```python
import torch

def _softmax(x, axis=-1):
    return torch.softmax(x, dim=axis)
def _gelu(x):
    return torch.nn.functional.gelu(x, approximate="tanh")
def _silu(x):
    return torch.nn.functional.silu(x)
def _sigmoid(x):
    return torch.sigmoid(x)
def _layernorm(x, axis=-1, eps=1e-5):
    m = x.mean(dim=axis, keepdim=True)
    v = x.var(dim=axis, keepdim=True, unbiased=False)
    return (x - m) / torch.sqrt(v + eps)
def _scatter_add(tgt, idx, src, axis=0):
    return tgt.clone().index_add_(axis, idx, src)
def _segment_sum(src, seg, n):
    out = torch.zeros((n,) + src.shape[1:], dtype=src.dtype)
    return out.index_add_(0, seg, src)
def _pad(x, pw):
    flat = tuple(v for pair in reversed(pw) for v in pair)
    return torch.nn.functional.pad(x, flat)

kernel_name = 'gated_mix__mha_block_cached__glu_gated_swiglu__decode'

def setup_inputs():

    _w = (lambda a: (lambda t: t.to(torch.bfloat16) if t.is_floating_point() else t)(torch.as_tensor(a)))
    def _A_setup():
        B, Sq, D = 4, 1, 512
        H, Sk, Dh = 32, 8192, 128
        return {
            "x": _w((lambda *s: torch.randn(*s, dtype=torch.bfloat16) * 0.05)(B, Sq, D)),
            "k_cache": _w((lambda *s: torch.randn(*s, dtype=torch.bfloat16) * 6.4)(B, H, Sk, Dh)),
            "v_cache": _w((lambda *s: torch.randn(*s, dtype=torch.bfloat16) * 0.2)(B, H, Sk, Dh)),
            "wq": _w((lambda *s: torch.randn(*s, dtype=torch.bfloat16) * 6.4)(D, H * Dh) * (D ** -0.5)),
            "wo": _w((lambda *s: torch.randn(*s, dtype=torch.bfloat16) * 0.2)(H * Dh, D) * ((H * Dh) ** -0.5)),
        }
    _A_in = _A_setup()

    def _B_setup():
        B, S, D, F = 4, 1, 512, 1792
        return {
            "x": _w((lambda *s: torch.randn(*s, dtype=torch.bfloat16) * 0.05)(B, S, D)),
            "w1": _w((lambda *s: torch.randn(*s, dtype=torch.bfloat16) * 0.8)(D, F) * (2.0 / D) ** 0.5),
            "w3": _w((lambda *s: torch.randn(*s, dtype=torch.bfloat16) * 0.8)(D, F) * (2.0 / D) ** 0.5),
            "w2": _w((lambda *s: torch.randn(*s, dtype=torch.bfloat16) * 0.8)(F, D) * (2.0 / F) ** 0.5),
        }
    _B_in = _B_setup()

    return {
        "A_x": _A_in["x"],
        "A_k_cache": _A_in["k_cache"],
        "A_v_cache": _A_in["v_cache"],
        "A_wq": _A_in["wq"],
        "A_wo": _A_in["wo"],
        "B_x": _B_in["x"],
        "B_w1": _B_in["w1"],
        "B_w3": _B_in["w3"],
        "B_w2": _B_in["w2"],
        "gate_w": _w((lambda *s: torch.randn(*s, dtype=torch.bfloat16) * 0.05)(512) * 16.0),
    }

def reference(A_x, A_k_cache, A_v_cache, A_wq, A_wo, B_x, B_w1, B_w3, B_w2, gate_w):
    def _A_ref(x, k_cache, v_cache, wq, wo):
        B, Sq, D = x.shape
        H, Dh = 32, 128
        q = (lambda x, *d: x.permute(*d))((x @ wq).reshape(B, Sq, H, Dh), 0, 2, 1, 3)
        scores = torch.einsum("bhqd,bhkd->bhqk", q, k_cache) * (Dh ** -0.5)
        p = _softmax(scores, axis=-1)
        o = torch.einsum("bhqk,bhkd->bhqd", p, v_cache)
        return (lambda x, *d: x.permute(*d))(o, 0, 2, 1, 3).reshape(B, Sq, H * Dh) @ wo

    def _B_ref(x, w1, w3, w2):
        gate = torch.einsum("bsd,df->bsf", x, w1)
        up = torch.einsum("bsd,df->bsf", x, w3)
        h = _silu(gate) * up
        out = torch.einsum("bsf,fd->bsd", h, w2)
        return out

    x = A_x
    g = _sigmoid((x * gate_w).sum(-1, keepdims=True))
    a = _A_ref(x, A_k_cache, A_v_cache, A_wq, A_wo)
    b = _B_ref(x, B_w1, B_w3, B_w2)
    return g * a + (1 - g) * b
```

```cpp
#include <hip/hip_runtime.h>
#include <hip/hip_bf16.h>

using u32x4 = __attribute__((ext_vector_type(4))) unsigned;
using f32x4 = __attribute__((ext_vector_type(4))) float;
using bf16x2 = __attribute__((ext_vector_type(2))) __bf16;

constexpr int B = 4, D = 512, H = 32, SK = 8192, DH = 128, F = 1792;
constexpr int NSPLIT = 4;
constexpr int KEYS_PER_WG = SK / NSPLIT;
constexpr int WAVES = 4;

__device__ __forceinline__ float bflo(unsigned u) { return __builtin_bit_cast(float, u << 16); }
__device__ __forceinline__ float bfhi(unsigned u) { return __builtin_bit_cast(float, u & 0xffff0000u); }
__device__ __forceinline__ float dot2(unsigned k, unsigned q, float acc) {
  return __builtin_amdgcn_fdot2_f32_bf16(__builtin_bit_cast(bf16x2, k), __builtin_bit_cast(bf16x2, q), acc, false);
}
__device__ __forceinline__ unsigned cvtpk(float lo, float hi) {
  unsigned r; asm volatile("v_cvt_pk_bf16_f32 %0, %1, %2" : "=v"(r) : "v"(lo), "v"(hi)); return r;
}
template <int CTRL> __device__ __forceinline__ float dpp(float x) {
  return __builtin_bit_cast(float, __builtin_amdgcn_mov_dpp(__builtin_bit_cast(int, x), CTRL, 0xf, 0xf, true));
}
constexpr int XOR1 = 0xB1, XOR2 = 0x4E, XOR7 = 0x141, XOR8 = 0x128;
__device__ __forceinline__ float row16_sum(float x) {
  x += dpp<XOR8>(x); x += dpp<XOR7>(x); x += dpp<XOR2>(x); x += dpp<XOR1>(x); return x;
}
__device__ __forceinline__ float xrow16_max(float x) {
  auto s = __builtin_amdgcn_permlane16_swap(__float_as_uint(x), __float_as_uint(x), false, false);
  x = fmaxf(__uint_as_float(s[0]), __uint_as_float(s[1]));
  auto t = __builtin_amdgcn_permlane32_swap(__float_as_uint(x), __float_as_uint(x), false, false);
  return fmaxf(__uint_as_float(t[0]), __uint_as_float(t[1]));
}
__device__ __forceinline__ float xrow16_sum(float x) {
  auto s = __builtin_amdgcn_permlane16_swap(__float_as_uint(x), __float_as_uint(x), false, false);
  x = __uint_as_float(s[0]) + __uint_as_float(s[1]);
  auto t = __builtin_amdgcn_permlane32_swap(__float_as_uint(x), __float_as_uint(x), false, false);
  return __uint_as_float(t[0]) + __uint_as_float(t[1]);
}

constexpr size_t WS_Q = 0;
constexpr size_t WS_Z = 65536;
constexpr size_t WS_ML = WS_Z + 28 * B * D * 4;
constexpr size_t WS_PO = WS_ML + B * H * NSPLIT * 2 * 4;
constexpr size_t WS_Y = WS_PO + B * H * NSPLIT * DH * 4;

__global__ void __launch_bounds__(256) k_pre(const unsigned short* __restrict__ x, const unsigned short* __restrict__ wq,
                                             const unsigned short* __restrict__ w1, const unsigned short* __restrict__ w3,
                                             const unsigned short* __restrict__ w2, unsigned char* __restrict__ ws) {
  __shared__ float xs[B][D];
  __shared__ float red[32][B][64];
  __shared__ float red2[32][B][64];
  __shared__ float hs[B][64];
  const int tid = threadIdx.x;
  for (int i = tid; i < B * D; i += 256) xs[i / D][i % D] = bflo(x[i]);
  __syncthreads();
  const int c8 = tid & 7, rg = tid >> 3;
  if (blockIdx.x < 64) {
    const int col0 = blockIdx.x * 64 + c8 * 8;
    float acc[B][8] = {};
#pragma unroll
    for (int i = 0; i < 16; ++i) {
      const int r = rg + 32 * i;
      const u32x4 w = *reinterpret_cast<const u32x4*>(wq + (size_t)r * (H * DH) + col0);
#pragma unroll
      for (int b = 0; b < B; ++b) {
        const float xv = xs[b][r];
#pragma unroll
        for (int k = 0; k < 4; ++k) { acc[b][2 * k] += xv * bflo(w[k]); acc[b][2 * k + 1] += xv * bfhi(w[k]); }
      }
    }
#pragma unroll
    for (int b = 0; b < B; ++b)
#pragma unroll
      for (int k = 0; k < 8; ++k) red[rg][b][c8 * 8 + k] = acc[b][k];
    __syncthreads();
    {
      const int b = tid >> 6, c = tid & 63;
      float s = 0.f;
      for (int g = 0; g < 32; ++g) s += red[g][b][c];
      reinterpret_cast<unsigned short*>(ws + WS_Q)[b * (H * DH) + blockIdx.x * 64 + c] = (unsigned short)(cvtpk(s, 0.f) & 0xffff);
    }
  } else {
    const int fb = blockIdx.x - 64;
    const int col0 = fb * 64 + c8 * 8;
    float a1[B][8] = {}, a3[B][8] = {};
#pragma unroll
    for (int i = 0; i < 16; ++i) {
      const int r = rg + 32 * i;
      const u32x4 u = *reinterpret_cast<const u32x4*>(w1 + (size_t)r * F + col0);
      const u32x4 v = *reinterpret_cast<const u32x4*>(w3 + (size_t)r * F + col0);
#pragma unroll
      for (int b = 0; b < B; ++b) {
        const float xv = xs[b][r];
#pragma unroll
        for (int k = 0; k < 4; ++k) {
          a1[b][2 * k] += xv * bflo(u[k]); a1[b][2 * k + 1] += xv * bfhi(u[k]);
          a3[b][2 * k] += xv * bflo(v[k]); a3[b][2 * k + 1] += xv * bfhi(v[k]);
        }
      }
    }
#pragma unroll
    for (int b = 0; b < B; ++b)
#pragma unroll
      for (int k = 0; k < 8; ++k) { red[rg][b][c8 * 8 + k] = a1[b][k]; red2[rg][b][c8 * 8 + k] = a3[b][k]; }
    __syncthreads();
    {
      const int b = tid >> 6, c = tid & 63;
      float g = 0.f, u = 0.f;
      for (int gg = 0; gg < 32; ++gg) { g += red[gg][b][c]; u += red2[gg][b][c]; }
      hs[b][c] = g / (1.f + __expf(-g)) * u;
    }
    __syncthreads();
    float z[B][2] = {};
    for (int f = 0; f < 64; ++f) {
      const unsigned w = *reinterpret_cast<const unsigned*>(w2 + (size_t)(fb * 64 + f) * D + 2 * tid);
#pragma unroll
      for (int b = 0; b < B; ++b) { z[b][0] += hs[b][f] * bflo(w); z[b][1] += hs[b][f] * bfhi(w); }
    }
    float* zp = reinterpret_cast<float*>(ws + WS_Z) + (size_t)fb * B * D;
#pragma unroll
    for (int b = 0; b < B; ++b) *reinterpret_cast<float2*>(zp + b * D + 2 * tid) = make_float2(z[b][0], z[b][1]);
  }
}

constexpr int U = 8;
__global__ void __launch_bounds__(256) k_attn(const unsigned short* __restrict__ kc, const unsigned short* __restrict__ vc,
                                              unsigned char* __restrict__ ws) {
  __shared__ float mrg[WAVES][4 + DH];
  const int bh = blockIdx.x / NSPLIT, s = blockIdx.x % NSPLIT;
  const int tid = threadIdx.x, lane = tid & 63, wave = __builtin_amdgcn_readfirstlane(tid >> 6), r = lane >> 4, c = lane & 15;
  constexpr float C2 = 0.088388347648318440f * 1.4426950408889634f;
  const u32x4 q = *reinterpret_cast<const u32x4*>(reinterpret_cast<const unsigned short*>(ws + WS_Q) + (size_t)bh * DH + 8 * c);
  constexpr int KEYS_PER_WAVE = KEYS_PER_WG / WAVES;
  constexpr int NIT = KEYS_PER_WAVE / (4 * U);
  const size_t base = ((size_t)bh * SK + (size_t)s * KEYS_PER_WG + (size_t)wave * KEYS_PER_WAVE) * DH + lane * 8;
  const u32x4* kp = reinterpret_cast<const u32x4*>(kc + base);
  const u32x4* vp = reinterpret_cast<const u32x4*>(vc + base);
  u32x4 Kn[U], Vn[U];
#pragma unroll
  for (int t = 0; t < U; ++t) { Kn[t] = __builtin_nontemporal_load(kp + t * 64); }
#pragma unroll
  for (int t = 0; t < U; ++t) { Vn[t] = __builtin_nontemporal_load(vp + t * 64); }
  float m = -INFINITY, l = 0.f, acc[8] = {};
  for (int it = 0; it < NIT; ++it) {
    u32x4 Kc[U], Vc[U];
#pragma unroll
    for (int t = 0; t < U; ++t) { Kc[t] = Kn[t]; Vc[t] = Vn[t]; }
    if (it + 1 < NIT) {
#pragma unroll
      for (int t = 0; t < U; ++t) { Kn[t] = __builtin_nontemporal_load(kp + ((it + 1) * U + t) * 64); }
#pragma unroll
      for (int t = 0; t < U; ++t) { Vn[t] = __builtin_nontemporal_load(vp + ((it + 1) * U + t) * 64); }
    }
    float sc[U];
#pragma unroll
    for (int t = 0; t < U; ++t) {
      float d = 0.f;
#pragma unroll
      for (int k = 0; k < 4; ++k) d = dot2(Kc[t][k], q[k], d);
      sc[t] = row16_sum(d) * C2;
    }
    float pm = sc[0];
#pragma unroll
    for (int t = 1; t < U; ++t) pm = fmaxf(pm, sc[t]);
    const float mn = fmaxf(m, pm);
    const float alpha = __builtin_amdgcn_exp2f(m - mn);
    m = mn;
    l *= alpha;
#pragma unroll
    for (int k = 0; k < 8; ++k) acc[k] *= alpha;
#pragma unroll
    for (int t = 0; t < U; ++t) {
      const float p = __builtin_amdgcn_exp2f(sc[t] - mn);
      l += p;
#pragma unroll
      for (int k = 0; k < 4; ++k) { acc[2 * k] += p * bflo(Vc[t][k]); acc[2 * k + 1] += p * bfhi(Vc[t][k]); }
    }
  }
  const float M = xrow16_max(m);
  const float w = __builtin_amdgcn_exp2f(m - M);
  l = xrow16_sum(l * w);
#pragma unroll
  for (int k = 0; k < 8; ++k) acc[k] = xrow16_sum(acc[k] * w);
  if (r == 0) {
    *reinterpret_cast<f32x4*>(&mrg[wave][4 + 8 * c]) = f32x4{acc[0], acc[1], acc[2], acc[3]};
    *reinterpret_cast<f32x4*>(&mrg[wave][4 + 8 * c + 4]) = f32x4{acc[4], acc[5], acc[6], acc[7]};
    if (c == 0) { mrg[wave][0] = M; mrg[wave][1] = l; }
  }
  __syncthreads();
  if (tid < DH) {
    float MM = -INFINITY;
#pragma unroll
    for (int wv = 0; wv < WAVES; ++wv) MM = fmaxf(MM, mrg[wv][0]);
    float L = 0.f, o = 0.f;
#pragma unroll
    for (int wv = 0; wv < WAVES; ++wv) { const float wt = __builtin_amdgcn_exp2f(mrg[wv][0] - MM); L += wt * mrg[wv][1]; o += wt * mrg[wv][4 + tid]; }
    reinterpret_cast<float*>(ws + WS_PO)[(size_t)blockIdx.x * DH + tid] = o;
    if (tid == 0) { reinterpret_cast<float*>(ws + WS_ML)[blockIdx.x * 2] = MM; reinterpret_cast<float*>(ws + WS_ML)[blockIdx.x * 2 + 1] = L; }
  }
}

__global__ void __launch_bounds__(256) k_post(const unsigned short* __restrict__ x, const unsigned short* __restrict__ wo,
                                              const unsigned short* __restrict__ gw, const unsigned char* __restrict__ ws,
                                              unsigned short* __restrict__ out) {
  __shared__ float os[B][H * DH];
  __shared__ float red[4][B][8];
  __shared__ float gs[B];
  const int tid = threadIdx.x, lane = tid & 63, wave = tid >> 6;
  const int n0 = blockIdx.x * 8;
  u32x4 w[16];
#pragma unroll
  for (int i = 0; i < 16; ++i) w[i] = *reinterpret_cast<const u32x4*>(wo + (size_t)(tid + 256 * i) * D + n0);
  const float* ml = reinterpret_cast<const float*>(ws + WS_ML);
  const float* po = reinterpret_cast<const float*>(ws + WS_PO);
  for (int i = tid; i < B * H * DH; i += 256) {
    const int bh = i / DH, d = i % DH;
    float M = -INFINITY;
#pragma unroll
    for (int s = 0; s < NSPLIT; ++s) M = fmaxf(M, ml[(bh * NSPLIT + s) * 2]);
    float L = 0.f, o = 0.f;
#pragma unroll
    for (int s = 0; s < NSPLIT; ++s) { const float wt = __builtin_amdgcn_exp2f(ml[(bh * NSPLIT + s) * 2] - M); L += wt * ml[(bh * NSPLIT + s) * 2 + 1]; o += wt * po[(size_t)(bh * NSPLIT + s) * DH + d]; }
    os[bh / H][(bh % H) * DH + d] = o / L;
  }
  {
    float g = 0.f;
    for (int i = lane; i < D; i += 64) g += bflo(x[wave * D + i]) * bflo(gw[i]);
    for (int o = 32; o > 0; o >>= 1) g += __shfl_xor(g, o);
    if (lane == 0) gs[wave] = 1.f / (1.f + __expf(-g));
  }
  __syncthreads();
  float acc[B][8] = {};
#pragma unroll
  for (int i = 0; i < 16; ++i)
#pragma unroll
    for (int b = 0; b < B; ++b) {
      const float ov = os[b][tid + 256 * i];
#pragma unroll
      for (int k = 0; k < 4; ++k) { acc[b][2 * k] += ov * bflo(w[i][k]); acc[b][2 * k + 1] += ov * bfhi(w[i][k]); }
    }
#pragma unroll
  for (int b = 0; b < B; ++b)
#pragma unroll
    for (int k = 0; k < 8; ++k) {
      float v = acc[b][k];
      for (int o = 32; o > 0; o >>= 1) v += __shfl_xor(v, o);
      if (lane == 0) red[wave][b][k] = v;
    }
  __syncthreads();
  if (tid < B * 8) {
    const int b = tid >> 3, k = tid & 7;
    const float a = red[0][b][k] + red[1][b][k] + red[2][b][k] + red[3][b][k];
    float z = 0.f;
    const float* zp = reinterpret_cast<const float*>(ws + WS_Z);
    for (int f = 0; f < 28; ++f) z += zp[(size_t)f * B * D + b * D + n0 + k];
    const float g = gs[b];
    out[b * D + n0 + k] = (unsigned short)(cvtpk(g * a + (1.f - g) * z, 0.f) & 0xffff);
  }
}

extern "C" void kernel_launch(void* const* d_in, const int* in_sizes, int n_in, void* d_out, int out_size, void* d_ws, size_t ws_size,
                              hipStream_t stream) {
  const unsigned short* x = (const unsigned short*)d_in[0];
  const unsigned short* kc = (const unsigned short*)d_in[1];
  const unsigned short* vc = (const unsigned short*)d_in[2];
  const unsigned short* wq = (const unsigned short*)d_in[3];
  const unsigned short* wo = (const unsigned short*)d_in[4];
  const unsigned short* w1 = (const unsigned short*)d_in[6];
  const unsigned short* w3 = (const unsigned short*)d_in[7];
  const unsigned short* w2 = (const unsigned short*)d_in[8];
  const unsigned short* gw = (const unsigned short*)d_in[9];
  unsigned char* ws = (unsigned char*)d_ws;
  k_pre<<<64 + 28, 256, 0, stream>>>(x, wq, w1, w3, w2, ws);
  k_attn<<<B * H * NSPLIT, 256, 0, stream>>>(kc, vc, ws);
  k_post<<<64, 256, 0, stream>>>(x, wo, gw, ws, (unsigned short*)d_out);
}
```

```cpp
#include <hip/hip_runtime.h>
#include <hip/hip_bf16.h>

#define GAS __attribute__((address_space(1)))
#define LAS __attribute__((address_space(3)))
typedef unsigned short u16;
typedef unsigned long long u64;
using u32x4 = __attribute__((ext_vector_type(4))) unsigned;
using u32x2 = __attribute__((ext_vector_type(2))) unsigned;
using f32x4 = __attribute__((ext_vector_type(4))) float;
using f32x2 = __attribute__((ext_vector_type(2))) float;
using bf16x2 = __attribute__((ext_vector_type(2))) __bf16;

constexpr int B = 4, D = 512, H = 32, SK = 8192, DH = 128, F = 1792;
constexpr int NCW = 4, NHW = 8, NWAVE = 1 + NCW + NHW + 1, NTHR = NWAVE * 64;
#define PACE 56
#define PACEQ 16
#define TK 72.0
#define TV 70.5
#define TOFF 100
#define WMID 20.0
#define WGAIN 5
#define WMIN 14
#define WMAX 27
#define CLRAUX 16
#define PCONT 0
#define XDEL 0
#define XSPR 0
#define XNT 0
constexpr int UNIT = 4096;
constexpr int NSLOT = 32, RING = NSLOT * UNIT;
constexpr int NKU = (SK / 2) / 16, NUNIT = 2 * NKU;
constexpr int NHC = 6, NHH = 4, NHELD = NHC * NCW + NHH * NHW;
constexpr int FS = 32;
constexpr int L_CTRL = RING;
constexpr int L_X = L_CTRL + 256;
constexpr int L_QP = L_X + D * 2;
constexpr int L_MP = L_QP + NHW * DH * 4;
constexpr int L_REC = L_MP + NHW * 64 * 4;
constexpr int L_SC = L_REC + NCW * DH * 4;
constexpr int L_YP = L_SC;
constexpr int LDS_BYTES = L_SC + NKU * 64;
static_assert(LDS_BYTES <= 159 * 1024 && NKU * 64 >= NHW * D * 4, "lds");
static_assert(WMIN <= (int)WMID && (int)WMID <= WMAX && WMAX <= 28, "window");
constexpr int WS_EPOCH = 0;
constexpr int WS_FLAG = 1024;
constexpr int WS_ML = 3072;
constexpr int WS_Y = 8192;
constexpr int WS_BYTES = WS_Y + B * 64 * 2048;
constexpr u64 TAG_STEP = 0x9E3779B97F4A7C15ull;
constexpr unsigned EMPTY = 0xFFFFFFFFu;
constexpr int SPIN_CAP = 1 << 18;
constexpr int NEVER = 1 << 20;

__device__ __forceinline__ float bflo(unsigned u) { return __builtin_bit_cast(float, u << 16); }
__device__ __forceinline__ float bfhi(unsigned u) { return __builtin_bit_cast(float, u & 0xffff0000u); }
__device__ __forceinline__ float dot2(unsigned a, unsigned b, float acc) {
  return __builtin_amdgcn_fdot2_f32_bf16(__builtin_bit_cast(bf16x2, a), __builtin_bit_cast(bf16x2, b), acc, false);
}
__device__ __forceinline__ unsigned pack_bf16(float lo, float hi) {
  const bf16x2 v = {(__bf16)lo, (__bf16)hi};
  return __builtin_bit_cast(unsigned, v);
}
__device__ __forceinline__ unsigned perm_lo(unsigned a, unsigned b) { return __builtin_amdgcn_perm(b, a, 0x05040100u); }
__device__ __forceinline__ unsigned perm_hi(unsigned a, unsigned b) { return __builtin_amdgcn_perm(b, a, 0x07060302u); }
template <int CTRL> __device__ __forceinline__ float dpp(float x) {
  return __builtin_bit_cast(float, __builtin_amdgcn_mov_dpp(__builtin_bit_cast(int, x), CTRL, 0xf, 0xf, true));
}
constexpr int XOR1 = 0xB1, ROR1 = 0x121, ROR2 = 0x122, ROR4 = 0x124, ROR8 = 0x128;
__device__ __forceinline__ float row16_sum(float x) { x += dpp<ROR8>(x); x += dpp<ROR4>(x); x += dpp<ROR2>(x); x += dpp<ROR1>(x); return x; }
__device__ __forceinline__ float swadd32(float a, float b) {
  auto t = __builtin_amdgcn_permlane32_swap(__float_as_uint(a), __float_as_uint(b), false, false);
  return __uint_as_float(t[0]) + __uint_as_float(t[1]);
}
__device__ __forceinline__ float swadd16(float a, float b) {
  auto t = __builtin_amdgcn_permlane16_swap(__float_as_uint(a), __float_as_uint(b), false, false);
  return __uint_as_float(t[0]) + __uint_as_float(t[1]);
}
__device__ __forceinline__ float xrow_sum(float x) { x = swadd16(x, x); return swadd32(x, x); }
__device__ __forceinline__ float xrow_max(float x) {
  auto s = __builtin_amdgcn_permlane16_swap(__float_as_uint(x), __float_as_uint(x), false, false);
  x = fmaxf(__uint_as_float(s[0]), __uint_as_float(s[1]));
  auto t = __builtin_amdgcn_permlane32_swap(__float_as_uint(x), __float_as_uint(x), false, false);
  return fmaxf(__uint_as_float(t[0]), __uint_as_float(t[1]));
}
__device__ __forceinline__ __amdgpu_buffer_rsrc_t mkrsrc(const void* p, int bytes) { return __builtin_amdgcn_make_buffer_rsrc((void*)p, 0, bytes, 0x00020000); }
__device__ __forceinline__ u32x4 ldw(__amdgpu_buffer_rsrc_t r, int voff, int soff) { return __builtin_amdgcn_raw_buffer_load_b128(r, voff, soff, 2); }
#define LDS_WAIT() asm volatile("s_waitcnt lgkmcnt(0)" ::: "memory")
#define VM_DRAIN() asm volatile("s_waitcnt vmcnt(0)" ::: "memory")
#define CFENCE() asm volatile("" ::: "memory")
#define BARRIER() asm volatile("s_waitcnt lgkmcnt(0)\n\ts_barrier" ::: "memory")
#define PAUSE(n) do { __builtin_amdgcn_sched_barrier(0); asm volatile("s_sleep %0" :: "i"(n) : "memory"); __builtin_amdgcn_sched_barrier(0); } while (0)

__device__ __forceinline__ void lds_arrive(LAS unsigned char* L, int idx, int lane) {
  LDS_WAIT();
  if (lane == 0) __hip_atomic_fetch_add((LAS int*)(L + L_CTRL) + idx, 1, __ATOMIC_RELAXED, __HIP_MEMORY_SCOPE_WORKGROUP);
}
__device__ __forceinline__ void lds_await(volatile LAS int* ctrl, int idx, int n) {
#pragma unroll 1
  for (int spin = 0; spin < SPIN_CAP; ++spin) { if (__builtin_amdgcn_readfirstlane(ctrl[idx]) >= n) break; __builtin_amdgcn_s_sleep(1); }
  CFENCE();
}

#define STR2(x) #x
#define STR(x) STR2(x)
constexpr int TAUK2 = 2 * (int)(TK * 100.0 * 256.0 / NUNIT), TAUV2 = 2 * (int)(TV * 100.0 * 256.0 / NUNIT), TAU02 = TAUK2 + TOFF * 256 / 16;
__device__ __forceinline__ void stream_run(int& u, int uend, unsigned& slot, unsigned& vo, int& due, int& w, int tau2, const unsigned char* sb, unsigned pa) {
  unsigned vo2, pv;
  asm volatile(
      "s_getpc_b64 s[40:41]\n"
      "1:\n\t"
      "s_add_u32 s40, s40, 3f-1b-%c[wmin]*128\n\t"
      "s_addc_u32 s41, s41, -1\n"
      "2:\n\t"
      "s_memrealtime s[44:45]\n\t"
      "s_lshl_b32 s42, %[w], 7\n\t"
      "s_add_u32 s42, s40, s42\n\t"
      "s_addc_u32 s43, s41, 0\n\t"
      "s_add_i32 s46, %[w], -1\n\t"
      "s_lshr_b32 s46, s46, 2\n\t"
      "s_sub_i32 s46, %[u], s46\n\t"
      "v_mov_b32 %[pv], s46\n\t"
      "v_add_u32 %[vo2], 0x1000, %[vo]\n\t"
      "s_add_u32 s47, %[slot], 0x1000\n\t"
      "s_mov_b32 m0, %[slot]\n\t"
      "s_setpc_b64 s[42:43]\n\t"
      ".p2align 7\n"
      "3:\n\t"
      ".set wcnt, %c[wmin]-1\n\t"
      ".rept %c[nvar]\n\t"
      "s_waitcnt (wcnt & 15) | ((wcnt >> 4) << 14) | 0x0F70\n\t"
      "global_load_lds_dwordx4 %[vo], %[sb] nt\n\t"
      "s_waitcnt (wcnt & 15) | ((wcnt >> 4) << 14) | 0x0F70\n\t"
      "global_load_lds_dwordx4 %[vo], %[sb] offset:1024 nt\n\t"
      "s_waitcnt (wcnt & 15) | ((wcnt >> 4) << 14) | 0x0F70\n\t"
      "global_load_lds_dwordx4 %[vo], %[sb] offset:2048 nt\n\t"
      "s_waitcnt (wcnt & 15) | ((wcnt >> 4) << 14) | 0x0F70\n\t"
      "global_load_lds_dwordx4 %[vo], %[sb] offset:3072 nt\n\t"
      "ds_write_b32 %[pa], %[pv]\n\t"
      "s_mov_b32 m0, s47\n\t"
      "s_waitcnt (wcnt & 15) | ((wcnt >> 4) << 14) | 0x0F70\n\t"
      "global_load_lds_dwordx4 %[vo2], %[sb] nt\n\t"
      "s_waitcnt (wcnt & 15) | ((wcnt >> 4) << 14) | 0x0F70\n\t"
      "global_load_lds_dwordx4 %[vo2], %[sb] offset:1024 nt\n\t"
      "s_waitcnt (wcnt & 15) | ((wcnt >> 4) << 14) | 0x0F70\n\t"
      "global_load_lds_dwordx4 %[vo2], %[sb] offset:2048 nt\n\t"
      "s_waitcnt (wcnt & 15) | ((wcnt >> 4) << 14) | 0x0F70\n\t"
      "global_load_lds_dwordx4 %[vo2], %[sb] offset:3072 nt\n\t"
      "s_branch 4f\n\t"
      ".p2align 7\n\t"
      ".set wcnt, wcnt+1\n\t"
      ".endr\n"
      "4:\n\t"
      "s_waitcnt lgkmcnt(0)\n\t"
      "v_add_u32 %[pv], 1, %[pv]\n\t"
      "s_lshl_b32 s44, s44, 8\n\t"
      "ds_write_b32 %[pa], %[pv]\n\t"
      "s_sub_i32 s44, s44, %[due]\n\t"
      "s_ashr_i32 s44, s44, 8\n\t"
      "s_mul_i32 s44, s44, %c[gain]\n\t"
      "s_add_i32 s44, s44, %c[wmid]\n\t"
      "s_ashr_i32 s44, s44, 8\n\t"
      "s_max_i32 s44, s44, %c[wmin]\n\t"
      "s_min_i32 %[w], s44, %c[wmax]\n\t"
      "s_add_i32 %[due], %[due], %[tau2]\n\t"
      "s_add_u32 %[slot], %[slot], 0x2000\n\t"
      "s_and_b32 %[slot], %[slot], %c[mask]\n\t"
      "v_add_u32 %[vo], 0x2000, %[vo]\n\t"
      "s_add_i32 %[u], %[u], 2\n\t"
      "s_cmp_lt_i32 %[u], %[uend]\n\t"
      "s_cbranch_scc1 2b"
      : [u] "+s"(u), [slot] "+s"(slot), [vo] "+v"(vo), [due] "+s"(due), [w] "+s"(w), [vo2] "=&v"(vo2), [pv] "=&v"(pv)
      : [uend] "s"(uend), [sb] "s"(sb), [pa] "v"(pa), [wmin] "i"(WMIN), [wmax] "i"(WMAX), [nvar] "i"(WMAX - WMIN + 1), [gain] "i"(WGAIN),
        [wmid] "i"((int)(WMID * 256)), [tau2] "s"(tau2), [mask] "i"(RING - 1)
      : "memory", "scc", "s40", "s41", "s42", "s43", "s44", "s45", "s46", "s47");
}
template <int CTRL> __device__ __forceinline__ int dppi(int x) { return __builtin_amdgcn_mov_dpp(x, CTRL, 0xf, 0xf, true); }

#define STAMP(i) do {} while (0)
#define STAMP_FLUSH() do {} while (0)
#define STAMP_LDS 0

__global__ void __launch_bounds__(NTHR) gated_mix_kernel(const u16* x, const u16* kc, const u16* vc, const u16* wq, const u16* wo, const u16* w1,
                                                         const u16* w3, const u16* w2, const u16* gw, u16* out, unsigned char* ws) {
  __shared__ __attribute__((aligned(16))) unsigned char lds_raw[LDS_BYTES + STAMP_LDS];
  LAS unsigned char* L = (LAS unsigned char*)lds_raw;
  volatile LAS int* ctrl = (volatile LAS int*)(L + L_CTRL);
  volatile LAS float* ctrlf = (volatile LAS float*)(L + L_CTRL);
  const int tid = threadIdx.x, lane = tid & 63, wave = __builtin_amdgcn_readfirstlane(tid >> 6);
  const int xg = blockIdx.x & 7, kk = blockIdx.x >> 3;
  const int b = kk >> 3, pj = 8 * xg + (kk & 7), h = pj >> 1, s = pj & 1;
  const int r4 = lane >> 4, c16 = lane & 15;
  STAMP(0);
  if (wave == 1) {
    const int wi = lane - 16;
    ctrl[lane] = (lane < 16 || lane >= 32) ? 0 : wi < NCW ? wi : wi < NCW + NHW ? NHC * NCW + wi - NCW : NEVER;
  }
  asm volatile("" : "+s"(x), "+s"(kc), "+s"(vc), "+s"(wq), "+s"(wo), "+s"(w1), "+s"(w3), "+s"(w2), "+s"(gw), "+s"(out), "+s"(ws));
  BARRIER();

  if (wave == 0) {
    __builtin_amdgcn_s_setprio(3);
    const size_t base = ((size_t)(b * H + h) * SK + (size_t)s * (SK / 2)) * DH * 2;
    const unsigned char* sb = (const unsigned char*)kc + base;
    const unsigned char* vb = (const unsigned char*)vc + base - NKU * UNIT;
    unsigned voff = lane * 16, slot = 0;
    int limit = NSLOT;
    int w = (int)WMID, u = 0;
    int due = (int)(unsigned)__builtin_amdgcn_s_memrealtime() * 256;
#pragma unroll 1
    for (;;) {
      stream_run(u, min(limit, u < NKU ? NKU : NUNIT), slot, voff, due, w, u == 0 ? TAU02 : u < NKU ? TAUK2 : TAUV2, u < NKU ? sb : vb, L_CTRL);
      if (u >= NUNIT) break;
#pragma unroll 1
      for (int spin = 0; spin < SPIN_CAP; ++spin) {
        int nx = ctrl[16 + c16];
        nx = min(nx, dppi<ROR8>(nx)); nx = min(nx, dppi<ROR4>(nx)); nx = min(nx, dppi<ROR2>(nx)); nx = min(nx, dppi<ROR1>(nx));
        limit = (__builtin_amdgcn_readfirstlane(nx) + NSLOT) & ~1;
        if (u < limit) break;
        VM_DRAIN();
        ctrl[0] = u;
        __builtin_amdgcn_s_sleep(1);
      }
    }
#define TAILPUB(n) if (4 * (n) < WMAX) { asm volatile("s_waitcnt vmcnt(%c0)" :: "i"(4 * (n)) : "memory"); ctrl[0] = NUNIT - (n); }
    TAILPUB(6) TAILPUB(5) TAILPUB(4) TAILPUB(3) TAILPUB(2) TAILPUB(1) TAILPUB(0)
    STAMP(1);
    return;
  }

  const __amdgpu_buffer_rsrc_t wsr = mkrsrc(ws, WS_BYTES);
  auto wg_max = [&]() __attribute__((always_inline)) {
    float mg = ctrlf[32 + (c16 < NCW + NHW ? c16 : 0)];
    mg = fmaxf(mg, dpp<ROR8>(mg)); mg = fmaxf(mg, dpp<ROR4>(mg)); mg = fmaxf(mg, dpp<ROR2>(mg)); mg = fmaxf(mg, dpp<ROR1>(mg));
    return mg;
  };

  if (wave == NWAVE - 1) {
#pragma unroll 1
    for (int i = 0; i < XDEL + XSPR * (kk & 31); ++i) PAUSE(4);
    const u32x4 xv = *(const GAS u32x4*)(x + b * D + 8 * lane), gv = *(const GAS u32x4*)(gw + 8 * lane);
    *(LAS u32x4*)(L + L_X + lane * 16) = xv;
    float gd = 0.f;
#pragma unroll
    for (int k = 0; k < 4; ++k) gd = dot2(xv[k], gv[k], gd);
    gd = xrow_sum(row16_sum(gd));
    const float g = 1.f / (1.f + __expf(-gd));
    ctrlf[48] = g;
    lds_arrive(L, 4, lane);
    u64 tag;
    asm volatile("s_load_dwordx2 %0, %1, 0x0\n\ts_waitcnt lgkmcnt(0)" : "=s"(tag) : "s"(ws + WS_EPOCH + b * 256) : "memory");
    tag += TAG_STEP;
#pragma unroll 1
    for (int spin = 0; spin < SPIN_CAP; ++spin) { if (__builtin_amdgcn_readfirstlane(ctrl[2]) >= NCW + NHW) break; __builtin_amdgcn_s_sleep(16); }
    CFENCE();
    const float mg = wg_max();
    float l = 0.f;
#pragma unroll 4
    for (int i = 0; i < NKU * 64 / 1024; ++i) {
      const f32x4 sc = *(const LAS f32x4*)(L + L_SC + i * 1024 + lane * 16);
      l += (__builtin_amdgcn_exp2f(sc[0] - mg) + __builtin_amdgcn_exp2f(sc[1] - mg)) + (__builtin_amdgcn_exp2f(sc[2] - mg) + __builtin_amdgcn_exp2f(sc[3] - mg));
    }
    l = xrow_sum(row16_sum(l));
    if (lane == 0) __builtin_amdgcn_raw_buffer_store_b64(u32x2{__float_as_uint(mg), __float_as_uint(l)}, wsr, WS_ML + (b * 64 + pj) * 8, 0, 16);
    const u32x4 e = {EMPTY, EMPTY, EMPTY, EMPTY};
#pragma unroll
    for (int i = 0; i < 2; ++i) __builtin_amdgcn_raw_buffer_store_b128(e, wsr, WS_Y + (b * 64 + pj) * 2048 + i * 1024 + lane * 16, 0, CLRAUX);
    VM_DRAIN();
    if (lane == 0) __hip_atomic_store((GAS u64*)(ws + WS_FLAG + (b * 64 + pj) * 8), tag, __ATOMIC_RELAXED, __HIP_MEMORY_SCOPE_AGENT);
#pragma unroll 1
    for (int i = 0; i < 12 + (kk & 31); ++i) PAUSE(12);
#pragma unroll 1
    for (int spin = 0; spin < SPIN_CAP; ++spin) {
      const u64 f = __hip_atomic_load((GAS u64*)(ws + WS_FLAG + (b * 64 + lane) * 8), __ATOMIC_RELAXED, __HIP_MEMORY_SCOPE_AGENT);
      if (__builtin_amdgcn_ballot_w64(f != tag) == 0ull) break;
      __builtin_amdgcn_s_sleep(16);
    }
    CFENCE();
    const u32x2 ml = __builtin_amdgcn_raw_buffer_load_b64(wsr, WS_ML + (b * 64 + (pj ^ 1)) * 8, 0, 16);
    if (pj == 0 && lane == 0) __hip_atomic_store((GAS u64*)(ws + WS_EPOCH + b * 256), tag, __ATOMIC_RELAXED, __HIP_MEMORY_SCOPE_AGENT);
    const float mo = __uint_as_float(ml[0]), mh = fmaxf(mg, mo), wt = __builtin_amdgcn_exp2f(mg - mh);
    ctrlf[49] = g * wt / (wt * l + __builtin_amdgcn_exp2f(mo - mh) * __uint_as_float(ml[1]));
    STAMP(4);
    BARRIER();
    BARRIER();
    const int off = PCONT ? WS_Y + (b * 64 + (lane >> 1)) * 2048 + pj * 32 + (lane & 1) * 16 : WS_Y + (b * 64 + pj) * 2048 + lane * 16;
    u32x4 ya, yb;
#pragma unroll 1
    for (int spin = 0; spin < SPIN_CAP; ++spin) {
      CFENCE();
      ya = __builtin_amdgcn_raw_buffer_load_b128(wsr, off, 0, 16);
      yb = __builtin_amdgcn_raw_buffer_load_b128(wsr, off + (PCONT ? 32 * 2048 : 1024), 0, 16);
      const unsigned mx = max(max(max(ya[0], ya[1]), max(ya[2], ya[3])), max(max(yb[0], yb[1]), max(yb[2], yb[3])));
      if (__builtin_amdgcn_ballot_w64(mx == EMPTY) == 0ull) break;
      __builtin_amdgcn_s_sleep(1);
    }
    float o[4];
#pragma unroll
    for (int k = 0; k < 4; ++k) o[k] = __uint_as_float(ya[k]) + __uint_as_float(yb[k]);
    float e0 = swadd32(o[0], o[2]), e1 = swadd32(o[1], o[3]);
    e0 = swadd16(e0, e0); e1 = swadd16(e1, e1);
    e0 += dpp<ROR8>(e0); e1 += dpp<ROR8>(e1); e0 += dpp<ROR4>(e0); e1 += dpp<ROR4>(e1); e0 += dpp<ROR2>(e0); e1 += dpp<ROR2>(e1);
    if ((lane & 30) == 0) *(GAS unsigned*)(out + b * D + 8 * pj + 4 * (lane & 1) + 2 * (lane >> 5)) = pack_bf16(e0, e1);
    STAMP(8);
    STAMP_FLUSH();
    return;
  }

  const int cw = wave - 1, hw = wave - 1 - NCW;
  int landed = 0;
  auto wait_unit = [&](int u) __attribute__((always_inline)) {
#pragma unroll 1
    for (int spin = 0; landed <= u && spin < SPIN_CAP; ++spin) {
      landed = __builtin_amdgcn_readfirstlane(ctrl[0]);
      if (landed > u) break;
      __builtin_amdgcn_s_sleep(1);
    }
    CFENCE();
  };
  auto take = [&](int u, int next, u32x4& a0, u32x4& a1, u32x4& a2, u32x4& a3) __attribute__((always_inline)) {
    wait_unit(u);
    const LAS u32x4* sp = (const LAS u32x4*)(L + (u & (NSLOT - 1)) * UNIT + lane * 16);
    a0 = sp[0]; a1 = sp[64]; a2 = sp[128]; a3 = sp[192];
    LDS_WAIT();
    ctrl[16 + cw] = next;
    asm volatile("" : "+v"(a0), "+v"(a1), "+v"(a2), "+v"(a3) :: "memory");
  };
  u32x4 q;
  auto build_q = [&]() __attribute__((always_inline)) {
    lds_await(ctrl, 1, NHW);
    const LAS f32x4* qp = (const LAS f32x4*)(L + L_QP + (2 * r4 * DH + 8 * c16) * 4);
    const f32x4 lo = qp[0] + qp[DH / 4], hi = qp[1] + qp[DH / 4 + 1];
    q[0] = pack_bf16(xrow_sum(lo[0]), xrow_sum(lo[1])); q[1] = pack_bf16(xrow_sum(lo[2]), xrow_sum(lo[3]));
    q[2] = pack_bf16(xrow_sum(hi[0]), xrow_sum(hi[1])); q[3] = pack_bf16(xrow_sum(hi[2]), xrow_sum(hi[3]));
  };
  constexpr float C2 = 0.088388347648318440f * 1.4426950408889634f;
  float mx = -INFINITY;
  auto score = [&](int u, const u32x4& k0, const u32x4& k1, const u32x4& k2, const u32x4& k3) __attribute__((always_inline)) {
    float d0 = 0.f, d1 = 0.f, d2 = 0.f, d3 = 0.f;
#pragma unroll
    for (int k = 0; k < 4; ++k) { d0 = dot2(k0[k], q[k], d0); d1 = dot2(k1[k], q[k], d1); d2 = dot2(k2[k], q[k], d2); d3 = dot2(k3[k], q[k], d3); }
    const f32x4 sc = {row16_sum(d0) * C2, row16_sum(d1) * C2, row16_sum(d2) * C2, row16_sum(d3) * C2};
    mx = fmaxf(fmaxf(mx, fmaxf(sc[0], sc[1])), fmaxf(sc[2], sc[3]));
    if (c16 == 0) *(LAS f32x4*)(L + L_SC + u * 64 + r4 * 16) = sc;
  };

  if (hw < 0) {
    {
      u32x4 hk[4 * NHC];
#pragma unroll
      for (int i = 0; i < NHC; ++i) take(cw + NCW * i, i + 1 < NHC ? cw + NCW * (i + 1) : NHELD + cw, hk[4 * i], hk[4 * i + 1], hk[4 * i + 2], hk[4 * i + 3]);
      STAMP(10);
      build_q();
      STAMP(3);
#pragma unroll
      for (int i = 0; i < NHC; ++i) { score(cw + NCW * i, hk[4 * i], hk[4 * i + 1], hk[4 * i + 2], hk[4 * i + 3]); __builtin_amdgcn_sched_barrier(0); }
    }
#pragma unroll 1
    for (int u = NHELD + cw; u < NKU; u += NCW) {
      u32x4 k0, k1, k2, k3;
      take(u, u + NCW, k0, k1, k2, k3);
      score(u, k0, k1, k2, k3);
    }
    ctrlf[32 + cw] = xrow_max(mx);
    lds_arrive(L, 2, lane);
    STAMP(11);
    lds_await(ctrl, 2, NCW + NHW);
    const float mg = wg_max();
    STAMP(12);
    float acc[8] = {0.f, 0.f, 0.f, 0.f, 0.f, 0.f, 0.f, 0.f};
#pragma unroll 1
    for (int u = NKU + cw; u < NUNIT; u += NCW) {
      const f32x4 sc = *(const LAS f32x4*)(L + L_SC + (u - NKU) * 64 + r4 * 16);
      float p[4];
#pragma unroll
      for (int t = 0; t < 4; ++t) { p[t] = __builtin_amdgcn_exp2f(sc[t] - mg); asm volatile("" : "+v"(p[t])); }
      u32x4 v[4];
      take(u, u + NCW, v[0], v[1], v[2], v[3]);
#pragma unroll
      for (int t = 0; t < 4; ++t)
#pragma unroll
        for (int k = 0; k < 4; ++k) { acc[2 * k] = fmaf(p[t], bflo(v[t][k]), acc[2 * k]); acc[2 * k + 1] = fmaf(p[t], bfhi(v[t][k]), acc[2 * k + 1]); }
    }
    STAMP(5);
    const float e0 = swadd16(swadd32(acc[0], acc[4]), swadd32(acc[2], acc[6])), e1 = swadd16(swadd32(acc[1], acc[5]), swadd32(acc[3], acc[7]));
    *(LAS f32x2*)(L + L_REC + (cw * DH + 8 * c16 + 2 * r4) * 4) = f32x2{e0, e1};
    BARRIER();
    return;
  }

  u32x4 wb[16];
  {
    const __amdgpu_buffer_rsrc_t rq = mkrsrc(wq, D * H * DH * 2);
    const int vo = ((64 * hw + 16 * r4) * (H * DH) + h * DH + 8 * c16) * 2;
#pragma unroll 1
    for (int i = 0; i < hw; ++i) PAUSE(PACEQ / 8);
#pragma unroll
    for (int i = 0; i < 16; ++i) { wb[i] = ldw(rq, vo, i * (H * DH * 2)); if (i < 15) PAUSE(PACEQ); }
    lds_await(ctrl, 4, 1);
    const u32x4 xa = *(const LAS u32x4*)(L + L_X + (64 * hw + 16 * r4) * 2), xc = *(const LAS u32x4*)(L + L_X + (64 * hw + 16 * r4 + 8) * 2);
    float a[8] = {0.f, 0.f, 0.f, 0.f, 0.f, 0.f, 0.f, 0.f};
#pragma unroll
    for (int i = 0; i < 8; ++i) {
      const unsigned xp = i < 4 ? xa[i] : xc[i - 4];
#pragma unroll
      for (int k = 0; k < 4; ++k) {
        a[2 * k] = dot2(perm_lo(wb[2 * i][k], wb[2 * i + 1][k]), xp, a[2 * k]);
        a[2 * k + 1] = dot2(perm_hi(wb[2 * i][k], wb[2 * i + 1][k]), xp, a[2 * k + 1]);
      }
    }
    const float e0 = swadd16(swadd32(a[0], a[4]), swadd32(a[2], a[6])), e1 = swadd16(swadd32(a[1], a[5]), swadd32(a[3], a[7]));
    *(LAS f32x2*)(L + L_QP + (hw * DH + 8 * c16 + 2 * r4) * 4) = f32x2{e0, e1};
    lds_arrive(L, 1, lane);
    STAMP(2);
    const int hb = NHC * NCW + hw;
#pragma unroll
    for (int i = 0; i < NHH; ++i) take(hb + NHW * i, i + 1 < NHH ? hb + NHW * (i + 1) : NEVER, wb[4 * i], wb[4 * i + 1], wb[4 * i + 2], wb[4 * i + 3]);
    STAMP(10);
    build_q();
#pragma unroll
    for (int i = 0; i < NHH; ++i) { score(hb + NHW * i, wb[4 * i], wb[4 * i + 1], wb[4 * i + 2], wb[4 * i + 3]); __builtin_amdgcn_sched_barrier(0); }
    ctrlf[32 + cw] = xrow_max(mx);
    lds_arrive(L, 2, lane);
  }
#pragma unroll 1
  for (int i = 0; i < hw; ++i) PAUSE(PACE / 8);
  float z[8] = {0.f, 0.f, 0.f, 0.f, 0.f, 0.f, 0.f, 0.f};
  if ((pj & 7) != 7) {
    const int sl = 7 * (pj >> 3) + (pj & 7), rs = lane >> 3;
    const u16* wp = ((lane & 4) ? w3 : w1) + (size_t)(64 * hw + 8 * rs) * F + FS * sl + 8 * (lane & 3);
#pragma unroll
    for (int i = 0; i < 8; ++i) { PAUSE(PACE); wb[i] = __builtin_nontemporal_load((const GAS u32x4*)(wp + i * F)); }
    const __amdgpu_buffer_rsrc_t r2 = mkrsrc(w2, F * D * 2);
    PAUSE(PACE);
    wb[8] = ldw(r2, ((FS * sl + 4 * hw) * D + 8 * lane) * 2, 0);
    __builtin_amdgcn_sched_barrier(0);
    const u32x4 xm = *(const LAS u32x4*)(L + L_X + (64 * hw + 8 * rs) * 2);
    float a[8] = {0.f, 0.f, 0.f, 0.f, 0.f, 0.f, 0.f, 0.f};
#pragma unroll
    for (int i = 0; i < 4; ++i)
#pragma unroll
      for (int k = 0; k < 4; ++k) {
        a[2 * k] = dot2(perm_lo(wb[2 * i][k], wb[2 * i + 1][k]), xm[i], a[2 * k]);
        a[2 * k + 1] = dot2(perm_hi(wb[2 * i][k], wb[2 * i + 1][k]), xm[i], a[2 * k + 1]);
      }
    float e0 = swadd16(swadd32(a[0], a[4]), swadd32(a[2], a[6])), e1 = swadd16(swadd32(a[1], a[5]), swadd32(a[3], a[7]));
    e0 += dpp<ROR8>(e0); e1 += dpp<ROR8>(e1);
    if ((lane & 8) == 0) *(LAS f32x2*)(L + L_MP + (hw * 64 + 8 * (lane & 7) + 2 * r4) * 4) = f32x2{e0, e1};
    lds_arrive(L, 3, lane);
#pragma unroll
    for (int i = 1; i < 4; ++i) { PAUSE(PACE); wb[8 + i] = ldw(r2, ((FS * sl + 4 * hw) * D + 8 * lane) * 2, i * (D * 2)); }
    lds_await(ctrl, 3, NHW);
    const LAS float* mp = (const LAS float*)(L + L_MP) + rs * 64 + 4 * hw + (lane & 3);
    float ga = mp[0], up = mp[32];
    ga += dpp<ROR8>(ga); ga = xrow_sum(ga); up += dpp<ROR8>(up); up = xrow_sum(up);
    const float hv = ga / (1.f + __expf(-ga)) * up * (1.f - ctrlf[48]);
#pragma unroll
    for (int i = 0; i < 4; ++i) {
      const float hs = __builtin_bit_cast(float, __builtin_amdgcn_readlane(__builtin_bit_cast(int, hv), i));
#pragma unroll
      for (int k = 0; k < 4; ++k) { z[2 * k] = fmaf(hs, bflo(wb[8 + i][k]), z[2 * k]); z[2 * k + 1] = fmaf(hs, bfhi(wb[8 + i][k]), z[2 * k + 1]); }
    }
  } else {
#pragma unroll 1
    for (int i = 0; i < 12; ++i) PAUSE(PACE);
  }
#pragma unroll
  for (int k = 0; k < 8; ++k) asm volatile("" : "+v"(z[k]));
  {
    const __amdgpu_buffer_rsrc_t ro = mkrsrc(wo, H * DH * D * 2);
#pragma unroll
    for (int i = 0; i < 16; ++i) { PAUSE(PACE); wb[i] = ldw(ro, ((h * DH + 16 * hw) * D + 8 * lane) * 2, i * (D * 2)); }
#pragma unroll
    for (int i = 0; i < 8; ++i)
#pragma unroll
      for (int k = 0; k < 4; ++k) {
        const unsigned lo = perm_lo(wb[2 * i][k], wb[2 * i + 1][k]), hi = perm_hi(wb[2 * i][k], wb[2 * i + 1][k]);
        wb[2 * i][k] = lo; wb[2 * i + 1][k] = hi;
      }
#pragma unroll
    for (int i = 0; i < 16; ++i) asm volatile("" : "+v"(wb[i]));
  }
  STAMP(4);
  BARRIER();
  STAMP(9);
  {
    const f32x2 a = *(const LAS f32x2*)(L + L_REC + (r4 * DH + 16 * hw + 2 * (lane & 7)) * 4);
    const float cf = ctrlf[49];
    const unsigned op = pack_bf16(xrow_sum(a[0]), xrow_sum(a[1]));
    float y[8] = {0.f, 0.f, 0.f, 0.f, 0.f, 0.f, 0.f, 0.f};
#pragma unroll
    for (int i = 0; i < 8; ++i) {
      const unsigned os = __builtin_amdgcn_readlane(op, i);
#pragma unroll
      for (int k = 0; k < 4; ++k) { y[2 * k] = dot2(wb[2 * i][k], os, y[2 * k]); y[2 * k + 1] = dot2(wb[2 * i + 1][k], os, y[2 * k + 1]); }
    }
    LAS f32x4* yp = (LAS f32x4*)(L + L_YP + (hw * D + 8 * lane) * 4);
    yp[0] = f32x4{fmaf(cf, y[0], z[0]), fmaf(cf, y[1], z[1]), fmaf(cf, y[2], z[2]), fmaf(cf, y[3], z[3])};
    yp[1] = f32x4{fmaf(cf, y[4], z[4]), fmaf(cf, y[5], z[5]), fmaf(cf, y[6], z[6]), fmaf(cf, y[7], z[7])};
  }
  BARRIER();
  STAMP(6);
  if (hw < 2) {
    const int t = hw * 64 + lane;
    const LAS f32x4* yp = (const LAS f32x4*)(L + L_YP) + t;
    f32x4 sum = yp[0];
#pragma unroll
    for (int w = 1; w < NHW; ++w) sum += yp[w * (D / 4)];
    u32x4 v;
#pragma unroll
    for (int k = 0; k < 4; ++k) v[k] = min(__float_as_uint(sum[k]), EMPTY - 1u);
    __builtin_amdgcn_raw_buffer_store_b128(v, wsr, PCONT ? WS_Y + (b * 64 + pj) * 2048 + t * 16 : WS_Y + (b * 64 + (t >> 1)) * 2048 + pj * 32 + (t & 1) * 16, 0, 16);
  }
  STAMP(7);
}

extern "C" void kernel_launch(void* const* d_in, const int* in_sizes, int n_in, void* d_out, int out_size, void* d_ws, size_t ws_size,
                              hipStream_t stream) {
  gated_mix_kernel<<<256, NTHR, 0, stream>>>((const u16*)d_in[0], (const u16*)d_in[1], (const u16*)d_in[2], (const u16*)d_in[3], (const u16*)d_in[4],
                                             (const u16*)d_in[6], (const u16*)d_in[7], (const u16*)d_in[8], (const u16*)d_in[9], (u16*)d_out,
                                             (unsigned char*)d_ws);
}
```

```cpp
#include <hip/hip_runtime.h>
#include <hip/hip_bf16.h>
#include <cstdio>

#define GAS __attribute__((address_space(1)))
#define LAS __attribute__((address_space(3)))
typedef unsigned short u16;
typedef unsigned long long u64;
using u32x4 = __attribute__((ext_vector_type(4))) unsigned;
using u32x2 = __attribute__((ext_vector_type(2))) unsigned;
using f32x4 = __attribute__((ext_vector_type(4))) float;
using f32x2 = __attribute__((ext_vector_type(2))) float;
using bf16x2 = __attribute__((ext_vector_type(2))) __bf16;

constexpr int B = 4, D = 512, H = 32, SK = 8192, DH = 128, F = 1792;
constexpr int NCW = 4, NHW = 8, NWAVE = 1 + NCW + NHW + 1, NTHR = NWAVE * 64;
#define PACE 56
#define PACEQ 16
#define TK 72.0
#define TV 70.5
#define TOFF 100
#define WMID 20.0
#define WGAIN 5
#define WMIN 14
#define WMAX 27
#define CLRAUX 16
#define PCONT 0
#define XDEL 0
#define XSPR 0
#define XNT 0
constexpr int UNIT = 4096;
constexpr int NSLOT = 32, RING = NSLOT * UNIT;
constexpr int NKU = (SK / 2) / 16, NUNIT = 2 * NKU;
constexpr int NHC = 6, NHH = 4, NHELD = NHC * NCW + NHH * NHW;
constexpr int FS = 32;
constexpr int L_CTRL = RING;
constexpr int L_X = L_CTRL + 256;
constexpr int L_QP = L_X + D * 2;
constexpr int L_MP = L_QP + NHW * DH * 4;
constexpr int L_REC = L_MP + NHW * 64 * 4;
constexpr int L_SC = L_REC + NCW * DH * 4;
constexpr int L_YP = L_SC;
constexpr int LDS_BYTES = L_SC + NKU * 64;
static_assert(LDS_BYTES <= 159 * 1024 && NKU * 64 >= NHW * D * 4, "lds");
static_assert(WMIN <= (int)WMID && (int)WMID <= WMAX && WMAX <= 28, "window");
constexpr int WS_EPOCH = 0;
constexpr int WS_FLAG = 1024;
constexpr int WS_ML = 3072;
constexpr int WS_Y = 8192;
constexpr int WS_BYTES = WS_Y + B * 64 * 2048;
constexpr u64 TAG_STEP = 0x9E3779B97F4A7C15ull;
constexpr unsigned EMPTY = 0xFFFFFFFFu;
constexpr int SPIN_CAP = 1 << 18;
constexpr int NEVER = 1 << 20;

__device__ __forceinline__ float bflo(unsigned u) { return __builtin_bit_cast(float, u << 16); }
__device__ __forceinline__ float bfhi(unsigned u) { return __builtin_bit_cast(float, u & 0xffff0000u); }
__device__ __forceinline__ float dot2(unsigned a, unsigned b, float acc) {
  return __builtin_amdgcn_fdot2_f32_bf16(__builtin_bit_cast(bf16x2, a), __builtin_bit_cast(bf16x2, b), acc, false);
}
__device__ __forceinline__ unsigned pack_bf16(float lo, float hi) {
  const bf16x2 v = {(__bf16)lo, (__bf16)hi};
  return __builtin_bit_cast(unsigned, v);
}
__device__ __forceinline__ unsigned perm_lo(unsigned a, unsigned b) { return __builtin_amdgcn_perm(b, a, 0x05040100u); }
__device__ __forceinline__ unsigned perm_hi(unsigned a, unsigned b) { return __builtin_amdgcn_perm(b, a, 0x07060302u); }
template <int CTRL> __device__ __forceinline__ float dpp(float x) {
  return __builtin_bit_cast(float, __builtin_amdgcn_mov_dpp(__builtin_bit_cast(int, x), CTRL, 0xf, 0xf, true));
}
constexpr int XOR1 = 0xB1, ROR1 = 0x121, ROR2 = 0x122, ROR4 = 0x124, ROR8 = 0x128;
__device__ __forceinline__ float row16_sum(float x) { x += dpp<ROR8>(x); x += dpp<ROR4>(x); x += dpp<ROR2>(x); x += dpp<ROR1>(x); return x; }
__device__ __forceinline__ float swadd32(float a, float b) {
  auto t = __builtin_amdgcn_permlane32_swap(__float_as_uint(a), __float_as_uint(b), false, false);
  return __uint_as_float(t[0]) + __uint_as_float(t[1]);
}
__device__ __forceinline__ float swadd16(float a, float b) {
  auto t = __builtin_amdgcn_permlane16_swap(__float_as_uint(a), __float_as_uint(b), false, false);
  return __uint_as_float(t[0]) + __uint_as_float(t[1]);
}
__device__ __forceinline__ float xrow_sum(float x) { x = swadd16(x, x); return swadd32(x, x); }
__device__ __forceinline__ float xrow_max(float x) {
  auto s = __builtin_amdgcn_permlane16_swap(__float_as_uint(x), __float_as_uint(x), false, false);
  x = fmaxf(__uint_as_float(s[0]), __uint_as_float(s[1]));
  auto t = __builtin_amdgcn_permlane32_swap(__float_as_uint(x), __float_as_uint(x), false, false);
  return fmaxf(__uint_as_float(t[0]), __uint_as_float(t[1]));
}
__device__ __forceinline__ __amdgpu_buffer_rsrc_t mkrsrc(const void* p, int bytes) { return __builtin_amdgcn_make_buffer_rsrc((void*)p, 0, bytes, 0x00020000); }
__device__ __forceinline__ u32x4 ldw(__amdgpu_buffer_rsrc_t r, int voff, int soff) { return __builtin_amdgcn_raw_buffer_load_b128(r, voff, soff, 2); }
#define LDS_WAIT() asm volatile("s_waitcnt lgkmcnt(0)" ::: "memory")
#define VM_DRAIN() asm volatile("s_waitcnt vmcnt(0)" ::: "memory")
#define CFENCE() asm volatile("" ::: "memory")
#define BARRIER() asm volatile("s_waitcnt lgkmcnt(0)\n\ts_barrier" ::: "memory")
#define PAUSE(n) do { __builtin_amdgcn_sched_barrier(0); asm volatile("s_sleep %0" :: "i"(n) : "memory"); __builtin_amdgcn_sched_barrier(0); } while (0)

__device__ __forceinline__ void lds_arrive(LAS unsigned char* L, int idx, int lane) {
  LDS_WAIT();
  if (lane == 0) __hip_atomic_fetch_add((LAS int*)(L + L_CTRL) + idx, 1, __ATOMIC_RELAXED, __HIP_MEMORY_SCOPE_WORKGROUP);
}
__device__ __forceinline__ void lds_await(volatile LAS int* ctrl, int idx, int n) {
#pragma unroll 1
  for (int spin = 0; spin < SPIN_CAP; ++spin) { if (__builtin_amdgcn_readfirstlane(ctrl[idx]) >= n) break; __builtin_amdgcn_s_sleep(1); }
  CFENCE();
}

#define STR2(x) #x
#define STR(x) STR2(x)
constexpr int TAUK2 = 2 * (int)(TK * 100.0 * 256.0 / NUNIT), TAUV2 = 2 * (int)(TV * 100.0 * 256.0 / NUNIT), TAU02 = TAUK2 + TOFF * 256 / 16;
__device__ __forceinline__ void stream_run(int& u, int uend, unsigned& slot, unsigned& vo, int& due, int& w, int tau2, const unsigned char* sb, unsigned pa) {
  unsigned vo2, pv;
  asm volatile(
      "s_getpc_b64 s[40:41]\n"
      "1:\n\t"
      "s_add_u32 s40, s40, 3f-1b-%c[wmin]*128\n\t"
      "s_addc_u32 s41, s41, -1\n"
      "2:\n\t"
      "s_memrealtime s[44:45]\n\t"
      "s_lshl_b32 s42, %[w], 7\n\t"
      "s_add_u32 s42, s40, s42\n\t"
      "s_addc_u32 s43, s41, 0\n\t"
      "s_add_i32 s46, %[w], -1\n\t"
      "s_lshr_b32 s46, s46, 2\n\t"
      "s_sub_i32 s46, %[u], s46\n\t"
      "v_mov_b32 %[pv], s46\n\t"
      "v_add_u32 %[vo2], 0x1000, %[vo]\n\t"
      "s_add_u32 s47, %[slot], 0x1000\n\t"
      "s_mov_b32 m0, %[slot]\n\t"
      "s_setpc_b64 s[42:43]\n\t"
      ".p2align 7\n"
      "3:\n\t"
      ".set wcnt, %c[wmin]-1\n\t"
      ".rept %c[nvar]\n\t"
      "s_waitcnt (wcnt & 15) | ((wcnt >> 4) << 14) | 0x0F70\n\t"
      "global_load_lds_dwordx4 %[vo], %[sb] nt\n\t"
      "s_waitcnt (wcnt & 15) | ((wcnt >> 4) << 14) | 0x0F70\n\t"
      "global_load_lds_dwordx4 %[vo], %[sb] offset:1024 nt\n\t"
      "s_waitcnt (wcnt & 15) | ((wcnt >> 4) << 14) | 0x0F70\n\t"
      "global_load_lds_dwordx4 %[vo], %[sb] offset:2048 nt\n\t"
      "s_waitcnt (wcnt & 15) | ((wcnt >> 4) << 14) | 0x0F70\n\t"
      "global_load_lds_dwordx4 %[vo], %[sb] offset:3072 nt\n\t"
      "ds_write_b32 %[pa], %[pv]\n\t"
      "s_mov_b32 m0, s47\n\t"
      "s_waitcnt (wcnt & 15) | ((wcnt >> 4) << 14) | 0x0F70\n\t"
      "global_load_lds_dwordx4 %[vo2], %[sb] nt\n\t"
      "s_waitcnt (wcnt & 15) | ((wcnt >> 4) << 14) | 0x0F70\n\t"
      "global_load_lds_dwordx4 %[vo2], %[sb] offset:1024 nt\n\t"
      "s_waitcnt (wcnt & 15) | ((wcnt >> 4) << 14) | 0x0F70\n\t"
      "global_load_lds_dwordx4 %[vo2], %[sb] offset:2048 nt\n\t"
      "s_waitcnt (wcnt & 15) | ((wcnt >> 4) << 14) | 0x0F70\n\t"
      "global_load_lds_dwordx4 %[vo2], %[sb] offset:3072 nt\n\t"
      "s_branch 4f\n\t"
      ".p2align 7\n\t"
      ".set wcnt, wcnt+1\n\t"
      ".endr\n"
      "4:\n\t"
      "s_waitcnt lgkmcnt(0)\n\t"
      "v_add_u32 %[pv], 1, %[pv]\n\t"
      "s_lshl_b32 s44, s44, 8\n\t"
      "ds_write_b32 %[pa], %[pv]\n\t"
      "s_sub_i32 s44, s44, %[due]\n\t"
      "s_ashr_i32 s44, s44, 8\n\t"
      "s_mul_i32 s44, s44, %c[gain]\n\t"
      "s_add_i32 s44, s44, %c[wmid]\n\t"
      "s_ashr_i32 s44, s44, 8\n\t"
      "s_max_i32 s44, s44, %c[wmin]\n\t"
      "s_min_i32 %[w], s44, %c[wmax]\n\t"
      "s_add_i32 %[due], %[due], %[tau2]\n\t"
      "s_add_u32 %[slot], %[slot], 0x2000\n\t"
      "s_and_b32 %[slot], %[slot], %c[mask]\n\t"
      "v_add_u32 %[vo], 0x2000, %[vo]\n\t"
      "s_add_i32 %[u], %[u], 2\n\t"
      "s_cmp_lt_i32 %[u], %[uend]\n\t"
      "s_cbranch_scc1 2b"
      : [u] "+s"(u), [slot] "+s"(slot), [vo] "+v"(vo), [due] "+s"(due), [w] "+s"(w), [vo2] "=&v"(vo2), [pv] "=&v"(pv)
      : [uend] "s"(uend), [sb] "s"(sb), [pa] "v"(pa), [wmin] "i"(WMIN), [wmax] "i"(WMAX), [nvar] "i"(WMAX - WMIN + 1), [gain] "i"(WGAIN),
        [wmid] "i"((int)(WMID * 256)), [tau2] "s"(tau2), [mask] "i"(RING - 1)
      : "memory", "scc", "s40", "s41", "s42", "s43", "s44", "s45", "s46", "s47");
}
template <int CTRL> __device__ __forceinline__ int dppi(int x) { return __builtin_amdgcn_mov_dpp(x, CTRL, 0xf, 0xf, true); }

#define STAMP(i) do {} while (0)
#define STAMP_FLUSH() do {} while (0)
#define STAMP_LDS 0

__global__ void __launch_bounds__(NTHR) gated_mix_kernel(const u16* x, const u16* kc, const u16* vc, const u16* wq, const u16* wo, const u16* w1,
                                                         const u16* w3, const u16* w2, const u16* gw, u16* out, unsigned char* ws) {
  __shared__ __attribute__((aligned(16))) unsigned char lds_raw[LDS_BYTES + STAMP_LDS];
  LAS unsigned char* L = (LAS unsigned char*)lds_raw;
  volatile LAS int* ctrl = (volatile LAS int*)(L + L_CTRL);
  volatile LAS float* ctrlf = (volatile LAS float*)(L + L_CTRL);
  const int tid = threadIdx.x, lane = tid & 63, wave = __builtin_amdgcn_readfirstlane(tid >> 6);
  const int xg = blockIdx.x & 7, kk = blockIdx.x >> 3;
  const int b = kk >> 3, pj = 8 * xg + (kk & 7), h = pj >> 1, s = pj & 1;
  const int r4 = lane >> 4, c16 = lane & 15;
  STAMP(0);
  if (wave == 1) {
    const int wi = lane - 16;
    ctrl[lane] = (lane < 16 || lane >= 32) ? 0 : wi < NCW ? wi : wi < NCW + NHW ? NHC * NCW + wi - NCW : NEVER;
  }
  asm volatile("" : "+s"(x), "+s"(kc), "+s"(vc), "+s"(wq), "+s"(wo), "+s"(w1), "+s"(w3), "+s"(w2), "+s"(gw), "+s"(out), "+s"(ws));
  BARRIER();

  if (wave == 0) {
    __builtin_amdgcn_s_setprio(3);
    const size_t base = ((size_t)(b * H + h) * SK + (size_t)s * (SK / 2)) * DH * 2;
    const unsigned char* sb = (const unsigned char*)kc + base;
    const unsigned char* vb = (const unsigned char*)vc + base - NKU * UNIT;
    unsigned voff = lane * 16, slot = 0;
    int limit = NSLOT;
    int w = (int)WMID, u = 0;
    int due = (int)(unsigned)__builtin_amdgcn_s_memrealtime() * 256;
#pragma unroll 1
    for (;;) {
      stream_run(u, min(limit, u < NKU ? NKU : NUNIT), slot, voff, due, w, u == 0 ? TAU02 : u < NKU ? TAUK2 : TAUV2, u < NKU ? sb : vb, L_CTRL);
      if (u >= NUNIT) break;
#pragma unroll 1
      for (int spin = 0; spin < SPIN_CAP; ++spin) {
        int nx = ctrl[16 + c16];
        nx = min(nx, dppi<ROR8>(nx)); nx = min(nx, dppi<ROR4>(nx)); nx = min(nx, dppi<ROR2>(nx)); nx = min(nx, dppi<ROR1>(nx));
        limit = (__builtin_amdgcn_readfirstlane(nx) + NSLOT) & ~1;
        if (u < limit) break;
        VM_DRAIN();
        ctrl[0] = u;
        __builtin_amdgcn_s_sleep(1);
      }
    }
#define TAILPUB(n) if (4 * (n) < WMAX) { asm volatile("s_waitcnt vmcnt(%c0)" :: "i"(4 * (n)) : "memory"); ctrl[0] = NUNIT - (n); }
    TAILPUB(6) TAILPUB(5) TAILPUB(4) TAILPUB(3) TAILPUB(2) TAILPUB(1) TAILPUB(0)
    STAMP(1);
    return;
  }

  const __amdgpu_buffer_rsrc_t wsr = mkrsrc(ws, WS_BYTES);
  auto wg_max = [&]() __attribute__((always_inline)) {
    float mg = ctrlf[32 + (c16 < NCW + NHW ? c16 : 0)];
    mg = fmaxf(mg, dpp<ROR8>(mg)); mg = fmaxf(mg, dpp<ROR4>(mg)); mg = fmaxf(mg, dpp<ROR2>(mg)); mg = fmaxf(mg, dpp<ROR1>(mg));
    return mg;
  };

  if (wave == NWAVE - 1) {
#pragma unroll 1
    for (int i = 0; i < XDEL + XSPR * (kk & 31); ++i) PAUSE(4);
    const u32x4 xv = *(const GAS u32x4*)(x + b * D + 8 * lane), gv = *(const GAS u32x4*)(gw + 8 * lane);
    *(LAS u32x4*)(L + L_X + lane * 16) = xv;
    float gd = 0.f;
#pragma unroll
    for (int k = 0; k < 4; ++k) gd = dot2(xv[k], gv[k], gd);
    gd = xrow_sum(row16_sum(gd));
    const float g = 1.f / (1.f + __expf(-gd));
    ctrlf[48] = g;
    lds_arrive(L, 4, lane);
    u64 tag;
    asm volatile("s_load_dwordx2 %0, %1, 0x0\n\ts_waitcnt lgkmcnt(0)" : "=s"(tag) : "s"(ws + WS_EPOCH + b * 256) : "memory");
    tag += TAG_STEP;
#pragma unroll 1
    for (int spin = 0; spin < SPIN_CAP; ++spin) { if (__builtin_amdgcn_readfirstlane(ctrl[2]) >= NCW + NHW) break; __builtin_amdgcn_s_sleep(16); }
    CFENCE();
    const float mg = wg_max();
    float l = 0.f;
#pragma unroll 4
    for (int i = 0; i < NKU * 64 / 1024; ++i) {
      const f32x4 sc = *(const LAS f32x4*)(L + L_SC + i * 1024 + lane * 16);
      l += (__builtin_amdgcn_exp2f(sc[0] - mg) + __builtin_amdgcn_exp2f(sc[1] - mg)) + (__builtin_amdgcn_exp2f(sc[2] - mg) + __builtin_amdgcn_exp2f(sc[3] - mg));
    }
    l = xrow_sum(row16_sum(l));
    if (lane == 0) __builtin_amdgcn_raw_buffer_store_b64(u32x2{__float_as_uint(mg), __float_as_uint(l)}, wsr, WS_ML + (b * 64 + pj) * 8, 0, 16);
    const u32x4 e = {EMPTY, EMPTY, EMPTY, EMPTY};
#pragma unroll
    for (int i = 0; i < 2; ++i) __builtin_amdgcn_raw_buffer_store_b128(e, wsr, WS_Y + (b * 64 + pj) * 2048 + i * 1024 + lane * 16, 0, CLRAUX);
    VM_DRAIN();
    if (lane == 0) __hip_atomic_store((GAS u64*)(ws + WS_FLAG + (b * 64 + pj) * 8), tag, __ATOMIC_RELAXED, __HIP_MEMORY_SCOPE_AGENT);
#pragma unroll 1
    for (int i = 0; i < 12 + (kk & 31); ++i) PAUSE(12);
#pragma unroll 1
    for (int spin = 0; spin < SPIN_CAP; ++spin) {
      const u64 f = __hip_atomic_load((GAS u64*)(ws + WS_FLAG + (b * 64 + lane) * 8), __ATOMIC_RELAXED, __HIP_MEMORY_SCOPE_AGENT);
      if (__builtin_amdgcn_ballot_w64(f != tag) == 0ull) break;
      __builtin_amdgcn_s_sleep(16);
    }
    CFENCE();
    const u32x2 ml = __builtin_amdgcn_raw_buffer_load_b64(wsr, WS_ML + (b * 64 + (pj ^ 1)) * 8, 0, 16);
    if (pj == 0 && lane == 0) __hip_atomic_store((GAS u64*)(ws + WS_EPOCH + b * 256), tag, __ATOMIC_RELAXED, __HIP_MEMORY_SCOPE_AGENT);
    const float mo = __uint_as_float(ml[0]), mh = fmaxf(mg, mo), wt = __builtin_amdgcn_exp2f(mg - mh);
    ctrlf[49] = g * wt / (wt * l + __builtin_amdgcn_exp2f(mo - mh) * __uint_as_float(ml[1]));
    STAMP(4);
    BARRIER();
    BARRIER();
    const int off = PCONT ? WS_Y + (b * 64 + (lane >> 1)) * 2048 + pj * 32 + (lane & 1) * 16 : WS_Y + (b * 64 + pj) * 2048 + lane * 16;
    u32x4 ya, yb;
#pragma unroll 1
    for (int spin = 0; spin < SPIN_CAP; ++spin) {
      CFENCE();
      ya = __builtin_amdgcn_raw_buffer_load_b128(wsr, off, 0, 16);
      yb = __builtin_amdgcn_raw_buffer_load_b128(wsr, off + (PCONT ? 32 * 2048 : 1024), 0, 16);
      const unsigned mx = max(max(max(ya[0], ya[1]), max(ya[2], ya[3])), max(max(yb[0], yb[1]), max(yb[2], yb[3])));
      if (__builtin_amdgcn_ballot_w64(mx == EMPTY) == 0ull) break;
      __builtin_amdgcn_s_sleep(1);
    }
    float o[4];
#pragma unroll
    for (int k = 0; k < 4; ++k) o[k] = __uint_as_float(ya[k]) + __uint_as_float(yb[k]);
    float e0 = swadd32(o[0], o[2]), e1 = swadd32(o[1], o[3]);
    e0 = swadd16(e0, e0); e1 = swadd16(e1, e1);
    e0 += dpp<ROR8>(e0); e1 += dpp<ROR8>(e1); e0 += dpp<ROR4>(e0); e1 += dpp<ROR4>(e1); e0 += dpp<ROR2>(e0); e1 += dpp<ROR2>(e1);
    if ((lane & 30) == 0) *(GAS unsigned*)(out + b * D + 8 * pj + 4 * (lane & 1) + 2 * (lane >> 5)) = pack_bf16(e0, e1);
    STAMP(8);
    STAMP_FLUSH();
    return;
  }

  const int cw = wave - 1, hw = wave - 1 - NCW;
  int landed = 0;
  auto wait_unit = [&](int u) __attribute__((always_inline)) {
#pragma unroll 1
    for (int spin = 0; landed <= u && spin < SPIN_CAP; ++spin) {
      landed = __builtin_amdgcn_readfirstlane(ctrl[0]);
      if (landed > u) break;
      __builtin_amdgcn_s_sleep(1);
    }
    CFENCE();
  };
  auto take = [&](int u, int next, u32x4& a0, u32x4& a1, u32x4& a2, u32x4& a3) __attribute__((always_inline)) {
    wait_unit(u);
    const LAS u32x4* sp = (const LAS u32x4*)(L + (u & (NSLOT - 1)) * UNIT + lane * 16);
    a0 = sp[0]; a1 = sp[64]; a2 = sp[128]; a3 = sp[192];
    LDS_WAIT();
    ctrl[16 + cw] = next;
    asm volatile("" : "+v"(a0), "+v"(a1), "+v"(a2), "+v"(a3) :: "memory");
  };
  u32x4 q;
  auto build_q = [&]() __attribute__((always_inline)) {
    lds_await(ctrl, 1, NHW);
    const LAS f32x4* qp = (const LAS f32x4*)(L + L_QP + (2 * r4 * DH + 8 * c16) * 4);
    const f32x4 lo = qp[0] + qp[DH / 4], hi = qp[1] + qp[DH / 4 + 1];
    q[0] = pack_bf16(xrow_sum(lo[0]), xrow_sum(lo[1])); q[1] = pack_bf16(xrow_sum(lo[2]), xrow_sum(lo[3]));
    q[2] = pack_bf16(xrow_sum(hi[0]), xrow_sum(hi[1])); q[3] = pack_bf16(xrow_sum(hi[2]), xrow_sum(hi[3]));
  };
  constexpr float C2 = 0.088388347648318440f * 1.4426950408889634f;
  float mx = -INFINITY;
  auto score = [&](int u, const u32x4& k0, const u32x4& k1, const u32x4& k2, const u32x4& k3) __attribute__((always_inline)) {
    float d0 = 0.f, d1 = 0.f, d2 = 0.f, d3 = 0.f;
#pragma unroll
    for (int k = 0; k < 4; ++k) { d0 = dot2(k0[k], q[k], d0); d1 = dot2(k1[k], q[k], d1); d2 = dot2(k2[k], q[k], d2); d3 = dot2(k3[k], q[k], d3); }
    const f32x4 sc = {row16_sum(d0) * C2, row16_sum(d1) * C2, row16_sum(d2) * C2, row16_sum(d3) * C2};
    mx = fmaxf(fmaxf(mx, fmaxf(sc[0], sc[1])), fmaxf(sc[2], sc[3]));
    if (c16 == 0) *(LAS f32x4*)(L + L_SC + u * 64 + r4 * 16) = sc;
  };

  if (hw < 0) {
    {
      u32x4 hk[4 * NHC];
#pragma unroll
      for (int i = 0; i < NHC; ++i) take(cw + NCW * i, i + 1 < NHC ? cw + NCW * (i + 1) : NHELD + cw, hk[4 * i], hk[4 * i + 1], hk[4 * i + 2], hk[4 * i + 3]);
      STAMP(10);
      build_q();
      STAMP(3);
#pragma unroll
      for (int i = 0; i < NHC; ++i) { score(cw + NCW * i, hk[4 * i], hk[4 * i + 1], hk[4 * i + 2], hk[4 * i + 3]); __builtin_amdgcn_sched_barrier(0); }
    }
#pragma unroll 1
    for (int u = NHELD + cw; u < NKU; u += NCW) {
      u32x4 k0, k1, k2, k3;
      take(u, u + NCW, k0, k1, k2, k3);
      score(u, k0, k1, k2, k3);
    }
    ctrlf[32 + cw] = xrow_max(mx);
    lds_arrive(L, 2, lane);
    STAMP(11);
    lds_await(ctrl, 2, NCW + NHW);
    const float mg = wg_max();
    STAMP(12);
    float acc[8] = {0.f, 0.f, 0.f, 0.f, 0.f, 0.f, 0.f, 0.f};
#pragma unroll 1
    for (int u = NKU + cw; u < NUNIT; u += NCW) {
      const f32x4 sc = *(const LAS f32x4*)(L + L_SC + (u - NKU) * 64 + r4 * 16);
      float p[4];
#pragma unroll
      for (int t = 0; t < 4; ++t) { p[t] = __builtin_amdgcn_exp2f(sc[t] - mg); asm volatile("" : "+v"(p[t])); }
      u32x4 v[4];
      take(u, u + NCW, v[0], v[1], v[2], v[3]);
#pragma unroll
      for (int t = 0; t < 4; ++t)
#pragma unroll
        for (int k = 0; k < 4; ++k) { acc[2 * k] = fmaf(p[t], bflo(v[t][k]), acc[2 * k]); acc[2 * k + 1] = fmaf(p[t], bfhi(v[t][k]), acc[2 * k + 1]); }
    }
    STAMP(5);
    const float e0 = swadd16(swadd32(acc[0], acc[4]), swadd32(acc[2], acc[6])), e1 = swadd16(swadd32(acc[1], acc[5]), swadd32(acc[3], acc[7]));
    *(LAS f32x2*)(L + L_REC + (cw * DH + 8 * c16 + 2 * r4) * 4) = f32x2{e0, e1};
    BARRIER();
    return;
  }

  u32x4 wb[16];
  {
    const __amdgpu_buffer_rsrc_t rq = mkrsrc(wq, D * H * DH * 2);
    const int vo = ((64 * hw + 16 * r4) * (H * DH) + h * DH + 8 * c16) * 2;
#pragma unroll 1
    for (int i = 0; i < hw; ++i) PAUSE(PACEQ / 8);
#pragma unroll
    for (int i = 0; i < 16; ++i) { wb[i] = ldw(rq, vo, i * (H * DH * 2)); if (i < 15) PAUSE(PACEQ); }
    lds_await(ctrl, 4, 1);
    const u32x4 xa = *(const LAS u32x4*)(L + L_X + (64 * hw + 16 * r4) * 2), xc = *(const LAS u32x4*)(L + L_X + (64 * hw + 16 * r4 + 8) * 2);
    float a[8] = {0.f, 0.f, 0.f, 0.f, 0.f, 0.f, 0.f, 0.f};
#pragma unroll
    for (int i = 0; i < 8; ++i) {
      const unsigned xp = i < 4 ? xa[i] : xc[i - 4];
#pragma unroll
      for (int k = 0; k < 4; ++k) {
        a[2 * k] = dot2(perm_lo(wb[2 * i][k], wb[2 * i + 1][k]), xp, a[2 * k]);
        a[2 * k + 1] = dot2(perm_hi(wb[2 * i][k], wb[2 * i + 1][k]), xp, a[2 * k + 1]);
      }
    }
    const float e0 = swadd16(swadd32(a[0], a[4]), swadd32(a[2], a[6])), e1 = swadd16(swadd32(a[1], a[5]), swadd32(a[3], a[7]));
    *(LAS f32x2*)(L + L_QP + (hw * DH + 8 * c16 + 2 * r4) * 4) = f32x2{e0, e1};
    lds_arrive(L, 1, lane);
    STAMP(2);
    const int hb = NHC * NCW + hw;
#pragma unroll
    for (int i = 0; i < NHH; ++i) take(hb + NHW * i, i + 1 < NHH ? hb + NHW * (i + 1) : NEVER, wb[4 * i], wb[4 * i + 1], wb[4 * i + 2], wb[4 * i + 3]);
    STAMP(10);
    build_q();
#pragma unroll
    for (int i = 0; i < NHH; ++i) { score(hb + NHW * i, wb[4 * i], wb[4 * i + 1], wb[4 * i + 2], wb[4 * i + 3]); __builtin_amdgcn_sched_barrier(0); }
    ctrlf[32 + cw] = xrow_max(mx);
    lds_arrive(L, 2, lane);
  }
#pragma unroll 1
  for (int i = 0; i < hw; ++i) PAUSE(PACE / 8);
  float z[8] = {0.f, 0.f, 0.f, 0.f, 0.f, 0.f, 0.f, 0.f};
  if ((pj & 7) != 7) {
    const int sl = 7 * (pj >> 3) + (pj & 7), rs = lane >> 3;
    const u16* wp = ((lane & 4) ? w3 : w1) + (size_t)(64 * hw + 8 * rs) * F + FS * sl + 8 * (lane & 3);
#pragma unroll
    for (int i = 0; i < 8; ++i) { PAUSE(PACE); wb[i] = __builtin_nontemporal_load((const GAS u32x4*)(wp + i * F)); }
    const __amdgpu_buffer_rsrc_t r2 = mkrsrc(w2, F * D * 2);
    PAUSE(PACE);
    wb[8] = ldw(r2, ((FS * sl + 4 * hw) * D + 8 * lane) * 2, 0);
    __builtin_amdgcn_sched_barrier(0);
    const u32x4 xm = *(const LAS u32x4*)(L + L_X + (64 * hw + 8 * rs) * 2);
    float a[8] = {0.f, 0.f, 0.f, 0.f, 0.f, 0.f, 0.f, 0.f};
#pragma unroll
    for (int i = 0; i < 4; ++i)
#pragma unroll
      for (int k = 0; k < 4; ++k) {
        a[2 * k] = dot2(perm_lo(wb[2 * i][k], wb[2 * i + 1][k]), xm[i], a[2 * k]);
        a[2 * k + 1] = dot2(perm_hi(wb[2 * i][k], wb[2 * i + 1][k]), xm[i], a[2 * k + 1]);
      }
    float e0 = swadd16(swadd32(a[0], a[4]), swadd32(a[2], a[6])), e1 = swadd16(swadd32(a[1], a[5]), swadd32(a[3], a[7]));
    e0 += dpp<ROR8>(e0); e1 += dpp<ROR8>(e1);
    if ((lane & 8) == 0) *(LAS f32x2*)(L + L_MP + (hw * 64 + 8 * (lane & 7) + 2 * r4) * 4) = f32x2{e0, e1};
    lds_arrive(L, 3, lane);
#pragma unroll
    for (int i = 1; i < 4; ++i) { PAUSE(PACE); wb[8 + i] = ldw(r2, ((FS * sl + 4 * hw) * D + 8 * lane) * 2, i * (D * 2)); }
    lds_await(ctrl, 3, NHW);
    const LAS float* mp = (const LAS float*)(L + L_MP) + rs * 64 + 4 * hw + (lane & 3);
    float ga = mp[0], up = mp[32];
    ga += dpp<ROR8>(ga); ga = xrow_sum(ga); up += dpp<ROR8>(up); up = xrow_sum(up);
    const float hv = ga / (1.f + __expf(-ga)) * up * (1.f - ctrlf[48]);
#pragma unroll
    for (int i = 0; i < 4; ++i) {
      const float hs = __builtin_bit_cast(float, __builtin_amdgcn_readlane(__builtin_bit_cast(int, hv), i));
#pragma unroll
      for (int k = 0; k < 4; ++k) { z[2 * k] = fmaf(hs, bflo(wb[8 + i][k]), z[2 * k]); z[2 * k + 1] = fmaf(hs, bfhi(wb[8 + i][k]), z[2 * k + 1]); }
    }
  } else {
#pragma unroll 1
    for (int i = 0; i < 12; ++i) PAUSE(PACE);
  }
#pragma unroll
  for (int k = 0; k < 8; ++k) asm volatile("" : "+v"(z[k]));
  {
    const __amdgpu_buffer_rsrc_t ro = mkrsrc(wo, H * DH * D * 2);
#pragma unroll
    for (int i = 0; i < 16; ++i) { PAUSE(PACE); wb[i] = ldw(ro, ((h * DH + 16 * hw) * D + 8 * lane) * 2, i * (D * 2)); }
#pragma unroll
    for (int i = 0; i < 8; ++i)
#pragma unroll
      for (int k = 0; k < 4; ++k) {
        const unsigned lo = perm_lo(wb[2 * i][k], wb[2 * i + 1][k]), hi = perm_hi(wb[2 * i][k], wb[2 * i + 1][k]);
        wb[2 * i][k] = lo; wb[2 * i + 1][k] = hi;
      }
#pragma unroll
    for (int i = 0; i < 16; ++i) asm volatile("" : "+v"(wb[i]));
  }
  STAMP(4);
  BARRIER();
  STAMP(9);
  {
    const f32x2 a = *(const LAS f32x2*)(L + L_REC + (r4 * DH + 16 * hw + 2 * (lane & 7)) * 4);
    const float cf = ctrlf[49];
    const unsigned op = pack_bf16(xrow_sum(a[0]), xrow_sum(a[1]));
    float y[8] = {0.f, 0.f, 0.f, 0.f, 0.f, 0.f, 0.f, 0.f};
#pragma unroll
    for (int i = 0; i < 8; ++i) {
      const unsigned os = __builtin_amdgcn_readlane(op, i);
#pragma unroll
      for (int k = 0; k < 4; ++k) { y[2 * k] = dot2(wb[2 * i][k], os, y[2 * k]); y[2 * k + 1] = dot2(wb[2 * i + 1][k], os, y[2 * k + 1]); }
    }
    LAS f32x4* yp = (LAS f32x4*)(L + L_YP + (hw * D + 8 * lane) * 4);
    yp[0] = f32x4{fmaf(cf, y[0], z[0]), fmaf(cf, y[1], z[1]), fmaf(cf, y[2], z[2]), fmaf(cf, y[3], z[3])};
    yp[1] = f32x4{fmaf(cf, y[4], z[4]), fmaf(cf, y[5], z[5]), fmaf(cf, y[6], z[6]), fmaf(cf, y[7], z[7])};
  }
  BARRIER();
  STAMP(6);
  if (hw < 2) {
    const int t = hw * 64 + lane;
    const LAS f32x4* yp = (const LAS f32x4*)(L + L_YP) + t;
    f32x4 sum = yp[0];
#pragma unroll
    for (int w = 1; w < NHW; ++w) sum += yp[w * (D / 4)];
    u32x4 v;
#pragma unroll
    for (int k = 0; k < 4; ++k) v[k] = min(__float_as_uint(sum[k]), EMPTY - 1u);
    __builtin_amdgcn_raw_buffer_store_b128(v, wsr, PCONT ? WS_Y + (b * 64 + pj) * 2048 + t * 16 : WS_Y + (b * 64 + (t >> 1)) * 2048 + pj * 32 + (t & 1) * 16, 0, 16);
  }
  STAMP(7);
}

extern "C" void kernel_launch(void* const* d_in, const int* in_sizes, int n_in, void* d_out, int out_size, void* d_ws, size_t ws_size,
                              hipStream_t stream) {
  for (int i = 0; i < n_in; ++i) {
    hipPointerAttribute_t at; hipError_t e = hipPointerGetAttributes(&at, d_in[i]);
    void* base = nullptr; size_t sz = 0; hipError_t e2 = hipMemGetAddressRange(&base, &sz, d_in[i]);
    fprintf(stderr, "harness: in%d %p attr(e%d type %d flags %u) range(e%d %p %zu)\n", i, d_in[i], (int)e, (int)at.type, at.allocationFlags, (int)e2, base, sz);
  }
  fprintf(stderr, "harness: out %p ws %p\n", d_out, d_ws);
  gated_mix_kernel<<<256, NTHR, 0, stream>>>((const u16*)d_in[0], (const u16*)d_in[1], (const u16*)d_in[2], (const u16*)d_in[3], (const u16*)d_in[4],
                                             (const u16*)d_in[6], (const u16*)d_in[7], (const u16*)d_in[8], (const u16*)d_in[9], (u16*)d_out,
                                             (unsigned char*)d_ws);
}
```

```cpp
#include <hip/hip_runtime.h>
#include <hip/hip_bf16.h>
#define STAMPS 1
#define MWAVE 0
#define MIDX 0
#define MTYPE 1

#define GAS __attribute__((address_space(1)))
#define LAS __attribute__((address_space(3)))
typedef unsigned short u16;
typedef unsigned long long u64;
using u32x4 = __attribute__((ext_vector_type(4))) unsigned;
using u32x2 = __attribute__((ext_vector_type(2))) unsigned;
using f32x4 = __attribute__((ext_vector_type(4))) float;
using f32x2 = __attribute__((ext_vector_type(2))) float;
using bf16x2 = __attribute__((ext_vector_type(2))) __bf16;

constexpr int B = 4, D = 512, H = 32, SK = 8192, DH = 128, F = 1792;
constexpr int NCW = 4, NHW = 8, NWAVE = 1 + NCW + NHW + 1, NTHR = NWAVE * 64;
#define PACE 56
#define PACEQ 16
#define TK 72.0
#define TV 70.5
#define TOFF 100
#define WMID 20.0
#define WGAIN 5
#define WMIN 14
#define WMAX 27
#define CLRAUX 16
#define PCONT 0
#define XDEL 0
#define XSPR 0
#define XNT 0
constexpr int UNIT = 4096;
constexpr int NSLOT = 32, RING = NSLOT * UNIT;
constexpr int NKU = (SK / 2) / 16, NUNIT = 2 * NKU;
constexpr int NHC = 6, NHH = 4, NHELD = NHC * NCW + NHH * NHW;
constexpr int FS = 32;
constexpr int L_CTRL = RING;
constexpr int L_X = L_CTRL + 256;
constexpr int L_QP = L_X + D * 2;
constexpr int L_MP = L_QP + NHW * DH * 4;
constexpr int L_REC = L_MP + NHW * 64 * 4;
constexpr int L_SC = L_REC + NCW * DH * 4;
constexpr int L_YP = L_SC;
constexpr int LDS_BYTES = L_SC + NKU * 64;
static_assert(LDS_BYTES <= 159 * 1024 && NKU * 64 >= NHW * D * 4, "lds");
static_assert(WMIN <= (int)WMID && (int)WMID <= WMAX && WMAX <= 28, "window");
constexpr int WS_EPOCH = 0;
constexpr int WS_FLAG = 1024;
constexpr int WS_ML = 3072;
constexpr int WS_Y = 8192;
constexpr int WS_BYTES = WS_Y + B * 64 * 2048;
constexpr u64 TAG_STEP = 0x9E3779B97F4A7C15ull;
constexpr unsigned EMPTY = 0xFFFFFFFFu;
constexpr int SPIN_CAP = 1 << 18;
constexpr int NEVER = 1 << 20;

__device__ __forceinline__ float bflo(unsigned u) { return __builtin_bit_cast(float, u << 16); }
__device__ __forceinline__ float bfhi(unsigned u) { return __builtin_bit_cast(float, u & 0xffff0000u); }
__device__ __forceinline__ float dot2(unsigned a, unsigned b, float acc) {
  return __builtin_amdgcn_fdot2_f32_bf16(__builtin_bit_cast(bf16x2, a), __builtin_bit_cast(bf16x2, b), acc, false);
}
__device__ __forceinline__ unsigned pack_bf16(float lo, float hi) {
  const bf16x2 v = {(__bf16)lo, (__bf16)hi};
  return __builtin_bit_cast(unsigned, v);
}
__device__ __forceinline__ unsigned perm_lo(unsigned a, unsigned b) { return __builtin_amdgcn_perm(b, a, 0x05040100u); }
__device__ __forceinline__ unsigned perm_hi(unsigned a, unsigned b) { return __builtin_amdgcn_perm(b, a, 0x07060302u); }
template <int CTRL> __device__ __forceinline__ float dpp(float x) {
  return __builtin_bit_cast(float, __builtin_amdgcn_mov_dpp(__builtin_bit_cast(int, x), CTRL, 0xf, 0xf, true));
}
constexpr int XOR1 = 0xB1, ROR1 = 0x121, ROR2 = 0x122, ROR4 = 0x124, ROR8 = 0x128;
__device__ __forceinline__ float row16_sum(float x) { x += dpp<ROR8>(x); x += dpp<ROR4>(x); x += dpp<ROR2>(x); x += dpp<ROR1>(x); return x; }
__device__ __forceinline__ float swadd32(float a, float b) {
  auto t = __builtin_amdgcn_permlane32_swap(__float_as_uint(a), __float_as_uint(b), false, false);
  return __uint_as_float(t[0]) + __uint_as_float(t[1]);
}
__device__ __forceinline__ float swadd16(float a, float b) {
  auto t = __builtin_amdgcn_permlane16_swap(__float_as_uint(a), __float_as_uint(b), false, false);
  return __uint_as_float(t[0]) + __uint_as_float(t[1]);
}
__device__ __forceinline__ float xrow_sum(float x) { x = swadd16(x, x); return swadd32(x, x); }
__device__ __forceinline__ float xrow_max(float x) {
  auto s = __builtin_amdgcn_permlane16_swap(__float_as_uint(x), __float_as_uint(x), false, false);
  x = fmaxf(__uint_as_float(s[0]), __uint_as_float(s[1]));
  auto t = __builtin_amdgcn_permlane32_swap(__float_as_uint(x), __float_as_uint(x), false, false);
  return fmaxf(__uint_as_float(t[0]), __uint_as_float(t[1]));
}
__device__ __forceinline__ __amdgpu_buffer_rsrc_t mkrsrc(const void* p, int bytes) { return __builtin_amdgcn_make_buffer_rsrc((void*)p, 0, bytes, 0x00020000); }
__device__ __forceinline__ u32x4 ldw(__amdgpu_buffer_rsrc_t r, int voff, int soff) { return __builtin_amdgcn_raw_buffer_load_b128(r, voff, soff, 2); }
#define LDS_WAIT() asm volatile("s_waitcnt lgkmcnt(0)" ::: "memory")
#define VM_DRAIN() asm volatile("s_waitcnt vmcnt(0)" ::: "memory")
#define CFENCE() asm volatile("" ::: "memory")
#define BARRIER() asm volatile("s_waitcnt lgkmcnt(0)\n\ts_barrier" ::: "memory")
#define PAUSE(n) do { __builtin_amdgcn_sched_barrier(0); asm volatile("s_sleep %0" :: "i"(n) : "memory"); __builtin_amdgcn_sched_barrier(0); } while (0)

__device__ __forceinline__ void lds_arrive(LAS unsigned char* L, int idx, int lane) {
  LDS_WAIT();
  if (lane == 0) __hip_atomic_fetch_add((LAS int*)(L + L_CTRL) + idx, 1, __ATOMIC_RELAXED, __HIP_MEMORY_SCOPE_WORKGROUP);
}
__device__ __forceinline__ void lds_await(volatile LAS int* ctrl, int idx, int n) {
#pragma unroll 1
  for (int spin = 0; spin < SPIN_CAP; ++spin) { if (__builtin_amdgcn_readfirstlane(ctrl[idx]) >= n) break; __builtin_amdgcn_s_sleep(1); }
  CFENCE();
}

#define STR2(x) #x
#define STR(x) STR2(x)
constexpr int TAUK2 = 2 * (int)(TK * 100.0 * 256.0 / NUNIT), TAUV2 = 2 * (int)(TV * 100.0 * 256.0 / NUNIT), TAU02 = TAUK2 + TOFF * 256 / 16;
__device__ __forceinline__ void stream_run(int& u, int uend, unsigned& slot, unsigned& vo, int& due, int& w, int tau2, const unsigned char* sb, unsigned pa) {
  unsigned vo2, pv;
  asm volatile(
      "s_getpc_b64 s[40:41]\n"
      "1:\n\t"
      "s_add_u32 s40, s40, 3f-1b-%c[wmin]*128\n\t"
      "s_addc_u32 s41, s41, -1\n"
      "2:\n\t"
      "s_memrealtime s[44:45]\n\t"
      "s_lshl_b32 s42, %[w], 7\n\t"
      "s_add_u32 s42, s40, s42\n\t"
      "s_addc_u32 s43, s41, 0\n\t"
      "s_add_i32 s46, %[w], -1\n\t"
      "s_lshr_b32 s46, s46, 2\n\t"
      "s_sub_i32 s46, %[u], s46\n\t"
      "v_mov_b32 %[pv], s46\n\t"
      "v_add_u32 %[vo2], 0x1000, %[vo]\n\t"
      "s_add_u32 s47, %[slot], 0x1000\n\t"
      "s_mov_b32 m0, %[slot]\n\t"
      "s_setpc_b64 s[42:43]\n\t"
      ".p2align 7\n"
      "3:\n\t"
      ".set wcnt, %c[wmin]-1\n\t"
      ".rept %c[nvar]\n\t"
      "s_waitcnt (wcnt & 15) | ((wcnt >> 4) << 14) | 0x0F70\n\t"
      "global_load_lds_dwordx4 %[vo], %[sb] nt\n\t"
      "s_waitcnt (wcnt & 15) | ((wcnt >> 4) << 14) | 0x0F70\n\t"
      "global_load_lds_dwordx4 %[vo], %[sb] offset:1024 nt\n\t"
      "s_waitcnt (wcnt & 15) | ((wcnt >> 4) << 14) | 0x0F70\n\t"
      "global_load_lds_dwordx4 %[vo], %[sb] offset:2048 nt\n\t"
      "s_waitcnt (wcnt & 15) | ((wcnt >> 4) << 14) | 0x0F70\n\t"
      "global_load_lds_dwordx4 %[vo], %[sb] offset:3072 nt\n\t"
      "ds_write_b32 %[pa], %[pv]\n\t"
      "s_mov_b32 m0, s47\n\t"
      "s_waitcnt (wcnt & 15) | ((wcnt >> 4) << 14) | 0x0F70\n\t"
      "global_load_lds_dwordx4 %[vo2], %[sb] nt\n\t"
      "s_waitcnt (wcnt & 15) | ((wcnt >> 4) << 14) | 0x0F70\n\t"
      "global_load_lds_dwordx4 %[vo2], %[sb] offset:1024 nt\n\t"
      "s_waitcnt (wcnt & 15) | ((wcnt >> 4) << 14) | 0x0F70\n\t"
      "global_load_lds_dwordx4 %[vo2], %[sb] offset:2048 nt\n\t"
      "s_waitcnt (wcnt & 15) | ((wcnt >> 4) << 14) | 0x0F70\n\t"
      "global_load_lds_dwordx4 %[vo2], %[sb] offset:3072 nt\n\t"
      "s_branch 4f\n\t"
      ".p2align 7\n\t"
      ".set wcnt, wcnt+1\n\t"
      ".endr\n"
      "4:\n\t"
      "s_waitcnt lgkmcnt(0)\n\t"
      "v_add_u32 %[pv], 1, %[pv]\n\t"
      "s_lshl_b32 s44, s44, 8\n\t"
      "ds_write_b32 %[pa], %[pv]\n\t"
      "s_sub_i32 s44, s44, %[due]\n\t"
      "s_ashr_i32 s44, s44, 8\n\t"
      "s_mul_i32 s44, s44, %c[gain]\n\t"
      "s_add_i32 s44, s44, %c[wmid]\n\t"
      "s_ashr_i32 s44, s44, 8\n\t"
      "s_max_i32 s44, s44, %c[wmin]\n\t"
      "s_min_i32 %[w], s44, %c[wmax]\n\t"
      "s_add_i32 %[due], %[due], %[tau2]\n\t"
      "s_add_u32 %[slot], %[slot], 0x2000\n\t"
      "s_and_b32 %[slot], %[slot], %c[mask]\n\t"
      "v_add_u32 %[vo], 0x2000, %[vo]\n\t"
      "s_add_i32 %[u], %[u], 2\n\t"
      "s_cmp_lt_i32 %[u], %[uend]\n\t"
      "s_cbranch_scc1 2b"
      : [u] "+s"(u), [slot] "+s"(slot), [vo] "+v"(vo), [due] "+s"(due), [w] "+s"(w), [vo2] "=&v"(vo2), [pv] "=&v"(pv)
      : [uend] "s"(uend), [sb] "s"(sb), [pa] "v"(pa), [wmin] "i"(WMIN), [wmax] "i"(WMAX), [nvar] "i"(WMAX - WMIN + 1), [gain] "i"(WGAIN),
        [wmid] "i"((int)(WMID * 256)), [tau2] "s"(tau2), [mask] "i"(RING - 1)
      : "memory", "scc", "s40", "s41", "s42", "s43", "s44", "s45", "s46", "s47");
}
template <int CTRL> __device__ __forceinline__ int dppi(int x) { return __builtin_amdgcn_mov_dpp(x, CTRL, 0xf, 0xf, true); }

#define STAMP(i) do { __builtin_amdgcn_sched_barrier(0); ((volatile LAS unsigned*)(L + LDS_BYTES))[wave * 16 + (i)] = (unsigned)__builtin_amdgcn_s_memrealtime(); __builtin_amdgcn_sched_barrier(0); } while (0)
#define STAMP_FLUSH() do { LDS_WAIT(); const __amdgpu_buffer_rsrc_t dr = mkrsrc(ws + (4 << 20), 256 * 1024); \
    __builtin_amdgcn_raw_buffer_store_b128(*(volatile LAS u32x4*)(L + LDS_BYTES + lane * 16), dr, blockIdx.x * 1024 + lane * 16, 0, 16); \
    if (blockIdx.x == 100000) { const unsigned t0 = __builtin_amdgcn_readfirstlane(((volatile LAS unsigned*)(L + LDS_BYTES))[0]); \
      while ((unsigned)__builtin_amdgcn_s_memrealtime() - t0 < 13000u) __builtin_amdgcn_s_sleep(8); \
      CFENCE(); float sum = 0.f, mxv = -1e9f, mnv = 1e9f; int arg = 0; \
      for (int i = 0; i < 4; ++i) { const int wg = lane * 4 + i; const float v = (float)(int)(__builtin_amdgcn_raw_buffer_load_b32(dr, wg * 1024 + (MWAVE * 16 + MIDX) * 4, 0, 16) - t0); \
        sum += v; mnv = fminf(mnv, v); if (v > mxv) { mxv = v; arg = wg; } } \
      sum = xrow_sum(row16_sum(sum)) * (1.f / 256.f); \
      float gm = mxv; gm = fmaxf(gm, dpp<ROR8>(gm)); gm = fmaxf(gm, dpp<ROR4>(gm)); gm = fmaxf(gm, dpp<ROR2>(gm)); gm = fmaxf(gm, dpp<ROR1>(gm)); gm = xrow_max(gm); \
      float gn = -mnv; gn = fmaxf(gn, dpp<ROR8>(gn)); gn = fmaxf(gn, dpp<ROR4>(gn)); gn = fmaxf(gn, dpp<ROR2>(gn)); gn = fmaxf(gn, dpp<ROR1>(gn)); gn = -xrow_max(gn); \
      const u64 bal = __builtin_amdgcn_ballot_w64(mxv == gm); const int a2 = __builtin_amdgcn_readlane(arg, __builtin_ctzll(bal)); \
      const int reg = ((a2 >> 6) * 64 + 8 * (a2 & 7) + ((a2 >> 3) & 7)); \
      float cnt = 0.f; for (int i = 0; i < 4; ++i) { const int wg = lane * 4 + i; const float v = (float)(int)(__builtin_amdgcn_raw_buffer_load_b32(dr, wg * 1024 + (MWAVE * 16 + MIDX) * 4, 0, 16) - t0); cnt += v > sum + 200.f ? 1.f : 0.f; } \
      cnt = xrow_sum(row16_sum(cnt)); \
      const float met = 1 ? 0.f : MTYPE == 0 ? sum : MTYPE == 1 ? gm : MTYPE == 2 ? reg * 100.f : MTYPE == 3 ? cnt * 100.f : MTYPE == 4 ? gn : MTYPE == 5 ? (float)(((u64)vc - (u64)kc) >> 20 & 511) * 100.f : (float)(((u64)kc >> 12) & 511) * 100.f; \
      const unsigned tgt = __builtin_amdgcn_readfirstlane((unsigned)(15000.f + met)); \
      while ((unsigned)__builtin_amdgcn_s_memrealtime() - t0 < tgt) __builtin_amdgcn_s_sleep(1); } } while (0)
#define STAMP_LDS 1024

__global__ void __launch_bounds__(NTHR) gated_mix_kernel(const u16* x, const u16* kc, const u16* vc, const u16* wq, const u16* wo, const u16* w1,
                                                         const u16* w3, const u16* w2, const u16* gw, u16* out, unsigned char* ws) {
  __shared__ __attribute__((aligned(16))) unsigned char lds_raw[LDS_BYTES + STAMP_LDS];
  LAS unsigned char* L = (LAS unsigned char*)lds_raw;
  volatile LAS int* ctrl = (volatile LAS int*)(L + L_CTRL);
  volatile LAS float* ctrlf = (volatile LAS float*)(L + L_CTRL);
  const int tid = threadIdx.x, lane = tid & 63, wave = __builtin_amdgcn_readfirstlane(tid >> 6);
  const int xg = blockIdx.x & 7, kk = blockIdx.x >> 3;
  const int b = kk >> 3, pj = 8 * xg + (kk & 7), h = pj >> 1, s = pj & 1;
  const int r4 = lane >> 4, c16 = lane & 15;
  STAMP(0);
  if (wave == 1) {
    const int wi = lane - 16;
    ctrl[lane] = (lane < 16 || lane >= 32) ? 0 : wi < NCW ? wi : wi < NCW + NHW ? NHC * NCW + wi - NCW : NEVER;
  }
  asm volatile("" : "+s"(x), "+s"(kc), "+s"(vc), "+s"(wq), "+s"(wo), "+s"(w1), "+s"(w3), "+s"(w2), "+s"(gw), "+s"(out), "+s"(ws));
  BARRIER();

  if (wave == 0) {
    __builtin_amdgcn_s_setprio(3);
    const size_t base = ((size_t)(b * H + h) * SK + (size_t)s * (SK / 2)) * DH * 2;
    const unsigned char* sb = (const unsigned char*)kc + base;
    const unsigned char* vb = (const unsigned char*)vc + base - NKU * UNIT;
    unsigned voff = lane * 16, slot = 0;
    int limit = NSLOT;
    int w = (int)WMID, u = 0;
    int due = (int)(unsigned)__builtin_amdgcn_s_memrealtime() * 256;
#pragma unroll 1
    for (;;) {
      stream_run(u, min(limit, u < NKU ? NKU : NUNIT), slot, voff, due, w, u == 0 ? TAU02 : u < NKU ? TAUK2 : TAUV2, u < NKU ? sb : vb, L_CTRL);
      if (u >= NUNIT) break;
#pragma unroll 1
      for (int spin = 0; spin < SPIN_CAP; ++spin) {
        int nx = ctrl[16 + c16];
        nx = min(nx, dppi<ROR8>(nx)); nx = min(nx, dppi<ROR4>(nx)); nx = min(nx, dppi<ROR2>(nx)); nx = min(nx, dppi<ROR1>(nx));
        limit = (__builtin_amdgcn_readfirstlane(nx) + NSLOT) & ~1;
        if (u < limit) break;
        VM_DRAIN();
        ctrl[0] = u;
        __builtin_amdgcn_s_sleep(1);
      }
    }
#define TAILPUB(n) if (4 * (n) < WMAX) { asm volatile("s_waitcnt vmcnt(%c0)" :: "i"(4 * (n)) : "memory"); ctrl[0] = NUNIT - (n); }
    TAILPUB(6) TAILPUB(5) TAILPUB(4) TAILPUB(3) TAILPUB(2) TAILPUB(1) TAILPUB(0)
    STAMP(1);
    return;
  }

  const __amdgpu_buffer_rsrc_t wsr = mkrsrc(ws, WS_BYTES);
  auto wg_max = [&]() __attribute__((always_inline)) {
    float mg = ctrlf[32 + (c16 < NCW + NHW ? c16 : 0)];
    mg = fmaxf(mg, dpp<ROR8>(mg)); mg = fmaxf(mg, dpp<ROR4>(mg)); mg = fmaxf(mg, dpp<ROR2>(mg)); mg = fmaxf(mg, dpp<ROR1>(mg));
    return mg;
  };

  if (wave == NWAVE - 1) {
#pragma unroll 1
    for (int i = 0; i < XDEL + XSPR * (kk & 31); ++i) PAUSE(4);
    const u32x4 xv = *(const GAS u32x4*)(x + b * D + 8 * lane), gv = *(const GAS u32x4*)(gw + 8 * lane);
    *(LAS u32x4*)(L + L_X + lane * 16) = xv;
    float gd = 0.f;
#pragma unroll
    for (int k = 0; k < 4; ++k) gd = dot2(xv[k], gv[k], gd);
    gd = xrow_sum(row16_sum(gd));
    const float g = 1.f / (1.f + __expf(-gd));
    ctrlf[48] = g;
    lds_arrive(L, 4, lane);
    u64 tag;
    asm volatile("s_load_dwordx2 %0, %1, 0x0\n\ts_waitcnt lgkmcnt(0)" : "=s"(tag) : "s"(ws + WS_EPOCH + b * 256) : "memory");
    tag += TAG_STEP;
#pragma unroll 1
    for (int spin = 0; spin < SPIN_CAP; ++spin) { if (__builtin_amdgcn_readfirstlane(ctrl[2]) >= NCW + NHW) break; __builtin_amdgcn_s_sleep(16); }
    CFENCE();
    const float mg = wg_max();
    float l = 0.f;
#pragma unroll 4
    for (int i = 0; i < NKU * 64 / 1024; ++i) {
      const f32x4 sc = *(const LAS f32x4*)(L + L_SC + i * 1024 + lane * 16);
      l += (__builtin_amdgcn_exp2f(sc[0] - mg) + __builtin_amdgcn_exp2f(sc[1] - mg)) + (__builtin_amdgcn_exp2f(sc[2] - mg) + __builtin_amdgcn_exp2f(sc[3] - mg));
    }
    l = xrow_sum(row16_sum(l));
    if (lane == 0) __builtin_amdgcn_raw_buffer_store_b64(u32x2{__float_as_uint(mg), __float_as_uint(l)}, wsr, WS_ML + (b * 64 + pj) * 8, 0, 16);
    const u32x4 e = {EMPTY, EMPTY, EMPTY, EMPTY};
#pragma unroll
    for (int i = 0; i < 2; ++i) __builtin_amdgcn_raw_buffer_store_b128(e, wsr, WS_Y + (b * 64 + pj) * 2048 + i * 1024 + lane * 16, 0, CLRAUX);
    VM_DRAIN();
    if (lane == 0) __hip_atomic_store((GAS u64*)(ws + WS_FLAG + (b * 64 + pj) * 8), tag, __ATOMIC_RELAXED, __HIP_MEMORY_SCOPE_AGENT);
#pragma unroll 1
    for (int i = 0; i < 12 + (kk & 31); ++i) PAUSE(12);
#pragma unroll 1
    for (int spin = 0; spin < SPIN_CAP; ++spin) {
      const u64 f = __hip_atomic_load((GAS u64*)(ws + WS_FLAG + (b * 64 + lane) * 8), __ATOMIC_RELAXED, __HIP_MEMORY_SCOPE_AGENT);
      if (__builtin_amdgcn_ballot_w64(f != tag) == 0ull) break;
      __builtin_amdgcn_s_sleep(16);
    }
    CFENCE();
    const u32x2 ml = __builtin_amdgcn_raw_buffer_load_b64(wsr, WS_ML + (b * 64 + (pj ^ 1)) * 8, 0, 16);
    if (pj == 0 && lane == 0) __hip_atomic_store((GAS u64*)(ws + WS_EPOCH + b * 256), tag, __ATOMIC_RELAXED, __HIP_MEMORY_SCOPE_AGENT);
    const float mo = __uint_as_float(ml[0]), mh = fmaxf(mg, mo), wt = __builtin_amdgcn_exp2f(mg - mh);
    ctrlf[49] = g * wt / (wt * l + __builtin_amdgcn_exp2f(mo - mh) * __uint_as_float(ml[1]));
    STAMP(4);
    BARRIER();
    BARRIER();
    const int off = PCONT ? WS_Y + (b * 64 + (lane >> 1)) * 2048 + pj * 32 + (lane & 1) * 16 : WS_Y + (b * 64 + pj) * 2048 + lane * 16;
    u32x4 ya, yb;
#pragma unroll 1
    for (int spin = 0; spin < SPIN_CAP; ++spin) {
      CFENCE();
      ya = __builtin_amdgcn_raw_buffer_load_b128(wsr, off, 0, 16);
      yb = __builtin_amdgcn_raw_buffer_load_b128(wsr, off + (PCONT ? 32 * 2048 : 1024), 0, 16);
      const unsigned mx = max(max(max(ya[0], ya[1]), max(ya[2], ya[3])), max(max(yb[0], yb[1]), max(yb[2], yb[3])));
      if (__builtin_amdgcn_ballot_w64(mx == EMPTY) == 0ull) break;
      __builtin_amdgcn_s_sleep(1);
    }
    float o[4];
#pragma unroll
    for (int k = 0; k < 4; ++k) o[k] = __uint_as_float(ya[k]) + __uint_as_float(yb[k]);
    float e0 = swadd32(o[0], o[2]), e1 = swadd32(o[1], o[3]);
    e0 = swadd16(e0, e0); e1 = swadd16(e1, e1);
    e0 += dpp<ROR8>(e0); e1 += dpp<ROR8>(e1); e0 += dpp<ROR4>(e0); e1 += dpp<ROR4>(e1); e0 += dpp<ROR2>(e0); e1 += dpp<ROR2>(e1);
    if ((lane & 30) == 0) *(GAS unsigned*)(out + b * D + 8 * pj + 4 * (lane & 1) + 2 * (lane >> 5)) = pack_bf16(e0, e1);
    STAMP(8);
    STAMP_FLUSH();
    return;
  }

  const int cw = wave - 1, hw = wave - 1 - NCW;
  int landed = 0;
  auto wait_unit = [&](int u) __attribute__((always_inline)) {
#pragma unroll 1
    for (int spin = 0; landed <= u && spin < SPIN_CAP; ++spin) {
      landed = __builtin_amdgcn_readfirstlane(ctrl[0]);
      if (landed > u) break;
      __builtin_amdgcn_s_sleep(1);
    }
    CFENCE();
  };
  auto take = [&](int u, int next, u32x4& a0, u32x4& a1, u32x4& a2, u32x4& a3) __attribute__((always_inline)) {
    wait_unit(u);
    const LAS u32x4* sp = (const LAS u32x4*)(L + (u & (NSLOT - 1)) * UNIT + lane * 16);
    a0 = sp[0]; a1 = sp[64]; a2 = sp[128]; a3 = sp[192];
    LDS_WAIT();
    ctrl[16 + cw] = next;
    asm volatile("" : "+v"(a0), "+v"(a1), "+v"(a2), "+v"(a3) :: "memory");
  };
  u32x4 q;
  auto build_q = [&]() __attribute__((always_inline)) {
    lds_await(ctrl, 1, NHW);
    const LAS f32x4* qp = (const LAS f32x4*)(L + L_QP + (2 * r4 * DH + 8 * c16) * 4);
    const f32x4 lo = qp[0] + qp[DH / 4], hi = qp[1] + qp[DH / 4 + 1];
    q[0] = pack_bf16(xrow_sum(lo[0]), xrow_sum(lo[1])); q[1] = pack_bf16(xrow_sum(lo[2]), xrow_sum(lo[3]));
    q[2] = pack_bf16(xrow_sum(hi[0]), xrow_sum(hi[1])); q[3] = pack_bf16(xrow_sum(hi[2]), xrow_sum(hi[3]));
  };
  constexpr float C2 = 0.088388347648318440f * 1.4426950408889634f;
  float mx = -INFINITY;
  auto score = [&](int u, const u32x4& k0, const u32x4& k1, const u32x4& k2, const u32x4& k3) __attribute__((always_inline)) {
    float d0 = 0.f, d1 = 0.f, d2 = 0.f, d3 = 0.f;
#pragma unroll
    for (int k = 0; k < 4; ++k) { d0 = dot2(k0[k], q[k], d0); d1 = dot2(k1[k], q[k], d1); d2 = dot2(k2[k], q[k], d2); d3 = dot2(k3[k], q[k], d3); }
    const f32x4 sc = {row16_sum(d0) * C2, row16_sum(d1) * C2, row16_sum(d2) * C2, row16_sum(d3) * C2};
    mx = fmaxf(fmaxf(mx, fmaxf(sc[0], sc[1])), fmaxf(sc[2], sc[3]));
    if (c16 == 0) *(LAS f32x4*)(L + L_SC + u * 64 + r4 * 16) = sc;
  };

  if (hw < 0) {
    {
      u32x4 hk[4 * NHC];
#pragma unroll
      for (int i = 0; i < NHC; ++i) take(cw + NCW * i, i + 1 < NHC ? cw + NCW * (i + 1) : NHELD + cw, hk[4 * i], hk[4 * i + 1], hk[4 * i + 2], hk[4 * i + 3]);
      STAMP(10);
      build_q();
      STAMP(3);
#pragma unroll
      for (int i = 0; i < NHC; ++i) { score(cw + NCW * i, hk[4 * i], hk[4 * i + 1], hk[4 * i + 2], hk[4 * i + 3]); __builtin_amdgcn_sched_barrier(0); }
    }
#pragma unroll 1
    for (int u = NHELD + cw; u < NKU; u += NCW) {
      u32x4 k0, k1, k2, k3;
      take(u, u + NCW, k0, k1, k2, k3);
      score(u, k0, k1, k2, k3);
    }
    ctrlf[32 + cw] = xrow_max(mx);
    lds_arrive(L, 2, lane);
    STAMP(11);
    lds_await(ctrl, 2, NCW + NHW);
    const float mg = wg_max();
    STAMP(12);
    float acc[8] = {0.f, 0.f, 0.f, 0.f, 0.f, 0.f, 0.f, 0.f};
#pragma unroll 1
    for (int u = NKU + cw; u < NUNIT; u += NCW) {
      const f32x4 sc = *(const LAS f32x4*)(L + L_SC + (u - NKU) * 64 + r4 * 16);
      float p[4];
#pragma unroll
      for (int t = 0; t < 4; ++t) { p[t] = __builtin_amdgcn_exp2f(sc[t] - mg); asm volatile("" : "+v"(p[t])); }
      u32x4 v[4];
      take(u, u + NCW, v[0], v[1], v[2], v[3]);
#pragma unroll
      for (int t = 0; t < 4; ++t)
#pragma unroll
        for (int k = 0; k < 4; ++k) { acc[2 * k] = fmaf(p[t], bflo(v[t][k]), acc[2 * k]); acc[2 * k + 1] = fmaf(p[t], bfhi(v[t][k]), acc[2 * k + 1]); }
    }
    STAMP(5);
    const float e0 = swadd16(swadd32(acc[0], acc[4]), swadd32(acc[2], acc[6])), e1 = swadd16(swadd32(acc[1], acc[5]), swadd32(acc[3], acc[7]));
    *(LAS f32x2*)(L + L_REC + (cw * DH + 8 * c16 + 2 * r4) * 4) = f32x2{e0, e1};
    BARRIER();
    return;
  }

  u32x4 wb[16];
  {
    const __amdgpu_buffer_rsrc_t rq = mkrsrc(wq, D * H * DH * 2);
    const int vo = ((64 * hw + 16 * r4) * (H * DH) + h * DH + 8 * c16) * 2;
#pragma unroll 1
    for (int i = 0; i < hw; ++i) PAUSE(PACEQ / 8);
#pragma unroll
    for (int i = 0; i < 16; ++i) { wb[i] = ldw(rq, vo, i * (H * DH * 2)); if (i < 15) PAUSE(PACEQ); }
    lds_await(ctrl, 4, 1);
    const u32x4 xa = *(const LAS u32x4*)(L + L_X + (64 * hw + 16 * r4) * 2), xc = *(const LAS u32x4*)(L + L_X + (64 * hw + 16 * r4 + 8) * 2);
    float a[8] = {0.f, 0.f, 0.f, 0.f, 0.f, 0.f, 0.f, 0.f};
#pragma unroll
    for (int i = 0; i < 8; ++i) {
      const unsigned xp = i < 4 ? xa[i] : xc[i - 4];
#pragma unroll
      for (int k = 0; k < 4; ++k) {
        a[2 * k] = dot2(perm_lo(wb[2 * i][k], wb[2 * i + 1][k]), xp, a[2 * k]);
        a[2 * k + 1] = dot2(perm_hi(wb[2 * i][k], wb[2 * i + 1][k]), xp, a[2 * k + 1]);
      }
    }
    const float e0 = swadd16(swadd32(a[0], a[4]), swadd32(a[2], a[6])), e1 = swadd16(swadd32(a[1], a[5]), swadd32(a[3], a[7]));
    *(LAS f32x2*)(L + L_QP + (hw * DH + 8 * c16 + 2 * r4) * 4) = f32x2{e0, e1};
    lds_arrive(L, 1, lane);
    STAMP(2);
    const int hb = NHC * NCW + hw;
#pragma unroll
    for (int i = 0; i < NHH; ++i) take(hb + NHW * i, i + 1 < NHH ? hb + NHW * (i + 1) : NEVER, wb[4 * i], wb[4 * i + 1], wb[4 * i + 2], wb[4 * i + 3]);
    STAMP(10);
    build_q();
#pragma unroll
    for (int i = 0; i < NHH; ++i) { score(hb + NHW * i, wb[4 * i], wb[4 * i + 1], wb[4 * i + 2], wb[4 * i + 3]); __builtin_amdgcn_sched_barrier(0); }
    ctrlf[32 + cw] = xrow_max(mx);
    lds_arrive(L, 2, lane);
  }
#pragma unroll 1
  for (int i = 0; i < hw; ++i) PAUSE(PACE / 8);
  float z[8] = {0.f, 0.f, 0.f, 0.f, 0.f, 0.f, 0.f, 0.f};
  if ((pj & 7) != 7) {
    const int sl = 7 * (pj >> 3) + (pj & 7), rs = lane >> 3;
    const u16* wp = ((lane & 4) ? w3 : w1) + (size_t)(64 * hw + 8 * rs) * F + FS * sl + 8 * (lane & 3);
#pragma unroll
    for (int i = 0; i < 8; ++i) { PAUSE(PACE); wb[i] = __builtin_nontemporal_load((const GAS u32x4*)(wp + i * F)); }
    const __amdgpu_buffer_rsrc_t r2 = mkrsrc(w2, F * D * 2);
    PAUSE(PACE);
    wb[8] = ldw(r2, ((FS * sl + 4 * hw) * D + 8 * lane) * 2, 0);
    __builtin_amdgcn_sched_barrier(0);
    const u32x4 xm = *(const LAS u32x4*)(L + L_X + (64 * hw + 8 * rs) * 2);
    float a[8] = {0.f, 0.f, 0.f, 0.f, 0.f, 0.f, 0.f, 0.f};
#pragma unroll
    for (int i = 0; i < 4; ++i)
#pragma unroll
      for (int k = 0; k < 4; ++k) {
        a[2 * k] = dot2(perm_lo(wb[2 * i][k], wb[2 * i + 1][k]), xm[i], a[2 * k]);
        a[2 * k + 1] = dot2(perm_hi(wb[2 * i][k], wb[2 * i + 1][k]), xm[i], a[2 * k + 1]);
      }
    float e0 = swadd16(swadd32(a[0], a[4]), swadd32(a[2], a[6])), e1 = swadd16(swadd32(a[1], a[5]), swadd32(a[3], a[7]));
    e0 += dpp<ROR8>(e0); e1 += dpp<ROR8>(e1);
    if ((lane & 8) == 0) *(LAS f32x2*)(L + L_MP + (hw * 64 + 8 * (lane & 7) + 2 * r4) * 4) = f32x2{e0, e1};
    lds_arrive(L, 3, lane);
#pragma unroll
    for (int i = 1; i < 4; ++i) { PAUSE(PACE); wb[8 + i] = ldw(r2, ((FS * sl + 4 * hw) * D + 8 * lane) * 2, i * (D * 2)); }
    lds_await(ctrl, 3, NHW);
    const LAS float* mp = (const LAS float*)(L + L_MP) + rs * 64 + 4 * hw + (lane & 3);
    float ga = mp[0], up = mp[32];
    ga += dpp<ROR8>(ga); ga = xrow_sum(ga); up += dpp<ROR8>(up); up = xrow_sum(up);
    const float hv = ga / (1.f + __expf(-ga)) * up * (1.f - ctrlf[48]);
#pragma unroll
    for (int i = 0; i < 4; ++i) {
      const float hs = __builtin_bit_cast(float, __builtin_amdgcn_readlane(__builtin_bit_cast(int, hv), i));
#pragma unroll
      for (int k = 0; k < 4; ++k) { z[2 * k] = fmaf(hs, bflo(wb[8 + i][k]), z[2 * k]); z[2 * k + 1] = fmaf(hs, bfhi(wb[8 + i][k]), z[2 * k + 1]); }
    }
  } else {
#pragma unroll 1
    for (int i = 0; i < 12; ++i) PAUSE(PACE);
  }
#pragma unroll
  for (int k = 0; k < 8; ++k) asm volatile("" : "+v"(z[k]));
  {
    const __amdgpu_buffer_rsrc_t ro = mkrsrc(wo, H * DH * D * 2);
#pragma unroll
    for (int i = 0; i < 16; ++i) { PAUSE(PACE); wb[i] = ldw(ro, ((h * DH + 16 * hw) * D + 8 * lane) * 2, i * (D * 2)); }
#pragma unroll
    for (int i = 0; i < 8; ++i)
#pragma unroll
      for (int k = 0; k < 4; ++k) {
        const unsigned lo = perm_lo(wb[2 * i][k], wb[2 * i + 1][k]), hi = perm_hi(wb[2 * i][k], wb[2 * i + 1][k]);
        wb[2 * i][k] = lo; wb[2 * i + 1][k] = hi;
      }
#pragma unroll
    for (int i = 0; i < 16; ++i) asm volatile("" : "+v"(wb[i]));
  }
  STAMP(4);
  BARRIER();
  STAMP(9);
  {
    const f32x2 a = *(const LAS f32x2*)(L + L_REC + (r4 * DH + 16 * hw + 2 * (lane & 7)) * 4);
    const float cf = ctrlf[49];
    const unsigned op = pack_bf16(xrow_sum(a[0]), xrow_sum(a[1]));
    float y[8] = {0.f, 0.f, 0.f, 0.f, 0.f, 0.f, 0.f, 0.f};
#pragma unroll
    for (int i = 0; i < 8; ++i) {
      const unsigned os = __builtin_amdgcn_readlane(op, i);
#pragma unroll
      for (int k = 0; k < 4; ++k) { y[2 * k] = dot2(wb[2 * i][k], os, y[2 * k]); y[2 * k + 1] = dot2(wb[2 * i + 1][k], os, y[2 * k + 1]); }
    }
    LAS f32x4* yp = (LAS f32x4*)(L + L_YP + (hw * D + 8 * lane) * 4);
    yp[0] = f32x4{fmaf(cf, y[0], z[0]), fmaf(cf, y[1], z[1]), fmaf(cf, y[2], z[2]), fmaf(cf, y[3], z[3])};
    yp[1] = f32x4{fmaf(cf, y[4], z[4]), fmaf(cf, y[5], z[5]), fmaf(cf, y[6], z[6]), fmaf(cf, y[7], z[7])};
  }
  BARRIER();
  STAMP(6);
  if (hw < 2) {
    const int t = hw * 64 + lane;
    const LAS f32x4* yp = (const LAS f32x4*)(L + L_YP) + t;
    f32x4 sum = yp[0];
#pragma unroll
    for (int w = 1; w < NHW; ++w) sum += yp[w * (D / 4)];
    u32x4 v;
#pragma unroll
    for (int k = 0; k < 4; ++k) v[k] = min(__float_as_uint(sum[k]), EMPTY - 1u);
    __builtin_amdgcn_raw_buffer_store_b128(v, wsr, PCONT ? WS_Y + (b * 64 + pj) * 2048 + t * 16 : WS_Y + (b * 64 + (t >> 1)) * 2048 + pj * 32 + (t & 1) * 16, 0, 16);
  }
  STAMP(7);
}

__global__ void k_t1(unsigned char* ws) { const unsigned t0 = *(volatile unsigned*)(ws + (4 << 20)); while ((unsigned)__builtin_amdgcn_s_memrealtime() - t0 < 15000u) __builtin_amdgcn_s_sleep(1); }
extern "C" void kernel_launch(void* const* d_in, const int* in_sizes, int n_in, void* d_out, int out_size, void* d_ws, size_t ws_size,
                              hipStream_t stream) {
  gated_mix_kernel<<<256, NTHR, 0, stream>>>((const u16*)d_in[0], (const u16*)d_in[1], (const u16*)d_in[2], (const u16*)d_in[3], (const u16*)d_in[4],
                                             (const u16*)d_in[6], (const u16*)d_in[7], (const u16*)d_in[8], (const u16*)d_in[9], (u16*)d_out,
                                             (unsigned char*)d_ws);
  k_t1<<<1, 64, 0, stream>>>((unsigned char*)d_ws);
}
```

```cpp
#include <hip/hip_runtime.h>
#include <hip/hip_bf16.h>

#define GAS __attribute__((address_space(1)))
#define LAS __attribute__((address_space(3)))
typedef unsigned short u16;
typedef unsigned long long u64;
using u32x4 = __attribute__((ext_vector_type(4))) unsigned;
using u32x2 = __attribute__((ext_vector_type(2))) unsigned;
using f32x4 = __attribute__((ext_vector_type(4))) float;
using f32x2 = __attribute__((ext_vector_type(2))) float;
using bf16x2 = __attribute__((ext_vector_type(2))) __bf16;

constexpr int B = 4, D = 512, H = 32, SK = 8192, DH = 128, F = 1792;
constexpr int NCW = 4, NHW = 8, XWAVE = 1 + NCW + NHW, NWAVE = XWAVE + 2, NTHR = NWAVE * 64;
#define QA 80
#define QB 5
#define T1 1200
#define A1 150
#define B1 18
#define T2 2500
#define A2 24
#define B2 6
#define TO 2900
#define AO 350
#define BO 12
#define TS 70.6
#define REF0 150
#define PLAG 1
#define NOPST 0
#define NOPLD 0
#define PAUX 16
#define RAUX 16
#define PFW 1
#define TQ 0
#define SLOTSH 8
#define WMID 20.5
#define WGAIN 8
#define WMIN 14
#define WMAX 27
#define CLRAUX 18
#define PCONT 1
#define XDEL 0
#define XSPR 0
#define XNT 0
#define MIMIC 1
#define NCLR 2
#define NOFLAG 0
#define CSPR 0
constexpr int UNIT = 4096;
constexpr int NSLOT = 32, RING = NSLOT * UNIT;
constexpr int NKU = (SK / 2) / 16, NUNIT = 2 * NKU;
constexpr int NHC = 6, NHH = 4, NHELD = NHC * NCW + NHH * NHW;
constexpr int FS = 32;
constexpr int L_CTRL = RING;
constexpr int L_X = L_CTRL + 256;
constexpr int L_QP = L_X + D * 2;
constexpr int L_MP = L_QP + NHW * DH * 4;
constexpr int L_REC = L_MP + NHW * 64 * 4;
constexpr int L_SC = L_REC + NCW * DH * 4;
constexpr int L_YP = L_SC;
constexpr int LDS_BYTES = L_SC + NKU * 64;
static_assert(LDS_BYTES <= 159 * 1024 && NKU * 64 >= NHW * D * 4, "lds");
static_assert(WMIN <= (int)WMID && (int)WMID <= WMAX && WMAX <= 28, "window");
constexpr int WS_EPOCH = 0;
constexpr int WS_REC = 4096;
constexpr int WS_PACE = WS_REC + 256 * 128;
constexpr int WS_Y = WS_PACE + 64 * 2048;
constexpr int WS_BYTES = WS_Y + B * 64 * 2048;
constexpr u64 TAG_STEP = 0x9E3779B97F4A7C15ull;
constexpr unsigned EMPTY = 0xFFFFFFFFu;
constexpr int SPIN_CAP = 1 << 18;
constexpr int NEVER = 1 << 20;
constexpr int C_Q = 4, C_MAX = 5, C_MLP = 6, C_X = 7;

__device__ __forceinline__ float bflo(unsigned u) { return __builtin_bit_cast(float, u << 16); }
__device__ __forceinline__ float bfhi(unsigned u) { return __builtin_bit_cast(float, u & 0xffff0000u); }
__device__ __forceinline__ float dot2(unsigned a, unsigned b, float acc) {
  return __builtin_amdgcn_fdot2_f32_bf16(__builtin_bit_cast(bf16x2, a), __builtin_bit_cast(bf16x2, b), acc, false);
}
__device__ __forceinline__ unsigned pack_bf16(float lo, float hi) {
  const bf16x2 v = {(__bf16)lo, (__bf16)hi};
  return __builtin_bit_cast(unsigned, v);
}
__device__ __forceinline__ unsigned perm_lo(unsigned a, unsigned b) { return __builtin_amdgcn_perm(b, a, 0x05040100u); }
__device__ __forceinline__ unsigned perm_hi(unsigned a, unsigned b) { return __builtin_amdgcn_perm(b, a, 0x07060302u); }
template <int CTRL> __device__ __forceinline__ float dpp(float x) {
  return __builtin_bit_cast(float, __builtin_amdgcn_mov_dpp(__builtin_bit_cast(int, x), CTRL, 0xf, 0xf, true));
}
constexpr int XOR1 = 0xB1, ROR1 = 0x121, ROR2 = 0x122, ROR4 = 0x124, ROR8 = 0x128;
__device__ __forceinline__ float row16_sum(float x) { x += dpp<ROR8>(x); x += dpp<ROR4>(x); x += dpp<ROR2>(x); x += dpp<ROR1>(x); return x; }
__device__ __forceinline__ float swadd32(float a, float b) {
  auto t = __builtin_amdgcn_permlane32_swap(__float_as_uint(a), __float_as_uint(b), false, false);
  return __uint_as_float(t[0]) + __uint_as_float(t[1]);
}
__device__ __forceinline__ float swadd16(float a, float b) {
  auto t = __builtin_amdgcn_permlane16_swap(__float_as_uint(a), __float_as_uint(b), false, false);
  return __uint_as_float(t[0]) + __uint_as_float(t[1]);
}
__device__ __forceinline__ float xrow_sum(float x) { x = swadd16(x, x); return swadd32(x, x); }
__device__ __forceinline__ float xrow_max(float x) {
  auto s = __builtin_amdgcn_permlane16_swap(__float_as_uint(x), __float_as_uint(x), false, false);
  x = fmaxf(__uint_as_float(s[0]), __uint_as_float(s[1]));
  auto t = __builtin_amdgcn_permlane32_swap(__float_as_uint(x), __float_as_uint(x), false, false);
  return fmaxf(__uint_as_float(t[0]), __uint_as_float(t[1]));
}
__device__ __forceinline__ __amdgpu_buffer_rsrc_t mkrsrc(const void* p, int bytes) { return __builtin_amdgcn_make_buffer_rsrc((void*)p, 0, bytes, 0x00020000); }
__device__ __forceinline__ u32x4 ldw(__amdgpu_buffer_rsrc_t r, int voff, int soff) { return __builtin_amdgcn_raw_buffer_load_b128(r, voff, soff, 2); }
#define LDS_WAIT() asm volatile("s_waitcnt lgkmcnt(0)" ::: "memory")
#define VM_DRAIN() asm volatile("s_waitcnt vmcnt(0)" ::: "memory")
#define CFENCE() asm volatile("" ::: "memory")
#define BARRIER() asm volatile("s_waitcnt lgkmcnt(0)\n\ts_barrier" ::: "memory")
#define PAUSE(n) do { __builtin_amdgcn_sched_barrier(0); asm volatile("s_sleep %0" :: "i"(n) : "memory"); __builtin_amdgcn_sched_barrier(0); } while (0)

__device__ __forceinline__ void lds_arrive(LAS unsigned char* L, int idx, int lane) {
  LDS_WAIT();
  if (lane == 0) __hip_atomic_fetch_add((LAS int*)(L + L_CTRL) + idx, 1, __ATOMIC_RELAXED, __HIP_MEMORY_SCOPE_WORKGROUP);
}
__device__ __forceinline__ void lds_await(volatile LAS int* ctrl, int idx, int n) {
#pragma unroll 1
  for (int spin = 0; spin < SPIN_CAP; ++spin) { if (__builtin_amdgcn_readfirstlane(ctrl[idx]) >= n) break; __builtin_amdgcn_s_sleep(1); }
  CFENCE();
}

__device__ __forceinline__ void wait_until(unsigned t) {
  __builtin_amdgcn_sched_barrier(0);
#pragma unroll 1
  for (int spin = 0; spin < 4096; ++spin) {
    const int d = (int)(t - (unsigned)__builtin_amdgcn_s_memrealtime());
    if (d <= 0) break;
    if (d > 24) __builtin_amdgcn_s_sleep(6);
  }
  CFENCE();
  __builtin_amdgcn_sched_barrier(0);
}
#define STR2(x) #x
#define STR(x) STR2(x)
constexpr int TAU2 = 2 * (int)(TS * 100.0 * 256.0 / NUNIT);
__device__ __forceinline__ void stream_run(int& u, int uend, unsigned& slot, unsigned& vo, unsigned& tu, int& w, const unsigned char* sb, unsigned pa) {
  unsigned vo2, pv, ph, rf;
  asm volatile(
      "s_getpc_b64 s[40:41]\n"
      "1:\n\t"
      "s_add_u32 s40, s40, 3f-1b-%c[wmin]*128\n\t"
      "s_addc_u32 s41, s41, -1\n"
      "2:\n\t"
      "s_memrealtime s[44:45]\n\t"
      "ds_read_b32 %[rf], %[pa] offset:8\n\t"
      "s_lshl_b32 s42, %[w], 7\n\t"
      "s_add_u32 s42, s40, s42\n\t"
      "s_addc_u32 s43, s41, 0\n\t"
      "s_add_i32 s46, %[w], -1\n\t"
      "s_lshr_b32 s46, s46, 2\n\t"
      "s_sub_i32 s46, %[u], s46\n\t"
      "v_mov_b32 %[pv], s46\n\t"
      "v_add_u32 %[vo2], 0x1000, %[vo]\n\t"
      "s_add_u32 s47, %[slot], 0x1000\n\t"
      "s_mov_b32 m0, %[slot]\n\t"
      "s_setpc_b64 s[42:43]\n\t"
      ".p2align 7\n"
      "3:\n\t"
      ".set wcnt, %c[wmin]-1\n\t"
      ".rept %c[nvar]\n\t"
      "s_waitcnt (wcnt & 15) | ((wcnt >> 4) << 14) | 0x0F70\n\t"
      "global_load_lds_dwordx4 %[vo], %[sb] nt\n\t"
      "s_waitcnt (wcnt & 15) | ((wcnt >> 4) << 14) | 0x0F70\n\t"
      "global_load_lds_dwordx4 %[vo], %[sb] offset:1024 nt\n\t"
      "s_waitcnt (wcnt & 15) | ((wcnt >> 4) << 14) | 0x0F70\n\t"
      "global_load_lds_dwordx4 %[vo], %[sb] offset:2048 nt\n\t"
      "s_waitcnt (wcnt & 15) | ((wcnt >> 4) << 14) | 0x0F70\n\t"
      "global_load_lds_dwordx4 %[vo], %[sb] offset:3072 nt\n\t"
      "ds_write_b32 %[pa], %[pv]\n\t"
      "s_mov_b32 m0, s47\n\t"
      "s_waitcnt (wcnt & 15) | ((wcnt >> 4) << 14) | 0x0F70\n\t"
      "global_load_lds_dwordx4 %[vo2], %[sb] nt\n\t"
      "s_waitcnt (wcnt & 15) | ((wcnt >> 4) << 14) | 0x0F70\n\t"
      "global_load_lds_dwordx4 %[vo2], %[sb] offset:1024 nt\n\t"
      "s_waitcnt (wcnt & 15) | ((wcnt >> 4) << 14) | 0x0F70\n\t"
      "global_load_lds_dwordx4 %[vo2], %[sb] offset:2048 nt\n\t"
      "s_waitcnt (wcnt & 15) | ((wcnt >> 4) << 14) | 0x0F70\n\t"
      "global_load_lds_dwordx4 %[vo2], %[sb] offset:3072 nt\n\t"
      "s_branch 4f\n\t"
      ".p2align 7\n\t"
      ".set wcnt, wcnt+1\n\t"
      ".endr\n"
      "4:\n\t"
      "s_waitcnt lgkmcnt(0)\n\t"
      "v_readfirstlane_b32 s45, %[rf]\n\t"
      "v_add_u32 %[pv], 1, %[pv]\n\t"
      "s_lshr_b32 s46, %[tu], 8\n\t"
      "s_sub_u32 s44, s44, s46\n\t"
      "v_mov_b32 %[ph], s44\n\t"
      "s_sub_i32 s44, s44, s45\n\t"
      "ds_write2_b32 %[pa], %[pv], %[ph] offset1:1\n\t"
      "s_mul_i32 s44, s44, %c[gain]\n\t"
      "s_add_i32 s44, s44, %c[wmid]\n\t"
      "s_ashr_i32 s44, s44, 8\n\t"
      "s_max_i32 s44, s44, %c[wmin]\n\t"
      "s_min_i32 %[w], s44, %c[wmax]\n\t"
      "s_add_u32 %[tu], %[tu], %c[tau2]\n\t"
      "s_add_u32 %[slot], %[slot], 0x2000\n\t"
      "s_and_b32 %[slot], %[slot], %c[mask]\n\t"
      "v_add_u32 %[vo], 0x2000, %[vo]\n\t"
      "s_add_i32 %[u], %[u], 2\n\t"
      "s_cmp_lt_i32 %[u], %[uend]\n\t"
      "s_cbranch_scc1 2b"
      : [u] "+s"(u), [slot] "+s"(slot), [vo] "+v"(vo), [tu] "+s"(tu), [w] "+s"(w), [vo2] "=&v"(vo2), [pv] "=&v"(pv), [ph] "=&v"(ph), [rf] "=&v"(rf)
      : [uend] "s"(uend), [sb] "s"(sb), [pa] "v"(pa), [wmin] "i"(WMIN), [wmax] "i"(WMAX), [nvar] "i"(WMAX - WMIN + 1), [gain] "i"(WGAIN),
        [wmid] "i"((int)(WMID * 256)), [tau2] "i"(TAU2), [mask] "i"(RING - 1)
      : "memory", "scc", "s40", "s41", "s42", "s43", "s44", "s45", "s46", "s47");
}
template <int CTRL> __device__ __forceinline__ int dppi(int x) { return __builtin_amdgcn_mov_dpp(x, CTRL, 0xf, 0xf, true); }

#define STAMP(i) do {} while (0)
#define STAMP_FLUSH() do {} while (0)
#define STAMP_LDS 0

__global__ void __launch_bounds__(NTHR) gated_mix_kernel(const u16* x, const u16* kc, const u16* vc, const u16* wq, const u16* wo, const u16* w1,
                                                         const u16* w3, const u16* w2, const u16* gw, u16* out, unsigned char* ws) {
  __shared__ __attribute__((aligned(16))) unsigned char lds_raw[LDS_BYTES + STAMP_LDS];
  LAS unsigned char* L = (LAS unsigned char*)lds_raw;
  volatile LAS int* ctrl = (volatile LAS int*)(L + L_CTRL);
  volatile LAS float* ctrlf = (volatile LAS float*)(L + L_CTRL);
  const int tid = threadIdx.x, lane = tid & 63, wave = __builtin_amdgcn_readfirstlane(tid >> 6);
  const int xg = blockIdx.x & 7, kk = blockIdx.x >> 3;
  const int b = kk >> 3, pj = 8 * xg + (kk & 7), h = pj >> 1, s = pj & 1;
  const int r4 = lane >> 4, c16 = lane & 15;
  if (wave < 14) STAMP(0);
  if (wave == 1) {
    const int wi = lane - 16;
    const int now = (int)(unsigned)__builtin_amdgcn_s_memrealtime() + REF0;
    ctrl[lane] = (lane == 1 || lane == 2) ? now : (lane < 16 || lane >= 32) ? 0 : wi < NCW ? wi : wi < NCW + NHW ? NHC * NCW + wi - NCW : NEVER;
  }
  asm volatile("" : "+s"(x), "+s"(kc), "+s"(vc), "+s"(wq), "+s"(wo), "+s"(w1), "+s"(w3), "+s"(w2), "+s"(gw), "+s"(out), "+s"(ws));
  BARRIER();

  if (wave == 0) {
    __builtin_amdgcn_s_setprio(3);
    const size_t base = ((size_t)(b * H + h) * SK + (size_t)s * (SK / 2)) * DH * 2;
    const unsigned char* sb = (const unsigned char*)kc + base;
    const unsigned char* vb = (const unsigned char*)vc + base - NKU * UNIT;
    unsigned voff = lane * 16, slot = 0;
    int limit = NSLOT;
    int w = (int)WMID, u = 0;
    unsigned tu = 0;
#pragma unroll 1
    for (;;) {
      stream_run(u, min(limit, u < NKU ? NKU : NUNIT), slot, voff, tu, w, u < NKU ? sb : vb, L_CTRL);
      if (u >= NUNIT) break;
#pragma unroll 1
      for (int spin = 0; spin < SPIN_CAP; ++spin) {
        int nx = ctrl[16 + c16];
        nx = min(nx, dppi<ROR8>(nx)); nx = min(nx, dppi<ROR4>(nx)); nx = min(nx, dppi<ROR2>(nx)); nx = min(nx, dppi<ROR1>(nx));
        limit = (__builtin_amdgcn_readfirstlane(nx) + NSLOT) & ~1;
        if (u < limit) break;
        VM_DRAIN();
        ctrl[0] = u;
        __builtin_amdgcn_s_sleep(1);
      }
    }
#define TAILPUB(n) if (4 * (n) < WMAX) { asm volatile("s_waitcnt vmcnt(%c0)" :: "i"(4 * (n)) : "memory"); ctrl[0] = NUNIT - (n); }
    TAILPUB(6) TAILPUB(5) TAILPUB(4) TAILPUB(3) TAILPUB(2) TAILPUB(1) TAILPUB(0)
    STAMP(1);
    return;
  }

  const __amdgpu_buffer_rsrc_t wsr = mkrsrc(ws, WS_BYTES);
  auto wg_max = [&]() __attribute__((always_inline)) {
    float mg = ctrlf[32 + (c16 < NCW + NHW ? c16 : 0)];
    mg = fmaxf(mg, dpp<ROR8>(mg)); mg = fmaxf(mg, dpp<ROR4>(mg)); mg = fmaxf(mg, dpp<ROR2>(mg)); mg = fmaxf(mg, dpp<ROR1>(mg));
    return mg;
  };

  if (wave == XWAVE + 1) {
    unsigned k = ((unsigned)__builtin_amdgcn_s_memrealtime() >> SLOTSH) + 1;
#pragma unroll 1
    for (int it = 0; it < 4096 >> (SLOTSH - 2); ++it, ++k) {
      wait_until(k << SLOTSH);
      if (__builtin_amdgcn_readfirstlane(ctrl[0]) >= NUNIT - 36) break;
      const int own = __builtin_amdgcn_readfirstlane(ctrl[1]);
      constexpr unsigned SM = (8192u >> SLOTSH) - 1u;
      if (!NOPST && blockIdx.x < 16 && lane < 8) __builtin_amdgcn_raw_buffer_store_b128(u32x4{(unsigned)own, (unsigned)own, (unsigned)own, (unsigned)own}, wsr, WS_PACE + (k & SM) * 2048 + blockIdx.x * 128 + lane * 16, 0, PAUX);
      if (it < PLAG || NOPLD) continue;
      int e[16];
      asm volatile("s_load_dword %0, %16, 0x0\n\ts_load_dword %1, %16, 0x80\n\ts_load_dword %2, %16, 0x100\n\ts_load_dword %3, %16, 0x180\n\t"
                   "s_load_dword %4, %16, 0x200\n\ts_load_dword %5, %16, 0x280\n\ts_load_dword %6, %16, 0x300\n\ts_load_dword %7, %16, 0x380\n\t"
                   "s_load_dword %8, %16, 0x400\n\ts_load_dword %9, %16, 0x480\n\ts_load_dword %10, %16, 0x500\n\ts_load_dword %11, %16, 0x580\n\t"
                   "s_load_dword %12, %16, 0x600\n\ts_load_dword %13, %16, 0x680\n\ts_load_dword %14, %16, 0x700\n\ts_load_dword %15, %16, 0x780\n\t"
                   "s_waitcnt lgkmcnt(0)"
                   : "=&s"(e[0]), "=&s"(e[1]), "=&s"(e[2]), "=&s"(e[3]), "=&s"(e[4]), "=&s"(e[5]), "=&s"(e[6]), "=&s"(e[7]),
                     "=&s"(e[8]), "=&s"(e[9]), "=&s"(e[10]), "=&s"(e[11]), "=&s"(e[12]), "=&s"(e[13]), "=&s"(e[14]), "=&s"(e[15])
                   : "s"(ws + WS_PACE + ((k - PLAG) & SM) * 2048) : "memory");
      int sum = 0, cnt = 0;
#pragma unroll
      for (int i = 0; i < 16; ++i) { const int d = e[i] - own; const bool ok = (unsigned)(d + 600) < 1200u; sum += ok ? d : 0; cnt += ok ? 1 : 0; }
      if (cnt >= 8) ctrl[2] = own + (int)((float)sum / (float)cnt);
    }
    return;
  }

  if (wave == XWAVE) {
#pragma unroll 1
    for (int i = 0; i < XDEL + XSPR * (kk & 31); ++i) PAUSE(4);
    const u32x4 xv = *(const GAS u32x4*)(x + b * D + 8 * lane), gv = *(const GAS u32x4*)(gw + 8 * lane);
    *(LAS u32x4*)(L + L_X + lane * 16) = xv;
    float gd = 0.f;
#pragma unroll
    for (int k = 0; k < 4; ++k) gd = dot2(xv[k], gv[k], gd);
    gd = xrow_sum(row16_sum(gd));
    const float g = 1.f / (1.f + __expf(-gd));
    ctrlf[48] = g;
    lds_arrive(L, C_X, lane);
    u64 tag;
    asm volatile("s_load_dwordx2 %0, %1, 0x0\n\ts_waitcnt lgkmcnt(0)" : "=s"(tag) : "s"(ws + WS_EPOCH + b * 256) : "memory");
    tag += TAG_STEP;
#pragma unroll 1
    for (int spin = 0; spin < SPIN_CAP; ++spin) { if (__builtin_amdgcn_readfirstlane(ctrl[C_MAX]) >= NCW + NHW) break; __builtin_amdgcn_s_sleep(16); }
    CFENCE();
    const float mg = wg_max();
    float l = 0.f;
#pragma unroll 4
    for (int i = 0; i < NKU * 64 / 1024; ++i) {
      const f32x4 sc = *(const LAS f32x4*)(L + L_SC + i * 1024 + lane * 16);
      l += (__builtin_amdgcn_exp2f(sc[0] - mg) + __builtin_amdgcn_exp2f(sc[1] - mg)) + (__builtin_amdgcn_exp2f(sc[2] - mg) + __builtin_amdgcn_exp2f(sc[3] - mg));
    }
    l = xrow_sum(row16_sum(l));
    const u32x4 e = {EMPTY, EMPTY, EMPTY, EMPTY};
#pragma unroll
    for (int i = 0; i < NCLR; ++i) __builtin_amdgcn_raw_buffer_store_b128(e, wsr, WS_Y + (b * 64 + pj) * 2048 + i * 1024 + lane * 16, 0, CLRAUX);
    VM_DRAIN();
    const unsigned tlo = (unsigned)tag, thi = (unsigned)(tag >> 32);
    if (lane < 8) __builtin_amdgcn_raw_buffer_store_b128(u32x4{__float_as_uint(mg), tlo, __float_as_uint(l), thi}, wsr, WS_REC + (b * 64 + pj) * 128 + lane * 16, 0, RAUX);
#pragma unroll 1
    for (int i = 0; i < 12 + (kk & 31); ++i) PAUSE(12);
    u32x4 rec;
#pragma unroll 1
    for (int spin = 0; spin < SPIN_CAP; ++spin) {
      CFENCE();
      rec = __builtin_amdgcn_raw_buffer_load_b128(wsr, WS_REC + (b * 64 + lane) * 128, 0, 16);
      if (__builtin_amdgcn_ballot_w64(rec[1] != tlo || rec[3] != thi) == 0ull) break;
      __builtin_amdgcn_s_sleep(16);
    }
    if (pj == 0 && lane < 8) __builtin_amdgcn_raw_buffer_store_b128(u32x4{tlo, thi, tlo, thi}, wsr, WS_EPOCH + b * 256 + lane * 16, 0, 16);
    const float mo = __uint_as_float(__builtin_amdgcn_readlane(rec[0], pj ^ 1)), lo = __uint_as_float(__builtin_amdgcn_readlane(rec[2], pj ^ 1));
    const float mh = fmaxf(mg, mo), wt = __builtin_amdgcn_exp2f(mg - mh);
    ctrlf[49] = g * wt / (wt * l + __builtin_amdgcn_exp2f(mo - mh) * lo);
    STAMP(4);
    BARRIER();
    BARRIER();
    const int off = PCONT ? WS_Y + (b * 64 + (lane >> 1)) * 2048 + pj * 32 + (lane & 1) * 16 : WS_Y + (b * 64 + pj) * 2048 + lane * 16;
    u32x4 ya, yb;
#pragma unroll 1
    for (int spin = 0; spin < SPIN_CAP; ++spin) {
      CFENCE();
      ya = __builtin_amdgcn_raw_buffer_load_b128(wsr, off, 0, 16);
      yb = __builtin_amdgcn_raw_buffer_load_b128(wsr, off + (PCONT ? 32 * 2048 : 1024), 0, 16);
      const unsigned mx = max(max(max(ya[0], ya[1]), max(ya[2], ya[3])), max(max(yb[0], yb[1]), max(yb[2], yb[3])));
      if (__builtin_amdgcn_ballot_w64(mx == EMPTY) == 0ull) break;
      __builtin_amdgcn_s_sleep(1);
    }
    float o[4];
#pragma unroll
    for (int k = 0; k < 4; ++k) o[k] = __uint_as_float(ya[k]) + __uint_as_float(yb[k]);
    float e0 = swadd32(o[0], o[2]), e1 = swadd32(o[1], o[3]);
    e0 = swadd16(e0, e0); e1 = swadd16(e1, e1);
    e0 += dpp<ROR8>(e0); e1 += dpp<ROR8>(e1); e0 += dpp<ROR4>(e0); e1 += dpp<ROR4>(e1); e0 += dpp<ROR2>(e0); e1 += dpp<ROR2>(e1);
    if ((lane & 30) == 0) __hip_atomic_store((GAS unsigned*)(out + b * D + 8 * pj + 4 * (lane & 1) + 2 * (lane >> 5)), pack_bf16(e0, e1), __ATOMIC_RELAXED, __HIP_MEMORY_SCOPE_AGENT);
    STAMP(8);
    STAMP_FLUSH();
    return;
  }

  const int cw = wave - 1, hw = wave - 1 - NCW;
  int landed = 0;
  auto wait_unit = [&](int u) __attribute__((always_inline)) {
#pragma unroll 1
    for (int spin = 0; landed <= u && spin < SPIN_CAP; ++spin) {
      landed = __builtin_amdgcn_readfirstlane(ctrl[0]);
      if (landed > u) break;
      __builtin_amdgcn_s_sleep(1);
    }
    CFENCE();
  };
  auto take = [&](int u, int next, u32x4& a0, u32x4& a1, u32x4& a2, u32x4& a3) __attribute__((always_inline)) {
    wait_unit(u);
    const LAS u32x4* sp = (const LAS u32x4*)(L + (u & (NSLOT - 1)) * UNIT + lane * 16);
    a0 = sp[0]; a1 = sp[64]; a2 = sp[128]; a3 = sp[192];
    LDS_WAIT();
    ctrl[16 + cw] = next;
    asm volatile("" : "+v"(a0), "+v"(a1), "+v"(a2), "+v"(a3) :: "memory");
  };
  u32x4 q;
  auto build_q = [&]() __attribute__((always_inline)) {
    lds_await(ctrl, C_Q, NHW);
    const LAS f32x4* qp = (const LAS f32x4*)(L + L_QP + (2 * r4 * DH + 8 * c16) * 4);
    const f32x4 lo = qp[0] + qp[DH / 4], hi = qp[1] + qp[DH / 4 + 1];
    q[0] = pack_bf16(xrow_sum(lo[0]), xrow_sum(lo[1])); q[1] = pack_bf16(xrow_sum(lo[2]), xrow_sum(lo[3]));
    q[2] = pack_bf16(xrow_sum(hi[0]), xrow_sum(hi[1])); q[3] = pack_bf16(xrow_sum(hi[2]), xrow_sum(hi[3]));
  };
  constexpr float SCALE = 0.088388347648318440f, LOG2E = 1.4426950408889634f, C2 = SCALE * LOG2E;
  float mx = -INFINITY;
  auto score = [&](int u, const u32x4& k0, const u32x4& k1, const u32x4& k2, const u32x4& k3) __attribute__((always_inline)) {
    float d0 = 0.f, d1 = 0.f, d2 = 0.f, d3 = 0.f;
#pragma unroll
    for (int k = 0; k < 4; ++k) { d0 = dot2(k0[k], q[k], d0); d1 = dot2(k1[k], q[k], d1); d2 = dot2(k2[k], q[k], d2); d3 = dot2(k3[k], q[k], d3); }
    const unsigned s01 = pack_bf16(row16_sum(d0), row16_sum(d1)), s23 = pack_bf16(row16_sum(d2), row16_sum(d3));
    const unsigned t01 = pack_bf16(bflo(s01) * SCALE, bfhi(s01) * SCALE), t23 = pack_bf16(bflo(s23) * SCALE, bfhi(s23) * SCALE);
    const f32x4 sc = {bflo(t01) * LOG2E, bfhi(t01) * LOG2E, bflo(t23) * LOG2E, bfhi(t23) * LOG2E};
    mx = fmaxf(fmaxf(mx, fmaxf(sc[0], sc[1])), fmaxf(sc[2], sc[3]));
    if (c16 == 0) *(LAS f32x4*)(L + L_SC + u * 64 + r4 * 16) = sc;
  };

  if (hw < 0) {
    {
      const __amdgpu_buffer_rsrc_t ro = mkrsrc(wo, H * DH * D * 2);
      const int vo = ((512 * xg + 16 * kk + 4 * cw) * D + 8 * lane) * 2;
      u32x4 t[4];
#pragma unroll
      for (int i = 0; i < 4; ++i) t[i] = __builtin_amdgcn_raw_buffer_load_b128(ro, vo, i * (D * 2), 2);
      asm volatile("" :: "v"(t[0]), "v"(t[1]), "v"(t[2]), "v"(t[3]) : "memory");
#pragma unroll
      for (int i = 0; i < 4; ++i) t[i] = __builtin_amdgcn_raw_buffer_load_b128(ro, vo, i * (D * 2), 16);
      asm volatile("" :: "v"(t[0]), "v"(t[1]), "v"(t[2]), "v"(t[3]) : "memory");
    }
    {
      u32x4 hk[4 * NHC];
#pragma unroll
      for (int i = 0; i < NHC; ++i) take(cw + NCW * i, i + 1 < NHC ? cw + NCW * (i + 1) : NHELD + cw, hk[4 * i], hk[4 * i + 1], hk[4 * i + 2], hk[4 * i + 3]);
      STAMP(10);
      build_q();
      STAMP(3);
#pragma unroll
      for (int i = 0; i < NHC; ++i) { score(cw + NCW * i, hk[4 * i], hk[4 * i + 1], hk[4 * i + 2], hk[4 * i + 3]); __builtin_amdgcn_sched_barrier(0); }
    }
#pragma unroll 1
    for (int u = NHELD + cw; u < NKU; u += NCW) {
      u32x4 k0, k1, k2, k3;
      take(u, u + NCW, k0, k1, k2, k3);
      score(u, k0, k1, k2, k3);
    }
    ctrlf[32 + cw] = xrow_max(mx);
    lds_arrive(L, C_MAX, lane);
    STAMP(11);
    lds_await(ctrl, C_MAX, NCW + NHW);
    const float mg = wg_max();
    STAMP(12);
    float acc[8] = {0.f, 0.f, 0.f, 0.f, 0.f, 0.f, 0.f, 0.f};
#pragma unroll 1
    for (int u = NKU + cw; u < NUNIT; u += NCW) {
      const f32x4 sc = *(const LAS f32x4*)(L + L_SC + (u - NKU) * 64 + r4 * 16);
      float p[4];
#pragma unroll
      for (int t = 0; t < 4; ++t) { p[t] = __builtin_amdgcn_exp2f(sc[t] - mg); asm volatile("" : "+v"(p[t])); }
      u32x4 v[4];
      take(u, u + NCW, v[0], v[1], v[2], v[3]);
#pragma unroll
      for (int t = 0; t < 4; ++t)
#pragma unroll
        for (int k = 0; k < 4; ++k) { acc[2 * k] = fmaf(p[t], bflo(v[t][k]), acc[2 * k]); acc[2 * k + 1] = fmaf(p[t], bfhi(v[t][k]), acc[2 * k + 1]); }
    }
    STAMP(5);
    const float e0 = swadd16(swadd32(acc[0], acc[4]), swadd32(acc[2], acc[6])), e1 = swadd16(swadd32(acc[1], acc[5]), swadd32(acc[3], acc[7]));
    *(LAS f32x2*)(L + L_REC + (cw * DH + 8 * c16 + 2 * r4) * 4) = f32x2{e0, e1};
    BARRIER();
    return;
  }

  u32x4 wb[16];
  const unsigned tstart = (unsigned)__builtin_amdgcn_s_memrealtime();
  {
    const __amdgpu_buffer_rsrc_t rq = mkrsrc(wq, D * H * DH * 2);
    const int vo = ((64 * hw + 16 * r4) * (H * DH) + h * DH + 8 * c16) * 2;
    {
      const __amdgpu_buffer_rsrc_t r1 = mkrsrc(w1, D * F * 2), r3 = mkrsrc(w3, D * F * 2), r2 = mkrsrc(w2, F * D * 2);
      const int oq = ((16 * kk + 2 * hw) * (H * DH) + 512 * xg + 8 * lane) * 2;
      const int o1 = (16 * kk + 2 * hw + (lane >> 5)) * (F * 2) + ((448 * xg) & ~127) + (lane & 31) * 16;
      const int o2 = ((224 * xg + 7 * kk + (hw < 7 ? hw : 6)) * D + 8 * lane) * 2;
#define PFPASS(AUX) { \
        u32x4 t[5] = {}; \
        if (PFW & 2) { t[0] = __builtin_amdgcn_raw_buffer_load_b128(rq, oq, 0, AUX); t[1] = __builtin_amdgcn_raw_buffer_load_b128(rq, oq, H * DH * 2, AUX); } \
        if (PFW & 4) { t[2] = __builtin_amdgcn_raw_buffer_load_b128(r1, o1, 0, AUX); t[3] = __builtin_amdgcn_raw_buffer_load_b128(r3, o1, 0, AUX); } \
        if (PFW & 8) t[4] = __builtin_amdgcn_raw_buffer_load_b128(r2, o2, 0, AUX); \
        asm volatile("" :: "v"(t[0]), "v"(t[1]), "v"(t[2]), "v"(t[3]), "v"(t[4]) : "memory"); \
      }
      PFPASS(2) PFPASS(16)
    }
#pragma unroll
    for (int i = 0; i < 16; ++i) { wait_until(tstart + TQ + hw * QA + i * QB); wb[i] = ldw(rq, vo, i * (H * DH * 2)); }
    lds_await(ctrl, C_X, 1);
    const u32x4 xa = *(const LAS u32x4*)(L + L_X + (64 * hw + 16 * r4) * 2), xc = *(const LAS u32x4*)(L + L_X + (64 * hw + 16 * r4 + 8) * 2);
    float a[8] = {0.f, 0.f, 0.f, 0.f, 0.f, 0.f, 0.f, 0.f};
#pragma unroll
    for (int i = 0; i < 8; ++i) {
      const unsigned xp = i < 4 ? xa[i] : xc[i - 4];
#pragma unroll
      for (int k = 0; k < 4; ++k) {
        a[2 * k] = dot2(perm_lo(wb[2 * i][k], wb[2 * i + 1][k]), xp, a[2 * k]);
        a[2 * k + 1] = dot2(perm_hi(wb[2 * i][k], wb[2 * i + 1][k]), xp, a[2 * k + 1]);
      }
    }
    const float e0 = swadd16(swadd32(a[0], a[4]), swadd32(a[2], a[6])), e1 = swadd16(swadd32(a[1], a[5]), swadd32(a[3], a[7]));
    *(LAS f32x2*)(L + L_QP + (hw * DH + 8 * c16 + 2 * r4) * 4) = f32x2{e0, e1};
    lds_arrive(L, C_Q, lane);
    STAMP(2);
    const int hb = NHC * NCW + hw;
#pragma unroll
    for (int i = 0; i < NHH; ++i) take(hb + NHW * i, i + 1 < NHH ? hb + NHW * (i + 1) : NEVER, wb[4 * i], wb[4 * i + 1], wb[4 * i + 2], wb[4 * i + 3]);
    STAMP(10);
    build_q();
#pragma unroll
    for (int i = 0; i < NHH; ++i) { score(hb + NHW * i, wb[4 * i], wb[4 * i + 1], wb[4 * i + 2], wb[4 * i + 3]); __builtin_amdgcn_sched_barrier(0); }
    ctrlf[32 + cw] = xrow_max(mx);
    lds_arrive(L, C_MAX, lane);
  }
  float z[8] = {0.f, 0.f, 0.f, 0.f, 0.f, 0.f, 0.f, 0.f};
  if ((pj & 7) != 7) {
    const int sl = 7 * (pj >> 3) + (pj & 7), rs = lane >> 3;
    const u16* wp = ((lane & 4) ? w3 : w1) + (size_t)(64 * hw + 8 * rs) * F + FS * sl + 8 * (lane & 3);
#pragma unroll
    for (int i = 0; i < 8; ++i) { wait_until(tstart + T1 + hw * A1 + i * B1); wb[i] = __builtin_nontemporal_load((const GAS u32x4*)(wp + i * F)); }
    const __amdgpu_buffer_rsrc_t r2 = mkrsrc(w2, F * D * 2);
    const u32x4 xm = *(const LAS u32x4*)(L + L_X + (64 * hw + 8 * rs) * 2);
    float a[8] = {0.f, 0.f, 0.f, 0.f, 0.f, 0.f, 0.f, 0.f};
#pragma unroll
    for (int i = 0; i < 4; ++i)
#pragma unroll
      for (int k = 0; k < 4; ++k) {
        a[2 * k] = dot2(perm_lo(wb[2 * i][k], wb[2 * i + 1][k]), xm[i], a[2 * k]);
        a[2 * k + 1] = dot2(perm_hi(wb[2 * i][k], wb[2 * i + 1][k]), xm[i], a[2 * k + 1]);
      }
    float e0 = swadd16(swadd32(a[0], a[4]), swadd32(a[2], a[6])), e1 = swadd16(swadd32(a[1], a[5]), swadd32(a[3], a[7]));
    e0 += dpp<ROR8>(e0); e1 += dpp<ROR8>(e1);
    if ((lane & 8) == 0) *(LAS f32x2*)(L + L_MP + (hw * 64 + 8 * (lane & 7) + 2 * r4) * 4) = f32x2{e0, e1};
    lds_arrive(L, C_MLP, lane);
#pragma unroll
    for (int i = 0; i < 4; ++i) { wait_until(tstart + T2 + hw * A2 + i * B2); wb[8 + i] = ldw(r2, ((FS * sl + 4 * hw) * D + 8 * lane) * 2, i * (D * 2)); }
    lds_await(ctrl, C_MLP, NHW);
    const LAS float* mp = (const LAS float*)(L + L_MP) + rs * 64 + 4 * hw + (lane & 3);
    float ga = mp[0], up = mp[32];
    ga += dpp<ROR8>(ga); ga = xrow_sum(ga); up += dpp<ROR8>(up); up = xrow_sum(up);
    const float hv = ga / (1.f + __expf(-ga)) * up * (1.f - ctrlf[48]);
#pragma unroll
    for (int i = 0; i < 4; ++i) {
      const float hs = __builtin_bit_cast(float, __builtin_amdgcn_readlane(__builtin_bit_cast(int, hv), i));
#pragma unroll
      for (int k = 0; k < 4; ++k) { z[2 * k] = fmaf(hs, bflo(wb[8 + i][k]), z[2 * k]); z[2 * k + 1] = fmaf(hs, bfhi(wb[8 + i][k]), z[2 * k + 1]); }
    }
  }
#pragma unroll
  for (int k = 0; k < 8; ++k) asm volatile("" : "+v"(z[k]));
  {
    const __amdgpu_buffer_rsrc_t ro = mkrsrc(wo, H * DH * D * 2);
#pragma unroll
    for (int i = 0; i < 16; ++i) { wait_until(tstart + TO + hw * AO + i * BO); wb[i] = ldw(ro, ((h * DH + 16 * hw) * D + 8 * lane) * 2, i * (D * 2)); }
#pragma unroll
    for (int i = 0; i < 8; ++i)
#pragma unroll
      for (int k = 0; k < 4; ++k) {
        const unsigned lo = perm_lo(wb[2 * i][k], wb[2 * i + 1][k]), hi = perm_hi(wb[2 * i][k], wb[2 * i + 1][k]);
        wb[2 * i][k] = lo; wb[2 * i + 1][k] = hi;
      }
#pragma unroll
    for (int i = 0; i < 16; ++i) asm volatile("" : "+v"(wb[i]));
  }
  STAMP(4);
  BARRIER();
  STAMP(9);
  {
    const f32x2 a = *(const LAS f32x2*)(L + L_REC + (r4 * DH + 16 * hw + 2 * (lane & 7)) * 4);
    const float cf = ctrlf[49];
    const unsigned op = pack_bf16(xrow_sum(a[0]), xrow_sum(a[1]));
    float y[8] = {0.f, 0.f, 0.f, 0.f, 0.f, 0.f, 0.f, 0.f};
#pragma unroll
    for (int i = 0; i < 8; ++i) {
      const unsigned os = __builtin_amdgcn_readlane(op, i);
#pragma unroll
      for (int k = 0; k < 4; ++k) { y[2 * k] = dot2(wb[2 * i][k], os, y[2 * k]); y[2 * k + 1] = dot2(wb[2 * i + 1][k], os, y[2 * k + 1]); }
    }
    LAS f32x4* yp = (LAS f32x4*)(L + L_YP + (hw * D + 8 * lane) * 4);
    yp[0] = f32x4{fmaf(cf, y[0], z[0]), fmaf(cf, y[1], z[1]), fmaf(cf, y[2], z[2]), fmaf(cf, y[3], z[3])};
    yp[1] = f32x4{fmaf(cf, y[4], z[4]), fmaf(cf, y[5], z[5]), fmaf(cf, y[6], z[6]), fmaf(cf, y[7], z[7])};
  }
  BARRIER();
  STAMP(6);
  if (hw < 2) {
    const int t = hw * 64 + lane;
    const LAS f32x4* yp = (const LAS f32x4*)(L + L_YP) + t;
    f32x4 sum = yp[0];
#pragma unroll
    for (int w = 1; w < NHW; ++w) sum += yp[w * (D / 4)];
    u32x4 v;
#pragma unroll
    for (int k = 0; k < 4; ++k) v[k] = min(__float_as_uint(sum[k]), EMPTY - 1u);
    __builtin_amdgcn_raw_buffer_store_b128(v, wsr, PCONT ? WS_Y + (b * 64 + pj) * 2048 + t * 16 : WS_Y + (b * 64 + (t >> 1)) * 2048 + pj * 32 + (t & 1) * 16, 0, 16);
  }
  STAMP(7);
}

extern "C" void kernel_launch(void* const* d_in, const int* in_sizes, int n_in, void* d_out, int out_size, void* d_ws, size_t ws_size,
                              hipStream_t stream) {
  gated_mix_kernel<<<256, NTHR, 0, stream>>>((const u16*)d_in[0], (const u16*)d_in[1], (const u16*)d_in[2], (const u16*)d_in[3], (const u16*)d_in[4],
                                             (const u16*)d_in[6], (const u16*)d_in[7], (const u16*)d_in[8], (const u16*)d_in[9], (u16*)d_out,
                                             (unsigned char*)d_ws);
}
```

```cpp
#include <hip/hip_runtime.h>
#include <hip/hip_bf16.h>
#define STAMPS 1
#define MWAVE 5
#define MIDX 9
#define MTYPE 0
#define RWAVE 0
#define RIDX 1
#define MSCALE 10.f

#define GAS __attribute__((address_space(1)))
#define LAS __attribute__((address_space(3)))
typedef unsigned short u16;
typedef unsigned long long u64;
using u32x4 = __attribute__((ext_vector_type(4))) unsigned;
using u32x2 = __attribute__((ext_vector_type(2))) unsigned;
using f32x4 = __attribute__((ext_vector_type(4))) float;
using f32x2 = __attribute__((ext_vector_type(2))) float;
using bf16x2 = __attribute__((ext_vector_type(2))) __bf16;

constexpr int B = 4, D = 512, H = 32, SK = 8192, DH = 128, F = 1792;
constexpr int NCW = 4, NHW = 8, XWAVE = 1 + NCW + NHW, NWAVE = XWAVE + 2, NTHR = NWAVE * 64;
#define QA 80
#define QB 5
#define T1 1200
#define A1 150
#define B1 18
#define T2 2500
#define A2 24
#define B2 6
#define TO 2900
#define AO 350
#define BO 12
#define TS 70.6
#define REF0 150
#define PLAG 1
#define NOPST 0
#define NOPLD 0
#define PAUX 16
#define RAUX 16
#define PFW 1
#define TQ 0
#define SLOTSH 8
#define WMID 20.5
#define WGAIN 8
#define WMIN 14
#define WMAX 27
#define CLRAUX 18
#define PCONT 1
#define XDEL 0
#define XSPR 0
#define XNT 0
#define MIMIC 1
#define NCLR 2
#define NOFLAG 0
#define CSPR 0
constexpr int UNIT = 4096;
constexpr int NSLOT = 32, RING = NSLOT * UNIT;
constexpr int NKU = (SK / 2) / 16, NUNIT = 2 * NKU;
constexpr int NHC = 6, NHH = 4, NHELD = NHC * NCW + NHH * NHW;
constexpr int FS = 32;
constexpr int L_CTRL = RING;
constexpr int L_X = L_CTRL + 256;
constexpr int L_QP = L_X + D * 2;
constexpr int L_MP = L_QP + NHW * DH * 4;
constexpr int L_REC = L_MP + NHW * 64 * 4;
constexpr int L_SC = L_REC + NCW * DH * 4;
constexpr int L_YP = L_SC;
constexpr int LDS_BYTES = L_SC + NKU * 64;
static_assert(LDS_BYTES <= 159 * 1024 && NKU * 64 >= NHW * D * 4, "lds");
static_assert(WMIN <= (int)WMID && (int)WMID <= WMAX && WMAX <= 28, "window");
constexpr int WS_EPOCH = 0;
constexpr int WS_REC = 4096;
constexpr int WS_PACE = WS_REC + 256 * 128;
constexpr int WS_Y = WS_PACE + 64 * 2048;
constexpr int WS_BYTES = WS_Y + B * 64 * 2048;
constexpr u64 TAG_STEP = 0x9E3779B97F4A7C15ull;
constexpr unsigned EMPTY = 0xFFFFFFFFu;
constexpr int SPIN_CAP = 1 << 18;
constexpr int NEVER = 1 << 20;
constexpr int C_Q = 4, C_MAX = 5, C_MLP = 6, C_X = 7;

__device__ __forceinline__ float bflo(unsigned u) { return __builtin_bit_cast(float, u << 16); }
__device__ __forceinline__ float bfhi(unsigned u) { return __builtin_bit_cast(float, u & 0xffff0000u); }
__device__ __forceinline__ float dot2(unsigned a, unsigned b, float acc) {
  return __builtin_amdgcn_fdot2_f32_bf16(__builtin_bit_cast(bf16x2, a), __builtin_bit_cast(bf16x2, b), acc, false);
}
__device__ __forceinline__ unsigned pack_bf16(float lo, float hi) {
  const bf16x2 v = {(__bf16)lo, (__bf16)hi};
  return __builtin_bit_cast(unsigned, v);
}
__device__ __forceinline__ unsigned perm_lo(unsigned a, unsigned b) { return __builtin_amdgcn_perm(b, a, 0x05040100u); }
__device__ __forceinline__ unsigned perm_hi(unsigned a, unsigned b) { return __builtin_amdgcn_perm(b, a, 0x07060302u); }
template <int CTRL> __device__ __forceinline__ float dpp(float x) {
  return __builtin_bit_cast(float, __builtin_amdgcn_mov_dpp(__builtin_bit_cast(int, x), CTRL, 0xf, 0xf, true));
}
constexpr int XOR1 = 0xB1, ROR1 = 0x121, ROR2 = 0x122, ROR4 = 0x124, ROR8 = 0x128;
__device__ __forceinline__ float row16_sum(float x) { x += dpp<ROR8>(x); x += dpp<ROR4>(x); x += dpp<ROR2>(x); x += dpp<ROR1>(x); return x; }
__device__ __forceinline__ float swadd32(float a, float b) {
  auto t = __builtin_amdgcn_permlane32_swap(__float_as_uint(a), __float_as_uint(b), false, false);
  return __uint_as_float(t[0]) + __uint_as_float(t[1]);
}
__device__ __forceinline__ float swadd16(float a, float b) {
  auto t = __builtin_amdgcn_permlane16_swap(__float_as_uint(a), __float_as_uint(b), false, false);
  return __uint_as_float(t[0]) + __uint_as_float(t[1]);
}
__device__ __forceinline__ float xrow_sum(float x) { x = swadd16(x, x); return swadd32(x, x); }
__device__ __forceinline__ float xrow_max(float x) {
  auto s = __builtin_amdgcn_permlane16_swap(__float_as_uint(x), __float_as_uint(x), false, false);
  x = fmaxf(__uint_as_float(s[0]), __uint_as_float(s[1]));
  auto t = __builtin_amdgcn_permlane32_swap(__float_as_uint(x), __float_as_uint(x), false, false);
  return fmaxf(__uint_as_float(t[0]), __uint_as_float(t[1]));
}
__device__ __forceinline__ __amdgpu_buffer_rsrc_t mkrsrc(const void* p, int bytes) { return __builtin_amdgcn_make_buffer_rsrc((void*)p, 0, bytes, 0x00020000); }
__device__ __forceinline__ u32x4 ldw(__amdgpu_buffer_rsrc_t r, int voff, int soff) { return __builtin_amdgcn_raw_buffer_load_b128(r, voff, soff, 2); }
#define LDS_WAIT() asm volatile("s_waitcnt lgkmcnt(0)" ::: "memory")
#define VM_DRAIN() asm volatile("s_waitcnt vmcnt(0)" ::: "memory")
#define CFENCE() asm volatile("" ::: "memory")
#define BARRIER() asm volatile("s_waitcnt lgkmcnt(0)\n\ts_barrier" ::: "memory")
#define PAUSE(n) do { __builtin_amdgcn_sched_barrier(0); asm volatile("s_sleep %0" :: "i"(n) : "memory"); __builtin_amdgcn_sched_barrier(0); } while (0)

__device__ __forceinline__ void lds_arrive(LAS unsigned char* L, int idx, int lane) {
  LDS_WAIT();
  if (lane == 0) __hip_atomic_fetch_add((LAS int*)(L + L_CTRL) + idx, 1, __ATOMIC_RELAXED, __HIP_MEMORY_SCOPE_WORKGROUP);
}
__device__ __forceinline__ void lds_await(volatile LAS int* ctrl, int idx, int n) {
#pragma unroll 1
  for (int spin = 0; spin < SPIN_CAP; ++spin) { if (__builtin_amdgcn_readfirstlane(ctrl[idx]) >= n) break; __builtin_amdgcn_s_sleep(1); }
  CFENCE();
}

__device__ __forceinline__ void wait_until(unsigned t) {
  __builtin_amdgcn_sched_barrier(0);
#pragma unroll 1
  for (int spin = 0; spin < 4096; ++spin) {
    const int d = (int)(t - (unsigned)__builtin_amdgcn_s_memrealtime());
    if (d <= 0) break;
    if (d > 24) __builtin_amdgcn_s_sleep(6);
  }
  CFENCE();
  __builtin_amdgcn_sched_barrier(0);
}
#define STR2(x) #x
#define STR(x) STR2(x)
constexpr int TAU2 = 2 * (int)(TS * 100.0 * 256.0 / NUNIT);
__device__ __forceinline__ void stream_run(int& u, int uend, unsigned& slot, unsigned& vo, unsigned& tu, int& w, const unsigned char* sb, unsigned pa) {
  unsigned vo2, pv, ph, rf;
  asm volatile(
      "s_getpc_b64 s[40:41]\n"
      "1:\n\t"
      "s_add_u32 s40, s40, 3f-1b-%c[wmin]*128\n\t"
      "s_addc_u32 s41, s41, -1\n"
      "2:\n\t"
      "s_memrealtime s[44:45]\n\t"
      "ds_read_b32 %[rf], %[pa] offset:8\n\t"
      "s_lshl_b32 s42, %[w], 7\n\t"
      "s_add_u32 s42, s40, s42\n\t"
      "s_addc_u32 s43, s41, 0\n\t"
      "s_add_i32 s46, %[w], -1\n\t"
      "s_lshr_b32 s46, s46, 2\n\t"
      "s_sub_i32 s46, %[u], s46\n\t"
      "v_mov_b32 %[pv], s46\n\t"
      "v_add_u32 %[vo2], 0x1000, %[vo]\n\t"
      "s_add_u32 s47, %[slot], 0x1000\n\t"
      "s_mov_b32 m0, %[slot]\n\t"
      "s_setpc_b64 s[42:43]\n\t"
      ".p2align 7\n"
      "3:\n\t"
      ".set wcnt, %c[wmin]-1\n\t"
      ".rept %c[nvar]\n\t"
      "s_waitcnt (wcnt & 15) | ((wcnt >> 4) << 14) | 0x0F70\n\t"
      "global_load_lds_dwordx4 %[vo], %[sb] nt\n\t"
      "s_waitcnt (wcnt & 15) | ((wcnt >> 4) << 14) | 0x0F70\n\t"
      "global_load_lds_dwordx4 %[vo], %[sb] offset:1024 nt\n\t"
      "s_waitcnt (wcnt & 15) | ((wcnt >> 4) << 14) | 0x0F70\n\t"
      "global_load_lds_dwordx4 %[vo], %[sb] offset:2048 nt\n\t"
      "s_waitcnt (wcnt & 15) | ((wcnt >> 4) << 14) | 0x0F70\n\t"
      "global_load_lds_dwordx4 %[vo], %[sb] offset:3072 nt\n\t"
      "ds_write_b32 %[pa], %[pv]\n\t"
      "s_mov_b32 m0, s47\n\t"
      "s_waitcnt (wcnt & 15) | ((wcnt >> 4) << 14) | 0x0F70\n\t"
      "global_load_lds_dwordx4 %[vo2], %[sb] nt\n\t"
      "s_waitcnt (wcnt & 15) | ((wcnt >> 4) << 14) | 0x0F70\n\t"
      "global_load_lds_dwordx4 %[vo2], %[sb] offset:1024 nt\n\t"
      "s_waitcnt (wcnt & 15) | ((wcnt >> 4) << 14) | 0x0F70\n\t"
      "global_load_lds_dwordx4 %[vo2], %[sb] offset:2048 nt\n\t"
      "s_waitcnt (wcnt & 15) | ((wcnt >> 4) << 14) | 0x0F70\n\t"
      "global_load_lds_dwordx4 %[vo2], %[sb] offset:3072 nt\n\t"
      "s_branch 4f\n\t"
      ".p2align 7\n\t"
      ".set wcnt, wcnt+1\n\t"
      ".endr\n"
      "4:\n\t"
      "s_waitcnt lgkmcnt(0)\n\t"
      "v_readfirstlane_b32 s45, %[rf]\n\t"
      "v_add_u32 %[pv], 1, %[pv]\n\t"
      "s_lshr_b32 s46, %[tu], 8\n\t"
      "s_sub_u32 s44, s44, s46\n\t"
      "v_mov_b32 %[ph], s44\n\t"
      "s_sub_i32 s44, s44, s45\n\t"
      "ds_write2_b32 %[pa], %[pv], %[ph] offset1:1\n\t"
      "s_mul_i32 s44, s44, %c[gain]\n\t"
      "s_add_i32 s44, s44, %c[wmid]\n\t"
      "s_ashr_i32 s44, s44, 8\n\t"
      "s_max_i32 s44, s44, %c[wmin]\n\t"
      "s_min_i32 %[w], s44, %c[wmax]\n\t"
      "s_add_u32 %[tu], %[tu], %c[tau2]\n\t"
      "s_add_u32 %[slot], %[slot], 0x2000\n\t"
      "s_and_b32 %[slot], %[slot], %c[mask]\n\t"
      "v_add_u32 %[vo], 0x2000, %[vo]\n\t"
      "s_add_i32 %[u], %[u], 2\n\t"
      "s_cmp_lt_i32 %[u], %[uend]\n\t"
      "s_cbranch_scc1 2b"
      : [u] "+s"(u), [slot] "+s"(slot), [vo] "+v"(vo), [tu] "+s"(tu), [w] "+s"(w), [vo2] "=&v"(vo2), [pv] "=&v"(pv), [ph] "=&v"(ph), [rf] "=&v"(rf)
      : [uend] "s"(uend), [sb] "s"(sb), [pa] "v"(pa), [wmin] "i"(WMIN), [wmax] "i"(WMAX), [nvar] "i"(WMAX - WMIN + 1), [gain] "i"(WGAIN),
        [wmid] "i"((int)(WMID * 256)), [tau2] "i"(TAU2), [mask] "i"(RING - 1)
      : "memory", "scc", "s40", "s41", "s42", "s43", "s44", "s45", "s46", "s47");
}
template <int CTRL> __device__ __forceinline__ int dppi(int x) { return __builtin_amdgcn_mov_dpp(x, CTRL, 0xf, 0xf, true); }

#define STAMP(i) do { __builtin_amdgcn_sched_barrier(0); ((volatile LAS unsigned*)(L + LDS_BYTES))[wave * 16 + (i)] = (unsigned)__builtin_amdgcn_s_memrealtime(); __builtin_amdgcn_sched_barrier(0); } while (0)
#define STAMP_FLUSH() do { LDS_WAIT(); const __amdgpu_buffer_rsrc_t dr = mkrsrc(ws + (4 << 20), 256 * 1024); \
    __builtin_amdgcn_raw_buffer_store_b128(*(volatile LAS u32x4*)(L + LDS_BYTES + lane * 16), dr, blockIdx.x * 1024 + lane * 16, 0, 16); \
    if (blockIdx.x == 0) { const unsigned t0 = __builtin_amdgcn_readfirstlane(((volatile LAS unsigned*)(L + LDS_BYTES))[0]); \
      while ((unsigned)__builtin_amdgcn_s_memrealtime() - t0 < 13000u) __builtin_amdgcn_s_sleep(8); \
      CFENCE(); float sum = 0.f, mxv = -1e9f; \
      for (int i = 0; i < 4; ++i) { const int wg = lane * 4 + i; \
        const unsigned ref = (RWAVE < 0) ? t0 : __builtin_amdgcn_raw_buffer_load_b32(dr, wg * 1024 + ((RWAVE < 0 ? 0 : RWAVE) * 16 + RIDX) * 4, 0, 16); \
        const float v = (float)(int)(__builtin_amdgcn_raw_buffer_load_b32(dr, wg * 1024 + (MWAVE * 16 + MIDX) * 4, 0, 16) - ref); \
        sum += v; mxv = fmaxf(mxv, v); } \
      sum = xrow_sum(row16_sum(sum)) * (1.f / 256.f); \
      float gm = mxv; gm = fmaxf(gm, dpp<ROR8>(gm)); gm = fmaxf(gm, dpp<ROR4>(gm)); gm = fmaxf(gm, dpp<ROR2>(gm)); gm = fmaxf(gm, dpp<ROR1>(gm)); gm = xrow_max(gm); \
      const float met = (MTYPE == 0 ? sum : gm) * MSCALE; \
      const unsigned tgt = __builtin_amdgcn_readfirstlane((unsigned)(15000.f + met)); \
      while ((unsigned)__builtin_amdgcn_s_memrealtime() - t0 < tgt) __builtin_amdgcn_s_sleep(1); } } while (0)
#define STAMP_LDS 1024

__global__ void __launch_bounds__(NTHR) gated_mix_kernel(const u16* x, const u16* kc, const u16* vc, const u16* wq, const u16* wo, const u16* w1,
                                                         const u16* w3, const u16* w2, const u16* gw, u16* out, unsigned char* ws) {
  __shared__ __attribute__((aligned(16))) unsigned char lds_raw[LDS_BYTES + STAMP_LDS];
  LAS unsigned char* L = (LAS unsigned char*)lds_raw;
  volatile LAS int* ctrl = (volatile LAS int*)(L + L_CTRL);
  volatile LAS float* ctrlf = (volatile LAS float*)(L + L_CTRL);
  const int tid = threadIdx.x, lane = tid & 63, wave = __builtin_amdgcn_readfirstlane(tid >> 6);
  const int xg = blockIdx.x & 7, kk = blockIdx.x >> 3;
  const int b = kk >> 3, pj = 8 * xg + (kk & 7), h = pj >> 1, s = pj & 1;
  const int r4 = lane >> 4, c16 = lane & 15;
  if (wave < 14) STAMP(0);
  if (wave == 1) {
    const int wi = lane - 16;
    const int now = (int)(unsigned)__builtin_amdgcn_s_memrealtime() + REF0;
    ctrl[lane] = (lane == 1 || lane == 2) ? now : (lane < 16 || lane >= 32) ? 0 : wi < NCW ? wi : wi < NCW + NHW ? NHC * NCW + wi - NCW : NEVER;
  }
  asm volatile("" : "+s"(x), "+s"(kc), "+s"(vc), "+s"(wq), "+s"(wo), "+s"(w1), "+s"(w3), "+s"(w2), "+s"(gw), "+s"(out), "+s"(ws));
  BARRIER();

  if (wave == 0) {
    __builtin_amdgcn_s_setprio(3);
    const size_t base = ((size_t)(b * H + h) * SK + (size_t)s * (SK / 2)) * DH * 2;
    const unsigned char* sb = (const unsigned char*)kc + base;
    const unsigned char* vb = (const unsigned char*)vc + base - NKU * UNIT;
    unsigned voff = lane * 16, slot = 0;
    int limit = NSLOT;
    int w = (int)WMID, u = 0;
    unsigned tu = 0;
#pragma unroll 1
    for (;;) {
      stream_run(u, min(limit, u < NKU ? NKU : NUNIT), slot, voff, tu, w, u < NKU ? sb : vb, L_CTRL);
      { const int ci = (u - 1) >> 5; LDS_WAIT();
        ((volatile LAS int*)(L + LDS_BYTES))[14 * 16 + ci] = ctrl[1] - ((volatile LAS int*)(L + LDS_BYTES))[0] + (int)((TS - 69.3) * 100 / 512 * u) ; ((volatile LAS int*)(L + LDS_BYTES))[15 * 16 + ci] = w; }
      if (u >= NUNIT) break;
#pragma unroll 1
      for (int spin = 0; spin < SPIN_CAP; ++spin) {
        int nx = ctrl[16 + c16];
        nx = min(nx, dppi<ROR8>(nx)); nx = min(nx, dppi<ROR4>(nx)); nx = min(nx, dppi<ROR2>(nx)); nx = min(nx, dppi<ROR1>(nx));
        limit = (__builtin_amdgcn_readfirstlane(nx) + NSLOT) & ~1;
        if (u < limit) break;
        VM_DRAIN();
        ctrl[0] = u;
        __builtin_amdgcn_s_sleep(1);
      }
    }
#define TAILPUB(n) if (4 * (n) < WMAX) { asm volatile("s_waitcnt vmcnt(%c0)" :: "i"(4 * (n)) : "memory"); ctrl[0] = NUNIT - (n); }
    TAILPUB(6) TAILPUB(5) TAILPUB(4) TAILPUB(3) TAILPUB(2) TAILPUB(1) TAILPUB(0)
    STAMP(1);
    return;
  }

  const __amdgpu_buffer_rsrc_t wsr = mkrsrc(ws, WS_BYTES);
  auto wg_max = [&]() __attribute__((always_inline)) {
    float mg = ctrlf[32 + (c16 < NCW + NHW ? c16 : 0)];
    mg = fmaxf(mg, dpp<ROR8>(mg)); mg = fmaxf(mg, dpp<ROR4>(mg)); mg = fmaxf(mg, dpp<ROR2>(mg)); mg = fmaxf(mg, dpp<ROR1>(mg));
    return mg;
  };

  if (wave == XWAVE + 1) {
    unsigned k = ((unsigned)__builtin_amdgcn_s_memrealtime() >> SLOTSH) + 1;
#pragma unroll 1
    for (int it = 0; it < 4096 >> (SLOTSH - 2); ++it, ++k) {
      wait_until(k << SLOTSH);
      if (__builtin_amdgcn_readfirstlane(ctrl[0]) >= NUNIT - 36) break;
      const int own = __builtin_amdgcn_readfirstlane(ctrl[1]);
      constexpr unsigned SM = (8192u >> SLOTSH) - 1u;
      if (!NOPST && blockIdx.x < 16 && lane < 8) __builtin_amdgcn_raw_buffer_store_b128(u32x4{(unsigned)own, (unsigned)own, (unsigned)own, (unsigned)own}, wsr, WS_PACE + (k & SM) * 2048 + blockIdx.x * 128 + lane * 16, 0, PAUX);
      if (it < PLAG || NOPLD) continue;
      int e[16];
      asm volatile("s_load_dword %0, %16, 0x0\n\ts_load_dword %1, %16, 0x80\n\ts_load_dword %2, %16, 0x100\n\ts_load_dword %3, %16, 0x180\n\t"
                   "s_load_dword %4, %16, 0x200\n\ts_load_dword %5, %16, 0x280\n\ts_load_dword %6, %16, 0x300\n\ts_load_dword %7, %16, 0x380\n\t"
                   "s_load_dword %8, %16, 0x400\n\ts_load_dword %9, %16, 0x480\n\ts_load_dword %10, %16, 0x500\n\ts_load_dword %11, %16, 0x580\n\t"
                   "s_load_dword %12, %16, 0x600\n\ts_load_dword %13, %16, 0x680\n\ts_load_dword %14, %16, 0x700\n\ts_load_dword %15, %16, 0x780\n\t"
                   "s_waitcnt lgkmcnt(0)"
                   : "=&s"(e[0]), "=&s"(e[1]), "=&s"(e[2]), "=&s"(e[3]), "=&s"(e[4]), "=&s"(e[5]), "=&s"(e[6]), "=&s"(e[7]),
                     "=&s"(e[8]), "=&s"(e[9]), "=&s"(e[10]), "=&s"(e[11]), "=&s"(e[12]), "=&s"(e[13]), "=&s"(e[14]), "=&s"(e[15])
                   : "s"(ws + WS_PACE + ((k - PLAG) & SM) * 2048) : "memory");
      int sum = 0, cnt = 0;
#pragma unroll
      for (int i = 0; i < 16; ++i) { const int d = e[i] - own; const bool ok = (unsigned)(d + 600) < 1200u; sum += ok ? d : 0; cnt += ok ? 1 : 0; }
      if (cnt >= 8) ctrl[2] = own + (int)((float)sum / (float)cnt);
    }
    return;
  }

  if (wave == XWAVE) {
#pragma unroll 1
    for (int i = 0; i < XDEL + XSPR * (kk & 31); ++i) PAUSE(4);
    const u32x4 xv = *(const GAS u32x4*)(x + b * D + 8 * lane), gv = *(const GAS u32x4*)(gw + 8 * lane);
    *(LAS u32x4*)(L + L_X + lane * 16) = xv;
    float gd = 0.f;
#pragma unroll
    for (int k = 0; k < 4; ++k) gd = dot2(xv[k], gv[k], gd);
    gd = xrow_sum(row16_sum(gd));
    const float g = 1.f / (1.f + __expf(-gd));
    ctrlf[48] = g;
    lds_arrive(L, C_X, lane);
    u64 tag;
    asm volatile("s_load_dwordx2 %0, %1, 0x0\n\ts_waitcnt lgkmcnt(0)" : "=s"(tag) : "s"(ws + WS_EPOCH + b * 256) : "memory");
    tag += TAG_STEP;
#pragma unroll 1
    for (int spin = 0; spin < SPIN_CAP; ++spin) { if (__builtin_amdgcn_readfirstlane(ctrl[C_MAX]) >= NCW + NHW) break; __builtin_amdgcn_s_sleep(16); }
    CFENCE();
    const float mg = wg_max();
    float l = 0.f;
#pragma unroll 4
    for (int i = 0; i < NKU * 64 / 1024; ++i) {
      const f32x4 sc = *(const LAS f32x4*)(L + L_SC + i * 1024 + lane * 16);
      l += (__builtin_amdgcn_exp2f(sc[0] - mg) + __builtin_amdgcn_exp2f(sc[1] - mg)) + (__builtin_amdgcn_exp2f(sc[2] - mg) + __builtin_amdgcn_exp2f(sc[3] - mg));
    }
    l = xrow_sum(row16_sum(l));
    const u32x4 e = {EMPTY, EMPTY, EMPTY, EMPTY};
#pragma unroll
    for (int i = 0; i < NCLR; ++i) __builtin_amdgcn_raw_buffer_store_b128(e, wsr, WS_Y + (b * 64 + pj) * 2048 + i * 1024 + lane * 16, 0, CLRAUX);
    VM_DRAIN();
    const unsigned tlo = (unsigned)tag, thi = (unsigned)(tag >> 32);
    if (lane < 8) __builtin_amdgcn_raw_buffer_store_b128(u32x4{__float_as_uint(mg), tlo, __float_as_uint(l), thi}, wsr, WS_REC + (b * 64 + pj) * 128 + lane * 16, 0, RAUX);
#pragma unroll 1
    for (int i = 0; i < 12 + (kk & 31); ++i) PAUSE(12);
    u32x4 rec;
#pragma unroll 1
    for (int spin = 0; spin < SPIN_CAP; ++spin) {
      CFENCE();
      rec = __builtin_amdgcn_raw_buffer_load_b128(wsr, WS_REC + (b * 64 + lane) * 128, 0, 16);
      if (__builtin_amdgcn_ballot_w64(rec[1] != tlo || rec[3] != thi) == 0ull) break;
      __builtin_amdgcn_s_sleep(16);
    }
    if (pj == 0 && lane < 8) __builtin_amdgcn_raw_buffer_store_b128(u32x4{tlo, thi, tlo, thi}, wsr, WS_EPOCH + b * 256 + lane * 16, 0, 16);
    const float mo = __uint_as_float(__builtin_amdgcn_readlane(rec[0], pj ^ 1)), lo = __uint_as_float(__builtin_amdgcn_readlane(rec[2], pj ^ 1));
    const float mh = fmaxf(mg, mo), wt = __builtin_amdgcn_exp2f(mg - mh);
    ctrlf[49] = g * wt / (wt * l + __builtin_amdgcn_exp2f(mo - mh) * lo);
    STAMP(4);
    BARRIER();
    BARRIER();
    const int off = PCONT ? WS_Y + (b * 64 + (lane >> 1)) * 2048 + pj * 32 + (lane & 1) * 16 : WS_Y + (b * 64 + pj) * 2048 + lane * 16;
    u32x4 ya, yb;
#pragma unroll 1
    for (int spin = 0; spin < SPIN_CAP; ++spin) {
      CFENCE();
      ya = __builtin_amdgcn_raw_buffer_load_b128(wsr, off, 0, 16);
      yb = __builtin_amdgcn_raw_buffer_load_b128(wsr, off + (PCONT ? 32 * 2048 : 1024), 0, 16);
      const unsigned mx = max(max(max(ya[0], ya[1]), max(ya[2], ya[3])), max(max(yb[0], yb[1]), max(yb[2], yb[3])));
      if (__builtin_amdgcn_ballot_w64(mx == EMPTY) == 0ull) break;
      __builtin_amdgcn_s_sleep(1);
    }
    float o[4];
#pragma unroll
    for (int k = 0; k < 4; ++k) o[k] = __uint_as_float(ya[k]) + __uint_as_float(yb[k]);
    float e0 = swadd32(o[0], o[2]), e1 = swadd32(o[1], o[3]);
    e0 = swadd16(e0, e0); e1 = swadd16(e1, e1);
    e0 += dpp<ROR8>(e0); e1 += dpp<ROR8>(e1); e0 += dpp<ROR4>(e0); e1 += dpp<ROR4>(e1); e0 += dpp<ROR2>(e0); e1 += dpp<ROR2>(e1);
    if ((lane & 30) == 0) __hip_atomic_store((GAS unsigned*)(out + b * D + 8 * pj + 4 * (lane & 1) + 2 * (lane >> 5)), pack_bf16(e0, e1), __ATOMIC_RELAXED, __HIP_MEMORY_SCOPE_AGENT);
    STAMP(8);
    STAMP_FLUSH();
    return;
  }

  const int cw = wave - 1, hw = wave - 1 - NCW;
  int landed = 0;
  auto wait_unit = [&](int u) __attribute__((always_inline)) {
#pragma unroll 1
    for (int spin = 0; landed <= u && spin < SPIN_CAP; ++spin) {
      landed = __builtin_amdgcn_readfirstlane(ctrl[0]);
      if (landed > u) break;
      __builtin_amdgcn_s_sleep(1);
    }
    CFENCE();
  };
  auto take = [&](int u, int next, u32x4& a0, u32x4& a1, u32x4& a2, u32x4& a3) __attribute__((always_inline)) {
    wait_unit(u);
    const LAS u32x4* sp = (const LAS u32x4*)(L + (u & (NSLOT - 1)) * UNIT + lane * 16);
    a0 = sp[0]; a1 = sp[64]; a2 = sp[128]; a3 = sp[192];
    LDS_WAIT();
    ctrl[16 + cw] = next;
    asm volatile("" : "+v"(a0), "+v"(a1), "+v"(a2), "+v"(a3) :: "memory");
  };
  u32x4 q;
  auto build_q = [&]() __attribute__((always_inline)) {
    lds_await(ctrl, C_Q, NHW);
    const LAS f32x4* qp = (const LAS f32x4*)(L + L_QP + (2 * r4 * DH + 8 * c16) * 4);
    const f32x4 lo = qp[0] + qp[DH / 4], hi = qp[1] + qp[DH / 4 + 1];
    q[0] = pack_bf16(xrow_sum(lo[0]), xrow_sum(lo[1])); q[1] = pack_bf16(xrow_sum(lo[2]), xrow_sum(lo[3]));
    q[2] = pack_bf16(xrow_sum(hi[0]), xrow_sum(hi[1])); q[3] = pack_bf16(xrow_sum(hi[2]), xrow_sum(hi[3]));
  };
  constexpr float SCALE = 0.088388347648318440f, LOG2E = 1.4426950408889634f, C2 = SCALE * LOG2E;
  float mx = -INFINITY;
  auto score = [&](int u, const u32x4& k0, const u32x4& k1, const u32x4& k2, const u32x4& k3) __attribute__((always_inline)) {
    float d0 = 0.f, d1 = 0.f, d2 = 0.f, d3 = 0.f;
#pragma unroll
    for (int k = 0; k < 4; ++k) { d0 = dot2(k0[k], q[k], d0); d1 = dot2(k1[k], q[k], d1); d2 = dot2(k2[k], q[k], d2); d3 = dot2(k3[k], q[k], d3); }
    const unsigned s01 = pack_bf16(row16_sum(d0), row16_sum(d1)), s23 = pack_bf16(row16_sum(d2), row16_sum(d3));
    const unsigned t01 = pack_bf16(bflo(s01) * SCALE, bfhi(s01) * SCALE), t23 = pack_bf16(bflo(s23) * SCALE, bfhi(s23) * SCALE);
    const f32x4 sc = {bflo(t01) * LOG2E, bfhi(t01) * LOG2E, bflo(t23) * LOG2E, bfhi(t23) * LOG2E};
    mx = fmaxf(fmaxf(mx, fmaxf(sc[0], sc[1])), fmaxf(sc[2], sc[3]));
    if (c16 == 0) *(LAS f32x4*)(L + L_SC + u * 64 + r4 * 16) = sc;
  };

  if (hw < 0) {
    {
      const __amdgpu_buffer_rsrc_t ro = mkrsrc(wo, H * DH * D * 2);
      const int vo = ((512 * xg + 16 * kk + 4 * cw) * D + 8 * lane) * 2;
      u32x4 t[4];
#pragma unroll
      for (int i = 0; i < 4; ++i) t[i] = __builtin_amdgcn_raw_buffer_load_b128(ro, vo, i * (D * 2), 2);
      asm volatile("" :: "v"(t[0]), "v"(t[1]), "v"(t[2]), "v"(t[3]) : "memory");
#pragma unroll
      for (int i = 0; i < 4; ++i) t[i] = __builtin_amdgcn_raw_buffer_load_b128(ro, vo, i * (D * 2), 16);
      asm volatile("" :: "v"(t[0]), "v"(t[1]), "v"(t[2]), "v"(t[3]) : "memory");
    }
    {
      u32x4 hk[4 * NHC];
#pragma unroll
      for (int i = 0; i < NHC; ++i) take(cw + NCW * i, i + 1 < NHC ? cw + NCW * (i + 1) : NHELD + cw, hk[4 * i], hk[4 * i + 1], hk[4 * i + 2], hk[4 * i + 3]);
      STAMP(10);
      build_q();
      STAMP(3);
#pragma unroll
      for (int i = 0; i < NHC; ++i) { score(cw + NCW * i, hk[4 * i], hk[4 * i + 1], hk[4 * i + 2], hk[4 * i + 3]); __builtin_amdgcn_sched_barrier(0); }
    }
#pragma unroll 1
    for (int u = NHELD + cw; u < NKU; u += NCW) {
      u32x4 k0, k1, k2, k3;
      take(u, u + NCW, k0, k1, k2, k3);
      score(u, k0, k1, k2, k3);
    }
    ctrlf[32 + cw] = xrow_max(mx);
    lds_arrive(L, C_MAX, lane);
    STAMP(11);
    lds_await(ctrl, C_MAX, NCW + NHW);
    const float mg = wg_max();
    STAMP(12);
    float acc[8] = {0.f, 0.f, 0.f, 0.f, 0.f, 0.f, 0.f, 0.f};
#pragma unroll 1
    for (int u = NKU + cw; u < NUNIT; u += NCW) {
      const f32x4 sc = *(const LAS f32x4*)(L + L_SC + (u - NKU) * 64 + r4 * 16);
      float p[4];
#pragma unroll
      for (int t = 0; t < 4; ++t) { p[t] = __builtin_amdgcn_exp2f(sc[t] - mg); asm volatile("" : "+v"(p[t])); }
      u32x4 v[4];
      take(u, u + NCW, v[0], v[1], v[2], v[3]);
#pragma unroll
      for (int t = 0; t < 4; ++t)
#pragma unroll
        for (int k = 0; k < 4; ++k) { acc[2 * k] = fmaf(p[t], bflo(v[t][k]), acc[2 * k]); acc[2 * k + 1] = fmaf(p[t], bfhi(v[t][k]), acc[2 * k + 1]); }
    }
    STAMP(5);
    const float e0 = swadd16(swadd32(acc[0], acc[4]), swadd32(acc[2], acc[6])), e1 = swadd16(swadd32(acc[1], acc[5]), swadd32(acc[3], acc[7]));
    *(LAS f32x2*)(L + L_REC + (cw * DH + 8 * c16 + 2 * r4) * 4) = f32x2{e0, e1};
    BARRIER();
    return;
  }

  u32x4 wb[16];
  const unsigned tstart = (unsigned)__builtin_amdgcn_s_memrealtime();
  {
    const __amdgpu_buffer_rsrc_t rq = mkrsrc(wq, D * H * DH * 2);
    const int vo = ((64 * hw + 16 * r4) * (H * DH) + h * DH + 8 * c16) * 2;
    {
      const __amdgpu_buffer_rsrc_t r1 = mkrsrc(w1, D * F * 2), r3 = mkrsrc(w3, D * F * 2), r2 = mkrsrc(w2, F * D * 2);
      const int oq = ((16 * kk + 2 * hw) * (H * DH) + 512 * xg + 8 * lane) * 2;
      const int o1 = (16 * kk + 2 * hw + (lane >> 5)) * (F * 2) + ((448 * xg) & ~127) + (lane & 31) * 16;
      const int o2 = ((224 * xg + 7 * kk + (hw < 7 ? hw : 6)) * D + 8 * lane) * 2;
#define PFPASS(AUX) { \
        u32x4 t[5] = {}; \
        if (PFW & 2) { t[0] = __builtin_amdgcn_raw_buffer_load_b128(rq, oq, 0, AUX); t[1] = __builtin_amdgcn_raw_buffer_load_b128(rq, oq, H * DH * 2, AUX); } \
        if (PFW & 4) { t[2] = __builtin_amdgcn_raw_buffer_load_b128(r1, o1, 0, AUX); t[3] = __builtin_amdgcn_raw_buffer_load_b128(r3, o1, 0, AUX); } \
        if (PFW & 8) t[4] = __builtin_amdgcn_raw_buffer_load_b128(r2, o2, 0, AUX); \
        asm volatile("" :: "v"(t[0]), "v"(t[1]), "v"(t[2]), "v"(t[3]), "v"(t[4]) : "memory"); \
      }
      PFPASS(2) PFPASS(16)
    }
#pragma unroll
    for (int i = 0; i < 16; ++i) { wait_until(tstart + TQ + hw * QA + i * QB); wb[i] = ldw(rq, vo, i * (H * DH * 2)); }
    lds_await(ctrl, C_X, 1);
    const u32x4 xa = *(const LAS u32x4*)(L + L_X + (64 * hw + 16 * r4) * 2), xc = *(const LAS u32x4*)(L + L_X + (64 * hw + 16 * r4 + 8) * 2);
    float a[8] = {0.f, 0.f, 0.f, 0.f, 0.f, 0.f, 0.f, 0.f};
#pragma unroll
    for (int i = 0; i < 8; ++i) {
      const unsigned xp = i < 4 ? xa[i] : xc[i - 4];
#pragma unroll
      for (int k = 0; k < 4; ++k) {
        a[2 * k] = dot2(perm_lo(wb[2 * i][k], wb[2 * i + 1][k]), xp, a[2 * k]);
        a[2 * k + 1] = dot2(perm_hi(wb[2 * i][k], wb[2 * i + 1][k]), xp, a[2 * k + 1]);
      }
    }
    const float e0 = swadd16(swadd32(a[0], a[4]), swadd32(a[2], a[6])), e1 = swadd16(swadd32(a[1], a[5]), swadd32(a[3], a[7]));
    *(LAS f32x2*)(L + L_QP + (hw * DH + 8 * c16 + 2 * r4) * 4) = f32x2{e0, e1};
    lds_arrive(L, C_Q, lane);
    STAMP(2);
    const int hb = NHC * NCW + hw;
#pragma unroll
    for (int i = 0; i < NHH; ++i) take(hb + NHW * i, i + 1 < NHH ? hb + NHW * (i + 1) : NEVER, wb[4 * i], wb[4 * i + 1], wb[4 * i + 2], wb[4 * i + 3]);
    STAMP(10);
    build_q();
#pragma unroll
    for (int i = 0; i < NHH; ++i) { score(hb + NHW * i, wb[4 * i], wb[4 * i + 1], wb[4 * i + 2], wb[4 * i + 3]); __builtin_amdgcn_sched_barrier(0); }
    ctrlf[32 + cw] = xrow_max(mx);
    lds_arrive(L, C_MAX, lane);
  }
  float z[8] = {0.f, 0.f, 0.f, 0.f, 0.f, 0.f, 0.f, 0.f};
  if ((pj & 7) != 7) {
    const int sl = 7 * (pj >> 3) + (pj & 7), rs = lane >> 3;
    const u16* wp = ((lane & 4) ? w3 : w1) + (size_t)(64 * hw + 8 * rs) * F + FS * sl + 8 * (lane & 3);
#pragma unroll
    for (int i = 0; i < 8; ++i) { wait_until(tstart + T1 + hw * A1 + i * B1); wb[i] = __builtin_nontemporal_load((const GAS u32x4*)(wp + i * F)); }
    const __amdgpu_buffer_rsrc_t r2 = mkrsrc(w2, F * D * 2);
    const u32x4 xm = *(const LAS u32x4*)(L + L_X + (64 * hw + 8 * rs) * 2);
    float a[8] = {0.f, 0.f, 0.f, 0.f, 0.f, 0.f, 0.f, 0.f};
#pragma unroll
    for (int i = 0; i < 4; ++i)
#pragma unroll
      for (int k = 0; k < 4; ++k) {
        a[2 * k] = dot2(perm_lo(wb[2 * i][k], wb[2 * i + 1][k]), xm[i], a[2 * k]);
        a[2 * k + 1] = dot2(perm_hi(wb[2 * i][k], wb[2 * i + 1][k]), xm[i], a[2 * k + 1]);
      }
    float e0 = swadd16(swadd32(a[0], a[4]), swadd32(a[2], a[6])), e1 = swadd16(swadd32(a[1], a[5]), swadd32(a[3], a[7]));
    e0 += dpp<ROR8>(e0); e1 += dpp<ROR8>(e1);
    if ((lane & 8) == 0) *(LAS f32x2*)(L + L_MP + (hw * 64 + 8 * (lane & 7) + 2 * r4) * 4) = f32x2{e0, e1};
    lds_arrive(L, C_MLP, lane);
#pragma unroll
    for (int i = 0; i < 4; ++i) { wait_until(tstart + T2 + hw * A2 + i * B2); wb[8 + i] = ldw(r2, ((FS * sl + 4 * hw) * D + 8 * lane) * 2, i * (D * 2)); }
    lds_await(ctrl, C_MLP, NHW);
    const LAS float* mp = (const LAS float*)(L + L_MP) + rs * 64 + 4 * hw + (lane & 3);
    float ga = mp[0], up = mp[32];
    ga += dpp<ROR8>(ga); ga = xrow_sum(ga); up += dpp<ROR8>(up); up = xrow_sum(up);
    const float hv = ga / (1.f + __expf(-ga)) * up * (1.f - ctrlf[48]);
#pragma unroll
    for (int i = 0; i < 4; ++i) {
      const float hs = __builtin_bit_cast(float, __builtin_amdgcn_readlane(__builtin_bit_cast(int, hv), i));
#pragma unroll
      for (int k = 0; k < 4; ++k) { z[2 * k] = fmaf(hs, bflo(wb[8 + i][k]), z[2 * k]); z[2 * k + 1] = fmaf(hs, bfhi(wb[8 + i][k]), z[2 * k + 1]); }
    }
  }
#pragma unroll
  for (int k = 0; k < 8; ++k) asm volatile("" : "+v"(z[k]));
  {
    const __amdgpu_buffer_rsrc_t ro = mkrsrc(wo, H * DH * D * 2);
#pragma unroll
    for (int i = 0; i < 16; ++i) { wait_until(tstart + TO + hw * AO + i * BO); wb[i] = ldw(ro, ((h * DH + 16 * hw) * D + 8 * lane) * 2, i * (D * 2)); }
#pragma unroll
    for (int i = 0; i < 8; ++i)
#pragma unroll
      for (int k = 0; k < 4; ++k) {
        const unsigned lo = perm_lo(wb[2 * i][k], wb[2 * i + 1][k]), hi = perm_hi(wb[2 * i][k], wb[2 * i + 1][k]);
        wb[2 * i][k] = lo; wb[2 * i + 1][k] = hi;
      }
#pragma unroll
    for (int i = 0; i < 16; ++i) asm volatile("" : "+v"(wb[i]));
  }
  STAMP(4);
  BARRIER();
  STAMP(9);
  {
    const f32x2 a = *(const LAS f32x2*)(L + L_REC + (r4 * DH + 16 * hw + 2 * (lane & 7)) * 4);
    const float cf = ctrlf[49];
    const unsigned op = pack_bf16(xrow_sum(a[0]), xrow_sum(a[1]));
    float y[8] = {0.f, 0.f, 0.f, 0.f, 0.f, 0.f, 0.f, 0.f};
#pragma unroll
    for (int i = 0; i < 8; ++i) {
      const unsigned os = __builtin_amdgcn_readlane(op, i);
#pragma unroll
      for (int k = 0; k < 4; ++k) { y[2 * k] = dot2(wb[2 * i][k], os, y[2 * k]); y[2 * k + 1] = dot2(wb[2 * i + 1][k], os, y[2 * k + 1]); }
    }
    LAS f32x4* yp = (LAS f32x4*)(L + L_YP + (hw * D + 8 * lane) * 4);
    yp[0] = f32x4{fmaf(cf, y[0], z[0]), fmaf(cf, y[1], z[1]), fmaf(cf, y[2], z[2]), fmaf(cf, y[3], z[3])};
    yp[1] = f32x4{fmaf(cf, y[4], z[4]), fmaf(cf, y[5], z[5]), fmaf(cf, y[6], z[6]), fmaf(cf, y[7], z[7])};
  }
  BARRIER();
  STAMP(6);
  if (hw < 2) {
    const int t = hw * 64 + lane;
    const LAS f32x4* yp = (const LAS f32x4*)(L + L_YP) + t;
    f32x4 sum = yp[0];
#pragma unroll
    for (int w = 1; w < NHW; ++w) sum += yp[w * (D / 4)];
    u32x4 v;
#pragma unroll
    for (int k = 0; k < 4; ++k) v[k] = min(__float_as_uint(sum[k]), EMPTY - 1u);
    __builtin_amdgcn_raw_buffer_store_b128(v, wsr, PCONT ? WS_Y + (b * 64 + pj) * 2048 + t * 16 : WS_Y + (b * 64 + (t >> 1)) * 2048 + pj * 32 + (t & 1) * 16, 0, 16);
  }
  STAMP(7);
}

extern "C" void kernel_launch(void* const* d_in, const int* in_sizes, int n_in, void* d_out, int out_size, void* d_ws, size_t ws_size,
                              hipStream_t stream) {
  gated_mix_kernel<<<256, NTHR, 0, stream>>>((const u16*)d_in[0], (const u16*)d_in[1], (const u16*)d_in[2], (const u16*)d_in[3], (const u16*)d_in[4],
                                             (const u16*)d_in[6], (const u16*)d_in[7], (const u16*)d_in[8], (const u16*)d_in[9], (u16*)d_out,
                                             (unsigned char*)d_ws);
}
```

```cpp
#include <hip/hip_runtime.h>
#include <hip/hip_bf16.h>

#define GAS __attribute__((address_space(1)))
#define LAS __attribute__((address_space(3)))
typedef unsigned short u16;
typedef unsigned long long u64;
using u32x4 = __attribute__((ext_vector_type(4))) unsigned;
using u32x2 = __attribute__((ext_vector_type(2))) unsigned;
using f32x4 = __attribute__((ext_vector_type(4))) float;
using f32x2 = __attribute__((ext_vector_type(2))) float;
using bf16x2 = __attribute__((ext_vector_type(2))) __bf16;

constexpr int B = 4, D = 512, H = 32, SK = 8192, DH = 128, F = 1792;
constexpr int NCW = 4, NHW = 8, XWAVE = 1 + NCW + NHW, NWAVE = XWAVE + 2, NTHR = NWAVE * 64;
#define QA 80
#define QB 5
#define T1 1200
#define A1 150
#define B1 18
#define T2 2500
#define A2 24
#define B2 6
#define TO 2900
#define AO 350
#define BO 12
#define TS 70.6
#define REF0 180
#define PLAG 1
#define NOPST 0
#define NOPLD 0
#define PAUX 16
#define RAUX 16
#define PFW 0
#define TQ 130
#define NINIT 6
#define CODE_KIB 23
#define FDEL 0
#define CODEPF 1
#define YST 16
#define YLD 16
#define RED16 0
#define MASKP 0
#define PSLP 1
#define OUTSC 0
#define OUT_STORE(p, v) *(GAS unsigned*)(p) = v
#define CLR0 1
#define MLPFULL 0
#define MLPPF 0
#define TP 1200
#define AP 12
#define GRID 0
#define B2BD 0
#define B2BAUX 16
#define WOB2B 0
#define TOUCHW 1
#define W0 21
#define BARPOS 12
#define SLOTSH 8
#define WMID 20.5
#define WGAIN 8
#define WMIN 14
#define WMAX 27
#define CLRAUX 16
#define PCONT 1
#define XDEL 0
#define XSPR 0
#define XNT 0
#define MIMIC 1
#define NCLR 2
#define NOFLAG 0
#define CSPR 0
constexpr int UNIT = 4096;
constexpr int NSLOT = 32, RING = NSLOT * UNIT;
constexpr int NKU = (SK / 2) / 16, NUNIT = 2 * NKU;
constexpr int NHC = 6, NHH = 4, NHELD = NHC * NCW + NHH * NHW;
constexpr int FS = 32;
constexpr int L_CTRL = RING;
constexpr int L_X = L_CTRL + 256;
constexpr int L_QP = L_X + D * 2;
constexpr int L_MP = L_QP + NHW * DH * 4;
constexpr int L_REC = L_MP + NHW * 64 * 4;
constexpr int L_SC = L_REC + NCW * DH * 4;
constexpr int L_JUNK = L_SC + NKU * 64;
constexpr int LDS_BYTES = L_JUNK + 1024;
static_assert(LDS_BYTES <= 159 * 1024, "lds");
static_assert(WMIN <= (int)WMID && (int)WMID <= WMAX && WMAX <= 28, "window");
constexpr int WS_EPOCH = 0;
constexpr int WS_REC = 4096;
constexpr int WS_PACE = WS_REC + 256 * 128;
constexpr int WS_Y = WS_PACE + 64 * 2048;
constexpr int WS_BYTES = WS_Y + B * 64 * 2048;
constexpr u64 TAG_STEP = 0x9E3779B97F4A7C15ull;
constexpr unsigned EMPTY = 0xFFFFFFFFu;
constexpr int SPIN_CAP = 1 << 18;
constexpr int NEVER = 1 << 20;
constexpr int C_Q = 4, C_MAX = 5, C_MLP = 6, C_X = 7;

__device__ __forceinline__ float bflo(unsigned u) { return __builtin_bit_cast(float, u << 16); }
__device__ __forceinline__ float bfhi(unsigned u) { return __builtin_bit_cast(float, u & 0xffff0000u); }
__device__ __forceinline__ float dot2(unsigned a, unsigned b, float acc) {
  return __builtin_amdgcn_fdot2_f32_bf16(__builtin_bit_cast(bf16x2, a), __builtin_bit_cast(bf16x2, b), acc, false);
}
__device__ __forceinline__ unsigned pack_bf16(float lo, float hi) {
  const bf16x2 v = {(__bf16)lo, (__bf16)hi};
  return __builtin_bit_cast(unsigned, v);
}
__device__ __forceinline__ unsigned perm_lo(unsigned a, unsigned b) { return __builtin_amdgcn_perm(b, a, 0x05040100u); }
__device__ __forceinline__ unsigned perm_hi(unsigned a, unsigned b) { return __builtin_amdgcn_perm(b, a, 0x07060302u); }
template <int CTRL> __device__ __forceinline__ float dpp(float x) {
  return __builtin_bit_cast(float, __builtin_amdgcn_mov_dpp(__builtin_bit_cast(int, x), CTRL, 0xf, 0xf, true));
}
constexpr int XOR1 = 0xB1, ROR1 = 0x121, ROR2 = 0x122, ROR4 = 0x124, ROR8 = 0x128;
__device__ __forceinline__ float row16_sum(float x) { x += dpp<ROR8>(x); x += dpp<ROR4>(x); x += dpp<ROR2>(x); x += dpp<ROR1>(x); return x; }
__device__ __forceinline__ float swadd32(float a, float b) {
  auto t = __builtin_amdgcn_permlane32_swap(__float_as_uint(a), __float_as_uint(b), false, false);
  return __uint_as_float(t[0]) + __uint_as_float(t[1]);
}
__device__ __forceinline__ float swadd16(float a, float b) {
  auto t = __builtin_amdgcn_permlane16_swap(__float_as_uint(a), __float_as_uint(b), false, false);
  return __uint_as_float(t[0]) + __uint_as_float(t[1]);
}
__device__ __forceinline__ float xrow_sum(float x) { x = swadd16(x, x); return swadd32(x, x); }
__device__ __forceinline__ float xrow_max(float x) {
  auto s = __builtin_amdgcn_permlane16_swap(__float_as_uint(x), __float_as_uint(x), false, false);
  x = fmaxf(__uint_as_float(s[0]), __uint_as_float(s[1]));
  auto t = __builtin_amdgcn_permlane32_swap(__float_as_uint(x), __float_as_uint(x), false, false);
  return fmaxf(__uint_as_float(t[0]), __uint_as_float(t[1]));
}
__device__ __forceinline__ __amdgpu_buffer_rsrc_t mkrsrc(const void* p, int bytes) { return __builtin_amdgcn_make_buffer_rsrc((void*)p, 0, bytes, 0x00020000); }
__device__ __forceinline__ u32x4 ldw(__amdgpu_buffer_rsrc_t r, int voff, int soff) { return __builtin_amdgcn_raw_buffer_load_b128(r, voff, soff, 2); }
#define LDS_WAIT() asm volatile("s_waitcnt lgkmcnt(0)" ::: "memory")
#define VM_DRAIN() asm volatile("s_waitcnt vmcnt(0)" ::: "memory")
#define CFENCE() asm volatile("" ::: "memory")
#define BARRIER() asm volatile("s_waitcnt lgkmcnt(0)\n\ts_barrier" ::: "memory")
#define PAUSE(n) do { __builtin_amdgcn_sched_barrier(0); asm volatile("s_sleep %0" :: "i"(n) : "memory"); __builtin_amdgcn_sched_barrier(0); } while (0)

__device__ __forceinline__ void lds_arrive(LAS unsigned char* L, int idx, int lane) {
  LDS_WAIT();
  if (lane == 0) __hip_atomic_fetch_add((LAS int*)(L + L_CTRL) + idx, 1, __ATOMIC_RELAXED, __HIP_MEMORY_SCOPE_WORKGROUP);
}
__device__ __forceinline__ void lds_await(volatile LAS int* ctrl, int idx, int n) {
#pragma unroll 1
  for (int spin = 0; spin < SPIN_CAP; ++spin) { if (__builtin_amdgcn_readfirstlane(ctrl[idx]) >= n) break; __builtin_amdgcn_s_sleep(1); }
  CFENCE();
}

__device__ __forceinline__ void wait_until(unsigned t) {
  __builtin_amdgcn_sched_barrier(0);
#pragma unroll 1
  for (int spin = 0; spin < 4096; ++spin) {
    const int d = (int)(t - (unsigned)__builtin_amdgcn_s_memrealtime());
    if (d <= 0) break;
    if (d > 24) __builtin_amdgcn_s_sleep(6);
  }
  CFENCE();
  __builtin_amdgcn_sched_barrier(0);
}
#define STR2(x) #x
#define STR(x) STR2(x)
constexpr int TAU2 = 2 * (int)(TS * 100.0 * 256.0 / NUNIT);
template <int CTRL> __device__ __forceinline__ int dppi(int x) { return __builtin_amdgcn_mov_dpp(x, CTRL, 0xf, 0xf, true); }

#define STAMP(i) do {} while (0)
#define STAMP_FLUSH() do {} while (0)
#define STAMP_LDS 0

__global__ void __launch_bounds__(NTHR) gated_mix_kernel(const u16* x, const u16* kc, const u16* vc, const u16* wq, const u16* wo, const u16* w1,
                                                         const u16* w3, const u16* w2, const u16* gw, u16* out, unsigned char* ws) {
  __shared__ __attribute__((aligned(16))) unsigned char lds_raw[LDS_BYTES + STAMP_LDS];
  LAS unsigned char* L = (LAS unsigned char*)lds_raw;
  volatile LAS int* ctrl = (volatile LAS int*)(L + L_CTRL);
  volatile LAS float* ctrlf = (volatile LAS float*)(L + L_CTRL);
#define DBG(x) ""
  {
    unsigned va, vb, vo, pa, nxa, t0, t1, t2, t3;
#define WAITW "s_waitcnt (wcnt & 15) | ((wcnt >> 4) << 14) | 0x0F70\n\t"
#define FETCH(lbl, v, sb, n, step, wait) \
        "s_mov_b32 s54, " n "\n" lbl ":\n\t" wait \
        "global_load_lds_dwordx4 " v ", " sb " nt\n\t" \
        "global_load_lds_dword " v ", " sb " sc1\n\t" \
        "s_sub_u32 s54, s54, 1\n\t" \
        "v_add_u32 " v ", " step ", " v "\n\t" \
        "s_cmp_lg_u32 s54, 0\n\t" \
        "s_cbranch_scc1 " lbl "b\n\t"
#define UNIT4(v) \
        WAITW "global_load_lds_dwordx4 " v ", s[40:41] nt\n\t" \
        WAITW "global_load_lds_dwordx4 " v ", s[40:41] offset:1024 nt\n\t" \
        WAITW "global_load_lds_dwordx4 " v ", s[40:41] offset:2048 nt\n\t" \
        WAITW "global_load_lds_dwordx4 " v ", s[40:41] offset:3072 nt\n\t"
    asm volatile(
        "s_getpc_b64 s[56:57]\n\t"
        "s_load_dwordx8 s[44:51], %[ka], 0x18\n\t"
        "s_load_dwordx2 s[52:53], %[ka], 0x38\n\t"
        "s_load_dwordx4 s[40:43], %[ka], 0x8\n\t"
        "v_readfirstlane_b32 s54, %[tid]\n\t"
        "s_mov_b32 m0, %c[jk]\n\t"
        "s_lshr_b32 s59, %[bid], 3\n\t"
        "s_cmp_ge_u32 s54, 64\n\t"
        "s_cbranch_scc1 90f\n\t"
        "s_min_u32 s54, s59, %c[nch]\n\t"
        "s_lshl_b32 s54, s54, 10\n\t"
        "v_lshlrev_b32 %[vo], 4, %[tid]\n\t"
        "s_and_b32 s56, s56, -128\n\t"
        "v_add_u32 %[t0], s54, %[vo]\n\t"
        "s_and_b32 s58, %[bid], 7\n\t"
        "global_load_lds_dwordx4 %[t0], s[56:57]\n\t"
        "s_setprio 3\n\t"
        "s_lshl_b32 s54, s59, 7\n\t"
        "s_add_u32 s54, s54, s58\n\t"
        "v_lshl_add_u32 %[va], s54, 10, %[vo]\n\t"
        "s_lshl_b32 s55, s58, 5\n\t"
        "s_add_u32 s55, s55, s59\n\t"
        "s_mul_i32 s54, s55, 7\n\t"
        "v_lshl_add_u32 %[vb], s54, 10, %[vo]\n\t"
        "s_waitcnt lgkmcnt(0)\n\t"
        FETCH("11", "%[va]", "s[44:45]", "16", "0x2000", "")
        FETCH("12", "%[vb]", "s[52:53]", "7", "0x400", "")
        ".if %c[barpos] == 12\n\ts_barrier\n\t.endif\n\t"
        "s_mul_i32 s54, s58, 448\n\t"
        "s_and_b32 s54, s54, -128\n\t"
        "s_mul_i32 s52, s59, 16 * 3584\n\t"
        "s_add_u32 s54, s54, s52\n\t"
        "v_lshrrev_b32 %[va], 5, %[tid]\n\t"
        "v_mul_u32_u24 %[va], 3584 - 512, %[va]\n\t"
        "v_add3_u32 %[va], %[va], %[vo], s54\n\t"
        "v_mov_b32 %[t0], %[va]\n\t"
        "v_lshl_add_u32 %[vb], s55, 14, %[vo]\n\t"
        ".set wcnt, %c[w0]\n\t"
        FETCH("13", "%[va]", "s[48:49]", "8", "0x1c00", WAITW)
        ".if %c[barpos] == 13\n\ts_barrier\n\t.endif\n\t"
        FETCH("14", "%[t0]", "s[50:51]", "8", "0x1c00", WAITW)
        ".if %c[barpos] == 14\n\ts_barrier\n\t.endif\n\t"
        FETCH("15", "%[vb]", "s[46:47]", "16", "0x400", WAITW)
        "s_and_b32 s54, %[bid], 0xc0\n\t"
        "s_lshl3_add_u32 s54, s58, s54\n\t"
        "s_and_b32 s55, s59, 7\n\t"
        "s_add_u32 s54, s54, s55\n\t"
        "s_lshl_b32 s54, s54, 20\n\t"
        "s_add_u32 s40, s40, s54\n\t"
        "s_addc_u32 s41, s41, 0\n\t"
        "s_add_u32 s42, s42, s54\n\t"
        "s_addc_u32 s43, s43, 0\n\t"
        "s_sub_u32 s42, s42, %c[kbytes]\n\t"
        "s_subb_u32 s43, s43, 0\n\t"
        ".set so, 0\n\t"
        ".rept 4\n\t"
        "s_movk_i32 m0, so\n\t"
        ".set o, 0\n\t"
        ".rept 4\n\t"
        WAITW
        "global_load_lds_dwordx4 %[vo], s[40:41] offset:o nt\n\t"
        ".set o, o + 1024\n\t"
        ".set wcnt, wcnt - 1\n\t"
        ".endr\n\t"
        "v_add_u32 %[vo], 0x1000, %[vo]\n\t"
        ".set so, so + 4096\n\t"
        ".endr\n\t"
        "s_getpc_b64 s[50:51]\n"
        "20:\n\t"
        "s_add_u32 s50, s50, 23f-20b-%c[wmin]*128\n\t"
        "s_addc_u32 s51, s51, -1\n\t"
        "v_mov_b32 %[pa], %c[ctl]\n\t"
        "v_and_b32 %[nxa], 0x3c0 >> 2, %[vo]\n\t"
        "v_lshrrev_b32 %[nxa], 2, %[nxa]\n\t"
        "v_add_u32 %[nxa], %c[ctl] + 64, %[nxa]\n\t"
        "s_mov_b32 s44, 4\n\t"
        "s_mov_b32 s45, %c[wmid] >> 8\n\t"
        "s_mov_b32 s46, 0x4000\n\t"
        "s_mov_b32 s47, 2 * %c[tau2]\n\t"
        "s_mov_b32 s48, %c[nslot]\n\t"
        "s_mov_b32 s49, %c[nku]\n"
        "22:\n\t"
        "s_memrealtime s[56:57]\n\t"
        "ds_read_b32 %[t3], %[pa] offset:8\n\t"
        "s_lshl_b32 s52, s45, 7\n\t"
        "s_add_u32 s52, s50, s52\n\t"
        "s_addc_u32 s53, s51, 0\n\t"
        "s_add_i32 s54, s45, -1\n\t"
        "s_lshr_b32 s54, s54, 2\n\t"
        "s_sub_i32 s54, s44, s54\n\t"
        "v_mov_b32 %[t1], s54\n\t"
        "v_add_u32 %[t0], 0x1000, %[vo]\n\t"
        "s_add_u32 s55, s46, 0x1000\n\t"
        "s_mov_b32 m0, s46\n\t"
        "s_setpc_b64 s[52:53]\n"
        "24:\n\t"
        "s_waitcnt lgkmcnt(0)\n\t"
        "v_readfirstlane_b32 s57, %[t3]\n\t"
        "v_add_u32 %[t1], 1, %[t1]\n\t"
        "s_lshr_b32 s54, s47, 8\n\t"
        "s_sub_u32 s56, s56, s54\n\t"
        "v_mov_b32 %[t2], s56\n\t"
        "s_sub_i32 s56, s56, s57\n\t"
        "ds_write2_b32 %[pa], %[t1], %[t2] offset1:1\n\t"
        "s_mul_i32 s56, s56, %c[gain]\n\t"
        "s_add_i32 s56, s56, %c[wmid]\n\t"
        "s_ashr_i32 s56, s56, 8\n\t"
        "s_max_i32 s56, s56, %c[wmin]\n\t"
        "s_min_i32 s45, s56, %c[wmax]\n\t"
        "s_add_u32 s47, s47, %c[tau2]\n\t"
        "s_add_u32 s46, s46, 0x2000\n\t"
        "s_and_b32 s46, s46, %c[mask]\n\t"
        "v_add_u32 %[vo], 0x2000, %[vo]\n\t"
        "s_add_i32 s44, s44, 2\n\t"
        "s_cmp_lt_i32 s44, s48\n\t"
        "s_cbranch_scc1 22b\n\t"
        DBG("s_add_i32 s54, s44, -1\n\ts_lshr_b32 s54, s54, 5\n\ts_lshl_b32 s54, s54, 2\n\ts_add_u32 s54, s54, %c[dbg] + 14 * 64\n\tv_mov_b32 %[t0], s54\n\tv_mov_b32 %[t1], s45\n\t"
            "ds_write2_b32 %[t0], %[t2], %[t1] offset1:16\n\t")
        "s_cmp_lt_i32 s44, s49\n\t"
        "s_cbranch_scc1 25f\n\t"
        "s_cmp_ge_i32 s44, 2 * %c[nku]\n\t"
        "s_cbranch_scc1 30f\n\t"
        "s_mov_b32 s49, 2 * %c[nku]\n\t"
        "s_mov_b64 s[40:41], s[42:43]\n"
        "25:\n\t"
        "s_mov_b32 s55, %c[cap]\n"
        "26:\n\t"
        "ds_read_b32 %[t0], %[nxa]\n\t"
        "s_waitcnt lgkmcnt(0)\n\t"
        "s_nop 1\n\t"
        "v_min_i32_dpp %[t0], %[t0], %[t0] row_ror:8 row_mask:0xf bank_mask:0xf\n\t"
        "s_nop 1\n\t"
        "v_min_i32_dpp %[t0], %[t0], %[t0] row_ror:4 row_mask:0xf bank_mask:0xf\n\t"
        "s_nop 1\n\t"
        "v_min_i32_dpp %[t0], %[t0], %[t0] row_ror:2 row_mask:0xf bank_mask:0xf\n\t"
        "s_nop 1\n\t"
        "v_min_i32_dpp %[t0], %[t0], %[t0] row_ror:1 row_mask:0xf bank_mask:0xf\n\t"
        "s_nop 1\n\t"
        "v_readfirstlane_b32 s48, %[t0]\n\t"
        "s_nop 1\n\t"
        "s_add_i32 s48, s48, %c[nslot]\n\t"
        "s_and_b32 s48, s48, -2\n\t"
        "s_min_i32 s48, s48, s49\n\t"
        "s_cmp_lt_i32 s44, s48\n\t"
        "s_cbranch_scc1 22b\n\t"
        "v_mov_b32 %[t1], s44\n\t"
        "s_waitcnt vmcnt(0)\n\t"
        "ds_write_b32 %[pa], %[t1]\n\t"
        "s_sleep 1\n\t"
        "s_sub_u32 s55, s55, 1\n\t"
        "s_cmp_lg_u32 s55, 0\n\t"
        "s_cbranch_scc1 26b\n\t"
        "s_branch 22b\n"
        "30:\n\t"
        ".set k, 6\n\t"
        ".rept 7\n\t"
        ".set wcnt, 4 * k\n\t"
        "v_mov_b32 %[t1], 2 * %c[nku] - k\n\t"
        WAITW
        "ds_write_b32 %[pa], %[t1]\n\t"
        ".set k, k - 1\n\t"
        ".endr\n\t"
        DBG("s_memrealtime s[56:57]\n\ts_waitcnt lgkmcnt(0)\n\tv_mov_b32 %[t0], %c[dbg] + 4\n\tv_mov_b32 %[t1], s56\n\tds_write_b32 %[t0], %[t1]\n\t")
        "s_waitcnt lgkmcnt(0)\n\t"
        "s_endpgm\n\t"
        ".p2align 7\n"
        "23:\n\t"
        ".set wcnt, %c[wmin]-1\n\t"
        ".rept %c[wmax] - %c[wmin] + 1\n\t"
        UNIT4("%[vo]")
        "ds_write_b32 %[pa], %[t1]\n\t"
        "s_mov_b32 m0, s55\n\t"
        UNIT4("%[t0]")
        "s_branch 24b\n\t"
        ".p2align 7\n\t"
        ".set wcnt, wcnt+1\n\t"
        ".endr\n"
        "90:\n\t"
        "s_waitcnt lgkmcnt(0)"
        : [va] "=&v"(va), [vb] "=&v"(vb), [vo] "=&v"(vo), [pa] "=&v"(pa), [nxa] "=&v"(nxa), [t0] "=&v"(t0), [t1] "=&v"(t1), [t2] "=&v"(t2), [t3] "=&v"(t3)
        : [tid] "v"(threadIdx.x), [bid] "s"(blockIdx.x), [ka] "s"(__builtin_amdgcn_kernarg_segment_ptr()), [nch] "i"(CODE_KIB - 1), [jk] "i"(L_JUNK),
          [w0] "i"(2 * (W0 - 1)), [kbytes] "i"(NKU * UNIT), [wmin] "i"(WMIN), [wmax] "i"(WMAX), [gain] "i"(WGAIN), [wmid] "i"((int)(WMID * 256)), [tau2] "i"(TAU2),
          [mask] "i"(RING - 1), [ctl] "i"(L_CTRL), [nslot] "i"(NSLOT), [nku] "i"(NKU), [cap] "i"(SPIN_CAP), [dbg] "i"(LDS_BYTES), [barpos] "i"(BARPOS)
        : "memory", "scc", "s40", "s41", "s42", "s43", "s44", "s45", "s46", "s47", "s48", "s49", "s50", "s51", "s52", "s53", "s54", "s55", "s56", "s57", "s58", "s59");
  }
  __builtin_amdgcn_sched_barrier(0);
  const int tid = threadIdx.x, lane = tid & 63, wave = __builtin_amdgcn_readfirstlane(tid >> 6);
  const int xg = blockIdx.x & 7, kk = blockIdx.x >> 3;
  const int b = kk >> 3, pj = 8 * xg + (kk & 7), h = pj >> 1, s = pj & 1;
  const int r4 = lane >> 4, c16 = lane & 15;
  asm volatile("" : "+s"(x), "+s"(wq), "+s"(wo), "+s"(w1), "+s"(w3), "+s"(w2), "+s"(gw), "+s"(out), "+s"(ws));
  if (wave == XWAVE) {
    const __amdgpu_buffer_rsrc_t wsr = mkrsrc(ws, WS_BYTES);
    const u32x4 e = {EMPTY, EMPTY, EMPTY, EMPTY};
#pragma unroll
    for (int i = 0; i < NCLR; ++i) __builtin_amdgcn_raw_buffer_store_b128(e, wsr, WS_Y + (b * 64 + pj) * 2048 + i * 1024 + lane * 16, 0, CLRAUX);
  }
  if (wave == 1) {
    const int wi = lane - 16;
    const int now = (int)(unsigned)__builtin_amdgcn_s_memrealtime() + REF0;
    ctrl[lane] = (lane == 1 || lane == 2) ? now : lane == 8 ? now - REF0 : (lane < 16 || lane >= 32) ? 0 : wi < NCW ? wi : wi < NCW + NHW ? NHC * NCW + wi - NCW : NEVER;
  }
  BARRIER();

  const __amdgpu_buffer_rsrc_t wsr = mkrsrc(ws, WS_BYTES);
  auto wg_max = [&]() __attribute__((always_inline)) {
    float mg = ctrlf[32 + (c16 < NCW + NHW ? c16 : 0)];
    mg = fmaxf(mg, dpp<ROR8>(mg)); mg = fmaxf(mg, dpp<ROR4>(mg)); mg = fmaxf(mg, dpp<ROR2>(mg)); mg = fmaxf(mg, dpp<ROR1>(mg));
    return mg;
  };

  if (wave == XWAVE + 1) {
    unsigned k = ((unsigned)__builtin_amdgcn_s_memrealtime() >> SLOTSH) + 1;
#pragma unroll 1
    for (int it = 0; it < 4096 >> (SLOTSH - 2); ++it, ++k) {
      wait_until(k << SLOTSH);
      if (__builtin_amdgcn_readfirstlane(ctrl[0]) >= NUNIT - 36) break;
      const int own = __builtin_amdgcn_readfirstlane(ctrl[1]);
      constexpr unsigned SM = (8192u >> SLOTSH) - 1u;
      if (!NOPST && blockIdx.x < 16 && lane < 8) __builtin_amdgcn_raw_buffer_store_b128(u32x4{(unsigned)own, (unsigned)own, (unsigned)own, (unsigned)own}, wsr, WS_PACE + (k & SM) * 2048 + blockIdx.x * 128 + lane * 16, 0, PAUX);
      if (it < PLAG || NOPLD) continue;
      int e[16];
      asm volatile("s_load_dword %0, %16, 0x0\n\ts_load_dword %1, %16, 0x80\n\ts_load_dword %2, %16, 0x100\n\ts_load_dword %3, %16, 0x180\n\t"
                   "s_load_dword %4, %16, 0x200\n\ts_load_dword %5, %16, 0x280\n\ts_load_dword %6, %16, 0x300\n\ts_load_dword %7, %16, 0x380\n\t"
                   "s_load_dword %8, %16, 0x400\n\ts_load_dword %9, %16, 0x480\n\ts_load_dword %10, %16, 0x500\n\ts_load_dword %11, %16, 0x580\n\t"
                   "s_load_dword %12, %16, 0x600\n\ts_load_dword %13, %16, 0x680\n\ts_load_dword %14, %16, 0x700\n\ts_load_dword %15, %16, 0x780\n\t"
                   "s_waitcnt lgkmcnt(0)"
                   : "=&s"(e[0]), "=&s"(e[1]), "=&s"(e[2]), "=&s"(e[3]), "=&s"(e[4]), "=&s"(e[5]), "=&s"(e[6]), "=&s"(e[7]),
                     "=&s"(e[8]), "=&s"(e[9]), "=&s"(e[10]), "=&s"(e[11]), "=&s"(e[12]), "=&s"(e[13]), "=&s"(e[14]), "=&s"(e[15])
                   : "s"(ws + WS_PACE + ((k - PLAG) & SM) * 2048) : "memory");
      int sum = 0, cnt = 0;
#pragma unroll
      for (int i = 0; i < 16; ++i) { const int d = e[i] - own; const bool ok = (unsigned)(d + 600) < 1200u; sum += ok ? d : 0; cnt += ok ? 1 : 0; }
      if (cnt >= 8) ctrl[2] = own + (int)((float)sum / (float)cnt);
    }
    return;
  }

  if (wave == XWAVE) {
#pragma unroll 1
    for (int i = 0; i < XDEL + XSPR * (kk & 31); ++i) PAUSE(4);
    const u32x4 xv = *(const GAS u32x4*)(x + b * D + 8 * lane), gv = *(const GAS u32x4*)(gw + 8 * lane);
    *(LAS u32x4*)(L + L_X + lane * 16) = xv;
    float gd = 0.f;
#pragma unroll
    for (int k = 0; k < 4; ++k) gd = dot2(xv[k], gv[k], gd);
    gd = xrow_sum(row16_sum(gd));
    const float g = 1.f / (1.f + __expf(-gd));
    ctrlf[48] = g;
    lds_arrive(L, C_X, lane);
    u64 tag;
    asm volatile("s_load_dwordx2 %0, %1, 0x0\n\ts_waitcnt lgkmcnt(0)" : "=s"(tag) : "s"(ws + WS_EPOCH + b * 256) : "memory");
    tag += TAG_STEP;
#pragma unroll 1
    for (int spin = 0; spin < SPIN_CAP; ++spin) { if (__builtin_amdgcn_readfirstlane(ctrl[C_MAX]) >= NCW + NHW) break; __builtin_amdgcn_s_sleep(16); }
    CFENCE();
    const float mg = wg_max();
    float l = 0.f;
#pragma unroll 4
    for (int i = 0; i < NKU * 64 / 1024; ++i) {
      const f32x4 sc = *(const LAS f32x4*)(L + L_SC + i * 1024 + lane * 16);
      l += (__builtin_amdgcn_exp2f(sc[0] - mg) + __builtin_amdgcn_exp2f(sc[1] - mg)) + (__builtin_amdgcn_exp2f(sc[2] - mg) + __builtin_amdgcn_exp2f(sc[3] - mg));
    }
    l = xrow_sum(row16_sum(l));
    VM_DRAIN();
    const unsigned tlo = (unsigned)tag, thi = (unsigned)(tag >> 32);
    if (lane < 8) __builtin_amdgcn_raw_buffer_store_b128(u32x4{__float_as_uint(mg), tlo, __float_as_uint(l), thi}, wsr, WS_REC + (b * 64 + pj) * 128 + lane * 16, 0, RAUX);
#pragma unroll 1
    for (int i = 0; i < 12 + (kk & 31); ++i) PAUSE(12);
    u32x4 rec;
#pragma unroll 1
    for (int spin = 0; spin < SPIN_CAP; ++spin) {
      CFENCE();
      rec = __builtin_amdgcn_raw_buffer_load_b128(wsr, WS_REC + (b * 64 + lane) * 128, 0, 16);
      if (__builtin_amdgcn_ballot_w64(rec[1] != tlo || rec[3] != thi) == 0ull) break;
      __builtin_amdgcn_s_sleep(16);
    }
    if (pj == 0 && lane < 8) __builtin_amdgcn_raw_buffer_store_b128(u32x4{tlo, thi, tlo, thi}, wsr, WS_EPOCH + b * 256 + lane * 16, 0, 16);
    const float mo = __uint_as_float(__builtin_amdgcn_readlane(rec[0], pj ^ 1)), lo = __uint_as_float(__builtin_amdgcn_readlane(rec[2], pj ^ 1));
    const float mh = fmaxf(mg, mo), wt = __builtin_amdgcn_exp2f(mg - mh);
    ctrlf[49] = g * wt / (wt * l + __builtin_amdgcn_exp2f(mo - mh) * lo);
    STAMP(4);
    BARRIER();
    if (FDEL) PAUSE(FDEL);
    STAMP(9);
    const int off = WS_Y + (b * 64 + (lane >> 1)) * 2048 + pj * 32 + (lane & 1) * 16;
    u32x4 ya = {EMPTY, EMPTY, EMPTY, EMPTY}, yb = ya;
#pragma unroll 1
    for (int spin = 0; spin < SPIN_CAP; ++spin) {
      CFENCE();
      ya = __builtin_amdgcn_raw_buffer_load_b128(wsr, off, 0, YLD);
      yb = __builtin_amdgcn_raw_buffer_load_b128(wsr, off + 32 * 2048, 0, YLD);
      const unsigned mx = max(max(max(ya[0], ya[1]), max(ya[2], ya[3])), max(max(yb[0], yb[1]), max(yb[2], yb[3])));
      if (__builtin_amdgcn_ballot_w64(mx == EMPTY) == 0ull) break;
      if (PSLP) __builtin_amdgcn_s_sleep(PSLP);
    }
    float o[4];
#pragma unroll
    for (int k = 0; k < 4; ++k) o[k] = __uint_as_float(ya[k]) + __uint_as_float(yb[k]);
    float e0 = swadd32(o[0], o[2]), e1 = swadd32(o[1], o[3]);
    e0 = swadd16(e0, e0); e1 = swadd16(e1, e1);
    e0 += dpp<ROR8>(e0); e1 += dpp<ROR8>(e1); e0 += dpp<ROR4>(e0); e1 += dpp<ROR4>(e1); e0 += dpp<ROR2>(e0); e1 += dpp<ROR2>(e1);
    if ((lane & 30) == 0) OUT_STORE(out + b * D + 64 * (pj >> 3) + 8 * (2 * (pj & 3) + (lane & 1)) + (pj & 4) + 2 * (lane >> 5), pack_bf16(e0, e1));
    STAMP(8);
    STAMP_FLUSH();
    return;
  }

  const int cw = wave - 1, hw = wave - 1 - NCW;
  int landed = 0;
  auto wait_unit = [&](int u) __attribute__((always_inline)) {
#pragma unroll 1
    for (int spin = 0; landed <= u && spin < SPIN_CAP; ++spin) {
      landed = __builtin_amdgcn_readfirstlane(ctrl[0]);
      if (landed > u) break;
      __builtin_amdgcn_s_sleep(1);
    }
    CFENCE();
  };
  auto take = [&](int u, int next, u32x4& a0, u32x4& a1, u32x4& a2, u32x4& a3) __attribute__((always_inline)) {
    wait_unit(u);
    const LAS u32x4* sp = (const LAS u32x4*)(L + (u & (NSLOT - 1)) * UNIT + lane * 16);
    a0 = sp[0]; a1 = sp[64]; a2 = sp[128]; a3 = sp[192];
    LDS_WAIT();
    ctrl[16 + cw] = next;
    asm volatile("" : "+v"(a0), "+v"(a1), "+v"(a2), "+v"(a3) :: "memory");
  };
  u32x4 q;
  auto build_q = [&]() __attribute__((always_inline)) {
    lds_await(ctrl, C_Q, NHW);
    const LAS f32x4* qp = (const LAS f32x4*)(L + L_QP + (2 * r4 * DH + 8 * c16) * 4);
    const f32x4 lo = qp[0] + qp[DH / 4], hi = qp[1] + qp[DH / 4 + 1];
    q[0] = pack_bf16(xrow_sum(lo[0]), xrow_sum(lo[1])); q[1] = pack_bf16(xrow_sum(lo[2]), xrow_sum(lo[3]));
    q[2] = pack_bf16(xrow_sum(hi[0]), xrow_sum(hi[1])); q[3] = pack_bf16(xrow_sum(hi[2]), xrow_sum(hi[3]));
  };
  constexpr float SCALE = 0.088388347648318440f, LOG2E = 1.4426950408889634f, C2 = SCALE * LOG2E;
  float mx = -INFINITY;
  auto score = [&](int u, const u32x4& k0, const u32x4& k1, const u32x4& k2, const u32x4& k3) __attribute__((always_inline)) {
    float d0 = 0.f, d1 = 0.f, d2 = 0.f, d3 = 0.f;
#pragma unroll
    for (int k = 0; k < 4; ++k) { d0 = dot2(k0[k], q[k], d0); d1 = dot2(k1[k], q[k], d1); d2 = dot2(k2[k], q[k], d2); d3 = dot2(k3[k], q[k], d3); }
    const unsigned s01 = pack_bf16(row16_sum(d0), row16_sum(d1)), s23 = pack_bf16(row16_sum(d2), row16_sum(d3));
    const unsigned t01 = pack_bf16(bflo(s01) * SCALE, bfhi(s01) * SCALE), t23 = pack_bf16(bflo(s23) * SCALE, bfhi(s23) * SCALE);
    const f32x4 sc = {bflo(t01) * LOG2E, bfhi(t01) * LOG2E, bflo(t23) * LOG2E, bfhi(t23) * LOG2E};
    mx = fmaxf(fmaxf(mx, fmaxf(sc[0], sc[1])), fmaxf(sc[2], sc[3]));
    if (c16 == 0) *(LAS f32x4*)(L + L_SC + u * 64 + r4 * 16) = sc;
  };

  if (hw < 0) {
    {
      u32x4 hk[4 * NHC];
#pragma unroll
      for (int i = 0; i < NHC; ++i) take(cw + NCW * i, i + 1 < NHC ? cw + NCW * (i + 1) : NHELD + cw, hk[4 * i], hk[4 * i + 1], hk[4 * i + 2], hk[4 * i + 3]);
      STAMP(10);
      build_q();
      STAMP(3);
#pragma unroll
      for (int i = 0; i < NHC; ++i) { score(cw + NCW * i, hk[4 * i], hk[4 * i + 1], hk[4 * i + 2], hk[4 * i + 3]); __builtin_amdgcn_sched_barrier(0); }
    }
#pragma unroll 1
    for (int u = NHELD + cw; u < NKU; u += NCW) {
      u32x4 k0, k1, k2, k3;
      take(u, u + NCW, k0, k1, k2, k3);
      score(u, k0, k1, k2, k3);
    }
    ctrlf[32 + cw] = xrow_max(mx);
    lds_arrive(L, C_MAX, lane);
    STAMP(11);
    lds_await(ctrl, C_MAX, NCW + NHW);
    const float mg = wg_max();
    STAMP(12);
    float acc[8] = {0.f, 0.f, 0.f, 0.f, 0.f, 0.f, 0.f, 0.f};
#pragma unroll 1
    for (int u = NKU + cw; u < NUNIT; u += NCW) {
      const f32x4 sc = *(const LAS f32x4*)(L + L_SC + (u - NKU) * 64 + r4 * 16);
      float p[4];
#pragma unroll
      for (int t = 0; t < 4; ++t) { p[t] = __builtin_amdgcn_exp2f(sc[t] - mg); asm volatile("" : "+v"(p[t])); }
      u32x4 v[4];
      take(u, u + NCW, v[0], v[1], v[2], v[3]);
#pragma unroll
      for (int t = 0; t < 4; ++t)
#pragma unroll
        for (int k = 0; k < 4; ++k) { acc[2 * k] = fmaf(p[t], bflo(v[t][k]), acc[2 * k]); acc[2 * k + 1] = fmaf(p[t], bfhi(v[t][k]), acc[2 * k + 1]); }
    }
    STAMP(5);
    const float e0 = swadd16(swadd32(acc[0], acc[4]), swadd32(acc[2], acc[6])), e1 = swadd16(swadd32(acc[1], acc[5]), swadd32(acc[3], acc[7]));
    *(LAS f32x2*)(L + L_REC + (cw * DH + 8 * c16 + 2 * r4) * 4) = f32x2{e0, e1};
    BARRIER();
    return;
  }

  u32x4 wb[16];
  const unsigned tstart = (unsigned)__builtin_amdgcn_readfirstlane(ctrl[8]);
  {
    const __amdgpu_buffer_rsrc_t rq = mkrsrc(wq, D * H * DH * 2);
    const int vo = ((64 * hw + 16 * r4) * (H * DH) + h * DH + 8 * c16) * 2;
#pragma unroll
    for (int i = 0; i < 16; ++i) { wait_until(tstart + TQ + hw * QA + i * QB); wb[i] = ldw(rq, vo, i * (H * DH * 2)); }
    lds_await(ctrl, C_X, 1);
    const u32x4 xa = *(const LAS u32x4*)(L + L_X + (64 * hw + 16 * r4) * 2), xc = *(const LAS u32x4*)(L + L_X + (64 * hw + 16 * r4 + 8) * 2);
    float a[8] = {0.f, 0.f, 0.f, 0.f, 0.f, 0.f, 0.f, 0.f};
#pragma unroll
    for (int i = 0; i < 8; ++i) {
      const unsigned xp = i < 4 ? xa[i] : xc[i - 4];
#pragma unroll
      for (int k = 0; k < 4; ++k) {
        a[2 * k] = dot2(perm_lo(wb[2 * i][k], wb[2 * i + 1][k]), xp, a[2 * k]);
        a[2 * k + 1] = dot2(perm_hi(wb[2 * i][k], wb[2 * i + 1][k]), xp, a[2 * k + 1]);
      }
    }
    const float e0 = swadd16(swadd32(a[0], a[4]), swadd32(a[2], a[6])), e1 = swadd16(swadd32(a[1], a[5]), swadd32(a[3], a[7]));
    *(LAS f32x2*)(L + L_QP + (hw * DH + 8 * c16 + 2 * r4) * 4) = f32x2{e0, e1};
    lds_arrive(L, C_Q, lane);
    STAMP(2);
    const int hb = NHC * NCW + hw;
#pragma unroll
    for (int i = 0; i < NHH; ++i) take(hb + NHW * i, i + 1 < NHH ? hb + NHW * (i + 1) : NEVER, wb[4 * i], wb[4 * i + 1], wb[4 * i + 2], wb[4 * i + 3]);
    STAMP(10);
    build_q();
#pragma unroll
    for (int i = 0; i < NHH; ++i) { score(hb + NHW * i, wb[4 * i], wb[4 * i + 1], wb[4 * i + 2], wb[4 * i + 3]); __builtin_amdgcn_sched_barrier(0); }
    ctrlf[32 + cw] = xrow_max(mx);
    lds_arrive(L, C_MAX, lane);
  }
  const unsigned tg = GRID ? ((tstart + T1) | (unsigned)(GRID - 1)) + 1u - T1 : tstart;
  float z[8] = {0.f, 0.f, 0.f, 0.f, 0.f, 0.f, 0.f, 0.f};
  if ((pj & 7) != 7) {
    const int sl = 7 * (pj >> 3) + (pj & 7), rs = lane >> 3;
    const u16* wp = ((lane & 4) ? w3 : w1) + (size_t)(64 * hw + 8 * rs) * F + FS * sl + 8 * (lane & 3);
#pragma unroll
    for (int i = 0; i < 8; ++i) { wait_until(tg + T1 + hw * A1 + i * B1); wb[i] = __builtin_nontemporal_load((const GAS u32x4*)(wp + i * F)); }
    const __amdgpu_buffer_rsrc_t r2 = mkrsrc(w2, F * D * 2);
    const u32x4 xm = *(const LAS u32x4*)(L + L_X + (64 * hw + 8 * rs) * 2);
    float a[8] = {0.f, 0.f, 0.f, 0.f, 0.f, 0.f, 0.f, 0.f};
#pragma unroll
    for (int i = 0; i < 4; ++i)
#pragma unroll
      for (int k = 0; k < 4; ++k) {
        a[2 * k] = dot2(perm_lo(wb[2 * i][k], wb[2 * i + 1][k]), xm[i], a[2 * k]);
        a[2 * k + 1] = dot2(perm_hi(wb[2 * i][k], wb[2 * i + 1][k]), xm[i], a[2 * k + 1]);
      }
    float e0 = swadd16(swadd32(a[0], a[4]), swadd32(a[2], a[6])), e1 = swadd16(swadd32(a[1], a[5]), swadd32(a[3], a[7]));
    e0 += dpp<ROR8>(e0); e1 += dpp<ROR8>(e1);
    if ((lane & 8) == 0) *(LAS f32x2*)(L + L_MP + (hw * 64 + 8 * (lane & 7) + 2 * r4) * 4) = f32x2{e0, e1};
    lds_arrive(L, C_MLP, lane);
#pragma unroll
    for (int i = 0; i < 4; ++i) { wait_until(tg + T2 + hw * A2 + i * B2); wb[8 + i] = ldw(r2, ((FS * sl + 4 * rs) * D + 64 * hw + 8 * (lane & 7)) * 2, i * (D * 2)); }
    lds_await(ctrl, C_MLP, NHW);
    const LAS float* mp = (const LAS float*)(L + L_MP) + 4 * rs + (lane & 3);
    float ga = 0.f, up = 0.f;
#pragma unroll
    for (int w = 0; w < NHW; ++w) { ga += mp[w * 64]; up += mp[w * 64 + 32]; }
    const float hv = ga / (1.f + __expf(-ga)) * up * (1.f - ctrlf[48]);
    const float hs[4] = {dpp<0x00>(hv), dpp<0x55>(hv), dpp<0xAA>(hv), dpp<0xFF>(hv)};
#pragma unroll
    for (int i = 0; i < 4; ++i)
#pragma unroll
      for (int k = 0; k < 4; ++k) { z[2 * k] = fmaf(hs[i], bflo(wb[8 + i][k]), z[2 * k]); z[2 * k + 1] = fmaf(hs[i], bfhi(wb[8 + i][k]), z[2 * k + 1]); }
  }
#pragma unroll
  for (int k = 0; k < 8; ++k) asm volatile("" : "+v"(z[k]));
  {
    const __amdgpu_buffer_rsrc_t ro = mkrsrc(wo, H * DH * D * 2);
#pragma unroll
    for (int i = 0; i < 16; ++i) { wait_until(tg + TO + hw * AO + i * BO); wb[i] = ldw(ro, ((h * DH + 16 * (lane >> 3)) * D + 64 * hw + 8 * (lane & 7)) * 2, i * (D * 2)); }
#pragma unroll
    for (int i = 0; i < 8; ++i)
#pragma unroll
      for (int k = 0; k < 4; ++k) {
        const unsigned lo = perm_lo(wb[2 * i][k], wb[2 * i + 1][k]), hi = perm_hi(wb[2 * i][k], wb[2 * i + 1][k]);
        wb[2 * i][k] = lo; wb[2 * i + 1][k] = hi;
      }
#pragma unroll
    for (int i = 0; i < 16; ++i) asm volatile("" : "+v"(wb[i]));
  }
  STAMP(4);
  BARRIER();
  STAMP(9);
  {
    const LAS f32x2* rec = (const LAS f32x2*)(L + L_REC) + lane;
    const f32x2 a = (rec[0] + rec[DH / 2]) + (rec[2 * DH / 2] + rec[3 * DH / 2]);
    const float cf = ctrlf[49];
    const int op = (int)pack_bf16(a[0], a[1]);
    const int olo = __builtin_amdgcn_update_dpp(op, op, 0x114, 0xf, 0xa, false);
    const int ohi = __builtin_amdgcn_update_dpp(op, op, 0x104, 0xf, 0x5, false);
    const unsigned os[8] = {(unsigned)dppi<0x00>(olo), (unsigned)dppi<0x55>(olo), (unsigned)dppi<0xAA>(olo), (unsigned)dppi<0xFF>(olo),
                            (unsigned)dppi<0x00>(ohi), (unsigned)dppi<0x55>(ohi), (unsigned)dppi<0xAA>(ohi), (unsigned)dppi<0xFF>(ohi)};
    float y[8] = {0.f, 0.f, 0.f, 0.f, 0.f, 0.f, 0.f, 0.f};
#pragma unroll
    for (int i = 0; i < 8; ++i)
#pragma unroll
      for (int k = 0; k < 4; ++k) { y[2 * k] = dot2(wb[2 * i][k], os[i], y[2 * k]); y[2 * k + 1] = dot2(wb[2 * i + 1][k], os[i], y[2 * k + 1]); }
    u32x4 v;
#pragma unroll
    for (int k = 0; k < 4; ++k) {
      float t = swadd32(fmaf(cf, y[k], z[k]), fmaf(cf, y[4 + k], z[4 + k]));
      t = swadd16(t, t); t += dpp<ROR8>(t);
      v[k] = min(__float_as_uint(t), EMPTY - 1u);
    }
    if ((lane & 24) == 0) __builtin_amdgcn_raw_buffer_store_b128(v, wsr, WS_Y + (b * 64 + pj) * 2048 + hw * 256 + (lane >> 5) * 128 + (lane & 7) * 16, 0, YST);
  }
  STAMP(7);
}

extern "C" void kernel_launch(void* const* d_in, const int* in_sizes, int n_in, void* d_out, int out_size, void* d_ws, size_t ws_size,
                              hipStream_t stream) {
  gated_mix_kernel<<<256, NTHR, 0, stream>>>((const u16*)d_in[0], (const u16*)d_in[1], (const u16*)d_in[2], (const u16*)d_in[3], (const u16*)d_in[4],
                                             (const u16*)d_in[6], (const u16*)d_in[7], (const u16*)d_in[8], (const u16*)d_in[9], (u16*)d_out,
                                             (unsigned char*)d_ws);
}
```

```cpp
#include <hip/hip_runtime.h>
#include <hip/hip_bf16.h>

#define GAS __attribute__((address_space(1)))
#define LAS __attribute__((address_space(3)))
typedef unsigned short u16;
typedef unsigned long long u64;
using u32x4 = __attribute__((ext_vector_type(4))) unsigned;
using u32x2 = __attribute__((ext_vector_type(2))) unsigned;
using f32x4 = __attribute__((ext_vector_type(4))) float;
using f32x2 = __attribute__((ext_vector_type(2))) float;
using bf16x2 = __attribute__((ext_vector_type(2))) __bf16;
using bf16x8 = __attribute__((ext_vector_type(8))) __bf16;

constexpr int B = 4, D = 512, H = 32, SK = 8192, DH = 128, F = 1792;
constexpr int NCW = 4, NHW = 8, XWAVE = 1 + NCW + NHW, NWAVE = XWAVE + 2, NTHR = NWAVE * 64;
#define QA 80
#define QB 5
#define T1 1200
#define A1 150
#define B1 18
#define T2 2500
#define A2 40
#define B2 4
#define TO 2900
#define AO 350
#define BO 8
#define TS 70.6
#define REF0 180
#define PLAG 1
#define NOPST 0
#define NOPLD 0
#define PAUX 16
#define RAUX 16
#define PFW 0
#define TQ 130
#define NINIT 6
#define CODE_KIB 21
#define FDEL 0
#define CODEPF 1
#define YST 16
#define YLD 16
#define RED16 0
#define MASKP 0
#define PSLP 1
#define OUTSC 1
#define OUT_STORE(p, v) __hip_atomic_store((GAS unsigned*)(p), v, __ATOMIC_RELAXED, __HIP_MEMORY_SCOPE_AGENT)
#define CLR0 1
#define MLPFULL 0
#define MLPPF 0
#define TP 1200
#define AP 12
#define GRID 0
#define B2BD 0
#define B2BAUX 16
#define WOB2B 0
#define TOUCHW 1
#define W0 30
#define BARPOS 12
#define NO_WO 0
#define TLBPF 0
#define TMODE 0
#define SPW 6
#define QS 2
#define S1 6
#define S2 1
#define SO 2
#define USL 0
#define SLOTSH 8
#define WMID 21.0
#define WGAIN 8
#define WMIN 14
#define WMAX 27
#define CLRAUX 16
#define PCONT 1
#define XDEL 20
#define XSPR 0
#define XNT 0
#define MIMIC 1
#define NCLR 2
#define NOFLAG 0
#define CSPR 0
constexpr int UNIT = 4096;
constexpr int NSLOT = 32, RING = NSLOT * UNIT;
constexpr int NKU = (SK / 2) / 16, NUNIT = 2 * NKU;
constexpr int NHC = 6, NHH = 4, NHELD = NHC * NCW + NHH * NHW;
constexpr int FS = 32;
constexpr int L_CTRL = RING;
constexpr int L_X = L_CTRL + 256;
constexpr int L_QP = L_X + D * 2;
constexpr int L_MP = L_QP + NHW * DH * 4;
constexpr int L_REC = L_MP + NHW * 64 * 4;
constexpr int L_H = L_REC + NCW * 272;
constexpr int L_SC = L_H + NHW * 128;
constexpr int LDS_BYTES = L_SC + NKU * 64;
static_assert(LDS_BYTES <= 159 * 1024, "lds");
static_assert(WMIN <= (int)WMID && (int)WMID <= WMAX && WMAX <= 28, "window");
constexpr int WS_EPOCH = 0;
constexpr int WS_REC = 4096;
constexpr int WS_PACE = WS_REC + 256 * 128;
constexpr int WS_Y = WS_PACE + 64 * 2048;
constexpr int WS_BYTES = WS_Y + B * 64 * 2048;
constexpr u64 TAG_STEP = 0x9E3779B97F4A7C15ull;
constexpr unsigned YMASK = 0xFFFu;
constexpr int SPIN_CAP = 1 << 18;
constexpr int NEVER = 1 << 20;
constexpr int C_Q = 4, C_MAX = 5, C_MLP = 6, C_X = 7, C_CF = 9;

__device__ __forceinline__ float bflo(unsigned u) { return __builtin_bit_cast(float, u << 16); }
__device__ __forceinline__ float bfhi(unsigned u) { return __builtin_bit_cast(float, u & 0xffff0000u); }
__device__ __forceinline__ float dot2(unsigned a, unsigned b, float acc) {
  return __builtin_amdgcn_fdot2_f32_bf16(__builtin_bit_cast(bf16x2, a), __builtin_bit_cast(bf16x2, b), acc, false);
}
__device__ __forceinline__ unsigned pack_bf16(float lo, float hi) {
  const bf16x2 v = {(__bf16)lo, (__bf16)hi};
  return __builtin_bit_cast(unsigned, v);
}
__device__ __forceinline__ unsigned perm_lo(unsigned a, unsigned b) { return __builtin_amdgcn_perm(b, a, 0x05040100u); }
__device__ __forceinline__ unsigned perm_hi(unsigned a, unsigned b) { return __builtin_amdgcn_perm(b, a, 0x07060302u); }
template <int CTRL> __device__ __forceinline__ float dpp(float x) {
  return __builtin_bit_cast(float, __builtin_amdgcn_mov_dpp(__builtin_bit_cast(int, x), CTRL, 0xf, 0xf, true));
}
constexpr int XOR1 = 0xB1, ROR1 = 0x121, ROR2 = 0x122, ROR4 = 0x124, ROR8 = 0x128;
__device__ __forceinline__ float row16_sum(float x) { x += dpp<ROR8>(x); x += dpp<ROR4>(x); x += dpp<ROR2>(x); x += dpp<ROR1>(x); return x; }
__device__ __forceinline__ float swadd32(float a, float b) {
  auto t = __builtin_amdgcn_permlane32_swap(__float_as_uint(a), __float_as_uint(b), false, false);
  return __uint_as_float(t[0]) + __uint_as_float(t[1]);
}
__device__ __forceinline__ float swadd16(float a, float b) {
  auto t = __builtin_amdgcn_permlane16_swap(__float_as_uint(a), __float_as_uint(b), false, false);
  return __uint_as_float(t[0]) + __uint_as_float(t[1]);
}
__device__ __forceinline__ float xrow_sum(float x) { x = swadd16(x, x); return swadd32(x, x); }
__device__ __forceinline__ float xrow_max(float x) {
  auto s = __builtin_amdgcn_permlane16_swap(__float_as_uint(x), __float_as_uint(x), false, false);
  x = fmaxf(__uint_as_float(s[0]), __uint_as_float(s[1]));
  auto t = __builtin_amdgcn_permlane32_swap(__float_as_uint(x), __float_as_uint(x), false, false);
  return fmaxf(__uint_as_float(t[0]), __uint_as_float(t[1]));
}
__device__ __forceinline__ __amdgpu_buffer_rsrc_t mkrsrc(const void* p, int bytes) { return __builtin_amdgcn_make_buffer_rsrc((void*)p, 0, bytes, 0x00020000); }
__device__ __forceinline__ u32x4 ldw(__amdgpu_buffer_rsrc_t r, int voff, int soff) { return __builtin_amdgcn_raw_buffer_load_b128(r, voff, soff, 2); }
#define LDS_WAIT() asm volatile("s_waitcnt lgkmcnt(0)" ::: "memory")
#define VM_DRAIN() asm volatile("s_waitcnt vmcnt(0)" ::: "memory")
#define CFENCE() asm volatile("" ::: "memory")
#define BARRIER() asm volatile("s_waitcnt lgkmcnt(0)\n\ts_barrier" ::: "memory")
#define PAUSE(n) do { __builtin_amdgcn_sched_barrier(0); asm volatile("s_sleep %0" :: "i"(n) : "memory"); __builtin_amdgcn_sched_barrier(0); } while (0)

__device__ __forceinline__ void lds_arrive(LAS unsigned char* L, int idx, int lane) {
  LDS_WAIT();
  if (lane == 0) __hip_atomic_fetch_add((LAS int*)(L + L_CTRL) + idx, 1, __ATOMIC_RELAXED, __HIP_MEMORY_SCOPE_WORKGROUP);
}
__device__ __forceinline__ void lds_await(volatile LAS int* ctrl, int idx, int n) {
#pragma unroll 1
  for (int spin = 0; spin < SPIN_CAP; ++spin) { if (__builtin_amdgcn_readfirstlane(ctrl[idx]) >= n) break; __builtin_amdgcn_s_sleep(1); }
  CFENCE();
}

__device__ __forceinline__ void wait_until(unsigned t) {
  __builtin_amdgcn_sched_barrier(0);
#pragma unroll 1
  for (int spin = 0; spin < 4096; ++spin) {
    const int d = (int)(t - (unsigned)__builtin_amdgcn_s_memrealtime());
    if (d <= 0) break;
    if (d > 24) __builtin_amdgcn_s_sleep(6);
  }
  CFENCE();
  __builtin_amdgcn_sched_barrier(0);
}
#define STR2(x) #x
#define STR(x) STR2(x)
constexpr int TAU2 = 2 * (int)(TS * 100.0 * 256.0 / NUNIT);
__device__ __forceinline__ f32x4 mfma(const u32x4& a, const u32x4& b, const f32x4& c) {
  return __builtin_amdgcn_mfma_f32_16x16x32_bf16(__builtin_bit_cast(bf16x8, a), __builtin_bit_cast(bf16x8, b), c, 0, 0, 0);
}
__device__ __forceinline__ u32x2 ldw2(__amdgpu_buffer_rsrc_t r, int voff, int soff) { return __builtin_amdgcn_raw_buffer_load_b64(r, voff, soff, 2); }
template <int CTRL> __device__ __forceinline__ int dppi(int x) { return __builtin_amdgcn_mov_dpp(x, CTRL, 0xf, 0xf, true); }

#define STAMP(i) do {} while (0)
#define STAMP_FLUSH() do {} while (0)
#define STAMP_LDS 0

__global__ void __launch_bounds__(NTHR) gated_mix_kernel(const u16* x, const u16* kc, const u16* vc, const u16* wq, const u16* wo, const u16* w1,
                                                         const u16* w3, const u16* w2, const u16* gw, u16* out, unsigned char* ws) {
  __shared__ __attribute__((aligned(16))) unsigned char lds_raw[LDS_BYTES + STAMP_LDS];
  LAS unsigned char* L = (LAS unsigned char*)lds_raw;
  volatile LAS int* ctrl = (volatile LAS int*)(L + L_CTRL);
  volatile LAS float* ctrlf = (volatile LAS float*)(L + L_CTRL);
#define DBG(x) ""
  {
    unsigned va, vb, vo, pa, nxa, t0, t1, t2, t3;
#define CODELD "global_load_lds_dwordx4 %[t0], s[56:57]\n\t"
#define ASTAMP(i) DBG("s_memrealtime s[60:61]\n\ts_waitcnt lgkmcnt(0)\n\tv_mov_b32 %[t1], %c[dbg] + 4 * " #i "\n\tv_mov_b32 %[t2], s60\n\tds_write_b32 %[t1], %[t2]\n\t")
#define WAITW "s_waitcnt (wcnt & 15) | ((wcnt >> 4) << 14) | 0x0F70\n\t"
#define SP(v, sb, wait) wait "global_load_lds_dword " v ", " sb " nt\n\tglobal_load_lds_dword " v ", " sb " sc1\n\t"
#define SW "s_waitcnt vmcnt(%c[spw])\n\t"
#define UNIT4(v) \
        WAITW "global_load_lds_dwordx4 " v ", s[40:41] nt\n\t" \
        WAITW "global_load_lds_dwordx4 " v ", s[40:41] offset:1024 nt\n\t" \
        WAITW "global_load_lds_dwordx4 " v ", s[40:41] offset:2048 nt\n\t" \
        WAITW "global_load_lds_dwordx4 " v ", s[40:41] offset:3072 nt\n\t"
    asm volatile(
        "s_getpc_b64 s[56:57]\n\t"
        "s_load_dwordx8 s[44:51], %[ka], 0x18\n\t"
        "s_load_dwordx2 s[52:53], %[ka], 0x38\n\t"
        "s_load_dwordx4 s[40:43], %[ka], 0x8\n\t"
        "v_readfirstlane_b32 s54, %[tid]\n\t"
        "s_mov_b32 m0, %c[jk]\n\t"
        "s_lshr_b32 s59, %[bid], 3\n\t"
        "s_cmp_ge_u32 s54, 64\n\t"
        "s_cbranch_scc1 90f\n\t"
        "s_min_u32 s54, s59, %c[nch]\n\t"
        "s_lshl_b32 s54, s54, 10\n\t"
        "v_lshlrev_b32 %[vo], 4, %[tid]\n\t"
        "s_and_b32 s56, s56, -128\n\t"
        "v_add_u32 %[t0], s54, %[vo]\n\t"
        "s_and_b32 s58, %[bid], 7\n\t"
        CODELD
        "s_setprio 3\n\t"
        "v_lshlrev_b32 %[t1], 7, %[tid]\n\t"
        "s_lshl_b32 s54, s59, 7\n\t"
        "s_add_u32 s54, s54, s58\n\t"
        "s_lshl_b32 s54, s54, 10\n\t"
        "v_lshrrev_b32 %[va], 3, %[tid]\n\t"
        "v_mul_u32_u24 %[va], 8192 - 1024, %[va]\n\t"
        "v_add3_u32 %[va], %[va], %[t1], s54\n\t"
        "v_add_u32 %[t2], 0x10000, %[va]\n\t"
        "s_lshl_b32 s55, s58, 5\n\t"
        "s_add_u32 s55, s55, s59\n\t"
        "s_mul_i32 s54, s55, 7 * 1024\n\t"
        "v_min_u32 %[t3], 55 * 128, %[t1]\n\t"
        "v_add_u32 %[t3], s54, %[t3]\n\t"
        "s_mul_i32 s54, s58, 448\n\t"
        "s_and_b32 s54, s54, -128\n\t"
        "s_mul_i32 s57, s59, 16 * 3584\n\t"
        "s_add_u32 s54, s54, s57\n\t"
        "v_lshrrev_b32 %[t0], 2, %[tid]\n\t"
        "v_mul_u32_u24 %[t0], 3584 - 512, %[t0]\n\t"
        "v_add3_u32 %[t0], %[t0], %[t1], s54\n\t"
        "v_lshl_add_u32 %[vb], s55, 14, %[t1]\n\t"
        "v_add_u32 %[t1], 0x2000, %[vb]\n\t"
        "s_and_b32 s54, %[bid], 0xc0\n\t"
        "s_lshl3_add_u32 s54, s58, s54\n\t"
        "s_and_b32 s56, s59, 7\n\t"
        "s_add_u32 s54, s54, s56\n\t"
        "s_lshl_b32 s56, s54, 20\n\t"
        "s_waitcnt lgkmcnt(0)\n\t"
        SP("%[va]", "s[44:45]", "") SP("%[t2]", "s[44:45]", "") SP("%[t3]", "s[52:53]", "")
        ".if %c[spw] <= 4\n\ts_barrier\n\t.endif\n\t"
        SP("%[t0]", "s[48:49]", SW)
        ".if %c[spw] == 6\n\ts_barrier\n\t.endif\n\t"
        SP("%[t0]", "s[50:51]", SW)
        ".if %c[spw] == 8\n\ts_barrier\n\t.endif\n\t"
        SP("%[vb]", "s[46:47]", SW) SP("%[t1]", "s[46:47]", SW)
        ".if %c[spw] > 8\n\ts_barrier\n\t.endif\n\t"
        ASTAMP(6)
        "s_add_u32 s40, s40, s56\n\t"
        "s_addc_u32 s41, s41, 0\n\t"
        "s_add_u32 s42, s42, s56\n\t"
        "s_addc_u32 s43, s43, 0\n\t"
        "s_sub_u32 s42, s42, %c[kbytes]\n\t"
        "s_subb_u32 s43, s43, 0\n\t"
        ".set so, 0\n\t"
        ".set wcnt, %c[spw]\n\t"
        ".rept 4\n\t"
        "s_movk_i32 m0, so\n\t"
        ".if (so & 4096) == 0\n\t"
        WAITW
        ".set wcnt, wcnt + 6\n\t"
        ".else\n\t"
        "s_nop 0\n\t"
        ".endif\n\t"
        "global_load_lds_dwordx4 %[vo], s[40:41] nt\n\t"
        "global_load_lds_dwordx4 %[vo], s[40:41] offset:1024 nt\n\t"
        "global_load_lds_dwordx4 %[vo], s[40:41] offset:2048 nt\n\t"
        "global_load_lds_dwordx4 %[vo], s[40:41] offset:3072 nt\n\t"
        "v_add_u32 %[vo], 0x1000, %[vo]\n\t"
        ".set so, so + 4096\n\t"
        ".endr\n\t"
        ".if %c[spw] <= 4\n\t"
        "s_waitcnt vmcnt(16)\n\t"
        ".endif\n\t"
        ASTAMP(7)
        "s_getpc_b64 s[50:51]\n"
        "20:\n\t"
        "s_add_u32 s50, s50, 23f-20b-%c[wmin]*128\n\t"
        "s_addc_u32 s51, s51, -1\n\t"
        "v_mov_b32 %[pa], %c[ctl]\n\t"
        "v_and_b32 %[nxa], 0x3c0 >> 2, %[vo]\n\t"
        "v_lshrrev_b32 %[nxa], 2, %[nxa]\n\t"
        "v_add_u32 %[nxa], %c[ctl] + 64, %[nxa]\n\t"
        "s_mov_b32 s44, 4\n\t"
        "s_mov_b32 s45, %c[wmid] >> 8\n\t"
        "s_mov_b32 s46, 0x4000\n\t"
        "s_mov_b32 s47, 2 * %c[tau2]\n\t"
        "s_mov_b32 s48, %c[nslot]\n\t"
        "s_mov_b32 s49, %c[nku]\n"
        "22:\n\t"
        "s_memrealtime s[56:57]\n\t"
        "ds_read_b32 %[t3], %[pa] offset:8\n\t"
        "s_lshl_b32 s52, s45, 7\n\t"
        "s_add_u32 s52, s50, s52\n\t"
        "s_addc_u32 s53, s51, 0\n\t"
        "s_add_i32 s54, s45, -1\n\t"
        "s_lshr_b32 s54, s54, 2\n\t"
        "s_sub_i32 s54, s44, s54\n\t"
        "v_mov_b32 %[t1], s54\n\t"
        "v_add_u32 %[t0], 0x1000, %[vo]\n\t"
        "s_add_u32 s55, s46, 0x1000\n\t"
        "s_mov_b32 m0, s46\n\t"
        "s_setpc_b64 s[52:53]\n"
        "24:\n\t"
        "s_waitcnt lgkmcnt(0)\n\t"
        "v_readfirstlane_b32 s57, %[t3]\n\t"
        "v_add_u32 %[t1], 1, %[t1]\n\t"
        "s_lshr_b32 s54, s47, 8\n\t"
        "s_sub_u32 s56, s56, s54\n\t"
        "v_mov_b32 %[t2], s56\n\t"
        "s_sub_i32 s56, s56, s57\n\t"
        "ds_write2_b32 %[pa], %[t1], %[t2] offset1:1\n\t"
        "s_mul_i32 s56, s56, %c[gain]\n\t"
        "s_add_i32 s56, s56, %c[wmid]\n\t"
        "s_ashr_i32 s56, s56, 8\n\t"
        "s_max_i32 s56, s56, %c[wmin]\n\t"
        "s_min_i32 s45, s56, %c[wmax]\n\t"
        "s_add_u32 s47, s47, %c[tau2]\n\t"
        "s_add_u32 s46, s46, 0x2000\n\t"
        "s_and_b32 s46, s46, %c[mask]\n\t"
        "v_add_u32 %[vo], 0x2000, %[vo]\n\t"
        "s_add_i32 s44, s44, 2\n\t"
        "s_cmp_lt_i32 s44, s48\n\t"
        "s_cbranch_scc1 22b\n\t"
        DBG("s_add_i32 s54, s44, -1\n\ts_lshr_b32 s54, s54, 5\n\ts_lshl_b32 s54, s54, 2\n\ts_add_u32 s54, s54, %c[dbg] + 14 * 64\n\tv_mov_b32 %[t0], s54\n\tv_mov_b32 %[t1], s45\n\t"
            "ds_write2_b32 %[t0], %[t2], %[t1] offset1:16\n\t")
        "s_cmp_lt_i32 s44, s49\n\t"
        "s_cbranch_scc1 25f\n\t"
        "s_cmp_ge_i32 s44, 2 * %c[nku]\n\t"
        "s_cbranch_scc1 30f\n\t"
        "s_mov_b32 s49, 2 * %c[nku]\n\t"
        "s_mov_b64 s[40:41], s[42:43]\n"
        "25:\n\t"
        "s_mov_b32 s55, %c[cap]\n"
        "26:\n\t"
        "ds_read_b32 %[t0], %[nxa]\n\t"
        "s_waitcnt lgkmcnt(0)\n\t"
        "s_nop 1\n\t"
        "v_min_i32_dpp %[t0], %[t0], %[t0] row_ror:8 row_mask:0xf bank_mask:0xf\n\t"
        "s_nop 1\n\t"
        "v_min_i32_dpp %[t0], %[t0], %[t0] row_ror:4 row_mask:0xf bank_mask:0xf\n\t"
        "s_nop 1\n\t"
        "v_min_i32_dpp %[t0], %[t0], %[t0] row_ror:2 row_mask:0xf bank_mask:0xf\n\t"
        "s_nop 1\n\t"
        "v_min_i32_dpp %[t0], %[t0], %[t0] row_ror:1 row_mask:0xf bank_mask:0xf\n\t"
        "s_nop 1\n\t"
        "v_readfirstlane_b32 s48, %[t0]\n\t"
        "s_nop 1\n\t"
        "s_add_i32 s48, s48, %c[nslot]\n\t"
        "s_and_b32 s48, s48, -2\n\t"
        "s_min_i32 s48, s48, s49\n\t"
        "s_cmp_lt_i32 s44, s48\n\t"
        "s_cbranch_scc1 22b\n\t"
        "v_mov_b32 %[t1], s44\n\t"
        "s_waitcnt vmcnt(0)\n\t"
        "ds_write_b32 %[pa], %[t1]\n\t"
        "s_sleep 1\n\t"
        "s_sub_u32 s55, s55, 1\n\t"
        "s_cmp_lg_u32 s55, 0\n\t"
        "s_cbranch_scc1 26b\n\t"
        "s_branch 22b\n"
        "30:\n\t"
        ".set k, 6\n\t"
        ".rept 7\n\t"
        ".set wcnt, 4 * k\n\t"
        "v_mov_b32 %[t1], 2 * %c[nku] - k\n\t"
        WAITW
        "ds_write_b32 %[pa], %[t1]\n\t"
        ".set k, k - 1\n\t"
        ".endr\n\t"
        DBG("s_memrealtime s[56:57]\n\ts_waitcnt lgkmcnt(0)\n\tv_mov_b32 %[t0], %c[dbg] + 4\n\tv_mov_b32 %[t1], s56\n\tds_write_b32 %[t0], %[t1]\n\t")
        "s_waitcnt lgkmcnt(0)\n\t"
        "s_endpgm\n\t"
        ".p2align 7\n"
        "23:\n\t"
        ".set wcnt, %c[wmin]-1\n\t"
        ".rept %c[wmax] - %c[wmin] + 1\n\t"
        UNIT4("%[vo]")
        "ds_write_b32 %[pa], %[t1]\n\t"
        "s_mov_b32 m0, s55\n\t"
        UNIT4("%[t0]")
        "s_branch 24b\n\t"
        ".p2align 7\n\t"
        ".set wcnt, wcnt+1\n\t"
        ".endr\n"
        "90:\n\t"
        "s_waitcnt lgkmcnt(0)"
        : [va] "=&v"(va), [vb] "=&v"(vb), [vo] "=&v"(vo), [pa] "=&v"(pa), [nxa] "=&v"(nxa), [t0] "=&v"(t0), [t1] "=&v"(t1), [t2] "=&v"(t2), [t3] "=&v"(t3)
        : [tid] "v"(threadIdx.x), [bid] "s"(blockIdx.x), [ka] "s"(__builtin_amdgcn_kernarg_segment_ptr()), [nch] "i"(CODE_KIB - 1), [jk] "i"(RING - UNIT),
          [spw] "i"(SPW), [kbytes] "i"(NKU * UNIT), [wmin] "i"(WMIN), [wmax] "i"(WMAX), [gain] "i"(WGAIN), [wmid] "i"((int)(WMID * 256)), [tau2] "i"(TAU2),
          [mask] "i"(RING - 1), [ctl] "i"(L_CTRL), [nslot] "i"(NSLOT), [nku] "i"(NKU), [cap] "i"(SPIN_CAP), [dbg] "i"(LDS_BYTES)
        : "memory", "scc", "s40", "s41", "s42", "s43", "s44", "s45", "s46", "s47", "s48", "s49", "s50", "s51", "s52", "s53", "s54", "s55", "s56", "s57", "s58", "s59", "s60", "s61");
  }
  __builtin_amdgcn_sched_barrier(0);
  const int tid = threadIdx.x, lane = tid & 63, wave = __builtin_amdgcn_readfirstlane(tid >> 6);
  const int xg = blockIdx.x & 7, kk = blockIdx.x >> 3;
  const int b = kk >> 3, pj = 8 * xg + (kk & 7), h = pj >> 1, s = pj & 1;
  const int r4 = lane >> 4, c16 = lane & 15;
  asm volatile("" : "+s"(x), "+s"(wq), "+s"(wo), "+s"(w1), "+s"(w3), "+s"(w2), "+s"(gw), "+s"(out), "+s"(ws));
  if (wave == 1) {
    const int wi = lane - 16;
    const int now = (int)(unsigned)__builtin_amdgcn_s_memrealtime() + REF0;
    ctrl[lane] = (lane == 1 || lane == 2) ? now : lane == 8 ? now - REF0 : (lane < 16 || lane >= 32) ? 0 : wi < NCW ? wi : wi < NCW + NHW ? NHC * NCW + wi - NCW : NEVER;
  }
  BARRIER();

  const __amdgpu_buffer_rsrc_t wsr = mkrsrc(ws, WS_BYTES);
  auto wg_max = [&]() __attribute__((always_inline)) {
    float mg = ctrlf[32 + (c16 < NCW + NHW ? c16 : 0)];
    mg = fmaxf(mg, dpp<ROR8>(mg)); mg = fmaxf(mg, dpp<ROR4>(mg)); mg = fmaxf(mg, dpp<ROR2>(mg)); mg = fmaxf(mg, dpp<ROR1>(mg));
    return mg;
  };

  if (wave == XWAVE + 1) {
    unsigned k = ((unsigned)__builtin_amdgcn_s_memrealtime() >> SLOTSH) + 1;
#pragma unroll 1
    for (int it = 0; it < 4096 >> (SLOTSH - 2); ++it, ++k) {
      wait_until(k << SLOTSH);
      if (__builtin_amdgcn_readfirstlane(ctrl[0]) >= NUNIT - 36) break;
      const int own = __builtin_amdgcn_readfirstlane(ctrl[1]);
      constexpr unsigned SM = (8192u >> SLOTSH) - 1u;
      if (!NOPST && blockIdx.x < 16 && lane < 8) __builtin_amdgcn_raw_buffer_store_b128(u32x4{(unsigned)own, (unsigned)own, (unsigned)own, (unsigned)own}, wsr, WS_PACE + (k & SM) * 2048 + blockIdx.x * 128 + lane * 16, 0, PAUX);
      if (it < PLAG || NOPLD) continue;
      int e[16];
      asm volatile("s_load_dword %0, %16, 0x0\n\ts_load_dword %1, %16, 0x80\n\ts_load_dword %2, %16, 0x100\n\ts_load_dword %3, %16, 0x180\n\t"
                   "s_load_dword %4, %16, 0x200\n\ts_load_dword %5, %16, 0x280\n\ts_load_dword %6, %16, 0x300\n\ts_load_dword %7, %16, 0x380\n\t"
                   "s_load_dword %8, %16, 0x400\n\ts_load_dword %9, %16, 0x480\n\ts_load_dword %10, %16, 0x500\n\ts_load_dword %11, %16, 0x580\n\t"
                   "s_load_dword %12, %16, 0x600\n\ts_load_dword %13, %16, 0x680\n\ts_load_dword %14, %16, 0x700\n\ts_load_dword %15, %16, 0x780\n\t"
                   "s_waitcnt lgkmcnt(0)"
                   : "=&s"(e[0]), "=&s"(e[1]), "=&s"(e[2]), "=&s"(e[3]), "=&s"(e[4]), "=&s"(e[5]), "=&s"(e[6]), "=&s"(e[7]),
                     "=&s"(e[8]), "=&s"(e[9]), "=&s"(e[10]), "=&s"(e[11]), "=&s"(e[12]), "=&s"(e[13]), "=&s"(e[14]), "=&s"(e[15])
                   : "s"(ws + WS_PACE + ((k - PLAG) & SM) * 2048) : "memory");
      int sum = 0, cnt = 0;
#pragma unroll
      for (int i = 0; i < 16; ++i) { const int d = e[i] - own; const bool ok = (unsigned)(d + 600) < 1200u; sum += ok ? d : 0; cnt += ok ? 1 : 0; }
      if (cnt >= 8) ctrl[2] = own + (int)((float)sum / (float)cnt);
    }
    return;
  }

  if (wave == XWAVE) {
#pragma unroll 1
    for (int i = 0; i < XDEL + XSPR * (kk & 31); ++i) PAUSE(4);
    const u32x4 xv = *(const GAS u32x4*)(x + b * D + 8 * lane), gv = *(const GAS u32x4*)(gw + 8 * lane);
    *(LAS u32x4*)(L + L_X + lane * 16) = xv;
    float gd = 0.f;
#pragma unroll
    for (int k = 0; k < 4; ++k) gd = dot2(xv[k], gv[k], gd);
    gd = xrow_sum(row16_sum(gd));
    const float g = 1.f / (1.f + __expf(-gd));
    ctrlf[48] = g;
    lds_arrive(L, C_X, lane);
    u64 tag;
    asm volatile("s_load_dwordx2 %0, %1, 0x0\n\ts_waitcnt lgkmcnt(0)" : "=s"(tag) : "s"(ws + WS_EPOCH + b * 256) : "memory");
    tag += TAG_STEP;
#pragma unroll 1
    for (int spin = 0; spin < SPIN_CAP; ++spin) { if (__builtin_amdgcn_readfirstlane(ctrl[C_MAX]) >= NCW + NHW) break; __builtin_amdgcn_s_sleep(16); }
    CFENCE();
    const float mg = wg_max();
    float l = 0.f;
#pragma unroll 4
    for (int i = 0; i < NKU * 64 / 1024; ++i) {
      const f32x4 sc = *(const LAS f32x4*)(L + L_SC + i * 1024 + lane * 16);
      l += (__builtin_amdgcn_exp2f(sc[0] - mg) + __builtin_amdgcn_exp2f(sc[1] - mg)) + (__builtin_amdgcn_exp2f(sc[2] - mg) + __builtin_amdgcn_exp2f(sc[3] - mg));
    }
    l = xrow_sum(row16_sum(l));
    const unsigned tlo = (unsigned)tag, thi = (unsigned)(tag >> 32);
    if (lane < 8) __builtin_amdgcn_raw_buffer_store_b128(u32x4{__float_as_uint(mg), tlo, __float_as_uint(l), thi}, wsr, WS_REC + (b * 64 + pj) * 128 + lane * 16, 0, 16);
#pragma unroll 1
    for (int i = 0; i < 12 + (kk & 31); ++i) PAUSE(12);
    u32x4 rec;
#pragma unroll 1
    for (int spin = 0; spin < SPIN_CAP; ++spin) {
      CFENCE();
      rec = __builtin_amdgcn_raw_buffer_load_b128(wsr, WS_REC + (b * 64 + (pj ^ 1)) * 128, 0, 16);
      if (__builtin_amdgcn_ballot_w64(rec[1] != tlo || rec[3] != thi) == 0ull) break;
      __builtin_amdgcn_s_sleep(16);
    }
    const float mo = __uint_as_float(rec[0]), lo = __uint_as_float(rec[2]);
    const float mh = fmaxf(mg, mo), wt = __builtin_amdgcn_exp2f(mg - mh);
    ctrlf[49] = g * wt / (wt * l + __builtin_amdgcn_exp2f(mo - mh) * lo);
    ctrl[50] = (int)(tlo & YMASK);
    lds_arrive(L, C_CF, lane);
    GAS unsigned* op = (GAS unsigned*)(out + b * D + 8 * pj + 4 * (lane & 1) + 2 * (lane >> 5));
    int off = WS_Y + (b * 64 + (lane >> 1)) * 2048 + pj * 32 + (lane & 1) * 16;
    asm volatile("" : "+v"(op), "+v"(off));
    STAMP(4);
    BARRIER();
    if (FDEL) PAUSE(FDEL);
    STAMP(9);
    u32x4 ya, yb;
#pragma unroll 1
    for (int spin = 0; spin < SPIN_CAP; ++spin) {
      CFENCE();
      ya = __builtin_amdgcn_raw_buffer_load_b128(wsr, off, 0, 16);
      yb = __builtin_amdgcn_raw_buffer_load_b128(wsr, off, 32 * 2048, 16);
      const unsigned bad = (((ya[0] ^ tlo) | (ya[1] ^ tlo)) | ((ya[2] ^ tlo) | (ya[3] ^ tlo)) | ((yb[0] ^ tlo) | (yb[1] ^ tlo)) | ((yb[2] ^ tlo) | (yb[3] ^ tlo))) & YMASK;
      if (__builtin_amdgcn_ballot_w64(bad != 0u) == 0ull) break;
      if (PSLP) __builtin_amdgcn_s_sleep(PSLP);
    }
    float o[4];
#pragma unroll
    for (int k = 0; k < 4; ++k) o[k] = __uint_as_float(ya[k] & ~YMASK) + __uint_as_float(yb[k] & ~YMASK);
    float e0 = swadd32(o[0], o[2]), e1 = swadd32(o[1], o[3]);
    e0 = swadd16(e0, e0); e1 = swadd16(e1, e1);
    e0 += dpp<ROR8>(e0); e1 += dpp<ROR8>(e1); e0 += dpp<ROR4>(e0); e1 += dpp<ROR4>(e1); e0 += dpp<ROR2>(e0); e1 += dpp<ROR2>(e1);
    if ((lane & 30) == 0) OUT_STORE(op, pack_bf16(e0, e1));
    if (pj == 0 && lane < 8) __builtin_amdgcn_raw_buffer_store_b128(u32x4{tlo, thi, tlo, thi}, wsr, WS_EPOCH + b * 256 + lane * 16, 0, 16);
    STAMP(8);
    STAMP_FLUSH();
    return;
  }

  const int cw = wave - 1, hw = wave - 1 - NCW;
  int landed = 0;
  auto wait_unit = [&](int u) __attribute__((always_inline)) {
#pragma unroll 1
    for (int spin = 0; landed <= u && spin < SPIN_CAP; ++spin) {
      landed = __builtin_amdgcn_readfirstlane(ctrl[0]);
      if (landed > u) break;
      if (USL) __builtin_amdgcn_s_sleep(USL);
    }
    CFENCE();
  };
  auto take = [&](int u, int next, u32x4& a0, u32x4& a1, u32x4& a2, u32x4& a3) __attribute__((always_inline)) {
    wait_unit(u);
    const LAS u32x4* sp = (const LAS u32x4*)(L + (u & (NSLOT - 1)) * UNIT + lane * 16);
    a0 = sp[0]; a1 = sp[64]; a2 = sp[128]; a3 = sp[192];
    LDS_WAIT();
    ctrl[16 + cw] = next;
    asm volatile("" : "+v"(a0), "+v"(a1), "+v"(a2), "+v"(a3) :: "memory");
  };
  u32x4 q;
  auto build_q = [&]() __attribute__((always_inline)) {
    lds_await(ctrl, C_Q, NHW);
    const LAS f32x4* qp = (const LAS f32x4*)(L + L_QP + (2 * r4 * DH + 8 * c16) * 4);
    const f32x4 lo = qp[0] + qp[DH / 4], hi = qp[1] + qp[DH / 4 + 1];
    q[0] = pack_bf16(xrow_sum(lo[0]), xrow_sum(lo[1])); q[1] = pack_bf16(xrow_sum(lo[2]), xrow_sum(lo[3]));
    q[2] = pack_bf16(xrow_sum(hi[0]), xrow_sum(hi[1])); q[3] = pack_bf16(xrow_sum(hi[2]), xrow_sum(hi[3]));
  };
  constexpr float SCALE = 0.088388347648318440f, LOG2E = 1.4426950408889634f, C2 = SCALE * LOG2E;
  float mx = -INFINITY;
  auto score = [&](int u, const u32x4& k0, const u32x4& k1, const u32x4& k2, const u32x4& k3) __attribute__((always_inline)) {
    float d0 = 0.f, d1 = 0.f, d2 = 0.f, d3 = 0.f;
#pragma unroll
    for (int k = 0; k < 4; ++k) { d0 = dot2(k0[k], q[k], d0); d1 = dot2(k1[k], q[k], d1); d2 = dot2(k2[k], q[k], d2); d3 = dot2(k3[k], q[k], d3); }
    const unsigned s01 = pack_bf16(row16_sum(d0), row16_sum(d1)), s23 = pack_bf16(row16_sum(d2), row16_sum(d3));
    const unsigned t01 = pack_bf16(bflo(s01) * SCALE, bfhi(s01) * SCALE), t23 = pack_bf16(bflo(s23) * SCALE, bfhi(s23) * SCALE);
    const f32x4 sc = {bflo(t01) * LOG2E, bfhi(t01) * LOG2E, bflo(t23) * LOG2E, bfhi(t23) * LOG2E};
    mx = fmaxf(fmaxf(mx, fmaxf(sc[0], sc[1])), fmaxf(sc[2], sc[3]));
    if (c16 == 0) *(LAS f32x4*)(L + L_SC + u * 64 + r4 * 16) = sc;
  };

  if (hw < 0) {
    {
      u32x4 hk[4 * NHC];
#pragma unroll
      for (int i = 0; i < NHC; ++i) take(cw + NCW * i, i + 1 < NHC ? cw + NCW * (i + 1) : NHELD + cw, hk[4 * i], hk[4 * i + 1], hk[4 * i + 2], hk[4 * i + 3]);
      STAMP(10);
      build_q();
      STAMP(3);
#pragma unroll
      for (int i = 0; i < NHC; ++i) { score(cw + NCW * i, hk[4 * i], hk[4 * i + 1], hk[4 * i + 2], hk[4 * i + 3]); __builtin_amdgcn_sched_barrier(0); }
    }
#pragma unroll 1
    for (int u = NHELD + cw; u < NKU; u += NCW) {
      u32x4 k0, k1, k2, k3;
      take(u, u + NCW, k0, k1, k2, k3);
      score(u, k0, k1, k2, k3);
    }
    ctrlf[32 + cw] = xrow_max(mx);
    lds_arrive(L, C_MAX, lane);
    STAMP(11);
    lds_await(ctrl, C_MAX, NCW + NHW);
    const float mg = wg_max();
    STAMP(12);
    float acc[8] = {0.f, 0.f, 0.f, 0.f, 0.f, 0.f, 0.f, 0.f};
    LAS unsigned* rp = (LAS unsigned*)(L + L_REC + cw * 272 + (8 * c16 + 2 * r4) * 2);
    asm volatile("" : "+v"(rp));
    float cf = 0.f;
#pragma unroll 1
    for (int u = NKU + cw; u < NUNIT; u += NCW) {
      const f32x4 sc = *(const LAS f32x4*)(L + L_SC + (u - NKU) * 64 + r4 * 16);
      float p[4];
#pragma unroll
      for (int t = 0; t < 4; ++t) { p[t] = __builtin_amdgcn_exp2f(sc[t] - mg); asm volatile("" : "+v"(p[t])); }
      if ((u & ~(NCW - 1)) == NUNIT - 32) { lds_await(ctrl, C_CF, 1); cf = ctrlf[49]; }
      u32x4 v[4];
      take(u, u + NCW, v[0], v[1], v[2], v[3]);
#pragma unroll
      for (int t = 0; t < 4; ++t)
#pragma unroll
        for (int k = 0; k < 4; ++k) { acc[2 * k] = fmaf(p[t], bflo(v[t][k]), acc[2 * k]); acc[2 * k + 1] = fmaf(p[t], bfhi(v[t][k]), acc[2 * k + 1]); }
    }
    STAMP(5);
    const float e0 = swadd16(swadd32(acc[0], acc[4]), swadd32(acc[2], acc[6])), e1 = swadd16(swadd32(acc[1], acc[5]), swadd32(acc[3], acc[7]));
    *rp = pack_bf16(e0 * cf, e1 * cf);
    BARRIER();
    return;
  }

  u32x4 wb[16];
  const unsigned tstart = (unsigned)__builtin_amdgcn_readfirstlane(ctrl[8]);
  {
    const __amdgpu_buffer_rsrc_t rq = mkrsrc(wq, D * H * DH * 2);
    const int vo = ((64 * hw + 16 * r4) * (H * DH) + h * DH + 8 * c16) * 2;
#pragma unroll
    for (int i = 0; i < 16; ++i) { if (i == 0) wait_until(tstart + TQ + hw * QA); else PAUSE(QS); wb[i] = ldw(rq, vo, i * (H * DH * 2)); }
    lds_await(ctrl, C_X, 1);
    const u32x4 xa = *(const LAS u32x4*)(L + L_X + (64 * hw + 16 * r4) * 2), xc = *(const LAS u32x4*)(L + L_X + (64 * hw + 16 * r4 + 8) * 2);
    float a[8] = {0.f, 0.f, 0.f, 0.f, 0.f, 0.f, 0.f, 0.f};
#pragma unroll
    for (int i = 0; i < 8; ++i) {
      const unsigned xp = i < 4 ? xa[i] : xc[i - 4];
#pragma unroll
      for (int k = 0; k < 4; ++k) {
        a[2 * k] = dot2(perm_lo(wb[2 * i][k], wb[2 * i + 1][k]), xp, a[2 * k]);
        a[2 * k + 1] = dot2(perm_hi(wb[2 * i][k], wb[2 * i + 1][k]), xp, a[2 * k + 1]);
      }
    }
    const float e0 = swadd16(swadd32(a[0], a[4]), swadd32(a[2], a[6])), e1 = swadd16(swadd32(a[1], a[5]), swadd32(a[3], a[7]));
    *(LAS f32x2*)(L + L_QP + (hw * DH + 8 * c16 + 2 * r4) * 4) = f32x2{e0, e1};
    lds_arrive(L, C_Q, lane);
    STAMP(2);
    const int hb = NHC * NCW + hw;
#pragma unroll
    for (int i = 0; i < NHH; ++i) take(hb + NHW * i, i + 1 < NHH ? hb + NHW * (i + 1) : NEVER, wb[4 * i], wb[4 * i + 1], wb[4 * i + 2], wb[4 * i + 3]);
    STAMP(10);
    build_q();
#pragma unroll
    for (int i = 0; i < NHH; ++i) { score(hb + NHW * i, wb[4 * i], wb[4 * i + 1], wb[4 * i + 2], wb[4 * i + 3]); __builtin_amdgcn_sched_barrier(0); }
    ctrlf[32 + cw] = xrow_max(mx);
    lds_arrive(L, C_MAX, lane);
  }
  f32x4 y[4] = {{0.f, 0.f, 0.f, 0.f}, {0.f, 0.f, 0.f, 0.f}, {0.f, 0.f, 0.f, 0.f}, {0.f, 0.f, 0.f, 0.f}};
  if ((pj & 7) != 7) {
    const int sl = 7 * (pj >> 3) + (pj & 7);
    const __amdgpu_buffer_rsrc_t r1 = mkrsrc(w1, D * F * 2), r3 = mkrsrc(w3, D * F * 2), r2 = mkrsrc(w2, F * D * 2);
    const int vo = ((64 * hw + (lane & 60)) * F + FS * sl + 8 * (lane & 3)) * 2;
#pragma unroll
    for (int i = 0; i < 8; ++i) { if (i == 0) wait_until(tstart + T1 + hw * A1); else PAUSE(S1); wb[i] = ldw(i < 4 ? r1 : r3, vo, (i & 3) * (F * 2)); }
    const u32x2 xm = *(const LAS u32x2*)(L + L_X + (64 * hw + (lane & 60)) * 2);
    float a[16];
#pragma unroll
    for (int m = 0; m < 2; ++m)
#pragma unroll
      for (int k = 0; k < 4; ++k) {
        a[8 * m + 2 * k] = dot2(perm_lo(wb[4 * m + 2][k], wb[4 * m + 3][k]), xm[1], dot2(perm_lo(wb[4 * m][k], wb[4 * m + 1][k]), xm[0], 0.f));
        a[8 * m + 2 * k + 1] = dot2(perm_hi(wb[4 * m + 2][k], wb[4 * m + 3][k]), xm[1], dot2(perm_hi(wb[4 * m][k], wb[4 * m + 1][k]), xm[0], 0.f));
      }
    f32x4 e;
#pragma unroll
    for (int k = 0; k < 4; ++k) {
      float t = swadd16(swadd32(a[k], a[8 + k]), swadd32(a[4 + k], a[12 + k]));
      t += dpp<ROR8>(t); e[k] = t + dpp<ROR4>(t);
    }
    if ((lane & 12) == 0) *(LAS f32x4*)(L + L_MP + (hw * 64 + (lane & 32) + 8 * (lane & 3) + 4 * (r4 & 1)) * 4) = e;
    lds_arrive(L, C_MLP, lane);
    u32x2 wr[8];
#pragma unroll
    for (int i = 0; i < 8; ++i) { if (i == 0) wait_until(tstart + T2 + hw * A2); else PAUSE(S2); wr[i] = ldw2(r2, ((FS * sl + 8 * r4) * D + 64 * hw + 4 * c16) * 2, i * (D * 2)); }
    lds_await(ctrl, C_MLP, NHW);
    const LAS float* mp = (const LAS float*)(L + L_MP) + (lane & 31);
    float ga = 0.f, up = 0.f;
#pragma unroll
    for (int w = 0; w < NHW; ++w) { ga += mp[w * 64]; up += mp[w * 64 + 32]; }
    const float hv = ga / (1.f + __expf(-ga)) * up * (1.f - ctrlf[48]);
    const unsigned hh = pack_bf16(hv, 0.f), hl = pack_bf16(hv - bflo(hh), 0.f);
    *(LAS u16*)(L + L_H + hw * 128 + lane * 2) = (u16)(lane < 32 ? hh : hl);
    LDS_WAIT();
    u32x4 ha = *(const LAS u32x4*)(L + L_H + hw * 128 + (c16 & 1) * 64 + r4 * 16);
    if (c16 >= 2) ha = u32x4{0u, 0u, 0u, 0u};
#pragma unroll
    for (int e = 0; e < 4; ++e) {
      u32x4 f;
#pragma unroll
      for (int k = 0; k < 4; ++k) f[k] = (e & 1) ? perm_hi(wr[2 * k][e >> 1], wr[2 * k + 1][e >> 1]) : perm_lo(wr[2 * k][e >> 1], wr[2 * k + 1][e >> 1]);
      y[e] = mfma(ha, f, y[e]);
    }
  }
#pragma unroll
  for (int e = 0; e < 4; ++e) asm volatile("" : "+v"(y[e]));
  {
    const __amdgpu_buffer_rsrc_t ro = mkrsrc(wo, H * DH * D * 2);
#pragma unroll
    for (int t = 0; t < 4; ++t) {
      u32x2 wr[8];
#pragma unroll
      for (int i = 0; i < 8; ++i) { if (t + i == 0) wait_until(tstart + TO + hw * AO); else PAUSE(SO); wr[i] = ldw2(ro, ((h * DH + 32 * r4) * D + 64 * hw + 4 * c16) * 2, (8 * t + i) * (D * 2)); }
#pragma unroll
      for (int e = 0; e < 4; ++e) {
#pragma unroll
        for (int k = 0; k < 4; ++k) wb[4 * t + e][k] = (e & 1) ? perm_hi(wr[2 * k][e >> 1], wr[2 * k + 1][e >> 1]) : perm_lo(wr[2 * k][e >> 1], wr[2 * k + 1][e >> 1]);
        asm volatile("" : "+v"(wb[4 * t + e]));
      }
    }
  }
  lds_await(ctrl, C_CF, 1);
  unsigned yt = (unsigned)ctrl[50];
  int yo = WS_Y + (b * 64 + pj) * 2048 + hw * 256 + c16 * 16;
  const LAS u32x4* rec = (const LAS u32x4*)(L + L_REC + (lane & 3) * 272 + r4 * 64);
  unsigned ym = ~YMASK;
  asm volatile("" : "+v"(yt), "+v"(yo), "+v"(rec), "+s"(ym));
  STAMP(4);
  BARRIER();
  STAMP(9);
  {
    const u32x4 a[4] = {rec[0], rec[1], rec[2], rec[3]};
#pragma unroll
    for (int t = 0; t < 4; ++t)
#pragma unroll
      for (int e = 0; e < 4; ++e) y[e] = mfma(a[t], wb[4 * t + e], y[e]);
    u32x4 v;
#pragma unroll
    for (int e = 0; e < 4; ++e) {
      const f32x2 t = y[e].xy + y[e].zw;
      unsigned r;
      asm("v_add_f32 %0, %1, %2\n\tv_add_u32 %0, %5, %0\n\tv_and_or_b32 %0, %0, %3, %4" : "=&v"(r) : "v"(t[0]), "v"(t[1]), "s"(ym), "v"(yt), "i"((YMASK + 1u) / 2u));
      v[e] = r;
    }
    if (lane < 16) __builtin_amdgcn_raw_buffer_store_b128(v, wsr, yo, 0, 16);
  }
  STAMP(7);
}

extern "C" void kernel_launch(void* const* d_in, const int* in_sizes, int n_in, void* d_out, int out_size, void* d_ws, size_t ws_size,
                              hipStream_t stream) {
  gated_mix_kernel<<<256, NTHR, 0, stream>>>((const u16*)d_in[0], (const u16*)d_in[1], (const u16*)d_in[2], (const u16*)d_in[3], (const u16*)d_in[4],
                                             (const u16*)d_in[6], (const u16*)d_in[7], (const u16*)d_in[8], (const u16*)d_in[9], (u16*)d_out,
                                             (unsigned char*)d_ws);
}
```

```cpp
#include <hip/hip_runtime.h>
#include <hip/hip_bf16.h>

#define GAS __attribute__((address_space(1)))
#define LAS __attribute__((address_space(3)))
typedef unsigned short u16;
typedef unsigned long long u64;
using u32x4 = __attribute__((ext_vector_type(4))) unsigned;
using u32x2 = __attribute__((ext_vector_type(2))) unsigned;
using f32x4 = __attribute__((ext_vector_type(4))) float;
using f32x2 = __attribute__((ext_vector_type(2))) float;
using bf16x2 = __attribute__((ext_vector_type(2))) __bf16;
using bf16x8 = __attribute__((ext_vector_type(8))) __bf16;

constexpr int B = 4, D = 512, H = 32, SK = 8192, DH = 128, F = 1792;
constexpr int NCW = 4, NHW = 8, XWAVE = 1 + NCW + NHW, NWAVE = XWAVE + 2, NTHR = NWAVE * 64;
#define QA 80
#define QB 5
#define T1 1200
#define A1 150
#define B1 18
#define T2 2500
#define A2 40
#define B2 4
#define TO 2900
#define AO 350
#define BO 8
#define TS 70.6
#define REF0 180
#define PLAG 1
#define NOPST 0
#define NOPLD 0
#define PAUX 16
#define RAUX 16
#define PFW 0
#define TQ 130
#define NINIT 6
#define CODE_KIB 21
#define FDEL 0
#define CODEPF 1
#define YST 16
#define YLD 16
#define RED16 0
#define MASKP 0
#define PSLP 0
#define OUTSC 0
#define OUT_STORE(p, v) *(GAS unsigned*)(p) = v
#define CLR0 1
#define MLPFULL 0
#define MLPPF 0
#define TP 1200
#define AP 12
#define GRID 0
#define B2BD 0
#define B2BAUX 16
#define WOB2B 0
#define TOUCHW 1
#define W0 30
#define BARPOS 12
#define NO_WO 0
#define TLBPF 0
#define TMODE 0
#define SPW 6
#define QS 2
#define S1 6
#define S2 1
#define SO 2
#define USL 0
#define JUMP 100
#define DECAY 6
#define HOLD0 10000
#define UEND 420
#define WGAIN2 18
#define WMID2 WMID
#define FOFF 150
#define FMIN 45
#define FMAX 150
#define NRED 64
#define SLOTSH 8
#define WMID 21.0
#define WGAIN 8
#define WMIN 14
#define WMAX 27
#define CLRAUX 16
#define PCONT 1
#define XDEL 20
#define XSPR 0
#define XNT 0
#define MIMIC 1
#define NCLR 2
#define NOFLAG 0
#define CSPR 0
constexpr int UNIT = 4096;
constexpr int NSLOT = 32, RING = NSLOT * UNIT;
constexpr int NKU = (SK / 2) / 16, NUNIT = 2 * NKU;
constexpr int NHC = 6, NHH = 4, NHELD = NHC * NCW + NHH * NHW;
constexpr int FS = 32;
constexpr int L_CTRL = RING;
constexpr int L_X = L_CTRL + 256;
constexpr int L_QP = L_X + D * 2;
constexpr int L_MP = L_QP + NHW * DH * 4;
constexpr int L_REC = L_MP + NHW * 64 * 4;
constexpr int L_H = L_REC + NCW * 272;
constexpr int L_SC = L_H + NHW * 128;
constexpr int LDS_BYTES = L_SC + NKU * 64;
static_assert(LDS_BYTES <= 159 * 1024, "lds");
static_assert(WMIN <= (int)WMID && (int)WMID <= WMAX && WMAX <= 28, "window");
constexpr int WS_EPOCH = 0;
constexpr int WS_REC = 4096;
constexpr int WS_PACE = WS_REC + 256 * 128;
constexpr int WS_Y = WS_PACE + 64 * 2048;
constexpr int WS_BYTES = WS_Y + B * 64 * 2048;
constexpr u64 TAG_STEP = 0x9E3779B97F4A7C15ull;
constexpr unsigned YMASK = 0xFFFu;
constexpr int SPIN_CAP = 1 << 18;
constexpr int NEVER = 1 << 20;
constexpr int C_Q = 4, C_MAX = 5, C_MLP = 6, C_X = 7, C_CF = 9;

__device__ __forceinline__ float bflo(unsigned u) { return __builtin_bit_cast(float, u << 16); }
__device__ __forceinline__ float bfhi(unsigned u) { return __builtin_bit_cast(float, u & 0xffff0000u); }
__device__ __forceinline__ float dot2(unsigned a, unsigned b, float acc) {
  return __builtin_amdgcn_fdot2_f32_bf16(__builtin_bit_cast(bf16x2, a), __builtin_bit_cast(bf16x2, b), acc, false);
}
__device__ __forceinline__ unsigned pack_bf16(float lo, float hi) {
  const bf16x2 v = {(__bf16)lo, (__bf16)hi};
  return __builtin_bit_cast(unsigned, v);
}
__device__ __forceinline__ unsigned perm_lo(unsigned a, unsigned b) { return __builtin_amdgcn_perm(b, a, 0x05040100u); }
__device__ __forceinline__ unsigned perm_hi(unsigned a, unsigned b) { return __builtin_amdgcn_perm(b, a, 0x07060302u); }
template <int CTRL> __device__ __forceinline__ float dpp(float x) {
  return __builtin_bit_cast(float, __builtin_amdgcn_mov_dpp(__builtin_bit_cast(int, x), CTRL, 0xf, 0xf, true));
}
constexpr int XOR1 = 0xB1, ROR1 = 0x121, ROR2 = 0x122, ROR4 = 0x124, ROR8 = 0x128;
__device__ __forceinline__ float row16_sum(float x) { x += dpp<ROR8>(x); x += dpp<ROR4>(x); x += dpp<ROR2>(x); x += dpp<ROR1>(x); return x; }
__device__ __forceinline__ float swadd32(float a, float b) {
  auto t = __builtin_amdgcn_permlane32_swap(__float_as_uint(a), __float_as_uint(b), false, false);
  return __uint_as_float(t[0]) + __uint_as_float(t[1]);
}
__device__ __forceinline__ float swadd16(float a, float b) {
  auto t = __builtin_amdgcn_permlane16_swap(__float_as_uint(a), __float_as_uint(b), false, false);
  return __uint_as_float(t[0]) + __uint_as_float(t[1]);
}
__device__ __forceinline__ float xrow_sum(float x) { x = swadd16(x, x); return swadd32(x, x); }
__device__ __forceinline__ float xrow_max(float x) {
  auto s = __builtin_amdgcn_permlane16_swap(__float_as_uint(x), __float_as_uint(x), false, false);
  x = fmaxf(__uint_as_float(s[0]), __uint_as_float(s[1]));
  auto t = __builtin_amdgcn_permlane32_swap(__float_as_uint(x), __float_as_uint(x), false, false);
  return fmaxf(__uint_as_float(t[0]), __uint_as_float(t[1]));
}
__device__ __forceinline__ __amdgpu_buffer_rsrc_t mkrsrc(const void* p, int bytes) { return __builtin_amdgcn_make_buffer_rsrc((void*)p, 0, bytes, 0x00020000); }
__device__ __forceinline__ u32x4 ldw(__amdgpu_buffer_rsrc_t r, int voff, int soff) { return __builtin_amdgcn_raw_buffer_load_b128(r, voff, soff, 2); }
#define LDS_WAIT() asm volatile("s_waitcnt lgkmcnt(0)" ::: "memory")
#define VM_DRAIN() asm volatile("s_waitcnt vmcnt(0)" ::: "memory")
#define CFENCE() asm volatile("" ::: "memory")
#define BARRIER() asm volatile("s_waitcnt lgkmcnt(0)\n\ts_barrier" ::: "memory")
#define PAUSE(n) do { __builtin_amdgcn_sched_barrier(0); asm volatile("s_sleep %0" :: "i"(n) : "memory"); __builtin_amdgcn_sched_barrier(0); } while (0)

__device__ __forceinline__ void lds_arrive(LAS unsigned char* L, int idx, int lane) {
  LDS_WAIT();
  if (lane == 0) __hip_atomic_fetch_add((LAS int*)(L + L_CTRL) + idx, 1, __ATOMIC_RELAXED, __HIP_MEMORY_SCOPE_WORKGROUP);
}
__device__ __forceinline__ void lds_await(volatile LAS int* ctrl, int idx, int n) {
#pragma unroll 1
  for (int spin = 0; spin < SPIN_CAP; ++spin) { if (__builtin_amdgcn_readfirstlane(ctrl[idx]) >= n) break; __builtin_amdgcn_s_sleep(1); }
  CFENCE();
}

__device__ __forceinline__ void wait_until(unsigned t) {
  __builtin_amdgcn_sched_barrier(0);
#pragma unroll 1
  for (int spin = 0; spin < 4096; ++spin) {
    const int d = (int)(t - (unsigned)__builtin_amdgcn_s_memrealtime());
    if (d <= 0) break;
    if (d > 24) __builtin_amdgcn_s_sleep(6);
  }
  CFENCE();
  __builtin_amdgcn_sched_barrier(0);
}
#define STR2(x) #x
#define STR(x) STR2(x)
constexpr int TAU2 = 2 * (int)(TS * 100.0 * 256.0 / NUNIT);
__device__ __forceinline__ f32x4 mfma(const u32x4& a, const u32x4& b, const f32x4& c) {
  return __builtin_amdgcn_mfma_f32_16x16x32_bf16(__builtin_bit_cast(bf16x8, a), __builtin_bit_cast(bf16x8, b), c, 0, 0, 0);
}
__device__ __forceinline__ u32x2 ldw2(__amdgpu_buffer_rsrc_t r, int voff, int soff) { return __builtin_amdgcn_raw_buffer_load_b64(r, voff, soff, 2); }
template <int CTRL> __device__ __forceinline__ int dppi(int x) { return __builtin_amdgcn_mov_dpp(x, CTRL, 0xf, 0xf, true); }

#define STAMP(i) do {} while (0)
#define STAMP_FLUSH() do {} while (0)
#define STAMP_LDS 0

__global__ void __launch_bounds__(NTHR) gated_mix_kernel(const u16* x, const u16* kc, const u16* vc, const u16* wq, const u16* wo, const u16* w1,
                                                         const u16* w3, const u16* w2, const u16* gw, u16* out, unsigned char* ws) {
  __shared__ __attribute__((aligned(16))) unsigned char lds_raw[LDS_BYTES + STAMP_LDS];
  LAS unsigned char* L = (LAS unsigned char*)lds_raw;
  volatile LAS int* ctrl = (volatile LAS int*)(L + L_CTRL);
  volatile LAS float* ctrlf = (volatile LAS float*)(L + L_CTRL);
#define DBG(x) ""
  {
    unsigned va, vb, vo, pa, nxa, t0, t1, t2, t3;
#define CODELD "global_load_lds_dwordx4 %[t0], s[56:57]\n\t"
#define ASTAMP(i) DBG("s_memrealtime s[60:61]\n\ts_waitcnt lgkmcnt(0)\n\tv_mov_b32 %[t1], %c[dbg] + 4 * " #i "\n\tv_mov_b32 %[t2], s60\n\tds_write_b32 %[t1], %[t2]\n\t")
#define WAITW "s_waitcnt (wcnt & 15) | ((wcnt >> 4) << 14) | 0x0F70\n\t"
#define SP(v, sb, wait) wait "global_load_lds_dword " v ", " sb " nt\n\tglobal_load_lds_dword " v ", " sb " sc1\n\t"
#define SW "s_waitcnt vmcnt(%c[spw])\n\t"
#define UNIT4(v) \
        WAITW "global_load_lds_dwordx4 " v ", s[40:41] nt\n\t" \
        WAITW "global_load_lds_dwordx4 " v ", s[40:41] offset:1024 nt\n\t" \
        WAITW "global_load_lds_dwordx4 " v ", s[40:41] offset:2048 nt\n\t" \
        WAITW "global_load_lds_dwordx4 " v ", s[40:41] offset:3072 nt\n\t"
    asm volatile(
        "s_getpc_b64 s[56:57]\n\t"
        "s_load_dwordx8 s[44:51], %[ka], 0x18\n\t"
        "s_load_dwordx2 s[52:53], %[ka], 0x38\n\t"
        "s_load_dwordx4 s[40:43], %[ka], 0x8\n\t"
        "v_readfirstlane_b32 s54, %[tid]\n\t"
        "s_mov_b32 m0, %c[jk]\n\t"
        "s_lshr_b32 s59, %[bid], 3\n\t"
        "s_cmp_ge_u32 s54, 64\n\t"
        "s_cbranch_scc1 90f\n\t"
        "s_min_u32 s54, s59, %c[nch]\n\t"
        "s_lshl_b32 s54, s54, 10\n\t"
        "v_lshlrev_b32 %[vo], 4, %[tid]\n\t"
        "s_and_b32 s56, s56, -128\n\t"
        "v_add_u32 %[t0], s54, %[vo]\n\t"
        "s_and_b32 s58, %[bid], 7\n\t"
        CODELD
        "s_setprio 3\n\t"
        "v_lshlrev_b32 %[t1], 7, %[tid]\n\t"
        "s_lshl_b32 s54, s59, 7\n\t"
        "s_add_u32 s54, s54, s58\n\t"
        "s_lshl_b32 s54, s54, 10\n\t"
        "v_lshrrev_b32 %[va], 3, %[tid]\n\t"
        "v_mul_u32_u24 %[va], 8192 - 1024, %[va]\n\t"
        "v_add3_u32 %[va], %[va], %[t1], s54\n\t"
        "v_add_u32 %[t2], 0x10000, %[va]\n\t"
        "s_lshl_b32 s55, s58, 5\n\t"
        "s_add_u32 s55, s55, s59\n\t"
        "s_mul_i32 s54, s55, 7 * 1024\n\t"
        "v_min_u32 %[t3], 55 * 128, %[t1]\n\t"
        "v_add_u32 %[t3], s54, %[t3]\n\t"
        "s_mul_i32 s54, s58, 448\n\t"
        "s_and_b32 s54, s54, -128\n\t"
        "s_mul_i32 s57, s59, 16 * 3584\n\t"
        "s_add_u32 s54, s54, s57\n\t"
        "v_lshrrev_b32 %[t0], 2, %[tid]\n\t"
        "v_mul_u32_u24 %[t0], 3584 - 512, %[t0]\n\t"
        "v_add3_u32 %[t0], %[t0], %[t1], s54\n\t"
        "v_lshl_add_u32 %[vb], s55, 14, %[t1]\n\t"
        "v_add_u32 %[t1], 0x2000, %[vb]\n\t"
        "s_and_b32 s54, %[bid], 0xc0\n\t"
        "s_lshl3_add_u32 s54, s58, s54\n\t"
        "s_and_b32 s56, s59, 7\n\t"
        "s_add_u32 s54, s54, s56\n\t"
        "s_lshl_b32 s56, s54, 20\n\t"
        "s_waitcnt lgkmcnt(0)\n\t"
        SP("%[va]", "s[44:45]", "") SP("%[t2]", "s[44:45]", "") SP("%[t3]", "s[52:53]", "")
        ".if %c[spw] <= 4\n\ts_barrier\n\t.endif\n\t"
        SP("%[t0]", "s[48:49]", SW)
        ".if %c[spw] == 6\n\ts_barrier\n\t.endif\n\t"
        SP("%[t0]", "s[50:51]", SW)
        ".if %c[spw] == 8\n\ts_barrier\n\t.endif\n\t"
        SP("%[vb]", "s[46:47]", SW) SP("%[t1]", "s[46:47]", SW)
        ".if %c[spw] > 8\n\ts_barrier\n\t.endif\n\t"
        ASTAMP(6)
        "s_add_u32 s40, s40, s56\n\t"
        "s_addc_u32 s41, s41, 0\n\t"
        "s_add_u32 s42, s42, s56\n\t"
        "s_addc_u32 s43, s43, 0\n\t"
        "s_sub_u32 s42, s42, %c[nku] * 4096\n\t"
        "s_subb_u32 s43, s43, 0\n\t"
        ".set so, 0\n\t"
        ".set wcnt, %c[spw]\n\t"
        ".rept 4\n\t"
        "s_movk_i32 m0, so\n\t"
        ".if (so & 4096) == 0\n\t"
        WAITW
        ".set wcnt, wcnt + 6\n\t"
        ".else\n\t"
        "s_nop 0\n\t"
        ".endif\n\t"
        "global_load_lds_dwordx4 %[vo], s[40:41] nt\n\t"
        "global_load_lds_dwordx4 %[vo], s[40:41] offset:1024 nt\n\t"
        "global_load_lds_dwordx4 %[vo], s[40:41] offset:2048 nt\n\t"
        "global_load_lds_dwordx4 %[vo], s[40:41] offset:3072 nt\n\t"
        "v_add_u32 %[vo], 0x1000, %[vo]\n\t"
        ".set so, so + 4096\n\t"
        ".endr\n\t"
        ".if %c[spw] <= 4\n\t"
        "s_waitcnt vmcnt(16)\n\t"
        ".endif\n\t"
        ASTAMP(7)
        "s_getpc_b64 s[50:51]\n"
        "20:\n\t"
        "s_add_u32 s50, s50, 23f-20b-%c[wmin]*128\n\t"
        "s_addc_u32 s51, s51, -1\n\t"
        "v_mov_b32 %[pa], %c[ctl]\n\t"
        "v_and_b32 %[nxa], 0x3c0 >> 2, %[vo]\n\t"
        "v_lshrrev_b32 %[nxa], 2, %[nxa]\n\t"
        "v_add_u32 %[nxa], %c[ctl] + 64, %[nxa]\n\t"
        "s_memrealtime s[58:59]\n\t"
        "s_mov_b32 s62, 0\n\t"
        "s_mov_b32 s63, %c[gain]\n\t"
        "s_mov_b32 s64, %c[wmid]\n\t"
        "s_mov_b32 s44, 4\n\t"
        "s_mov_b32 s45, %c[wmid] >> 8\n\t"
        "s_mov_b32 s46, 0x4000\n\t"
        "s_mov_b32 s47, 2 * %c[tau2]\n\t"
        "s_mov_b32 s48, %c[nslot]\n\t"
        "s_mov_b32 s49, %c[nku]\n\t"
        "s_waitcnt lgkmcnt(0)\n\t"
        "s_mov_b32 s59, 10000\n"
        "22:\n\t"
        "s_memrealtime s[56:57]\n\t"
        "ds_read_b32 %[t3], %[pa] offset:8\n\t"
        "s_lshl_b32 s52, s45, 7\n\t"
        "s_add_u32 s52, s50, s52\n\t"
        "s_addc_u32 s53, s51, 0\n\t"
        "s_add_i32 s54, s45, -1\n\t"
        "s_lshr_b32 s54, s54, 2\n\t"
        "s_sub_i32 s54, s44, s54\n\t"
        "v_mov_b32 %[t1], s54\n\t"
        "v_add_u32 %[t0], 0x1000, %[vo]\n\t"
        "s_add_u32 s55, s46, 0x1000\n\t"
        "s_mov_b32 m0, s46\n\t"
        "s_setpc_b64 s[52:53]\n"
        "24:\n\t"
        "s_waitcnt lgkmcnt(0)\n\t"
        "v_readfirstlane_b32 s57, %[t3]\n\t"
        "v_add_u32 %[t1], 1, %[t1]\n\t"
        "s_lshr_b32 s54, s47, 8\n\t"
        "s_sub_u32 s56, s56, s54\n\t"
        "v_mov_b32 %[t2], s56\n\t"
        "s_sub_i32 s54, s56, s58\n\t"
        "s_mov_b32 s58, s56\n\t"
        "s_sub_i32 s56, s56, s57\n\t"
        "ds_write2_b32 %[pa], %[t1], %[t2] offset1:1\n\t"
        "s_add_i32 s59, s59, s54\n\t"
        "s_add_i32 s59, s59, %c[decay]\n\t"
        "s_cmp_gt_i32 s54, %c[jump]\n\t"
        "s_cselect_b32 s59, s62, s59\n\t"
        "s_min_i32 s62, s56, s59\n\t"
        "s_mul_i32 s56, s62, s63\n\t"
        "s_add_i32 s56, s56, s64\n\t"
        "s_ashr_i32 s56, s56, 8\n\t"
        "s_max_i32 s56, s56, %c[wmin]\n\t"
        "s_min_i32 s45, s56, %c[wmax]\n\t"
        "s_add_u32 s47, s47, %c[tau2]\n\t"
        "s_add_u32 s46, s46, 0x2000\n\t"
        "s_and_b32 s46, s46, %c[nslot] * 4096 - 1\n\t"
        "v_add_u32 %[vo], 0x2000, %[vo]\n\t"
        "s_add_i32 s44, s44, 2\n\t"
        "s_cmp_lt_i32 s44, s48\n\t"
        "s_cbranch_scc1 22b\n\t"
        DBG("s_add_i32 s54, s44, -1\n\ts_lshr_b32 s54, s54, 5\n\ts_lshl_b32 s54, s54, 2\n\ts_add_u32 s54, s54, %c[dbg] + 14 * 64\n\tv_mov_b32 %[t0], s54\n\tv_mov_b32 %[t1], s45\n\t"
            "ds_write2_b32 %[t0], %[t2], %[t1] offset1:16\n\t")
        "s_cmp_lt_i32 s44, s49\n\t"
        "s_cbranch_scc1 25f\n\t"
        "s_cmp_ge_i32 s44, 2 * %c[nku]\n\t"
        "s_cbranch_scc1 30f\n\t"
        "s_mov_b32 s49, 2 * %c[nku]\n\t"
        "s_mov_b64 s[40:41], s[42:43]\n"
        "25:\n\t"
        "s_cmp_ge_i32 s44, %c[uend]\n\t"
        "s_cselect_b32 s63, %c[gain2], s63\n\t"
        "s_cselect_b32 s64, %c[wmid2], s64\n\t"
        "s_mov_b32 s55, %c[cap]\n"
        "26:\n\t"
        "ds_read_b32 %[t0], %[nxa]\n\t"
        "s_waitcnt lgkmcnt(0)\n\t"
        "s_nop 1\n\t"
        "v_min_i32_dpp %[t0], %[t0], %[t0] row_ror:8 row_mask:0xf bank_mask:0xf\n\t"
        "s_nop 1\n\t"
        "v_min_i32_dpp %[t0], %[t0], %[t0] row_ror:4 row_mask:0xf bank_mask:0xf\n\t"
        "s_nop 1\n\t"
        "v_min_i32_dpp %[t0], %[t0], %[t0] row_ror:2 row_mask:0xf bank_mask:0xf\n\t"
        "s_nop 1\n\t"
        "v_min_i32_dpp %[t0], %[t0], %[t0] row_ror:1 row_mask:0xf bank_mask:0xf\n\t"
        "s_nop 1\n\t"
        "v_readfirstlane_b32 s48, %[t0]\n\t"
        "s_nop 1\n\t"
        "s_add_i32 s48, s48, %c[nslot]\n\t"
        "s_and_b32 s48, s48, -2\n\t"
        "s_min_i32 s48, s48, s49\n\t"
        "s_cmp_lt_i32 s44, s48\n\t"
        "s_cbranch_scc1 22b\n\t"
        "v_mov_b32 %[t1], s44\n\t"
        "s_waitcnt vmcnt(0)\n\t"
        "ds_write_b32 %[pa], %[t1]\n\t"
        "s_sleep 1\n\t"
        "s_sub_u32 s55, s55, 1\n\t"
        "s_cmp_lg_u32 s55, 0\n\t"
        "s_cbranch_scc1 26b\n\t"
        "s_branch 22b\n"
        "30:\n\t"
        ".set k, 6\n\t"
        ".rept 7\n\t"
        ".set wcnt, 4 * k\n\t"
        "v_mov_b32 %[t1], 2 * %c[nku] - k\n\t"
        WAITW
        "ds_write_b32 %[pa], %[t1]\n\t"
        ".set k, k - 1\n\t"
        ".endr\n\t"
        DBG("s_memrealtime s[56:57]\n\ts_waitcnt lgkmcnt(0)\n\tv_mov_b32 %[t0], %c[dbg] + 4\n\tv_mov_b32 %[t1], s56\n\tds_write_b32 %[t0], %[t1]\n\t")
        "s_waitcnt lgkmcnt(0)\n\t"
        "s_endpgm\n\t"
        ".p2align 7\n"
        "23:\n\t"
        ".set wcnt, %c[wmin]-1\n\t"
        ".rept %c[wmax] - %c[wmin] + 1\n\t"
        UNIT4("%[vo]")
        "ds_write_b32 %[pa], %[t1]\n\t"
        "s_mov_b32 m0, s55\n\t"
        UNIT4("%[t0]")
        "s_branch 24b\n\t"
        ".p2align 7\n\t"
        ".set wcnt, wcnt+1\n\t"
        ".endr\n"
        "90:\n\t"
        "s_waitcnt lgkmcnt(0)"
        : [va] "=&v"(va), [vb] "=&v"(vb), [vo] "=&v"(vo), [pa] "=&v"(pa), [nxa] "=&v"(nxa), [t0] "=&v"(t0), [t1] "=&v"(t1), [t2] "=&v"(t2), [t3] "=&v"(t3)
        : [tid] "v"(threadIdx.x), [bid] "s"(blockIdx.x), [ka] "s"(__builtin_amdgcn_kernarg_segment_ptr()), [nch] "i"(CODE_KIB - 1), [jk] "i"(RING - UNIT),
          [spw] "i"(SPW), [wmin] "i"(WMIN), [wmax] "i"(WMAX), [gain] "i"(WGAIN), [wmid] "i"((int)(WMID * 256)), [tau2] "i"(TAU2),
          [ctl] "i"(L_CTRL), [nslot] "i"(NSLOT), [nku] "i"(NKU), [cap] "i"(SPIN_CAP), [dbg] "i"(LDS_BYTES), [jump] "i"(JUMP), [decay] "i"(DECAY), [uend] "i"(UEND), [gain2] "i"(WGAIN2), [wmid2] "i"((int)(WMID2 * 256))
        : "memory", "scc", "s40", "s41", "s42", "s43", "s44", "s45", "s46", "s47", "s48", "s49", "s50", "s51", "s52", "s53", "s54", "s55", "s56", "s57", "s58", "s59", "s60", "s61", "s62", "s63", "s64");
  }
  __builtin_amdgcn_sched_barrier(0);
  const int tid = threadIdx.x, lane = tid & 63, wave = __builtin_amdgcn_readfirstlane(tid >> 6);
  const int xg = blockIdx.x & 7, kk = blockIdx.x >> 3;
  const int b = kk >> 3, pj = 8 * xg + (kk & 7), h = pj >> 1, s = pj & 1;
  const int r4 = lane >> 4, c16 = lane & 15;
  asm volatile("" : "+s"(x), "+s"(wq), "+s"(wo), "+s"(w1), "+s"(w3), "+s"(w2), "+s"(gw), "+s"(out), "+s"(ws));
  if (wave == 1) {
    const int wi = lane - 16;
    const int now = (int)(unsigned)__builtin_amdgcn_s_memrealtime() + REF0;
    ctrl[lane] = (lane == 1 || lane == 2) ? now : lane == 8 ? now - REF0 : (lane < 16 || lane >= 32) ? 0 : wi < NCW ? wi : wi < NCW + NHW ? NHC * NCW + wi - NCW : NEVER;
  }
  BARRIER();

  const __amdgpu_buffer_rsrc_t wsr = mkrsrc(ws, WS_BYTES);
  auto wg_max = [&]() __attribute__((always_inline)) {
    float mg = ctrlf[32 + (c16 < NCW + NHW ? c16 : 0)];
    mg = fmaxf(mg, dpp<ROR8>(mg)); mg = fmaxf(mg, dpp<ROR4>(mg)); mg = fmaxf(mg, dpp<ROR2>(mg)); mg = fmaxf(mg, dpp<ROR1>(mg));
    return mg;
  };

  if (wave == XWAVE + 1) {
    unsigned k = ((unsigned)__builtin_amdgcn_s_memrealtime() >> SLOTSH) + 1;
#pragma unroll 1
    for (int it = 0; it < 4096 >> (SLOTSH - 2); ++it, ++k) {
      wait_until(k << SLOTSH);
      if (__builtin_amdgcn_readfirstlane(ctrl[0]) >= NUNIT - 36) break;
      const int own = __builtin_amdgcn_readfirstlane(ctrl[1]);
      constexpr unsigned SM = (8192u >> SLOTSH) - 1u;
      if (!NOPST && blockIdx.x < 16 && lane < 8) __builtin_amdgcn_raw_buffer_store_b128(u32x4{(unsigned)own, (unsigned)own, (unsigned)own, (unsigned)own}, wsr, WS_PACE + (k & SM) * 2048 + blockIdx.x * 128 + lane * 16, 0, PAUX);
      if (it < PLAG || NOPLD) continue;
      int e[16];
      asm volatile("s_load_dword %0, %16, 0x0\n\ts_load_dword %1, %16, 0x80\n\ts_load_dword %2, %16, 0x100\n\ts_load_dword %3, %16, 0x180\n\t"
                   "s_load_dword %4, %16, 0x200\n\ts_load_dword %5, %16, 0x280\n\ts_load_dword %6, %16, 0x300\n\ts_load_dword %7, %16, 0x380\n\t"
                   "s_load_dword %8, %16, 0x400\n\ts_load_dword %9, %16, 0x480\n\ts_load_dword %10, %16, 0x500\n\ts_load_dword %11, %16, 0x580\n\t"
                   "s_load_dword %12, %16, 0x600\n\ts_load_dword %13, %16, 0x680\n\ts_load_dword %14, %16, 0x700\n\ts_load_dword %15, %16, 0x780\n\t"
                   "s_waitcnt lgkmcnt(0)"
                   : "=&s"(e[0]), "=&s"(e[1]), "=&s"(e[2]), "=&s"(e[3]), "=&s"(e[4]), "=&s"(e[5]), "=&s"(e[6]), "=&s"(e[7]),
                     "=&s"(e[8]), "=&s"(e[9]), "=&s"(e[10]), "=&s"(e[11]), "=&s"(e[12]), "=&s"(e[13]), "=&s"(e[14]), "=&s"(e[15])
                   : "s"(ws + WS_PACE + ((k - PLAG) & SM) * 2048) : "memory");
      int sum = 0, cnt = 0;
#pragma unroll
      for (int i = 0; i < 16; ++i) { const int d = e[i] - own; const bool ok = (unsigned)(d + 600) < 1200u; sum += ok ? d : 0; cnt += ok ? 1 : 0; }
      if (cnt >= 8) ctrl[2] = own + (int)((float)sum / (float)cnt);
    }
    return;
  }

  if (wave == XWAVE) {
#pragma unroll 1
    for (int i = 0; i < XDEL + XSPR * (kk & 31); ++i) PAUSE(4);
    const u32x4 xv = *(const GAS u32x4*)(x + b * D + 8 * lane), gv = *(const GAS u32x4*)(gw + 8 * lane);
    *(LAS u32x4*)(L + L_X + lane * 16) = xv;
    float gd = 0.f;
#pragma unroll
    for (int k = 0; k < 4; ++k) gd = dot2(xv[k], gv[k], gd);
    gd = xrow_sum(row16_sum(gd));
    const float g = 1.f / (1.f + __expf(-gd));
    ctrlf[48] = g;
    lds_arrive(L, C_X, lane);
    u64 tag;
    asm volatile("s_load_dwordx2 %0, %1, 0x0\n\ts_waitcnt lgkmcnt(0)" : "=s"(tag) : "s"(ws + WS_EPOCH + b * 256) : "memory");
    tag += TAG_STEP;
#pragma unroll 1
    for (int spin = 0; spin < SPIN_CAP; ++spin) { if (__builtin_amdgcn_readfirstlane(ctrl[C_MAX]) >= NCW + NHW) break; __builtin_amdgcn_s_sleep(16); }
    CFENCE();
    const float mg = wg_max();
    float l = 0.f;
#pragma unroll 4
    for (int i = 0; i < NKU * 64 / 1024; ++i) {
      const f32x4 sc = *(const LAS f32x4*)(L + L_SC + i * 1024 + lane * 16);
      l += (__builtin_amdgcn_exp2f(sc[0] - mg) + __builtin_amdgcn_exp2f(sc[1] - mg)) + (__builtin_amdgcn_exp2f(sc[2] - mg) + __builtin_amdgcn_exp2f(sc[3] - mg));
    }
    l = xrow_sum(row16_sum(l));
    const unsigned tlo = (unsigned)tag, thi = (unsigned)(tag >> 32);
    if (lane < 8) __builtin_amdgcn_raw_buffer_store_b128(u32x4{__float_as_uint(mg), tlo, __float_as_uint(l), thi}, wsr, WS_REC + (b * 64 + pj) * 128 + lane * 16, 0, 16);
#pragma unroll 1
    for (int i = 0; i < 12 + (kk & 31); ++i) PAUSE(12);
    u32x4 rec;
#pragma unroll 1
    for (int spin = 0; spin < SPIN_CAP; ++spin) {
      CFENCE();
      rec = __builtin_amdgcn_raw_buffer_load_b128(wsr, WS_REC + (b * 64 + (pj ^ 1)) * 128, 0, 16);
      if (__builtin_amdgcn_ballot_w64(rec[1] != tlo || rec[3] != thi) == 0ull) break;
      __builtin_amdgcn_s_sleep(16);
    }
    const float mo = __uint_as_float(rec[0]), lo = __uint_as_float(rec[2]);
    const float mh = fmaxf(mg, mo), wt = __builtin_amdgcn_exp2f(mg - mh);
    ctrlf[49] = g * wt / (wt * l + __builtin_amdgcn_exp2f(mo - mh) * lo);
    ctrl[50] = (int)(tlo & YMASK);
    lds_arrive(L, C_CF, lane);
    GAS unsigned* op = (GAS unsigned*)(out + b * D + 8 * pj + 4 * (lane & 1) + 2 * (lane >> 5));
    int off = WS_Y + (b * 64 + (lane >> 1)) * 2048 + pj * 32 + (lane & 1) * 16;
    asm volatile("" : "+v"(op), "+v"(off));
    if ((pj & (64 / NRED - 1)) != 0) { STAMP_FLUSH(); return; }
    STAMP(4);
    BARRIER();
    {
      const unsigned now = (unsigned)__builtin_amdgcn_s_memrealtime();
      const int d = __builtin_amdgcn_readfirstlane(ctrl[2]) + (int)(TS * 100) + FOFF - (int)now;
      const unsigned t = now + (unsigned)min(max(d, FMIN), FMAX);
#pragma unroll 1
      for (int spin = 0; spin < 4096 && (int)(t - (unsigned)__builtin_amdgcn_s_memrealtime()) > 0; ++spin) {}
    }
    STAMP(9);
    constexpr int NL = 128 / NRED;
    u32x4 yv[NL];
#pragma unroll 1
    for (int spin = 0; spin < SPIN_CAP; ++spin) {
      CFENCE();
      unsigned bad = 0;
#pragma unroll
      for (int i = 0; i < NL; ++i) yv[i] = __builtin_amdgcn_raw_buffer_load_b128(wsr, off, i * (64 / NL) * 2048, 16);
#pragma unroll
      for (int i = 0; i < NL; ++i) bad |= ((yv[i][0] ^ tlo) | (yv[i][1] ^ tlo)) | ((yv[i][2] ^ tlo) | (yv[i][3] ^ tlo));
      bad &= YMASK;
      if (__builtin_amdgcn_ballot_w64(bad != 0u) == 0ull) break;
      if (PSLP) __builtin_amdgcn_s_sleep(PSLP);
    }
    float o[4];
#pragma unroll
    for (int k = 0; k < 4; ++k) {
      float t[NL];
#pragma unroll
      for (int i = 0; i < NL; ++i) t[i] = __uint_as_float(yv[i][k] & ~YMASK);
#pragma unroll
      for (int st = NL / 2; st >= 1; st /= 2)
#pragma unroll
        for (int i = 0; i < st; ++i) t[i] += t[i + st];
      o[k] = t[0];
    }
    float e0 = swadd32(o[0], o[2]), e1 = swadd32(o[1], o[3]);
    e0 = swadd16(e0, e0); e1 = swadd16(e1, e1);
    e0 += dpp<ROR8>(e0); e1 += dpp<ROR8>(e1); e0 += dpp<ROR4>(e0); e1 += dpp<ROR4>(e1); e0 += dpp<ROR2>(e0); e1 += dpp<ROR2>(e1);
    if ((lane & 30) == 0) OUT_STORE(op, pack_bf16(e0, e1));
    if (pj == 0 && lane < 8) __builtin_amdgcn_raw_buffer_store_b128(u32x4{tlo, thi, tlo, thi}, wsr, WS_EPOCH + b * 256 + lane * 16, 0, 16);
    STAMP(8);
    STAMP_FLUSH();
    return;
  }

  const int cw = wave - 1, hw = wave - 1 - NCW;
  int landed = 0;
  auto wait_unit = [&](int u) __attribute__((always_inline)) {
#pragma unroll 1
    for (int spin = 0; landed <= u && spin < SPIN_CAP; ++spin) {
      landed = __builtin_amdgcn_readfirstlane(ctrl[0]);
      if (landed > u) break;
      if (USL) __builtin_amdgcn_s_sleep(USL);
    }
    CFENCE();
  };
  auto take = [&](int u, int next, u32x4& a0, u32x4& a1, u32x4& a2, u32x4& a3) __attribute__((always_inline)) {
    wait_unit(u);
    const LAS u32x4* sp = (const LAS u32x4*)(L + (u & (NSLOT - 1)) * UNIT + lane * 16);
    a0 = sp[0]; a1 = sp[64]; a2 = sp[128]; a3 = sp[192];
    LDS_WAIT();
    ctrl[16 + cw] = next;
    asm volatile("" : "+v"(a0), "+v"(a1), "+v"(a2), "+v"(a3) :: "memory");
  };
  u32x4 q;
  auto build_q = [&]() __attribute__((always_inline)) {
    lds_await(ctrl, C_Q, NHW);
    const LAS f32x4* qp = (const LAS f32x4*)(L + L_QP + (2 * r4 * DH + 8 * c16) * 4);
    const f32x4 lo = qp[0] + qp[DH / 4], hi = qp[1] + qp[DH / 4 + 1];
    q[0] = pack_bf16(xrow_sum(lo[0]), xrow_sum(lo[1])); q[1] = pack_bf16(xrow_sum(lo[2]), xrow_sum(lo[3]));
    q[2] = pack_bf16(xrow_sum(hi[0]), xrow_sum(hi[1])); q[3] = pack_bf16(xrow_sum(hi[2]), xrow_sum(hi[3]));
  };
  constexpr float SCALE = 0.088388347648318440f, LOG2E = 1.4426950408889634f, C2 = SCALE * LOG2E;
  float mx = -INFINITY;
  auto score = [&](int u, const u32x4& k0, const u32x4& k1, const u32x4& k2, const u32x4& k3) __attribute__((always_inline)) {
    float d0 = 0.f, d1 = 0.f, d2 = 0.f, d3 = 0.f;
#pragma unroll
    for (int k = 0; k < 4; ++k) { d0 = dot2(k0[k], q[k], d0); d1 = dot2(k1[k], q[k], d1); d2 = dot2(k2[k], q[k], d2); d3 = dot2(k3[k], q[k], d3); }
    const unsigned s01 = pack_bf16(row16_sum(d0), row16_sum(d1)), s23 = pack_bf16(row16_sum(d2), row16_sum(d3));
    const unsigned t01 = pack_bf16(bflo(s01) * SCALE, bfhi(s01) * SCALE), t23 = pack_bf16(bflo(s23) * SCALE, bfhi(s23) * SCALE);
    const f32x4 sc = {bflo(t01) * LOG2E, bfhi(t01) * LOG2E, bflo(t23) * LOG2E, bfhi(t23) * LOG2E};
    mx = fmaxf(fmaxf(mx, fmaxf(sc[0], sc[1])), fmaxf(sc[2], sc[3]));
    if (c16 == 0) *(LAS f32x4*)(L + L_SC + u * 64 + r4 * 16) = sc;
  };

  if (hw < 0) {
    {
      u32x4 hk[4 * NHC];
#pragma unroll
      for (int i = 0; i < NHC; ++i) take(cw + NCW * i, i + 1 < NHC ? cw + NCW * (i + 1) : NHELD + cw, hk[4 * i], hk[4 * i + 1], hk[4 * i + 2], hk[4 * i + 3]);
      STAMP(10);
      build_q();
      STAMP(3);
#pragma unroll
      for (int i = 0; i < NHC; ++i) { score(cw + NCW * i, hk[4 * i], hk[4 * i + 1], hk[4 * i + 2], hk[4 * i + 3]); __builtin_amdgcn_sched_barrier(0); }
    }
#pragma unroll 1
    for (int u = NHELD + cw; u < NKU; u += NCW) {
      u32x4 k0, k1, k2, k3;
      take(u, u + NCW, k0, k1, k2, k3);
      score(u, k0, k1, k2, k3);
    }
    ctrlf[32 + cw] = xrow_max(mx);
    lds_arrive(L, C_MAX, lane);
    STAMP(11);
    lds_await(ctrl, C_MAX, NCW + NHW);
    const float mg = wg_max();
    STAMP(12);
    float acc[8] = {0.f, 0.f, 0.f, 0.f, 0.f, 0.f, 0.f, 0.f};
    LAS unsigned* rp = (LAS unsigned*)(L + L_REC + cw * 272 + (8 * c16 + 2 * r4) * 2);
    asm volatile("" : "+v"(rp));
    float cf = 0.f;
#pragma unroll 1
    for (int u = NKU + cw; u < NUNIT; u += NCW) {
      const f32x4 sc = *(const LAS f32x4*)(L + L_SC + (u - NKU) * 64 + r4 * 16);
      float p[4];
#pragma unroll
      for (int t = 0; t < 4; ++t) { p[t] = __builtin_amdgcn_exp2f(sc[t] - mg); asm volatile("" : "+v"(p[t])); }
      if ((u & ~(NCW - 1)) == NUNIT - 32) { lds_await(ctrl, C_CF, 1); cf = ctrlf[49]; }
      u32x4 v[4];
      take(u, u + NCW, v[0], v[1], v[2], v[3]);
#pragma unroll
      for (int t = 0; t < 4; ++t)
#pragma unroll
        for (int k = 0; k < 4; ++k) { acc[2 * k] = fmaf(p[t], bflo(v[t][k]), acc[2 * k]); acc[2 * k + 1] = fmaf(p[t], bfhi(v[t][k]), acc[2 * k + 1]); }
    }
    STAMP(5);
    const float e0 = swadd16(swadd32(acc[0], acc[4]), swadd32(acc[2], acc[6])), e1 = swadd16(swadd32(acc[1], acc[5]), swadd32(acc[3], acc[7]));
    *rp = pack_bf16(e0 * cf, e1 * cf);
    BARRIER();
    return;
  }

  u32x4 wb[16];
  const unsigned tstart = (unsigned)__builtin_amdgcn_readfirstlane(ctrl[8]);
  {
    const __amdgpu_buffer_rsrc_t rq = mkrsrc(wq, D * H * DH * 2);
    const int vo = ((64 * hw + 16 * r4) * (H * DH) + h * DH + 8 * c16) * 2;
#pragma unroll
    for (int i = 0; i < 16; ++i) { if (i == 0) wait_until(tstart + TQ + hw * QA); else PAUSE(QS); wb[i] = ldw(rq, vo, i * (H * DH * 2)); }
    lds_await(ctrl, C_X, 1);
    const u32x4 xa = *(const LAS u32x4*)(L + L_X + (64 * hw + 16 * r4) * 2), xc = *(const LAS u32x4*)(L + L_X + (64 * hw + 16 * r4 + 8) * 2);
    float a[8] = {0.f, 0.f, 0.f, 0.f, 0.f, 0.f, 0.f, 0.f};
#pragma unroll
    for (int i = 0; i < 8; ++i) {
      const unsigned xp = i < 4 ? xa[i] : xc[i - 4];
#pragma unroll
      for (int k = 0; k < 4; ++k) {
        a[2 * k] = dot2(perm_lo(wb[2 * i][k], wb[2 * i + 1][k]), xp, a[2 * k]);
        a[2 * k + 1] = dot2(perm_hi(wb[2 * i][k], wb[2 * i + 1][k]), xp, a[2 * k + 1]);
      }
    }
    const float e0 = swadd16(swadd32(a[0], a[4]), swadd32(a[2], a[6])), e1 = swadd16(swadd32(a[1], a[5]), swadd32(a[3], a[7]));
    *(LAS f32x2*)(L + L_QP + (hw * DH + 8 * c16 + 2 * r4) * 4) = f32x2{e0, e1};
    lds_arrive(L, C_Q, lane);
    STAMP(2);
    const int hb = NHC * NCW + hw;
#pragma unroll
    for (int i = 0; i < NHH; ++i) take(hb + NHW * i, i + 1 < NHH ? hb + NHW * (i + 1) : NEVER, wb[4 * i], wb[4 * i + 1], wb[4 * i + 2], wb[4 * i + 3]);
    STAMP(10);
    build_q();
#pragma unroll
    for (int i = 0; i < NHH; ++i) { score(hb + NHW * i, wb[4 * i], wb[4 * i + 1], wb[4 * i + 2], wb[4 * i + 3]); __builtin_amdgcn_sched_barrier(0); }
    ctrlf[32 + cw] = xrow_max(mx);
    lds_arrive(L, C_MAX, lane);
  }
  f32x4 y[4] = {{0.f, 0.f, 0.f, 0.f}, {0.f, 0.f, 0.f, 0.f}, {0.f, 0.f, 0.f, 0.f}, {0.f, 0.f, 0.f, 0.f}};
  if ((pj & 7) != 7) {
    const int sl = 7 * (pj >> 3) + (pj & 7);
    const __amdgpu_buffer_rsrc_t r1 = mkrsrc(w1, D * F * 2), r3 = mkrsrc(w3, D * F * 2), r2 = mkrsrc(w2, F * D * 2);
    const int vo = ((64 * hw + (lane & 60)) * F + FS * sl + 8 * (lane & 3)) * 2;
#pragma unroll
    for (int i = 0; i < 8; ++i) { if (i == 0) wait_until(tstart + T1 + hw * A1); else PAUSE(S1); wb[i] = ldw(i < 4 ? r1 : r3, vo, (i & 3) * (F * 2)); }
    const u32x2 xm = *(const LAS u32x2*)(L + L_X + (64 * hw + (lane & 60)) * 2);
    float a[16];
#pragma unroll
    for (int m = 0; m < 2; ++m)
#pragma unroll
      for (int k = 0; k < 4; ++k) {
        a[8 * m + 2 * k] = dot2(perm_lo(wb[4 * m + 2][k], wb[4 * m + 3][k]), xm[1], dot2(perm_lo(wb[4 * m][k], wb[4 * m + 1][k]), xm[0], 0.f));
        a[8 * m + 2 * k + 1] = dot2(perm_hi(wb[4 * m + 2][k], wb[4 * m + 3][k]), xm[1], dot2(perm_hi(wb[4 * m][k], wb[4 * m + 1][k]), xm[0], 0.f));
      }
    f32x4 e;
#pragma unroll
    for (int k = 0; k < 4; ++k) {
      float t = swadd16(swadd32(a[k], a[8 + k]), swadd32(a[4 + k], a[12 + k]));
      t += dpp<ROR8>(t); e[k] = t + dpp<ROR4>(t);
    }
    if ((lane & 12) == 0) *(LAS f32x4*)(L + L_MP + (hw * 64 + (lane & 32) + 8 * (lane & 3) + 4 * (r4 & 1)) * 4) = e;
    lds_arrive(L, C_MLP, lane);
    u32x2 wr[8];
#pragma unroll
    for (int i = 0; i < 8; ++i) { if (i == 0) wait_until(tstart + T2 + hw * A2); else PAUSE(S2); wr[i] = ldw2(r2, ((FS * sl + 8 * r4) * D + 64 * hw + 4 * c16) * 2, i * (D * 2)); }
    lds_await(ctrl, C_MLP, NHW);
    const LAS float* mp = (const LAS float*)(L + L_MP) + (lane & 31);
    float ga = 0.f, up = 0.f;
#pragma unroll
    for (int w = 0; w < NHW; ++w) { ga += mp[w * 64]; up += mp[w * 64 + 32]; }
    const float hv = ga / (1.f + __expf(-ga)) * up * (1.f - ctrlf[48]);
    const unsigned hh = pack_bf16(hv, 0.f), hl = pack_bf16(hv - bflo(hh), 0.f);
    *(LAS u16*)(L + L_H + hw * 128 + lane * 2) = (u16)(lane < 32 ? hh : hl);
    LDS_WAIT();
    u32x4 ha = *(const LAS u32x4*)(L + L_H + hw * 128 + (c16 & 1) * 64 + r4 * 16);
    if (c16 >= 2) ha = u32x4{0u, 0u, 0u, 0u};
#pragma unroll
    for (int e = 0; e < 4; ++e) {
      u32x4 f;
#pragma unroll
      for (int k = 0; k < 4; ++k) f[k] = (e & 1) ? perm_hi(wr[2 * k][e >> 1], wr[2 * k + 1][e >> 1]) : perm_lo(wr[2 * k][e >> 1], wr[2 * k + 1][e >> 1]);
      y[e] = mfma(ha, f, y[e]);
    }
  }
#pragma unroll
  for (int e = 0; e < 4; ++e) asm volatile("" : "+v"(y[e]));
  {
    const __amdgpu_buffer_rsrc_t ro = mkrsrc(wo, H * DH * D * 2);
#pragma unroll
    for (int t = 0; t < 4; ++t) {
      u32x2 wr[8];
#pragma unroll
      for (int i = 0; i < 8; ++i) { if (t + i == 0) wait_until(tstart + TO + hw * AO); else PAUSE(SO); wr[i] = ldw2(ro, ((h * DH + 32 * r4) * D + 64 * hw + 4 * c16) * 2, (8 * t + i) * (D * 2)); }
#pragma unroll
      for (int e = 0; e < 4; ++e) {
#pragma unroll
        for (int k = 0; k < 4; ++k) wb[4 * t + e][k] = (e & 1) ? perm_hi(wr[2 * k][e >> 1], wr[2 * k + 1][e >> 1]) : perm_lo(wr[2 * k][e >> 1], wr[2 * k + 1][e >> 1]);
        asm volatile("" : "+v"(wb[4 * t + e]));
      }
    }
  }
  lds_await(ctrl, C_CF, 1);
  unsigned yt = (unsigned)ctrl[50];
  int yo = WS_Y + (b * 64 + pj) * 2048 + hw * 256 + c16 * 16;
  const LAS u32x4* rec = (const LAS u32x4*)(L + L_REC + (lane & 3) * 272 + r4 * 64);
  unsigned ym = ~YMASK;
  asm volatile("" : "+v"(yt), "+v"(yo), "+v"(rec), "+s"(ym));
  STAMP(4);
  BARRIER();
  STAMP(9);
  {
    const u32x4 a[4] = {rec[0], rec[1], rec[2], rec[3]};
#pragma unroll
    for (int t = 0; t < 4; ++t)
#pragma unroll
      for (int e = 0; e < 4; ++e) y[e] = mfma(a[t], wb[4 * t + e], y[e]);
    u32x4 v;
#pragma unroll
    for (int e = 0; e < 4; ++e) {
      const f32x2 t = y[e].xy + y[e].zw;
      unsigned r;
      asm("v_add_f32 %0, %1, %2\n\tv_add_u32 %0, %5, %0\n\tv_and_or_b32 %0, %0, %3, %4" : "=&v"(r) : "v"(t[0]), "v"(t[1]), "s"(ym), "v"(yt), "i"((YMASK + 1u) / 2u));
      v[e] = r;
    }
    if (lane < 16) __builtin_amdgcn_raw_buffer_store_b128(v, wsr, yo, 0, 16);
  }
  STAMP(7);
}

extern "C" void kernel_launch(void* const* d_in, const int* in_sizes, int n_in, void* d_out, int out_size, void* d_ws, size_t ws_size,
                              hipStream_t stream) {
  gated_mix_kernel<<<256, NTHR, 0, stream>>>((const u16*)d_in[0], (const u16*)d_in[1], (const u16*)d_in[2], (const u16*)d_in[3], (const u16*)d_in[4],
                                             (const u16*)d_in[6], (const u16*)d_in[7], (const u16*)d_in[8], (const u16*)d_in[9], (u16*)d_out,
                                             (unsigned char*)d_ws);
}
```

```cpp
#include <hip/hip_runtime.h>
#include <hip/hip_bf16.h>
#define STAMPS 1
#define MWAVE 0
#define MIDX 0
#define MTYPE 0
#define RWAVE 0
#define RIDX 0
#define MSCALE 0.f

#define GAS __attribute__((address_space(1)))
#define LAS __attribute__((address_space(3)))
typedef unsigned short u16;
typedef unsigned long long u64;
using u32x4 = __attribute__((ext_vector_type(4))) unsigned;
using u32x2 = __attribute__((ext_vector_type(2))) unsigned;
using f32x4 = __attribute__((ext_vector_type(4))) float;
using f32x2 = __attribute__((ext_vector_type(2))) float;
using bf16x2 = __attribute__((ext_vector_type(2))) __bf16;
using bf16x8 = __attribute__((ext_vector_type(8))) __bf16;

constexpr int B = 4, D = 512, H = 32, SK = 8192, DH = 128, F = 1792;
constexpr int NCW = 4, NHW = 8, XWAVE = 1 + NCW + NHW, NWAVE = XWAVE + 2, NTHR = NWAVE * 64;
#define QA 80
#define QB 5
#define T1 1200
#define A1 150
#define B1 18
#define T2 2500
#define A2 40
#define B2 4
#define TO 2900
#define AO 350
#define BO 8
#define TS 70.6
#define REF0 180
#define PLAG 1
#define NOPST 0
#define NOPLD 0
#define PAUX 16
#define RAUX 16
#define PFW 0
#define TQ 130
#define NINIT 6
#define CODE_KIB 21
#define FDEL 0
#define CODEPF 1
#define YST 16
#define YLD 16
#define RED16 0
#define MASKP 0
#define PSLP 0
#define OUTSC 0
#define OUT_STORE(p, v) *(GAS unsigned*)(p) = v
#define CLR0 1
#define MLPFULL 0
#define MLPPF 0
#define TP 1200
#define AP 12
#define GRID 0
#define B2BD 0
#define B2BAUX 16
#define WOB2B 0
#define TOUCHW 1
#define W0 30
#define BARPOS 12
#define NO_WO 0
#define TLBPF 0
#define TMODE 0
#define SPW 4
#define QS 2
#define S1 6
#define S2 1
#define SO 2
#define USL 0
#define JUMP 100
#define DECAY 6
#define HOLD0 10000
#define UEND 420
#define WGAIN2 18
#define WMID2 WMID
#define FOFF 150
#define FMIN 45
#define FMAX 150
#define NRED 64
#define SLOTSH 8
#define WMID 20.5
#define WGAIN 10
#define WMIN 14
#define WMAX 27
#define CLRAUX 16
#define PCONT 1
#define XDEL 20
#define XSPR 0
#define XNT 0
#define MIMIC 1
#define NCLR 2
#define NOFLAG 0
#define CSPR 0
constexpr int UNIT = 4096;
constexpr int NSLOT = 32, RING = NSLOT * UNIT;
constexpr int NKU = (SK / 2) / 16, NUNIT = 2 * NKU;
constexpr int NHC = 6, NHH = 4, NHELD = NHC * NCW + NHH * NHW;
constexpr int FS = 32;
constexpr int L_CTRL = RING;
constexpr int L_X = L_CTRL + 256;
constexpr int L_QP = L_X + D * 2;
constexpr int L_MP = L_QP + NHW * DH * 4;
constexpr int L_REC = L_MP + NHW * 64 * 4;
constexpr int L_H = L_REC + NCW * 272;
constexpr int L_SC = L_H + NHW * 128;
constexpr int LDS_BYTES = L_SC + NKU * 64;
static_assert(LDS_BYTES <= 159 * 1024, "lds");
static_assert(WMIN <= (int)WMID && (int)WMID <= WMAX && WMAX <= 28, "window");
constexpr int WS_EPOCH = 0;
constexpr int WS_REC = 4096;
constexpr int WS_PACE = WS_REC + 256 * 128;
constexpr int WS_Y = WS_PACE + 64 * 2048;
constexpr int WS_BYTES = WS_Y + B * 64 * 2048;
constexpr u64 TAG_STEP = 0x9E3779B97F4A7C15ull;
constexpr unsigned YMASK = 0xFFFu;
constexpr int SPIN_CAP = 1 << 18;
constexpr int NEVER = 1 << 20;
constexpr int C_Q = 4, C_MAX = 5, C_MLP = 6, C_X = 7, C_CF = 9;

__device__ __forceinline__ float bflo(unsigned u) { return __builtin_bit_cast(float, u << 16); }
__device__ __forceinline__ float bfhi(unsigned u) { return __builtin_bit_cast(float, u & 0xffff0000u); }
__device__ __forceinline__ float dot2(unsigned a, unsigned b, float acc) {
  return __builtin_amdgcn_fdot2_f32_bf16(__builtin_bit_cast(bf16x2, a), __builtin_bit_cast(bf16x2, b), acc, false);
}
__device__ __forceinline__ unsigned pack_bf16(float lo, float hi) {
  const bf16x2 v = {(__bf16)lo, (__bf16)hi};
  return __builtin_bit_cast(unsigned, v);
}
__device__ __forceinline__ unsigned perm_lo(unsigned a, unsigned b) { return __builtin_amdgcn_perm(b, a, 0x05040100u); }
__device__ __forceinline__ unsigned perm_hi(unsigned a, unsigned b) { return __builtin_amdgcn_perm(b, a, 0x07060302u); }
template <int CTRL> __device__ __forceinline__ float dpp(float x) {
  return __builtin_bit_cast(float, __builtin_amdgcn_mov_dpp(__builtin_bit_cast(int, x), CTRL, 0xf, 0xf, true));
}
constexpr int XOR1 = 0xB1, ROR1 = 0x121, ROR2 = 0x122, ROR4 = 0x124, ROR8 = 0x128;
__device__ __forceinline__ float row16_sum(float x) { x += dpp<ROR8>(x); x += dpp<ROR4>(x); x += dpp<ROR2>(x); x += dpp<ROR1>(x); return x; }
__device__ __forceinline__ float swadd32(float a, float b) {
  auto t = __builtin_amdgcn_permlane32_swap(__float_as_uint(a), __float_as_uint(b), false, false);
  return __uint_as_float(t[0]) + __uint_as_float(t[1]);
}
__device__ __forceinline__ float swadd16(float a, float b) {
  auto t = __builtin_amdgcn_permlane16_swap(__float_as_uint(a), __float_as_uint(b), false, false);
  return __uint_as_float(t[0]) + __uint_as_float(t[1]);
}
__device__ __forceinline__ float xrow_sum(float x) { x = swadd16(x, x); return swadd32(x, x); }
__device__ __forceinline__ float xrow_max(float x) {
  auto s = __builtin_amdgcn_permlane16_swap(__float_as_uint(x), __float_as_uint(x), false, false);
  x = fmaxf(__uint_as_float(s[0]), __uint_as_float(s[1]));
  auto t = __builtin_amdgcn_permlane32_swap(__float_as_uint(x), __float_as_uint(x), false, false);
  return fmaxf(__uint_as_float(t[0]), __uint_as_float(t[1]));
}
__device__ __forceinline__ __amdgpu_buffer_rsrc_t mkrsrc(const void* p, int bytes) { return __builtin_amdgcn_make_buffer_rsrc((void*)p, 0, bytes, 0x00020000); }
__device__ __forceinline__ u32x4 ldw(__amdgpu_buffer_rsrc_t r, int voff, int soff) { return __builtin_amdgcn_raw_buffer_load_b128(r, voff, soff, 2); }
#define LDS_WAIT() asm volatile("s_waitcnt lgkmcnt(0)" ::: "memory")
#define VM_DRAIN() asm volatile("s_waitcnt vmcnt(0)" ::: "memory")
#define CFENCE() asm volatile("" ::: "memory")
#define BARRIER() asm volatile("s_waitcnt lgkmcnt(0)\n\ts_barrier" ::: "memory")
#define PAUSE(n) do { __builtin_amdgcn_sched_barrier(0); asm volatile("s_sleep %0" :: "i"(n) : "memory"); __builtin_amdgcn_sched_barrier(0); } while (0)

__device__ __forceinline__ void lds_arrive(LAS unsigned char* L, int idx, int lane) {
  LDS_WAIT();
  if (lane == 0) __hip_atomic_fetch_add((LAS int*)(L + L_CTRL) + idx, 1, __ATOMIC_RELAXED, __HIP_MEMORY_SCOPE_WORKGROUP);
}
__device__ __forceinline__ void lds_await(volatile LAS int* ctrl, int idx, int n) {
#pragma unroll 1
  for (int spin = 0; spin < SPIN_CAP; ++spin) { if (__builtin_amdgcn_readfirstlane(ctrl[idx]) >= n) break; __builtin_amdgcn_s_sleep(1); }
  CFENCE();
}

__device__ __forceinline__ void wait_until(unsigned t) {
  __builtin_amdgcn_sched_barrier(0);
#pragma unroll 1
  for (int spin = 0; spin < 4096; ++spin) {
    const int d = (int)(t - (unsigned)__builtin_amdgcn_s_memrealtime());
    if (d <= 0) break;
    if (d > 24) __builtin_amdgcn_s_sleep(6);
  }
  CFENCE();
  __builtin_amdgcn_sched_barrier(0);
}
#define STR2(x) #x
#define STR(x) STR2(x)
constexpr int TAU2 = 2 * (int)(TS * 100.0 * 256.0 / NUNIT);
__device__ __forceinline__ f32x4 mfma(const u32x4& a, const u32x4& b, const f32x4& c) {
  return __builtin_amdgcn_mfma_f32_16x16x32_bf16(__builtin_bit_cast(bf16x8, a), __builtin_bit_cast(bf16x8, b), c, 0, 0, 0);
}
__device__ __forceinline__ u32x2 ldw2(__amdgpu_buffer_rsrc_t r, int voff, int soff) { return __builtin_amdgcn_raw_buffer_load_b64(r, voff, soff, 2); }
template <int CTRL> __device__ __forceinline__ int dppi(int x) { return __builtin_amdgcn_mov_dpp(x, CTRL, 0xf, 0xf, true); }

#define STAMP(i) do { __builtin_amdgcn_sched_barrier(0); ((volatile LAS unsigned*)(L + LDS_BYTES))[wave * 16 + (i)] = (unsigned)__builtin_amdgcn_s_memrealtime(); __builtin_amdgcn_sched_barrier(0); } while (0)
#define STAMP_FLUSH() do { LDS_WAIT(); const __amdgpu_buffer_rsrc_t dr = mkrsrc(ws + (4 << 20), 256 * 1024); \
    __builtin_amdgcn_raw_buffer_store_b128(*(volatile LAS u32x4*)(L + LDS_BYTES + lane * 16), dr, blockIdx.x * 1024 + lane * 16, 0, 16); \
    if (blockIdx.x == 0) { const unsigned t0 = __builtin_amdgcn_readfirstlane(((volatile LAS unsigned*)(L + LDS_BYTES))[0]); \
      while ((unsigned)__builtin_amdgcn_s_memrealtime() - t0 < 13000u) __builtin_amdgcn_s_sleep(8); \
      CFENCE(); float sum = 0.f, mxv = -1e9f; \
      for (int i = 0; i < 4; ++i) { const int wg = lane * 4 + i; \
        const unsigned ref = (RWAVE < 0) ? t0 : __builtin_amdgcn_raw_buffer_load_b32(dr, wg * 1024 + ((RWAVE < 0 ? 0 : RWAVE) * 16 + RIDX) * 4, 0, 16); \
        const float v = (float)(int)(__builtin_amdgcn_raw_buffer_load_b32(dr, wg * 1024 + (MWAVE * 16 + MIDX) * 4, 0, 16) - ref); \
        sum += v; mxv = fmaxf(mxv, v); } \
      sum = xrow_sum(row16_sum(sum)) * (1.f / 256.f); \
      float gm = mxv; gm = fmaxf(gm, dpp<ROR8>(gm)); gm = fmaxf(gm, dpp<ROR4>(gm)); gm = fmaxf(gm, dpp<ROR2>(gm)); gm = fmaxf(gm, dpp<ROR1>(gm)); gm = xrow_max(gm); \
      const float met = (MTYPE == 0 ? sum : gm) * MSCALE; \
      const unsigned tgt = __builtin_amdgcn_readfirstlane((unsigned)(15000.f + met)); \
      while ((unsigned)__builtin_amdgcn_s_memrealtime() - t0 < tgt) __builtin_amdgcn_s_sleep(1); } } while (0)
#define STAMP_LDS 1024

__global__ void __launch_bounds__(NTHR) gated_mix_kernel(const u16* x, const u16* kc, const u16* vc, const u16* wq, const u16* wo, const u16* w1,
                                                         const u16* w3, const u16* w2, const u16* gw, u16* out, unsigned char* ws) {
  __shared__ __attribute__((aligned(16))) unsigned char lds_raw[LDS_BYTES + STAMP_LDS];
  LAS unsigned char* L = (LAS unsigned char*)lds_raw;
  volatile LAS int* ctrl = (volatile LAS int*)(L + L_CTRL);
  volatile LAS float* ctrlf = (volatile LAS float*)(L + L_CTRL);
  u64 t_first; asm volatile("s_memrealtime %0\n\ts_waitcnt lgkmcnt(0)" : "=s"(t_first));
  __builtin_amdgcn_sched_barrier(0);
  if (threadIdx.x < 14 * 64) ((volatile LAS unsigned*)(L + LDS_BYTES))[(threadIdx.x >> 6) * 16] = (unsigned)t_first;
#define DBG(x) x
  {
    unsigned va, vb, vo, pa, nxa, t0, t1, t2, t3;
#define CODELD "global_load_lds_dwordx4 %[t0], s[56:57]\n\t"
#define ASTAMP(i) DBG("s_memrealtime s[60:61]\n\ts_waitcnt lgkmcnt(0)\n\tv_mov_b32 %[t1], %c[dbg] + 4 * " #i "\n\tv_mov_b32 %[t2], s60\n\tds_write_b32 %[t1], %[t2]\n\t")
#define WAITW "s_waitcnt (wcnt & 15) | ((wcnt >> 4) << 14) | 0x0F70\n\t"
#define SP(v, sb, wait) wait "global_load_lds_dword " v ", " sb " nt\n\tglobal_load_lds_dword " v ", " sb " sc1\n\t"
#define SW "s_waitcnt vmcnt(%c[spw])\n\t"
#define UNIT4(v) \
        WAITW "global_load_lds_dwordx4 " v ", s[40:41] nt\n\t" \
        WAITW "global_load_lds_dwordx4 " v ", s[40:41] offset:1024 nt\n\t" \
        WAITW "global_load_lds_dwordx4 " v ", s[40:41] offset:2048 nt\n\t" \
        WAITW "global_load_lds_dwordx4 " v ", s[40:41] offset:3072 nt\n\t"
    asm volatile(
        "s_getpc_b64 s[56:57]\n\t"
        "s_load_dwordx8 s[44:51], %[ka], 0x18\n\t"
        "s_load_dwordx2 s[52:53], %[ka], 0x38\n\t"
        "s_load_dwordx4 s[40:43], %[ka], 0x8\n\t"
        "v_readfirstlane_b32 s54, %[tid]\n\t"
        "s_mov_b32 m0, %c[jk]\n\t"
        "s_lshr_b32 s59, %[bid], 3\n\t"
        "s_cmp_ge_u32 s54, 64\n\t"
        "s_cbranch_scc1 90f\n\t"
        "s_min_u32 s54, s59, %c[nch]\n\t"
        "s_lshl_b32 s54, s54, 10\n\t"
        "v_lshlrev_b32 %[vo], 4, %[tid]\n\t"
        "s_and_b32 s56, s56, -128\n\t"
        "v_add_u32 %[t0], s54, %[vo]\n\t"
        "s_and_b32 s58, %[bid], 7\n\t"
        CODELD
        "s_setprio 3\n\t"
        "v_lshlrev_b32 %[t1], 7, %[tid]\n\t"
        "s_lshl_b32 s54, s59, 7\n\t"
        "s_add_u32 s54, s54, s58\n\t"
        "s_lshl_b32 s54, s54, 10\n\t"
        "v_lshrrev_b32 %[va], 3, %[tid]\n\t"
        "v_mul_u32_u24 %[va], 8192 - 1024, %[va]\n\t"
        "v_add3_u32 %[va], %[va], %[t1], s54\n\t"
        "v_add_u32 %[t2], 0x10000, %[va]\n\t"
        "s_lshl_b32 s55, s58, 5\n\t"
        "s_add_u32 s55, s55, s59\n\t"
        "s_mul_i32 s54, s55, 7 * 1024\n\t"
        "v_min_u32 %[t3], 55 * 128, %[t1]\n\t"
        "v_add_u32 %[t3], s54, %[t3]\n\t"
        "s_mul_i32 s54, s58, 448\n\t"
        "s_and_b32 s54, s54, -128\n\t"
        "s_mul_i32 s57, s59, 16 * 3584\n\t"
        "s_add_u32 s54, s54, s57\n\t"
        "v_lshrrev_b32 %[t0], 2, %[tid]\n\t"
        "v_mul_u32_u24 %[t0], 3584 - 512, %[t0]\n\t"
        "v_add3_u32 %[t0], %[t0], %[t1], s54\n\t"
        "v_lshl_add_u32 %[vb], s55, 14, %[t1]\n\t"
        "v_add_u32 %[t1], 0x2000, %[vb]\n\t"
        "s_and_b32 s54, %[bid], 0xc0\n\t"
        "s_lshl3_add_u32 s54, s58, s54\n\t"
        "s_and_b32 s56, s59, 7\n\t"
        "s_add_u32 s54, s54, s56\n\t"
        "s_lshl_b32 s56, s54, 20\n\t"
        "s_waitcnt lgkmcnt(0)\n\t"
        SP("%[va]", "s[44:45]", "") SP("%[t2]", "s[44:45]", "") SP("%[t3]", "s[52:53]", "")
        ".if %c[spw] <= 4\n\ts_barrier\n\t.endif\n\t"
        SP("%[t0]", "s[48:49]", SW)
        ".if %c[spw] == 6\n\ts_barrier\n\t.endif\n\t"
        SP("%[t0]", "s[50:51]", SW)
        ".if %c[spw] == 8\n\ts_barrier\n\t.endif\n\t"
        SP("%[vb]", "s[46:47]", SW) SP("%[t1]", "s[46:47]", SW)
        ".if %c[spw] > 8\n\ts_barrier\n\t.endif\n\t"
        ASTAMP(6)
        "s_add_u32 s40, s40, s56\n\t"
        "s_addc_u32 s41, s41, 0\n\t"
        "s_add_u32 s42, s42, s56\n\t"
        "s_addc_u32 s43, s43, 0\n\t"
        "s_sub_u32 s42, s42, %c[nku] * 4096\n\t"
        "s_subb_u32 s43, s43, 0\n\t"
        ".set so, 0\n\t"
        ".set wcnt, %c[spw]\n\t"
        ".rept 4\n\t"
        "s_movk_i32 m0, so\n\t"
        ".if (so & 4096) == 0\n\t"
        WAITW
        ".set wcnt, wcnt + 6\n\t"
        ".else\n\t"
        "s_nop 0\n\t"
        ".endif\n\t"
        "global_load_lds_dwordx4 %[vo], s[40:41] nt\n\t"
        "global_load_lds_dwordx4 %[vo], s[40:41] offset:1024 nt\n\t"
        "global_load_lds_dwordx4 %[vo], s[40:41] offset:2048 nt\n\t"
        "global_load_lds_dwordx4 %[vo], s[40:41] offset:3072 nt\n\t"
        "v_add_u32 %[vo], 0x1000, %[vo]\n\t"
        ".set so, so + 4096\n\t"
        ".endr\n\t"
        ".if %c[spw] <= 4\n\t"
        "s_waitcnt vmcnt(16)\n\t"
        ".endif\n\t"
        ASTAMP(7)
        "s_getpc_b64 s[50:51]\n"
        "20:\n\t"
        "s_add_u32 s50, s50, 23f-20b-%c[wmin]*128\n\t"
        "s_addc_u32 s51, s51, -1\n\t"
        "v_mov_b32 %[pa], %c[ctl]\n\t"
        "v_and_b32 %[nxa], 0x3c0 >> 2, %[vo]\n\t"
        "v_lshrrev_b32 %[nxa], 2, %[nxa]\n\t"
        "v_add_u32 %[nxa], %c[ctl] + 64, %[nxa]\n\t"
        "s_memrealtime s[58:59]\n\t"
        "s_mov_b32 s62, 0\n\t"
        "s_mov_b32 s63, %c[gain]\n\t"
        "s_mov_b32 s64, %c[wmid]\n\t"
        "s_mov_b32 s44, 4\n\t"
        "s_mov_b32 s45, %c[wmid] >> 8\n\t"
        "s_mov_b32 s46, 0x4000\n\t"
        "s_mov_b32 s47, 2 * %c[tau2]\n\t"
        "s_mov_b32 s48, %c[nslot]\n\t"
        "s_mov_b32 s49, %c[nku]\n\t"
        "s_waitcnt lgkmcnt(0)\n\t"
        "s_mov_b32 s59, 10000\n"
        "22:\n\t"
        "s_memrealtime s[56:57]\n\t"
        "ds_read_b32 %[t3], %[pa] offset:8\n\t"
        "s_lshl_b32 s52, s45, 7\n\t"
        "s_add_u32 s52, s50, s52\n\t"
        "s_addc_u32 s53, s51, 0\n\t"
        "s_add_i32 s54, s45, -1\n\t"
        "s_lshr_b32 s54, s54, 2\n\t"
        "s_sub_i32 s54, s44, s54\n\t"
        "v_mov_b32 %[t1], s54\n\t"
        "v_add_u32 %[t0], 0x1000, %[vo]\n\t"
        "s_add_u32 s55, s46, 0x1000\n\t"
        "s_mov_b32 m0, s46\n\t"
        "s_setpc_b64 s[52:53]\n"
        "24:\n\t"
        "s_waitcnt lgkmcnt(0)\n\t"
        "v_readfirstlane_b32 s57, %[t3]\n\t"
        "v_add_u32 %[t1], 1, %[t1]\n\t"
        "s_lshr_b32 s54, s47, 8\n\t"
        "s_sub_u32 s56, s56, s54\n\t"
        "v_mov_b32 %[t2], s56\n\t"
        "s_sub_i32 s54, s56, s58\n\t"
        "s_mov_b32 s58, s56\n\t"
        "s_sub_i32 s56, s56, s57\n\t"
        "ds_write2_b32 %[pa], %[t1], %[t2] offset1:1\n\t"
        "s_add_i32 s59, s59, s54\n\t"
        "s_add_i32 s59, s59, %c[decay]\n\t"
        "s_cmp_gt_i32 s54, %c[jump]\n\t"
        "s_cselect_b32 s59, s62, s59\n\t"
        "s_min_i32 s62, s56, s59\n\t"
        "s_mul_i32 s56, s62, s63\n\t"
        "s_add_i32 s56, s56, s64\n\t"
        "s_ashr_i32 s56, s56, 8\n\t"
        "s_max_i32 s56, s56, %c[wmin]\n\t"
        "s_min_i32 s45, s56, %c[wmax]\n\t"
        "s_add_u32 s47, s47, %c[tau2]\n\t"
        "s_add_u32 s46, s46, 0x2000\n\t"
        "s_and_b32 s46, s46, %c[nslot] * 4096 - 1\n\t"
        "v_add_u32 %[vo], 0x2000, %[vo]\n\t"
        "s_add_i32 s44, s44, 2\n\t"
        "s_cmp_lt_i32 s44, s48\n\t"
        "s_cbranch_scc1 22b\n\t"
        DBG("s_add_i32 s54, s44, -1\n\ts_lshr_b32 s54, s54, 5\n\ts_lshl_b32 s54, s54, 2\n\ts_add_u32 s54, s54, %c[dbg] + 14 * 64\n\tv_mov_b32 %[t0], s54\n\tv_mov_b32 %[t1], s45\n\t"
            "ds_write2_b32 %[t0], %[t2], %[t1] offset1:16\n\t")
        "s_cmp_lt_i32 s44, s49\n\t"
        "s_cbranch_scc1 25f\n\t"
        "s_cmp_ge_i32 s44, 2 * %c[nku]\n\t"
        "s_cbranch_scc1 30f\n\t"
        "s_mov_b32 s49, 2 * %c[nku]\n\t"
        "s_mov_b64 s[40:41], s[42:43]\n"
        "25:\n\t"
        "s_cmp_ge_i32 s44, %c[uend]\n\t"
        "s_cselect_b32 s63, %c[gain2], s63\n\t"
        "s_cselect_b32 s64, %c[wmid2], s64\n\t"
        "s_mov_b32 s55, %c[cap]\n"
        "26:\n\t"
        "ds_read_b32 %[t0], %[nxa]\n\t"
        "s_waitcnt lgkmcnt(0)\n\t"
        "s_nop 1\n\t"
        "v_min_i32_dpp %[t0], %[t0], %[t0] row_ror:8 row_mask:0xf bank_mask:0xf\n\t"
        "s_nop 1\n\t"
        "v_min_i32_dpp %[t0], %[t0], %[t0] row_ror:4 row_mask:0xf bank_mask:0xf\n\t"
        "s_nop 1\n\t"
        "v_min_i32_dpp %[t0], %[t0], %[t0] row_ror:2 row_mask:0xf bank_mask:0xf\n\t"
        "s_nop 1\n\t"
        "v_min_i32_dpp %[t0], %[t0], %[t0] row_ror:1 row_mask:0xf bank_mask:0xf\n\t"
        "s_nop 1\n\t"
        "v_readfirstlane_b32 s48, %[t0]\n\t"
        "s_nop 1\n\t"
        "s_add_i32 s48, s48, %c[nslot]\n\t"
        "s_and_b32 s48, s48, -2\n\t"
        "s_min_i32 s48, s48, s49\n\t"
        "s_cmp_lt_i32 s44, s48\n\t"
        "s_cbranch_scc1 22b\n\t"
        "v_mov_b32 %[t1], s44\n\t"
        "s_waitcnt vmcnt(0)\n\t"
        "ds_write_b32 %[pa], %[t1]\n\t"
        "s_sleep 1\n\t"
        "s_sub_u32 s55, s55, 1\n\t"
        "s_cmp_lg_u32 s55, 0\n\t"
        "s_cbranch_scc1 26b\n\t"
        "s_branch 22b\n"
        "30:\n\t"
        ".set k, 6\n\t"
        ".rept 7\n\t"
        ".set wcnt, 4 * k\n\t"
        "v_mov_b32 %[t1], 2 * %c[nku] - k\n\t"
        WAITW
        "ds_write_b32 %[pa], %[t1]\n\t"
        ".set k, k - 1\n\t"
        ".endr\n\t"
        DBG("s_memrealtime s[56:57]\n\ts_waitcnt lgkmcnt(0)\n\tv_mov_b32 %[t0], %c[dbg] + 4\n\tv_mov_b32 %[t1], s56\n\tds_write_b32 %[t0], %[t1]\n\t")
        "s_waitcnt lgkmcnt(0)\n\t"
        "s_endpgm\n\t"
        ".p2align 7\n"
        "23:\n\t"
        ".set wcnt, %c[wmin]-1\n\t"
        ".rept %c[wmax] - %c[wmin] + 1\n\t"
        UNIT4("%[vo]")
        "ds_write_b32 %[pa], %[t1]\n\t"
        "s_mov_b32 m0, s55\n\t"
        UNIT4("%[t0]")
        "s_branch 24b\n\t"
        ".p2align 7\n\t"
        ".set wcnt, wcnt+1\n\t"
        ".endr\n"
        "90:\n\t"
        "s_waitcnt lgkmcnt(0)"
        : [va] "=&v"(va), [vb] "=&v"(vb), [vo] "=&v"(vo), [pa] "=&v"(pa), [nxa] "=&v"(nxa), [t0] "=&v"(t0), [t1] "=&v"(t1), [t2] "=&v"(t2), [t3] "=&v"(t3)
        : [tid] "v"(threadIdx.x), [bid] "s"(blockIdx.x), [ka] "s"(__builtin_amdgcn_kernarg_segment_ptr()), [nch] "i"(CODE_KIB - 1), [jk] "i"(RING - UNIT),
          [spw] "i"(SPW), [wmin] "i"(WMIN), [wmax] "i"(WMAX), [gain] "i"(WGAIN), [wmid] "i"((int)(WMID * 256)), [tau2] "i"(TAU2),
          [ctl] "i"(L_CTRL), [nslot] "i"(NSLOT), [nku] "i"(NKU), [cap] "i"(SPIN_CAP), [dbg] "i"(LDS_BYTES), [jump] "i"(JUMP), [decay] "i"(DECAY), [uend] "i"(UEND), [gain2] "i"(WGAIN2), [wmid2] "i"((int)(WMID2 * 256))
        : "memory", "scc", "s40", "s41", "s42", "s43", "s44", "s45", "s46", "s47", "s48", "s49", "s50", "s51", "s52", "s53", "s54", "s55", "s56", "s57", "s58", "s59", "s60", "s61", "s62", "s63", "s64");
  }
  __builtin_amdgcn_sched_barrier(0);
  const int tid = threadIdx.x, lane = tid & 63, wave = __builtin_amdgcn_readfirstlane(tid >> 6);
  const int xg = blockIdx.x & 7, kk = blockIdx.x >> 3;
  const int b = kk >> 3, pj = 8 * xg + (kk & 7), h = pj >> 1, s = pj & 1;
  const int r4 = lane >> 4, c16 = lane & 15;
  asm volatile("" : "+s"(x), "+s"(wq), "+s"(wo), "+s"(w1), "+s"(w3), "+s"(w2), "+s"(gw), "+s"(out), "+s"(ws));
  if (wave == 1) {
    const int wi = lane - 16;
    const int now = (int)(unsigned)__builtin_amdgcn_s_memrealtime() + REF0;
    ctrl[lane] = (lane == 1 || lane == 2) ? now : lane == 8 ? now - REF0 : (lane < 16 || lane >= 32) ? 0 : wi < NCW ? wi : wi < NCW + NHW ? NHC * NCW + wi - NCW : NEVER;
  }
  BARRIER();

  const __amdgpu_buffer_rsrc_t wsr = mkrsrc(ws, WS_BYTES);
  auto wg_max = [&]() __attribute__((always_inline)) {
    float mg = ctrlf[32 + (c16 < NCW + NHW ? c16 : 0)];
    mg = fmaxf(mg, dpp<ROR8>(mg)); mg = fmaxf(mg, dpp<ROR4>(mg)); mg = fmaxf(mg, dpp<ROR2>(mg)); mg = fmaxf(mg, dpp<ROR1>(mg));
    return mg;
  };

  if (wave == XWAVE + 1) {
    unsigned k = ((unsigned)__builtin_amdgcn_s_memrealtime() >> SLOTSH) + 1;
#pragma unroll 1
    for (int it = 0; it < 4096 >> (SLOTSH - 2); ++it, ++k) {
      wait_until(k << SLOTSH);
      if (__builtin_amdgcn_readfirstlane(ctrl[0]) >= NUNIT - 36) break;
      const int own = __builtin_amdgcn_readfirstlane(ctrl[1]);
      constexpr unsigned SM = (8192u >> SLOTSH) - 1u;
      if (!NOPST && blockIdx.x < 16 && lane < 8) __builtin_amdgcn_raw_buffer_store_b128(u32x4{(unsigned)own, (unsigned)own, (unsigned)own, (unsigned)own}, wsr, WS_PACE + (k & SM) * 2048 + blockIdx.x * 128 + lane * 16, 0, PAUX);
      if (it < PLAG || NOPLD) continue;
      int e[16];
      asm volatile("s_load_dword %0, %16, 0x0\n\ts_load_dword %1, %16, 0x80\n\ts_load_dword %2, %16, 0x100\n\ts_load_dword %3, %16, 0x180\n\t"
                   "s_load_dword %4, %16, 0x200\n\ts_load_dword %5, %16, 0x280\n\ts_load_dword %6, %16, 0x300\n\ts_load_dword %7, %16, 0x380\n\t"
                   "s_load_dword %8, %16, 0x400\n\ts_load_dword %9, %16, 0x480\n\ts_load_dword %10, %16, 0x500\n\ts_load_dword %11, %16, 0x580\n\t"
                   "s_load_dword %12, %16, 0x600\n\ts_load_dword %13, %16, 0x680\n\ts_load_dword %14, %16, 0x700\n\ts_load_dword %15, %16, 0x780\n\t"
                   "s_waitcnt lgkmcnt(0)"
                   : "=&s"(e[0]), "=&s"(e[1]), "=&s"(e[2]), "=&s"(e[3]), "=&s"(e[4]), "=&s"(e[5]), "=&s"(e[6]), "=&s"(e[7]),
                     "=&s"(e[8]), "=&s"(e[9]), "=&s"(e[10]), "=&s"(e[11]), "=&s"(e[12]), "=&s"(e[13]), "=&s"(e[14]), "=&s"(e[15])
                   : "s"(ws + WS_PACE + ((k - PLAG) & SM) * 2048) : "memory");
      int sum = 0, cnt = 0;
#pragma unroll
      for (int i = 0; i < 16; ++i) { const int d = e[i] - own; const bool ok = (unsigned)(d + 600) < 1200u; sum += ok ? d : 0; cnt += ok ? 1 : 0; }
      if (cnt >= 8) ctrl[2] = own + (int)((float)sum / (float)cnt);
    }
    return;
  }

  if (wave == XWAVE) {
#pragma unroll 1
    for (int i = 0; i < XDEL + XSPR * (kk & 31); ++i) PAUSE(4);
    const u32x4 xv = *(const GAS u32x4*)(x + b * D + 8 * lane), gv = *(const GAS u32x4*)(gw + 8 * lane);
    *(LAS u32x4*)(L + L_X + lane * 16) = xv;
    float gd = 0.f;
#pragma unroll
    for (int k = 0; k < 4; ++k) gd = dot2(xv[k], gv[k], gd);
    gd = xrow_sum(row16_sum(gd));
    const float g = 1.f / (1.f + __expf(-gd));
    ctrlf[48] = g;
    lds_arrive(L, C_X, lane);
    u64 tag;
    asm volatile("s_load_dwordx2 %0, %1, 0x0\n\ts_waitcnt lgkmcnt(0)" : "=s"(tag) : "s"(ws + WS_EPOCH + b * 256) : "memory");
    tag += TAG_STEP;
#pragma unroll 1
    for (int spin = 0; spin < SPIN_CAP; ++spin) { if (__builtin_amdgcn_readfirstlane(ctrl[C_MAX]) >= NCW + NHW) break; __builtin_amdgcn_s_sleep(16); }
    CFENCE();
    const float mg = wg_max();
    float l = 0.f;
#pragma unroll 4
    for (int i = 0; i < NKU * 64 / 1024; ++i) {
      const f32x4 sc = *(const LAS f32x4*)(L + L_SC + i * 1024 + lane * 16);
      l += (__builtin_amdgcn_exp2f(sc[0] - mg) + __builtin_amdgcn_exp2f(sc[1] - mg)) + (__builtin_amdgcn_exp2f(sc[2] - mg) + __builtin_amdgcn_exp2f(sc[3] - mg));
    }
    l = xrow_sum(row16_sum(l));
    const unsigned tlo = (unsigned)tag, thi = (unsigned)(tag >> 32);
    if (lane < 8) __builtin_amdgcn_raw_buffer_store_b128(u32x4{__float_as_uint(mg), tlo, __float_as_uint(l), thi}, wsr, WS_REC + (b * 64 + pj) * 128 + lane * 16, 0, 16);
#pragma unroll 1
    for (int i = 0; i < 12 + (kk & 31); ++i) PAUSE(12);
    u32x4 rec;
#pragma unroll 1
    for (int spin = 0; spin < SPIN_CAP; ++spin) {
      CFENCE();
      rec = __builtin_amdgcn_raw_buffer_load_b128(wsr, WS_REC + (b * 64 + (pj ^ 1)) * 128, 0, 16);
      if (__builtin_amdgcn_ballot_w64(rec[1] != tlo || rec[3] != thi) == 0ull) break;
      __builtin_amdgcn_s_sleep(16);
    }
    const float mo = __uint_as_float(rec[0]), lo = __uint_as_float(rec[2]);
    const float mh = fmaxf(mg, mo), wt = __builtin_amdgcn_exp2f(mg - mh);
    ctrlf[49] = g * wt / (wt * l + __builtin_amdgcn_exp2f(mo - mh) * lo);
    ctrl[50] = (int)(tlo & YMASK);
    lds_arrive(L, C_CF, lane);
    GAS unsigned* op = (GAS unsigned*)(out + b * D + 8 * pj + 4 * (lane & 1) + 2 * (lane >> 5));
    int off = WS_Y + (b * 64 + (lane >> 1)) * 2048 + pj * 32 + (lane & 1) * 16;
    asm volatile("" : "+v"(op), "+v"(off));
    if ((pj & (64 / NRED - 1)) != 0) return;
    STAMP(4);
    BARRIER();
    {
      const unsigned now = (unsigned)__builtin_amdgcn_s_memrealtime();
      const int d = __builtin_amdgcn_readfirstlane(ctrl[2]) + (int)(TS * 100) + FOFF - (int)now;
      const unsigned t = now + (unsigned)min(max(d, FMIN), FMAX);
#pragma unroll 1
      for (int spin = 0; spin < 4096 && (int)(t - (unsigned)__builtin_amdgcn_s_memrealtime()) > 0; ++spin) {}
    }
    STAMP(9);
    constexpr int NL = 128 / NRED;
    u32x4 yv[NL];
#pragma unroll 1
    for (int spin = 0; spin < SPIN_CAP; ++spin) {
      CFENCE();
      unsigned bad = 0;
#pragma unroll
      for (int i = 0; i < NL; ++i) yv[i] = __builtin_amdgcn_raw_buffer_load_b128(wsr, off, i * (64 / NL) * 2048, 16);
#pragma unroll
      for (int i = 0; i < NL; ++i) bad |= ((yv[i][0] ^ tlo) | (yv[i][1] ^ tlo)) | ((yv[i][2] ^ tlo) | (yv[i][3] ^ tlo));
      bad &= YMASK;
      { const u64 bal = __builtin_amdgcn_ballot_w64(bad != 0u); if (spin < 5) STAMP(10 + spin); ((volatile LAS unsigned*)(L + LDS_BYTES))[wave * 16 + 15] = spin + 1 + (bal == 1234567 ? 1 : 0); }
      if (__builtin_amdgcn_ballot_w64(bad != 0u) == 0ull) break;
      if (PSLP) __builtin_amdgcn_s_sleep(PSLP);
    }
    float o[4];
#pragma unroll
    for (int k = 0; k < 4; ++k) {
      float t[NL];
#pragma unroll
      for (int i = 0; i < NL; ++i) t[i] = __uint_as_float(yv[i][k] & ~YMASK);
#pragma unroll
      for (int st = NL / 2; st >= 1; st /= 2)
#pragma unroll
        for (int i = 0; i < st; ++i) t[i] += t[i + st];
      o[k] = t[0];
    }
    float e0 = swadd32(o[0], o[2]), e1 = swadd32(o[1], o[3]);
    e0 = swadd16(e0, e0); e1 = swadd16(e1, e1);
    e0 += dpp<ROR8>(e0); e1 += dpp<ROR8>(e1); e0 += dpp<ROR4>(e0); e1 += dpp<ROR4>(e1); e0 += dpp<ROR2>(e0); e1 += dpp<ROR2>(e1);
    if ((lane & 30) == 0) OUT_STORE(op, pack_bf16(e0, e1));
    if (pj == 0 && lane < 8) __builtin_amdgcn_raw_buffer_store_b128(u32x4{tlo, thi, tlo, thi}, wsr, WS_EPOCH + b * 256 + lane * 16, 0, 16);
    STAMP(7);
    VM_DRAIN();
    STAMP(8);
    STAMP_FLUSH();
    return;
  }

  const int cw = wave - 1, hw = wave - 1 - NCW;
  int landed = 0;
  auto wait_unit = [&](int u) __attribute__((always_inline)) {
#pragma unroll 1
    for (int spin = 0; landed <= u && spin < SPIN_CAP; ++spin) {
      landed = __builtin_amdgcn_readfirstlane(ctrl[0]);
      if (landed > u) break;
      if (USL) __builtin_amdgcn_s_sleep(USL);
    }
    CFENCE();
  };
  auto take = [&](int u, int next, u32x4& a0, u32x4& a1, u32x4& a2, u32x4& a3) __attribute__((always_inline)) {
    wait_unit(u);
    const LAS u32x4* sp = (const LAS u32x4*)(L + (u & (NSLOT - 1)) * UNIT + lane * 16);
    a0 = sp[0]; a1 = sp[64]; a2 = sp[128]; a3 = sp[192];
    LDS_WAIT();
    ctrl[16 + cw] = next;
    asm volatile("" : "+v"(a0), "+v"(a1), "+v"(a2), "+v"(a3) :: "memory");
  };
  u32x4 q;
  auto build_q = [&]() __attribute__((always_inline)) {
    lds_await(ctrl, C_Q, NHW);
    const LAS f32x4* qp = (const LAS f32x4*)(L + L_QP + (2 * r4 * DH + 8 * c16) * 4);
    const f32x4 lo = qp[0] + qp[DH / 4], hi = qp[1] + qp[DH / 4 + 1];
    q[0] = pack_bf16(xrow_sum(lo[0]), xrow_sum(lo[1])); q[1] = pack_bf16(xrow_sum(lo[2]), xrow_sum(lo[3]));
    q[2] = pack_bf16(xrow_sum(hi[0]), xrow_sum(hi[1])); q[3] = pack_bf16(xrow_sum(hi[2]), xrow_sum(hi[3]));
  };
  constexpr float SCALE = 0.088388347648318440f, LOG2E = 1.4426950408889634f, C2 = SCALE * LOG2E;
  float mx = -INFINITY;
  auto score = [&](int u, const u32x4& k0, const u32x4& k1, const u32x4& k2, const u32x4& k3) __attribute__((always_inline)) {
    float d0 = 0.f, d1 = 0.f, d2 = 0.f, d3 = 0.f;
#pragma unroll
    for (int k = 0; k < 4; ++k) { d0 = dot2(k0[k], q[k], d0); d1 = dot2(k1[k], q[k], d1); d2 = dot2(k2[k], q[k], d2); d3 = dot2(k3[k], q[k], d3); }
    const unsigned s01 = pack_bf16(row16_sum(d0), row16_sum(d1)), s23 = pack_bf16(row16_sum(d2), row16_sum(d3));
    const unsigned t01 = pack_bf16(bflo(s01) * SCALE, bfhi(s01) * SCALE), t23 = pack_bf16(bflo(s23) * SCALE, bfhi(s23) * SCALE);
    const f32x4 sc = {bflo(t01) * LOG2E, bfhi(t01) * LOG2E, bflo(t23) * LOG2E, bfhi(t23) * LOG2E};
    mx = fmaxf(fmaxf(mx, fmaxf(sc[0], sc[1])), fmaxf(sc[2], sc[3]));
    if (c16 == 0) *(LAS f32x4*)(L + L_SC + u * 64 + r4 * 16) = sc;
  };

  if (hw < 0) {
    {
      u32x4 hk[4 * NHC];
#pragma unroll
      for (int i = 0; i < NHC; ++i) take(cw + NCW * i, i + 1 < NHC ? cw + NCW * (i + 1) : NHELD + cw, hk[4 * i], hk[4 * i + 1], hk[4 * i + 2], hk[4 * i + 3]);
      STAMP(10);
      build_q();
      STAMP(3);
#pragma unroll
      for (int i = 0; i < NHC; ++i) { score(cw + NCW * i, hk[4 * i], hk[4 * i + 1], hk[4 * i + 2], hk[4 * i + 3]); __builtin_amdgcn_sched_barrier(0); }
    }
#pragma unroll 1
    for (int u = NHELD + cw; u < NKU; u += NCW) {
      u32x4 k0, k1, k2, k3;
      take(u, u + NCW, k0, k1, k2, k3);
      score(u, k0, k1, k2, k3);
    }
    ctrlf[32 + cw] = xrow_max(mx);
    lds_arrive(L, C_MAX, lane);
    STAMP(11);
    lds_await(ctrl, C_MAX, NCW + NHW);
    const float mg = wg_max();
    STAMP(12);
    float acc[8] = {0.f, 0.f, 0.f, 0.f, 0.f, 0.f, 0.f, 0.f};
    LAS unsigned* rp = (LAS unsigned*)(L + L_REC + cw * 272 + (8 * c16 + 2 * r4) * 2);
    asm volatile("" : "+v"(rp));
    float cf = 0.f;
#pragma unroll 1
    for (int u = NKU + cw; u < NUNIT; u += NCW) {
      const f32x4 sc = *(const LAS f32x4*)(L + L_SC + (u - NKU) * 64 + r4 * 16);
      float p[4];
#pragma unroll
      for (int t = 0; t < 4; ++t) { p[t] = __builtin_amdgcn_exp2f(sc[t] - mg); asm volatile("" : "+v"(p[t])); }
      if ((u & ~(NCW - 1)) == NUNIT - 32) { lds_await(ctrl, C_CF, 1); cf = ctrlf[49]; }
      u32x4 v[4];
      take(u, u + NCW, v[0], v[1], v[2], v[3]);
#pragma unroll
      for (int t = 0; t < 4; ++t)
#pragma unroll
        for (int k = 0; k < 4; ++k) { acc[2 * k] = fmaf(p[t], bflo(v[t][k]), acc[2 * k]); acc[2 * k + 1] = fmaf(p[t], bfhi(v[t][k]), acc[2 * k + 1]); }
    }
    STAMP(5);
    const float e0 = swadd16(swadd32(acc[0], acc[4]), swadd32(acc[2], acc[6])), e1 = swadd16(swadd32(acc[1], acc[5]), swadd32(acc[3], acc[7]));
    *rp = pack_bf16(e0 * cf, e1 * cf);
    BARRIER();
    return;
  }

  u32x4 wb[16];
  const unsigned tstart = (unsigned)__builtin_amdgcn_readfirstlane(ctrl[8]);
  {
    const __amdgpu_buffer_rsrc_t rq = mkrsrc(wq, D * H * DH * 2);
    const int vo = ((64 * hw + 16 * r4) * (H * DH) + h * DH + 8 * c16) * 2;
#pragma unroll
    for (int i = 0; i < 16; ++i) { if (i == 0) wait_until(tstart + TQ + hw * QA); else PAUSE(QS); wb[i] = ldw(rq, vo, i * (H * DH * 2)); }
    lds_await(ctrl, C_X, 1);
    const u32x4 xa = *(const LAS u32x4*)(L + L_X + (64 * hw + 16 * r4) * 2), xc = *(const LAS u32x4*)(L + L_X + (64 * hw + 16 * r4 + 8) * 2);
    float a[8] = {0.f, 0.f, 0.f, 0.f, 0.f, 0.f, 0.f, 0.f};
#pragma unroll
    for (int i = 0; i < 8; ++i) {
      const unsigned xp = i < 4 ? xa[i] : xc[i - 4];
#pragma unroll
      for (int k = 0; k < 4; ++k) {
        a[2 * k] = dot2(perm_lo(wb[2 * i][k], wb[2 * i + 1][k]), xp, a[2 * k]);
        a[2 * k + 1] = dot2(perm_hi(wb[2 * i][k], wb[2 * i + 1][k]), xp, a[2 * k + 1]);
      }
    }
    const float e0 = swadd16(swadd32(a[0], a[4]), swadd32(a[2], a[6])), e1 = swadd16(swadd32(a[1], a[5]), swadd32(a[3], a[7]));
    *(LAS f32x2*)(L + L_QP + (hw * DH + 8 * c16 + 2 * r4) * 4) = f32x2{e0, e1};
    lds_arrive(L, C_Q, lane);
    STAMP(2);
    const int hb = NHC * NCW + hw;
#pragma unroll
    for (int i = 0; i < NHH; ++i) take(hb + NHW * i, i + 1 < NHH ? hb + NHW * (i + 1) : NEVER, wb[4 * i], wb[4 * i + 1], wb[4 * i + 2], wb[4 * i + 3]);
    STAMP(10);
    build_q();
#pragma unroll
    for (int i = 0; i < NHH; ++i) { score(hb + NHW * i, wb[4 * i], wb[4 * i + 1], wb[4 * i + 2], wb[4 * i + 3]); __builtin_amdgcn_sched_barrier(0); }
    ctrlf[32 + cw] = xrow_max(mx);
    lds_arrive(L, C_MAX, lane);
  }
  f32x4 y[4] = {{0.f, 0.f, 0.f, 0.f}, {0.f, 0.f, 0.f, 0.f}, {0.f, 0.f, 0.f, 0.f}, {0.f, 0.f, 0.f, 0.f}};
  if ((pj & 7) != 7) {
    const int sl = 7 * (pj >> 3) + (pj & 7);
    const __amdgpu_buffer_rsrc_t r1 = mkrsrc(w1, D * F * 2), r3 = mkrsrc(w3, D * F * 2), r2 = mkrsrc(w2, F * D * 2);
    const int vo = ((64 * hw + (lane & 60)) * F + FS * sl + 8 * (lane & 3)) * 2;
#pragma unroll
    for (int i = 0; i < 8; ++i) { if (i == 0) wait_until(tstart + T1 + hw * A1); else PAUSE(S1); wb[i] = ldw(i < 4 ? r1 : r3, vo, (i & 3) * (F * 2)); }
    const u32x2 xm = *(const LAS u32x2*)(L + L_X + (64 * hw + (lane & 60)) * 2);
    float a[16];
#pragma unroll
    for (int m = 0; m < 2; ++m)
#pragma unroll
      for (int k = 0; k < 4; ++k) {
        a[8 * m + 2 * k] = dot2(perm_lo(wb[4 * m + 2][k], wb[4 * m + 3][k]), xm[1], dot2(perm_lo(wb[4 * m][k], wb[4 * m + 1][k]), xm[0], 0.f));
        a[8 * m + 2 * k + 1] = dot2(perm_hi(wb[4 * m + 2][k], wb[4 * m + 3][k]), xm[1], dot2(perm_hi(wb[4 * m][k], wb[4 * m + 1][k]), xm[0], 0.f));
      }
    f32x4 e;
#pragma unroll
    for (int k = 0; k < 4; ++k) {
      float t = swadd16(swadd32(a[k], a[8 + k]), swadd32(a[4 + k], a[12 + k]));
      t += dpp<ROR8>(t); e[k] = t + dpp<ROR4>(t);
    }
    if ((lane & 12) == 0) *(LAS f32x4*)(L + L_MP + (hw * 64 + (lane & 32) + 8 * (lane & 3) + 4 * (r4 & 1)) * 4) = e;
    lds_arrive(L, C_MLP, lane);
    u32x2 wr[8];
#pragma unroll
    for (int i = 0; i < 8; ++i) { if (i == 0) wait_until(tstart + T2 + hw * A2); else PAUSE(S2); wr[i] = ldw2(r2, ((FS * sl + 8 * r4) * D + 64 * hw + 4 * c16) * 2, i * (D * 2)); }
    lds_await(ctrl, C_MLP, NHW);
    const LAS float* mp = (const LAS float*)(L + L_MP) + (lane & 31);
    float ga = 0.f, up = 0.f;
#pragma unroll
    for (int w = 0; w < NHW; ++w) { ga += mp[w * 64]; up += mp[w * 64 + 32]; }
    const float hv = ga / (1.f + __expf(-ga)) * up * (1.f - ctrlf[48]);
    const unsigned hh = pack_bf16(hv, 0.f), hl = pack_bf16(hv - bflo(hh), 0.f);
    *(LAS u16*)(L + L_H + hw * 128 + lane * 2) = (u16)(lane < 32 ? hh : hl);
    LDS_WAIT();
    u32x4 ha = *(const LAS u32x4*)(L + L_H + hw * 128 + (c16 & 1) * 64 + r4 * 16);
    if (c16 >= 2) ha = u32x4{0u, 0u, 0u, 0u};
#pragma unroll
    for (int e = 0; e < 4; ++e) {
      u32x4 f;
#pragma unroll
      for (int k = 0; k < 4; ++k) f[k] = (e & 1) ? perm_hi(wr[2 * k][e >> 1], wr[2 * k + 1][e >> 1]) : perm_lo(wr[2 * k][e >> 1], wr[2 * k + 1][e >> 1]);
      y[e] = mfma(ha, f, y[e]);
    }
  }
#pragma unroll
  for (int e = 0; e < 4; ++e) asm volatile("" : "+v"(y[e]));
  {
    const __amdgpu_buffer_rsrc_t ro = mkrsrc(wo, H * DH * D * 2);
#pragma unroll
    for (int t = 0; t < 4; ++t) {
      u32x2 wr[8];
#pragma unroll
      for (int i = 0; i < 8; ++i) { if (t + i == 0) wait_until(tstart + TO + hw * AO); else PAUSE(SO); wr[i] = ldw2(ro, ((h * DH + 32 * r4) * D + 64 * hw + 4 * c16) * 2, (8 * t + i) * (D * 2)); }
#pragma unroll
      for (int e = 0; e < 4; ++e) {
#pragma unroll
        for (int k = 0; k < 4; ++k) wb[4 * t + e][k] = (e & 1) ? perm_hi(wr[2 * k][e >> 1], wr[2 * k + 1][e >> 1]) : perm_lo(wr[2 * k][e >> 1], wr[2 * k + 1][e >> 1]);
        asm volatile("" : "+v"(wb[4 * t + e]));
      }
    }
  }
  lds_await(ctrl, C_CF, 1);
  unsigned yt = (unsigned)ctrl[50];
  int yo = WS_Y + (b * 64 + pj) * 2048 + hw * 256 + c16 * 16;
  const LAS u32x4* rec = (const LAS u32x4*)(L + L_REC + (lane & 3) * 272 + r4 * 64);
  unsigned ym = ~YMASK;
  asm volatile("" : "+v"(yt), "+v"(yo), "+v"(rec), "+s"(ym));
  STAMP(4);
  BARRIER();
  STAMP(9);
  {
    const u32x4 a[4] = {rec[0], rec[1], rec[2], rec[3]};
#pragma unroll
    for (int t = 0; t < 4; ++t)
#pragma unroll
      for (int e = 0; e < 4; ++e) y[e] = mfma(a[t], wb[4 * t + e], y[e]);
    u32x4 v;
#pragma unroll
    for (int e = 0; e < 4; ++e) {
      const f32x2 t = y[e].xy + y[e].zw;
      unsigned r;
      asm("v_add_f32 %0, %1, %2\n\tv_add_u32 %0, %5, %0\n\tv_and_or_b32 %0, %0, %3, %4" : "=&v"(r) : "v"(t[0]), "v"(t[1]), "s"(ym), "v"(yt), "i"((YMASK + 1u) / 2u));
      v[e] = r;
    }
    if (lane < 16) __builtin_amdgcn_raw_buffer_store_b128(v, wsr, yo, 0, 16);
  }
  STAMP(7);
}

extern "C" void kernel_launch(void* const* d_in, const int* in_sizes, int n_in, void* d_out, int out_size, void* d_ws, size_t ws_size,
                              hipStream_t stream) {
  gated_mix_kernel<<<256, NTHR, 0, stream>>>((const u16*)d_in[0], (const u16*)d_in[1], (const u16*)d_in[2], (const u16*)d_in[3], (const u16*)d_in[4],
                                             (const u16*)d_in[6], (const u16*)d_in[7], (const u16*)d_in[8], (const u16*)d_in[9], (u16*)d_out,
                                             (unsigned char*)d_ws);
}
```

```cpp
#include <hip/hip_runtime.h>
#include <hip/hip_bf16.h>

#define GAS __attribute__((address_space(1)))
#define LAS __attribute__((address_space(3)))
typedef unsigned short u16;
typedef unsigned long long u64;
using u32x4 = __attribute__((ext_vector_type(4))) unsigned;
using u32x2 = __attribute__((ext_vector_type(2))) unsigned;
using f32x4 = __attribute__((ext_vector_type(4))) float;
using f32x2 = __attribute__((ext_vector_type(2))) float;
using bf16x2 = __attribute__((ext_vector_type(2))) __bf16;
using bf16x8 = __attribute__((ext_vector_type(8))) __bf16;

constexpr int B = 4, D = 512, H = 32, SK = 8192, DH = 128, F = 1792;
constexpr int NCW = 4, NHW = 8, XWAVE = 1 + NCW + NHW, NWAVE = XWAVE + 2, NTHR = NWAVE * 64;
#define QA 80
#define QB 5
#define T1 1200
#define A1 150
#define B1 18
#define T2 2500
#define A2 40
#define B2 4
#define TO 2900
#define AO 350
#define BO 8
#define TS 70.6
#define REF0 180
#define PLAG 1
#define NOPST 0
#define NOPLD 0
#define PAUX 16
#define RAUX 16
#define PFW 0
#define TQ 130
#define NINIT 6
#define CODE_KIB 21
#define FDEL 0
#define CODEPF 1
#define YST 16
#define YLD 16
#define RED16 0
#define MASKP 0
#define PSLP 0
#define OUTSC 0
#define OUT_STORE(p, v) *(GAS unsigned*)(p) = v
#define CLR0 1
#define MLPFULL 0
#define MLPPF 0
#define TP 1200
#define AP 12
#define GRID 0
#define B2BD 0
#define B2BAUX 16
#define WOB2B 0
#define TOUCHW 1
#define W0 30
#define BARPOS 12
#define NO_WO 0
#define TLBPF 0
#define TMODE 0
#define SPW 4
#define QS 2
#define S1 6
#define S2 1
#define SO 2
#define USL 0
#define JUMP 100
#define DECAY 6
#define HOLD0 10000
#define UEND 420
#define WGAIN2 18
#define WMID2 WMID
#define FOFF 150
#define FMIN 45
#define FMAX 150
#define NRED 64
#define SLOTSH 8
#define WMID 20.5
#define WGAIN 8
#define WMIN 14
#define WMAX 27
#define CLRAUX 16
#define PCONT 1
#define XDEL 20
#define XSPR 0
#define XNT 0
#define MIMIC 1
#define NCLR 2
#define NOFLAG 0
#define CSPR 0
constexpr int UNIT = 4096;
constexpr int NSLOT = 32, RING = NSLOT * UNIT;
constexpr int NKU = (SK / 2) / 16, NUNIT = 2 * NKU;
constexpr int NHC = 6, NHH = 4, NHELD = NHC * NCW + NHH * NHW;
constexpr int FS = 32;
constexpr int L_CTRL = RING;
constexpr int L_X = L_CTRL + 256;
constexpr int L_QP = L_X + D * 2;
constexpr int L_MP = L_QP + NHW * DH * 4;
constexpr int L_REC = L_MP + NHW * 64 * 4;
constexpr int L_H = L_REC + NCW * 272;
constexpr int L_SC = L_H + NHW * 128;
constexpr int LDS_BYTES = L_SC + NKU * 64;
static_assert(LDS_BYTES <= 159 * 1024, "lds");
static_assert(WMIN <= (int)WMID && (int)WMID <= WMAX && WMAX <= 28, "window");
constexpr int WS_EPOCH = 0;
constexpr int WS_REC = 4096;
constexpr int WS_PACE = WS_REC + 256 * 128;
constexpr int WS_Y = WS_PACE + 64 * 2048;
constexpr int WS_BYTES = WS_Y + B * 64 * 2048;
constexpr u64 TAG_STEP = 0x9E3779B97F4A7C15ull;
constexpr unsigned YMASK = 0xFFFu;
constexpr int SPIN_CAP = 1 << 18;
constexpr int NEVER = 1 << 20;
constexpr int C_Q = 4, C_MAX = 5, C_MLP = 6, C_X = 7, C_CF = 9;

__device__ __forceinline__ float bflo(unsigned u) { return __builtin_bit_cast(float, u << 16); }
__device__ __forceinline__ float bfhi(unsigned u) { return __builtin_bit_cast(float, u & 0xffff0000u); }
__device__ __forceinline__ float dot2(unsigned a, unsigned b, float acc) {
  return __builtin_amdgcn_fdot2_f32_bf16(__builtin_bit_cast(bf16x2, a), __builtin_bit_cast(bf16x2, b), acc, false);
}
__device__ __forceinline__ unsigned pack_bf16(float lo, float hi) {
  const bf16x2 v = {(__bf16)lo, (__bf16)hi};
  return __builtin_bit_cast(unsigned, v);
}
__device__ __forceinline__ unsigned perm_lo(unsigned a, unsigned b) { return __builtin_amdgcn_perm(b, a, 0x05040100u); }
__device__ __forceinline__ unsigned perm_hi(unsigned a, unsigned b) { return __builtin_amdgcn_perm(b, a, 0x07060302u); }
template <int CTRL> __device__ __forceinline__ float dpp(float x) {
  return __builtin_bit_cast(float, __builtin_amdgcn_mov_dpp(__builtin_bit_cast(int, x), CTRL, 0xf, 0xf, true));
}
constexpr int XOR1 = 0xB1, ROR1 = 0x121, ROR2 = 0x122, ROR4 = 0x124, ROR8 = 0x128;
__device__ __forceinline__ float row16_sum(float x) { x += dpp<ROR8>(x); x += dpp<ROR4>(x); x += dpp<ROR2>(x); x += dpp<ROR1>(x); return x; }
__device__ __forceinline__ float swadd32(float a, float b) {
  auto t = __builtin_amdgcn_permlane32_swap(__float_as_uint(a), __float_as_uint(b), false, false);
  return __uint_as_float(t[0]) + __uint_as_float(t[1]);
}
__device__ __forceinline__ float swadd16(float a, float b) {
  auto t = __builtin_amdgcn_permlane16_swap(__float_as_uint(a), __float_as_uint(b), false, false);
  return __uint_as_float(t[0]) + __uint_as_float(t[1]);
}
__device__ __forceinline__ float xrow_sum(float x) { x = swadd16(x, x); return swadd32(x, x); }
__device__ __forceinline__ float xrow_max(float x) {
  auto s = __builtin_amdgcn_permlane16_swap(__float_as_uint(x), __float_as_uint(x), false, false);
  x = fmaxf(__uint_as_float(s[0]), __uint_as_float(s[1]));
  auto t = __builtin_amdgcn_permlane32_swap(__float_as_uint(x), __float_as_uint(x), false, false);
  return fmaxf(__uint_as_float(t[0]), __uint_as_float(t[1]));
}
__device__ __forceinline__ __amdgpu_buffer_rsrc_t mkrsrc(const void* p, int bytes) { return __builtin_amdgcn_make_buffer_rsrc((void*)p, 0, bytes, 0x00020000); }
__device__ __forceinline__ u32x4 ldw(__amdgpu_buffer_rsrc_t r, int voff, int soff) { return __builtin_amdgcn_raw_buffer_load_b128(r, voff, soff, 2); }
#define LDS_WAIT() asm volatile("s_waitcnt lgkmcnt(0)" ::: "memory")
#define VM_DRAIN() asm volatile("s_waitcnt vmcnt(0)" ::: "memory")
#define CFENCE() asm volatile("" ::: "memory")
#define BARRIER() asm volatile("s_waitcnt lgkmcnt(0)\n\ts_barrier" ::: "memory")
#define PAUSE(n) do { __builtin_amdgcn_sched_barrier(0); asm volatile("s_sleep %0" :: "i"(n) : "memory"); __builtin_amdgcn_sched_barrier(0); } while (0)

__device__ __forceinline__ void lds_arrive(LAS unsigned char* L, int idx, int lane) {
  LDS_WAIT();
  if (lane == 0) __hip_atomic_fetch_add((LAS int*)(L + L_CTRL) + idx, 1, __ATOMIC_RELAXED, __HIP_MEMORY_SCOPE_WORKGROUP);
}
__device__ __forceinline__ void lds_await(volatile LAS int* ctrl, int idx, int n) {
#pragma unroll 1
  for (int spin = 0; spin < SPIN_CAP; ++spin) { if (__builtin_amdgcn_readfirstlane(ctrl[idx]) >= n) break; __builtin_amdgcn_s_sleep(1); }
  CFENCE();
}

__device__ __forceinline__ void wait_until(unsigned t) {
  __builtin_amdgcn_sched_barrier(0);
#pragma unroll 1
  for (int spin = 0; spin < 4096; ++spin) {
    const int d = (int)(t - (unsigned)__builtin_amdgcn_s_memrealtime());
    if (d <= 0) break;
    if (d > 24) __builtin_amdgcn_s_sleep(6);
  }
  CFENCE();
  __builtin_amdgcn_sched_barrier(0);
}
#define STR2(x) #x
#define STR(x) STR2(x)
constexpr int TAU2 = 2 * (int)(TS * 100.0 * 256.0 / NUNIT);
__device__ __forceinline__ f32x4 mfma(const u32x4& a, const u32x4& b, const f32x4& c) {
  return __builtin_amdgcn_mfma_f32_16x16x32_bf16(__builtin_bit_cast(bf16x8, a), __builtin_bit_cast(bf16x8, b), c, 0, 0, 0);
}
__device__ __forceinline__ u32x2 ldw2(__amdgpu_buffer_rsrc_t r, int voff, int soff) { return __builtin_amdgcn_raw_buffer_load_b64(r, voff, soff, 2); }
template <int CTRL> __device__ __forceinline__ int dppi(int x) { return __builtin_amdgcn_mov_dpp(x, CTRL, 0xf, 0xf, true); }

#define STAMP(i) do {} while (0)
#define STAMP_FLUSH() do {} while (0)
#define STAMP_LDS 0

__global__ void __launch_bounds__(NTHR) gated_mix_kernel(const u16* x, const u16* kc, const u16* vc, const u16* wq, const u16* wo, const u16* w1,
                                                         const u16* w3, const u16* w2, const u16* gw, u16* out, unsigned char* ws) {
  __shared__ __attribute__((aligned(16))) unsigned char lds_raw[LDS_BYTES + STAMP_LDS];
  LAS unsigned char* L = (LAS unsigned char*)lds_raw;
  volatile LAS int* ctrl = (volatile LAS int*)(L + L_CTRL);
  volatile LAS float* ctrlf = (volatile LAS float*)(L + L_CTRL);
#define DBG(x) ""
  {
    unsigned va, vb, vo, pa, nxa, t0, t1, t2, t3;
#define CODELD "global_load_lds_dwordx4 %[t0], s[56:57]\n\t"
#define ASTAMP(i) DBG("s_memrealtime s[60:61]\n\ts_waitcnt lgkmcnt(0)\n\tv_mov_b32 %[t1], %c[dbg] + 4 * " #i "\n\tv_mov_b32 %[t2], s60\n\tds_write_b32 %[t1], %[t2]\n\t")
#define WAITW "s_waitcnt (wcnt & 15) | ((wcnt >> 4) << 14) | 0x0F70\n\t"
#define SP(v, sb, wait) wait "global_load_lds_dword " v ", " sb " nt\n\tglobal_load_lds_dword " v ", " sb " sc1\n\t"
#define SW "s_waitcnt vmcnt(%c[spw])\n\t"
#define UNIT4(v) \
        WAITW "global_load_lds_dwordx4 " v ", s[40:41] nt\n\t" \
        WAITW "global_load_lds_dwordx4 " v ", s[40:41] offset:1024 nt\n\t" \
        WAITW "global_load_lds_dwordx4 " v ", s[40:41] offset:2048 nt\n\t" \
        WAITW "global_load_lds_dwordx4 " v ", s[40:41] offset:3072 nt\n\t"
    asm volatile(
        "s_getpc_b64 s[56:57]\n\t"
        "s_load_dwordx8 s[44:51], %[ka], 0x18\n\t"
        "s_load_dwordx2 s[52:53], %[ka], 0x38\n\t"
        "s_load_dwordx4 s[40:43], %[ka], 0x8\n\t"
        "v_readfirstlane_b32 s54, %[tid]\n\t"
        "s_mov_b32 m0, %c[jk]\n\t"
        "s_lshr_b32 s59, %[bid], 3\n\t"
        "s_cmp_ge_u32 s54, 64\n\t"
        "s_cbranch_scc1 90f\n\t"
        "s_min_u32 s54, s59, %c[nch]\n\t"
        "s_lshl_b32 s54, s54, 10\n\t"
        "v_lshlrev_b32 %[vo], 4, %[tid]\n\t"
        "s_and_b32 s56, s56, -128\n\t"
        "v_add_u32 %[t0], s54, %[vo]\n\t"
        "s_and_b32 s58, %[bid], 7\n\t"
        CODELD
        "s_setprio 3\n\t"
        "v_lshlrev_b32 %[t1], 7, %[tid]\n\t"
        "s_lshl_b32 s54, s59, 7\n\t"
        "s_add_u32 s54, s54, s58\n\t"
        "s_lshl_b32 s54, s54, 10\n\t"
        "v_lshrrev_b32 %[va], 3, %[tid]\n\t"
        "v_mul_u32_u24 %[va], 8192 - 1024, %[va]\n\t"
        "v_add3_u32 %[va], %[va], %[t1], s54\n\t"
        "v_add_u32 %[t2], 0x10000, %[va]\n\t"
        "s_lshl_b32 s55, s58, 5\n\t"
        "s_add_u32 s55, s55, s59\n\t"
        "s_mul_i32 s54, s55, 7 * 1024\n\t"
        "v_min_u32 %[t3], 55 * 128, %[t1]\n\t"
        "v_add_u32 %[t3], s54, %[t3]\n\t"
        "s_mul_i32 s54, s58, 448\n\t"
        "s_and_b32 s54, s54, -128\n\t"
        "s_mul_i32 s57, s59, 16 * 3584\n\t"
        "s_add_u32 s54, s54, s57\n\t"
        "v_lshrrev_b32 %[t0], 2, %[tid]\n\t"
        "v_mul_u32_u24 %[t0], 3584 - 512, %[t0]\n\t"
        "v_add3_u32 %[t0], %[t0], %[t1], s54\n\t"
        "v_lshl_add_u32 %[vb], s55, 14, %[t1]\n\t"
        "v_add_u32 %[t1], 0x2000, %[vb]\n\t"
        "s_and_b32 s54, %[bid], 0xc0\n\t"
        "s_lshl3_add_u32 s54, s58, s54\n\t"
        "s_and_b32 s56, s59, 7\n\t"
        "s_add_u32 s54, s54, s56\n\t"
        "s_lshl_b32 s56, s54, 20\n\t"
        "s_waitcnt lgkmcnt(0)\n\t"
        SP("%[va]", "s[44:45]", "") SP("%[t2]", "s[44:45]", "") SP("%[t3]", "s[52:53]", "")
        ".if %c[spw] <= 4\n\ts_barrier\n\t.endif\n\t"
        SP("%[t0]", "s[48:49]", SW)
        ".if %c[spw] == 6\n\ts_barrier\n\t.endif\n\t"
        SP("%[t0]", "s[50:51]", SW)
        ".if %c[spw] == 8\n\ts_barrier\n\t.endif\n\t"
        SP("%[vb]", "s[46:47]", SW) SP("%[t1]", "s[46:47]", SW)
        ".if %c[spw] > 8\n\ts_barrier\n\t.endif\n\t"
        ASTAMP(6)
        "s_add_u32 s40, s40, s56\n\t"
        "s_addc_u32 s41, s41, 0\n\t"
        "s_add_u32 s42, s42, s56\n\t"
        "s_addc_u32 s43, s43, 0\n\t"
        "s_sub_u32 s42, s42, %c[nku] * 4096\n\t"
        "s_subb_u32 s43, s43, 0\n\t"
        ".set so, 0\n\t"
        ".set wcnt, %c[spw]\n\t"
        ".rept 4\n\t"
        "s_movk_i32 m0, so\n\t"
        ".if (so & 4096) == 0\n\t"
        WAITW
        ".set wcnt, wcnt + 6\n\t"
        ".else\n\t"
        "s_nop 0\n\t"
        ".endif\n\t"
        "global_load_lds_dwordx4 %[vo], s[40:41] nt\n\t"
        "global_load_lds_dwordx4 %[vo], s[40:41] offset:1024 nt\n\t"
        "global_load_lds_dwordx4 %[vo], s[40:41] offset:2048 nt\n\t"
        "global_load_lds_dwordx4 %[vo], s[40:41] offset:3072 nt\n\t"
        "v_add_u32 %[vo], 0x1000, %[vo]\n\t"
        ".set so, so + 4096\n\t"
        ".endr\n\t"
        ".if %c[spw] <= 4\n\t"
        "s_waitcnt vmcnt(16)\n\t"
        ".endif\n\t"
        ASTAMP(7)
        "s_getpc_b64 s[50:51]\n"
        "20:\n\t"
        "s_add_u32 s50, s50, 23f-20b-%c[wmin]*128\n\t"
        "s_addc_u32 s51, s51, -1\n\t"
        "v_mov_b32 %[pa], %c[ctl]\n\t"
        "v_and_b32 %[nxa], 0x3c0 >> 2, %[vo]\n\t"
        "v_lshrrev_b32 %[nxa], 2, %[nxa]\n\t"
        "v_add_u32 %[nxa], %c[ctl] + 64, %[nxa]\n\t"
        "s_memrealtime s[58:59]\n\t"
        "s_mov_b32 s62, 0\n\t"
        "s_mov_b32 s63, %c[gain]\n\t"
        "s_mov_b32 s64, %c[wmid]\n\t"
        "s_mov_b32 s44, 4\n\t"
        "s_mov_b32 s45, %c[wmid] >> 8\n\t"
        "s_mov_b32 s46, 0x4000\n\t"
        "s_mov_b32 s47, 2 * %c[tau2]\n\t"
        "s_mov_b32 s48, %c[nslot]\n\t"
        "s_mov_b32 s49, %c[nku]\n\t"
        "s_waitcnt lgkmcnt(0)\n\t"
        "s_mov_b32 s59, 10000\n"
        "22:\n\t"
        "s_memrealtime s[56:57]\n\t"
        "ds_read_b32 %[t3], %[pa] offset:8\n\t"
        "s_lshl_b32 s52, s45, 7\n\t"
        "s_add_u32 s52, s50, s52\n\t"
        "s_addc_u32 s53, s51, 0\n\t"
        "s_add_i32 s54, s45, -1\n\t"
        "s_lshr_b32 s54, s54, 2\n\t"
        "s_sub_i32 s54, s44, s54\n\t"
        "v_mov_b32 %[t1], s54\n\t"
        "v_add_u32 %[t0], 0x1000, %[vo]\n\t"
        "s_add_u32 s55, s46, 0x1000\n\t"
        "s_mov_b32 m0, s46\n\t"
        "s_setpc_b64 s[52:53]\n"
        "24:\n\t"
        "s_waitcnt lgkmcnt(0)\n\t"
        "v_readfirstlane_b32 s57, %[t3]\n\t"
        "v_add_u32 %[t1], 1, %[t1]\n\t"
        "s_lshr_b32 s54, s47, 8\n\t"
        "s_sub_u32 s56, s56, s54\n\t"
        "v_mov_b32 %[t2], s56\n\t"
        "s_sub_i32 s54, s56, s58\n\t"
        "s_mov_b32 s58, s56\n\t"
        "s_sub_i32 s56, s56, s57\n\t"
        "ds_write2_b32 %[pa], %[t1], %[t2] offset1:1\n\t"
        "s_add_i32 s59, s59, s54\n\t"
        "s_add_i32 s59, s59, %c[decay]\n\t"
        "s_cmp_gt_i32 s54, %c[jump]\n\t"
        "s_cselect_b32 s59, s62, s59\n\t"
        "s_min_i32 s62, s56, s59\n\t"
        "s_mul_i32 s56, s62, s63\n\t"
        "s_add_i32 s56, s56, s64\n\t"
        "s_ashr_i32 s56, s56, 8\n\t"
        "s_max_i32 s56, s56, %c[wmin]\n\t"
        "s_min_i32 s45, s56, %c[wmax]\n\t"
        "s_add_u32 s47, s47, %c[tau2]\n\t"
        "s_add_u32 s46, s46, 0x2000\n\t"
        "s_and_b32 s46, s46, %c[nslot] * 4096 - 1\n\t"
        "v_add_u32 %[vo], 0x2000, %[vo]\n\t"
        "s_add_i32 s44, s44, 2\n\t"
        "s_cmp_lt_i32 s44, s48\n\t"
        "s_cbranch_scc1 22b\n\t"
        DBG("s_add_i32 s54, s44, -1\n\ts_lshr_b32 s54, s54, 5\n\ts_lshl_b32 s54, s54, 2\n\ts_add_u32 s54, s54, %c[dbg] + 14 * 64\n\tv_mov_b32 %[t0], s54\n\tv_mov_b32 %[t1], s45\n\t"
            "ds_write2_b32 %[t0], %[t2], %[t1] offset1:16\n\t")
        "s_cmp_lt_i32 s44, s49\n\t"
        "s_cbranch_scc1 25f\n\t"
        "s_cmp_ge_i32 s44, 2 * %c[nku]\n\t"
        "s_cbranch_scc1 30f\n\t"
        "s_mov_b32 s49, 2 * %c[nku]\n\t"
        "s_mov_b64 s[40:41], s[42:43]\n"
        "25:\n\t"
        "s_cmp_ge_i32 s44, %c[uend]\n\t"
        "s_cselect_b32 s63, %c[gain2], s63\n\t"
        "s_cselect_b32 s64, %c[wmid2], s64\n\t"
        "s_mov_b32 s55, %c[cap]\n"
        "26:\n\t"
        "ds_read_b32 %[t0], %[nxa]\n\t"
        "s_waitcnt lgkmcnt(0)\n\t"
        "s_nop 1\n\t"
        "v_min_i32_dpp %[t0], %[t0], %[t0] row_ror:8 row_mask:0xf bank_mask:0xf\n\t"
        "s_nop 1\n\t"
        "v_min_i32_dpp %[t0], %[t0], %[t0] row_ror:4 row_mask:0xf bank_mask:0xf\n\t"
        "s_nop 1\n\t"
        "v_min_i32_dpp %[t0], %[t0], %[t0] row_ror:2 row_mask:0xf bank_mask:0xf\n\t"
        "s_nop 1\n\t"
        "v_min_i32_dpp %[t0], %[t0], %[t0] row_ror:1 row_mask:0xf bank_mask:0xf\n\t"
        "s_nop 1\n\t"
        "v_readfirstlane_b32 s48, %[t0]\n\t"
        "s_nop 1\n\t"
        "s_add_i32 s48, s48, %c[nslot]\n\t"
        "s_and_b32 s48, s48, -2\n\t"
        "s_min_i32 s48, s48, s49\n\t"
        "s_cmp_lt_i32 s44, s48\n\t"
        "s_cbranch_scc1 22b\n\t"
        "v_mov_b32 %[t1], s44\n\t"
        "s_waitcnt vmcnt(0)\n\t"
        "ds_write_b32 %[pa], %[t1]\n\t"
        "s_sleep 1\n\t"
        "s_sub_u32 s55, s55, 1\n\t"
        "s_cmp_lg_u32 s55, 0\n\t"
        "s_cbranch_scc1 26b\n\t"
        "s_branch 22b\n"
        "30:\n\t"
        ".set k, 6\n\t"
        ".rept 7\n\t"
        ".set wcnt, 4 * k\n\t"
        "v_mov_b32 %[t1], 2 * %c[nku] - k\n\t"
        WAITW
        "ds_write_b32 %[pa], %[t1]\n\t"
        ".set k, k - 1\n\t"
        ".endr\n\t"
        DBG("s_memrealtime s[56:57]\n\ts_waitcnt lgkmcnt(0)\n\tv_mov_b32 %[t0], %c[dbg] + 4\n\tv_mov_b32 %[t1], s56\n\tds_write_b32 %[t0], %[t1]\n\t")
        "s_waitcnt lgkmcnt(0)\n\t"
        "s_endpgm\n\t"
        ".p2align 7\n"
        "23:\n\t"
        ".set wcnt, %c[wmin]-1\n\t"
        ".rept %c[wmax] - %c[wmin] + 1\n\t"
        UNIT4("%[vo]")
        "ds_write_b32 %[pa], %[t1]\n\t"
        "s_mov_b32 m0, s55\n\t"
        UNIT4("%[t0]")
        "s_branch 24b\n\t"
        ".p2align 7\n\t"
        ".set wcnt, wcnt+1\n\t"
        ".endr\n"
        "90:\n\t"
        "s_waitcnt lgkmcnt(0)"
        : [va] "=&v"(va), [vb] "=&v"(vb), [vo] "=&v"(vo), [pa] "=&v"(pa), [nxa] "=&v"(nxa), [t0] "=&v"(t0), [t1] "=&v"(t1), [t2] "=&v"(t2), [t3] "=&v"(t3)
        : [tid] "v"(threadIdx.x), [bid] "s"(blockIdx.x), [ka] "s"(__builtin_amdgcn_kernarg_segment_ptr()), [nch] "i"(CODE_KIB - 1), [jk] "i"(RING - UNIT),
          [spw] "i"(SPW), [wmin] "i"(WMIN), [wmax] "i"(WMAX), [gain] "i"(WGAIN), [wmid] "i"((int)(WMID * 256)), [tau2] "i"(TAU2),
          [ctl] "i"(L_CTRL), [nslot] "i"(NSLOT), [nku] "i"(NKU), [cap] "i"(SPIN_CAP), [dbg] "i"(LDS_BYTES), [jump] "i"(JUMP), [decay] "i"(DECAY), [uend] "i"(UEND), [gain2] "i"(WGAIN2), [wmid2] "i"((int)(WMID2 * 256))
        : "memory", "scc", "s40", "s41", "s42", "s43", "s44", "s45", "s46", "s47", "s48", "s49", "s50", "s51", "s52", "s53", "s54", "s55", "s56", "s57", "s58", "s59", "s60", "s61", "s62", "s63", "s64");
  }
  __builtin_amdgcn_sched_barrier(0);
  const int tid = threadIdx.x, lane = tid & 63, wave = __builtin_amdgcn_readfirstlane(tid >> 6);
  const int xg = blockIdx.x & 7, kk = blockIdx.x >> 3;
  const int b = kk >> 3, pj = 8 * xg + (kk & 7), h = pj >> 1, s = pj & 1;
  const int r4 = lane >> 4, c16 = lane & 15;
  asm volatile("" : "+s"(x), "+s"(wq), "+s"(wo), "+s"(w1), "+s"(w3), "+s"(w2), "+s"(gw), "+s"(out), "+s"(ws));
  if (wave == 1) {
    const int wi = lane - 16;
    const int now = (int)(unsigned)__builtin_amdgcn_s_memrealtime() + REF0;
    ctrl[lane] = (lane == 1 || lane == 2) ? now : lane == 8 ? now - REF0 : (lane < 16 || lane >= 32) ? 0 : wi < NCW ? wi : wi < NCW + NHW ? NHC * NCW + wi - NCW : NEVER;
  }
  BARRIER();

  const __amdgpu_buffer_rsrc_t wsr = mkrsrc(ws, WS_BYTES);
  auto wg_max = [&]() __attribute__((always_inline)) {
    float mg = ctrlf[32 + (c16 < NCW + NHW ? c16 : 0)];
    mg = fmaxf(mg, dpp<ROR8>(mg)); mg = fmaxf(mg, dpp<ROR4>(mg)); mg = fmaxf(mg, dpp<ROR2>(mg)); mg = fmaxf(mg, dpp<ROR1>(mg));
    return mg;
  };

  if (wave == XWAVE + 1) {
    unsigned k = ((unsigned)__builtin_amdgcn_s_memrealtime() >> SLOTSH) + 1;
#pragma unroll 1
    for (int it = 0; it < 4096 >> (SLOTSH - 2); ++it, ++k) {
      wait_until(k << SLOTSH);
      if (__builtin_amdgcn_readfirstlane(ctrl[0]) >= NUNIT - 36) break;
      const int own = __builtin_amdgcn_readfirstlane(ctrl[1]);
      constexpr unsigned SM = (8192u >> SLOTSH) - 1u;
      if (!NOPST && blockIdx.x < 16 && lane < 8) __builtin_amdgcn_raw_buffer_store_b128(u32x4{(unsigned)own, (unsigned)own, (unsigned)own, (unsigned)own}, wsr, WS_PACE + (k & SM) * 2048 + blockIdx.x * 128 + lane * 16, 0, PAUX);
      if (it < PLAG || NOPLD) continue;
      int e[16];
      asm volatile("s_load_dword %0, %16, 0x0\n\ts_load_dword %1, %16, 0x80\n\ts_load_dword %2, %16, 0x100\n\ts_load_dword %3, %16, 0x180\n\t"
                   "s_load_dword %4, %16, 0x200\n\ts_load_dword %5, %16, 0x280\n\ts_load_dword %6, %16, 0x300\n\ts_load_dword %7, %16, 0x380\n\t"
                   "s_load_dword %8, %16, 0x400\n\ts_load_dword %9, %16, 0x480\n\ts_load_dword %10, %16, 0x500\n\ts_load_dword %11, %16, 0x580\n\t"
                   "s_load_dword %12, %16, 0x600\n\ts_load_dword %13, %16, 0x680\n\ts_load_dword %14, %16, 0x700\n\ts_load_dword %15, %16, 0x780\n\t"
                   "s_waitcnt lgkmcnt(0)"
                   : "=&s"(e[0]), "=&s"(e[1]), "=&s"(e[2]), "=&s"(e[3]), "=&s"(e[4]), "=&s"(e[5]), "=&s"(e[6]), "=&s"(e[7]),
                     "=&s"(e[8]), "=&s"(e[9]), "=&s"(e[10]), "=&s"(e[11]), "=&s"(e[12]), "=&s"(e[13]), "=&s"(e[14]), "=&s"(e[15])
                   : "s"(ws + WS_PACE + ((k - PLAG) & SM) * 2048) : "memory");
      int sum = 0, cnt = 0;
#pragma unroll
      for (int i = 0; i < 16; ++i) { const int d = e[i] - own; const bool ok = (unsigned)(d + 600) < 1200u; sum += ok ? d : 0; cnt += ok ? 1 : 0; }
      if (cnt >= 8) ctrl[2] = own + (int)((float)sum / (float)cnt);
    }
    return;
  }

  if (wave == XWAVE) {
#pragma unroll 1
    for (int i = 0; i < XDEL + XSPR * (kk & 31); ++i) PAUSE(4);
    const u32x4 xv = *(const GAS u32x4*)(x + b * D + 8 * lane), gv = *(const GAS u32x4*)(gw + 8 * lane);
    *(LAS u32x4*)(L + L_X + lane * 16) = xv;
    float gd = 0.f;
#pragma unroll
    for (int k = 0; k < 4; ++k) gd = dot2(xv[k], gv[k], gd);
    gd = xrow_sum(row16_sum(gd));
    const float g = 1.f / (1.f + __expf(-gd));
    ctrlf[48] = g;
    lds_arrive(L, C_X, lane);
    u64 tag;
    asm volatile("s_load_dwordx2 %0, %1, 0x0\n\ts_waitcnt lgkmcnt(0)" : "=s"(tag) : "s"(ws + WS_EPOCH + b * 256) : "memory");
    tag += TAG_STEP;
#pragma unroll 1
    for (int spin = 0; spin < SPIN_CAP; ++spin) { if (__builtin_amdgcn_readfirstlane(ctrl[C_MAX]) >= NCW + NHW) break; __builtin_amdgcn_s_sleep(16); }
    CFENCE();
    const float mg = wg_max();
    float l = 0.f;
#pragma unroll 4
    for (int i = 0; i < NKU * 64 / 1024; ++i) {
      const f32x4 sc = *(const LAS f32x4*)(L + L_SC + i * 1024 + lane * 16);
      l += (__builtin_amdgcn_exp2f(sc[0] - mg) + __builtin_amdgcn_exp2f(sc[1] - mg)) + (__builtin_amdgcn_exp2f(sc[2] - mg) + __builtin_amdgcn_exp2f(sc[3] - mg));
    }
    l = xrow_sum(row16_sum(l));
    const unsigned tlo = (unsigned)tag, thi = (unsigned)(tag >> 32);
    if (lane < 8) __builtin_amdgcn_raw_buffer_store_b128(u32x4{__float_as_uint(mg), tlo, __float_as_uint(l), thi}, wsr, WS_REC + (b * 64 + pj) * 128 + lane * 16, 0, 16);
#pragma unroll 1
    for (int i = 0; i < 12 + (kk & 31); ++i) PAUSE(12);
    u32x4 rec;
#pragma unroll 1
    for (int spin = 0; spin < SPIN_CAP; ++spin) {
      CFENCE();
      rec = __builtin_amdgcn_raw_buffer_load_b128(wsr, WS_REC + (b * 64 + (pj ^ 1)) * 128, 0, 16);
      if (__builtin_amdgcn_ballot_w64(rec[1] != tlo || rec[3] != thi) == 0ull) break;
      __builtin_amdgcn_s_sleep(16);
    }
    const float mo = __uint_as_float(rec[0]), lo = __uint_as_float(rec[2]);
    const float mh = fmaxf(mg, mo), wt = __builtin_amdgcn_exp2f(mg - mh);
    ctrlf[49] = g * wt / (wt * l + __builtin_amdgcn_exp2f(mo - mh) * lo);
    ctrl[50] = (int)(tlo & YMASK);
    lds_arrive(L, C_CF, lane);
    GAS unsigned* op = (GAS unsigned*)(out + b * D + 8 * pj + 4 * (lane & 1) + 2 * (lane >> 5));
    int off = WS_Y + (b * 64 + (lane >> 1)) * 2048 + pj * 32 + (lane & 1) * 16;
    asm volatile("" : "+v"(op), "+v"(off));
    if ((pj & (64 / NRED - 1)) != 0) return;
    STAMP(4);
    BARRIER();
    {
      const unsigned now = (unsigned)__builtin_amdgcn_s_memrealtime();
      const int d = __builtin_amdgcn_readfirstlane(ctrl[2]) + (int)(TS * 100) + FOFF - (int)now;
      const unsigned t = now + (unsigned)min(max(d, FMIN), FMAX);
#pragma unroll 1
      for (int spin = 0; spin < 4096 && (int)(t - (unsigned)__builtin_amdgcn_s_memrealtime()) > 0; ++spin) {}
    }
    STAMP(9);
    constexpr int NL = 128 / NRED;
    u32x4 yv[NL];
#pragma unroll 1
    for (int spin = 0; spin < SPIN_CAP; ++spin) {
      CFENCE();
      unsigned bad = 0;
#pragma unroll
      for (int i = 0; i < NL; ++i) yv[i] = __builtin_amdgcn_raw_buffer_load_b128(wsr, off, i * (64 / NL) * 2048, 16);
#pragma unroll
      for (int i = 0; i < NL; ++i) bad |= ((yv[i][0] ^ tlo) | (yv[i][1] ^ tlo)) | ((yv[i][2] ^ tlo) | (yv[i][3] ^ tlo));
      bad &= YMASK;
      if (__builtin_amdgcn_ballot_w64(bad != 0u) == 0ull) break;
      if (PSLP) __builtin_amdgcn_s_sleep(PSLP);
    }
    float o[4];
#pragma unroll
    for (int k = 0; k < 4; ++k) {
      float t[NL];
#pragma unroll
      for (int i = 0; i < NL; ++i) t[i] = __uint_as_float(yv[i][k] & ~YMASK);
#pragma unroll
      for (int st = NL / 2; st >= 1; st /= 2)
#pragma unroll
        for (int i = 0; i < st; ++i) t[i] += t[i + st];
      o[k] = t[0];
    }
    float e0 = swadd32(o[0], o[2]), e1 = swadd32(o[1], o[3]);
    e0 = swadd16(e0, e0); e1 = swadd16(e1, e1);
    e0 += dpp<ROR8>(e0); e1 += dpp<ROR8>(e1); e0 += dpp<ROR4>(e0); e1 += dpp<ROR4>(e1); e0 += dpp<ROR2>(e0); e1 += dpp<ROR2>(e1);
    if ((lane & 30) == 0) OUT_STORE(op, pack_bf16(e0, e1));
    if (pj == 0 && lane < 8) __builtin_amdgcn_raw_buffer_store_b128(u32x4{tlo, thi, tlo, thi}, wsr, WS_EPOCH + b * 256 + lane * 16, 0, 16);
    STAMP(8);
    STAMP_FLUSH();
    return;
  }

  const int cw = wave - 1, hw = wave - 1 - NCW;
  int landed = 0;
  auto wait_unit = [&](int u) __attribute__((always_inline)) {
#pragma unroll 1
    for (int spin = 0; landed <= u && spin < SPIN_CAP; ++spin) {
      landed = __builtin_amdgcn_readfirstlane(ctrl[0]);
      if (landed > u) break;
      if (USL) __builtin_amdgcn_s_sleep(USL);
    }
    CFENCE();
  };
  auto take = [&](int u, int next, u32x4& a0, u32x4& a1, u32x4& a2, u32x4& a3) __attribute__((always_inline)) {
    wait_unit(u);
    const LAS u32x4* sp = (const LAS u32x4*)(L + (u & (NSLOT - 1)) * UNIT + lane * 16);
    a0 = sp[0]; a1 = sp[64]; a2 = sp[128]; a3 = sp[192];
    LDS_WAIT();
    ctrl[16 + cw] = next;
    asm volatile("" : "+v"(a0), "+v"(a1), "+v"(a2), "+v"(a3) :: "memory");
  };
  u32x4 q;
  auto build_q = [&]() __attribute__((always_inline)) {
    lds_await(ctrl, C_Q, NHW);
    const LAS f32x4* qp = (const LAS f32x4*)(L + L_QP + (2 * r4 * DH + 8 * c16) * 4);
    const f32x4 lo = qp[0] + qp[DH / 4], hi = qp[1] + qp[DH / 4 + 1];
    q[0] = pack_bf16(xrow_sum(lo[0]), xrow_sum(lo[1])); q[1] = pack_bf16(xrow_sum(lo[2]), xrow_sum(lo[3]));
    q[2] = pack_bf16(xrow_sum(hi[0]), xrow_sum(hi[1])); q[3] = pack_bf16(xrow_sum(hi[2]), xrow_sum(hi[3]));
  };
  constexpr float SCALE = 0.088388347648318440f, LOG2E = 1.4426950408889634f, C2 = SCALE * LOG2E;
  float mx = -INFINITY;
  auto score = [&](int u, const u32x4& k0, const u32x4& k1, const u32x4& k2, const u32x4& k3) __attribute__((always_inline)) {
    float d0 = 0.f, d1 = 0.f, d2 = 0.f, d3 = 0.f;
#pragma unroll
    for (int k = 0; k < 4; ++k) { d0 = dot2(k0[k], q[k], d0); d1 = dot2(k1[k], q[k], d1); d2 = dot2(k2[k], q[k], d2); d3 = dot2(k3[k], q[k], d3); }
    const unsigned s01 = pack_bf16(row16_sum(d0), row16_sum(d1)), s23 = pack_bf16(row16_sum(d2), row16_sum(d3));
    const unsigned t01 = pack_bf16(bflo(s01) * SCALE, bfhi(s01) * SCALE), t23 = pack_bf16(bflo(s23) * SCALE, bfhi(s23) * SCALE);
    const f32x4 sc = {bflo(t01) * LOG2E, bfhi(t01) * LOG2E, bflo(t23) * LOG2E, bfhi(t23) * LOG2E};
    mx = fmaxf(fmaxf(mx, fmaxf(sc[0], sc[1])), fmaxf(sc[2], sc[3]));
    if (c16 == 0) *(LAS f32x4*)(L + L_SC + u * 64 + r4 * 16) = sc;
  };

  if (hw < 0) {
    {
      u32x4 hk[4 * NHC];
#pragma unroll
      for (int i = 0; i < NHC; ++i) take(cw + NCW * i, i + 1 < NHC ? cw + NCW * (i + 1) : NHELD + cw, hk[4 * i], hk[4 * i + 1], hk[4 * i + 2], hk[4 * i + 3]);
      STAMP(10);
      build_q();
      STAMP(3);
#pragma unroll
      for (int i = 0; i < NHC; ++i) { score(cw + NCW * i, hk[4 * i], hk[4 * i + 1], hk[4 * i + 2], hk[4 * i + 3]); __builtin_amdgcn_sched_barrier(0); }
    }
#pragma unroll 1
    for (int u = NHELD + cw; u < NKU; u += NCW) {
      u32x4 k0, k1, k2, k3;
      take(u, u + NCW, k0, k1, k2, k3);
      score(u, k0, k1, k2, k3);
    }
    ctrlf[32 + cw] = xrow_max(mx);
    lds_arrive(L, C_MAX, lane);
    STAMP(11);
    lds_await(ctrl, C_MAX, NCW + NHW);
    const float mg = wg_max();
    STAMP(12);
    float acc[8] = {0.f, 0.f, 0.f, 0.f, 0.f, 0.f, 0.f, 0.f};
    LAS unsigned* rp = (LAS unsigned*)(L + L_REC + cw * 272 + (8 * c16 + 2 * r4) * 2);
    asm volatile("" : "+v"(rp));
    float cf = 0.f;
#pragma unroll 1
    for (int u = NKU + cw; u < NUNIT; u += NCW) {
      const f32x4 sc = *(const LAS f32x4*)(L + L_SC + (u - NKU) * 64 + r4 * 16);
      float p[4];
#pragma unroll
      for (int t = 0; t < 4; ++t) { p[t] = __builtin_amdgcn_exp2f(sc[t] - mg); asm volatile("" : "+v"(p[t])); }
      if ((u & ~(NCW - 1)) == NUNIT - 32) { lds_await(ctrl, C_CF, 1); cf = ctrlf[49]; }
      u32x4 v[4];
      take(u, u + NCW, v[0], v[1], v[2], v[3]);
#pragma unroll
      for (int t = 0; t < 4; ++t)
#pragma unroll
        for (int k = 0; k < 4; ++k) { acc[2 * k] = fmaf(p[t], bflo(v[t][k]), acc[2 * k]); acc[2 * k + 1] = fmaf(p[t], bfhi(v[t][k]), acc[2 * k + 1]); }
    }
    STAMP(5);
    const float e0 = swadd16(swadd32(acc[0], acc[4]), swadd32(acc[2], acc[6])), e1 = swadd16(swadd32(acc[1], acc[5]), swadd32(acc[3], acc[7]));
    *rp = pack_bf16(e0 * cf, e1 * cf);
    BARRIER();
    return;
  }

  u32x4 wb[16];
  const unsigned tstart = (unsigned)__builtin_amdgcn_readfirstlane(ctrl[8]);
  {
    const __amdgpu_buffer_rsrc_t rq = mkrsrc(wq, D * H * DH * 2);
    const int vo = ((64 * hw + 16 * r4) * (H * DH) + h * DH + 8 * c16) * 2;
#pragma unroll
    for (int i = 0; i < 16; ++i) { if (i == 0) wait_until(tstart + TQ + hw * QA); else PAUSE(QS); wb[i] = ldw(rq, vo, i * (H * DH * 2)); }
    lds_await(ctrl, C_X, 1);
    const u32x4 xa = *(const LAS u32x4*)(L + L_X + (64 * hw + 16 * r4) * 2), xc = *(const LAS u32x4*)(L + L_X + (64 * hw + 16 * r4 + 8) * 2);
    float a[8] = {0.f, 0.f, 0.f, 0.f, 0.f, 0.f, 0.f, 0.f};
#pragma unroll
    for (int i = 0; i < 8; ++i) {
      const unsigned xp = i < 4 ? xa[i] : xc[i - 4];
#pragma unroll
      for (int k = 0; k < 4; ++k) {
        a[2 * k] = dot2(perm_lo(wb[2 * i][k], wb[2 * i + 1][k]), xp, a[2 * k]);
        a[2 * k + 1] = dot2(perm_hi(wb[2 * i][k], wb[2 * i + 1][k]), xp, a[2 * k + 1]);
      }
    }
    const float e0 = swadd16(swadd32(a[0], a[4]), swadd32(a[2], a[6])), e1 = swadd16(swadd32(a[1], a[5]), swadd32(a[3], a[7]));
    *(LAS f32x2*)(L + L_QP + (hw * DH + 8 * c16 + 2 * r4) * 4) = f32x2{e0, e1};
    lds_arrive(L, C_Q, lane);
    STAMP(2);
    const int hb = NHC * NCW + hw;
#pragma unroll
    for (int i = 0; i < NHH; ++i) take(hb + NHW * i, i + 1 < NHH ? hb + NHW * (i + 1) : NEVER, wb[4 * i], wb[4 * i + 1], wb[4 * i + 2], wb[4 * i + 3]);
    STAMP(10);
    build_q();
#pragma unroll
    for (int i = 0; i < NHH; ++i) { score(hb + NHW * i, wb[4 * i], wb[4 * i + 1], wb[4 * i + 2], wb[4 * i + 3]); __builtin_amdgcn_sched_barrier(0); }
    ctrlf[32 + cw] = xrow_max(mx);
    lds_arrive(L, C_MAX, lane);
  }
  f32x4 y[4] = {{0.f, 0.f, 0.f, 0.f}, {0.f, 0.f, 0.f, 0.f}, {0.f, 0.f, 0.f, 0.f}, {0.f, 0.f, 0.f, 0.f}};
  if ((pj & 7) != 7) {
    const int sl = 7 * (pj >> 3) + (pj & 7);
    const __amdgpu_buffer_rsrc_t r1 = mkrsrc(w1, D * F * 2), r3 = mkrsrc(w3, D * F * 2), r2 = mkrsrc(w2, F * D * 2);
    const int vo = ((64 * hw + (lane & 60)) * F + FS * sl + 8 * (lane & 3)) * 2;
#pragma unroll
    for (int i = 0; i < 8; ++i) { if (i == 0) wait_until(tstart + T1 + hw * A1); else PAUSE(S1); wb[i] = ldw(i < 4 ? r1 : r3, vo, (i & 3) * (F * 2)); }
    const u32x2 xm = *(const LAS u32x2*)(L + L_X + (64 * hw + (lane & 60)) * 2);
    float a[16];
#pragma unroll
    for (int m = 0; m < 2; ++m)
#pragma unroll
      for (int k = 0; k < 4; ++k) {
        a[8 * m + 2 * k] = dot2(perm_lo(wb[4 * m + 2][k], wb[4 * m + 3][k]), xm[1], dot2(perm_lo(wb[4 * m][k], wb[4 * m + 1][k]), xm[0], 0.f));
        a[8 * m + 2 * k + 1] = dot2(perm_hi(wb[4 * m + 2][k], wb[4 * m + 3][k]), xm[1], dot2(perm_hi(wb[4 * m][k], wb[4 * m + 1][k]), xm[0], 0.f));
      }
    f32x4 e;
#pragma unroll
    for (int k = 0; k < 4; ++k) {
      float t = swadd16(swadd32(a[k], a[8 + k]), swadd32(a[4 + k], a[12 + k]));
      t += dpp<ROR8>(t); e[k] = t + dpp<ROR4>(t);
    }
    if ((lane & 12) == 0) *(LAS f32x4*)(L + L_MP + (hw * 64 + (lane & 32) + 8 * (lane & 3) + 4 * (r4 & 1)) * 4) = e;
    lds_arrive(L, C_MLP, lane);
    u32x2 wr[8];
#pragma unroll
    for (int i = 0; i < 8; ++i) { if (i == 0) wait_until(tstart + T2 + hw * A2); else PAUSE(S2); wr[i] = ldw2(r2, ((FS * sl + 8 * r4) * D + 64 * hw + 4 * c16) * 2, i * (D * 2)); }
    lds_await(ctrl, C_MLP, NHW);
    const LAS float* mp = (const LAS float*)(L + L_MP) + (lane & 31);
    float ga = 0.f, up = 0.f;
#pragma unroll
    for (int w = 0; w < NHW; ++w) { ga += mp[w * 64]; up += mp[w * 64 + 32]; }
    const float hv = ga / (1.f + __expf(-ga)) * up * (1.f - ctrlf[48]);
    const unsigned hh = pack_bf16(hv, 0.f), hl = pack_bf16(hv - bflo(hh), 0.f);
    *(LAS u16*)(L + L_H + hw * 128 + lane * 2) = (u16)(lane < 32 ? hh : hl);
    LDS_WAIT();
    u32x4 ha = *(const LAS u32x4*)(L + L_H + hw * 128 + (c16 & 1) * 64 + r4 * 16);
    if (c16 >= 2) ha = u32x4{0u, 0u, 0u, 0u};
#pragma unroll
    for (int e = 0; e < 4; ++e) {
      u32x4 f;
#pragma unroll
      for (int k = 0; k < 4; ++k) f[k] = (e & 1) ? perm_hi(wr[2 * k][e >> 1], wr[2 * k + 1][e >> 1]) : perm_lo(wr[2 * k][e >> 1], wr[2 * k + 1][e >> 1]);
      y[e] = mfma(ha, f, y[e]);
    }
  }
#pragma unroll
  for (int e = 0; e < 4; ++e) asm volatile("" : "+v"(y[e]));
  {
    const __amdgpu_buffer_rsrc_t ro = mkrsrc(wo, H * DH * D * 2);
#pragma unroll
    for (int t = 0; t < 4; ++t) {
      u32x2 wr[8];
#pragma unroll
      for (int i = 0; i < 8; ++i) { if (t + i == 0) wait_until(tstart + TO + hw * AO); else PAUSE(SO); wr[i] = ldw2(ro, ((h * DH + 32 * r4) * D + 64 * hw + 4 * c16) * 2, (8 * t + i) * (D * 2)); }
#pragma unroll
      for (int e = 0; e < 4; ++e) {
#pragma unroll
        for (int k = 0; k < 4; ++k) wb[4 * t + e][k] = (e & 1) ? perm_hi(wr[2 * k][e >> 1], wr[2 * k + 1][e >> 1]) : perm_lo(wr[2 * k][e >> 1], wr[2 * k + 1][e >> 1]);
        asm volatile("" : "+v"(wb[4 * t + e]));
      }
    }
  }
  lds_await(ctrl, C_CF, 1);
  unsigned yt = (unsigned)ctrl[50];
  int yo = WS_Y + (b * 64 + pj) * 2048 + hw * 256 + c16 * 16;
  const LAS u32x4* rec = (const LAS u32x4*)(L + L_REC + (lane & 3) * 272 + r4 * 64);
  unsigned ym = ~YMASK;
  asm volatile("" : "+v"(yt), "+v"(yo), "+v"(rec), "+s"(ym));
  STAMP(4);
  BARRIER();
  STAMP(9);
  {
    const u32x4 a[4] = {rec[0], rec[1], rec[2], rec[3]};
#pragma unroll
    for (int t = 0; t < 4; ++t)
#pragma unroll
      for (int e = 0; e < 4; ++e) y[e] = mfma(a[t], wb[4 * t + e], y[e]);
    u32x4 v;
#pragma unroll
    for (int e = 0; e < 4; ++e) {
      const f32x2 t = y[e].xy + y[e].zw;
      unsigned r;
      asm("v_add_f32 %0, %1, %2\n\tv_add_u32 %0, %5, %0\n\tv_and_or_b32 %0, %0, %3, %4" : "=&v"(r) : "v"(t[0]), "v"(t[1]), "s"(ym), "v"(yt), "i"((YMASK + 1u) / 2u));
      v[e] = r;
    }
    if (lane < 16) __builtin_amdgcn_raw_buffer_store_b128(v, wsr, yo, 0, 16);
  }
  STAMP(7);
}

extern "C" void kernel_launch(void* const* d_in, const int* in_sizes, int n_in, void* d_out, int out_size, void* d_ws, size_t ws_size,
                              hipStream_t stream) {
  gated_mix_kernel<<<256, NTHR, 0, stream>>>((const u16*)d_in[0], (const u16*)d_in[1], (const u16*)d_in[2], (const u16*)d_in[3], (const u16*)d_in[4],
                                             (const u16*)d_in[6], (const u16*)d_in[7], (const u16*)d_in[8], (const u16*)d_in[9], (u16*)d_out,
                                             (unsigned char*)d_ws);
}
```

```cpp
#include <hip/hip_runtime.h>
#include <hip/hip_bf16.h>
#define STAMPS 1
#define MWAVE 0
#define MIDX 1
#define MTYPE 2
#define RWAVE -1
#define RIDX 0
#define MSCALE 20.f

#define GAS __attribute__((address_space(1)))
#define LAS __attribute__((address_space(3)))
typedef unsigned short u16;
typedef unsigned long long u64;
using u32x4 = __attribute__((ext_vector_type(4))) unsigned;
using u32x2 = __attribute__((ext_vector_type(2))) unsigned;
using f32x4 = __attribute__((ext_vector_type(4))) float;
using f32x2 = __attribute__((ext_vector_type(2))) float;
using bf16x2 = __attribute__((ext_vector_type(2))) __bf16;
using bf16x8 = __attribute__((ext_vector_type(8))) __bf16;

constexpr int B = 4, D = 512, H = 32, SK = 8192, DH = 128, F = 1792;
constexpr int NCW = 4, NHW = 8, XWAVE = 1 + NCW + NHW, NWAVE = XWAVE + 2, NTHR = NWAVE * 64;
#define QA 80
#define QB 5
#define T1 1200
#define A1 150
#define B1 18
#define T2 2500
#define A2 40
#define B2 4
#define TO 2900
#define AO 350
#define BO 8
#define TS 70.6
#define REF0 180
#define PLAG 1
#define NOPST 0
#define NOPLD 0
#define PAUX 16
#define RAUX 16
#define PFW 0
#define TQ 130
#define NINIT 6
#define CODE_KIB 21
#define FDEL 0
#define CODEPF 1
#define YST 16
#define YLD 16
#define RED16 0
#define MASKP 0
#define PSLP 0
#define OUTSC 0
#define OUT_STORE(p, v) *(GAS unsigned*)(p) = v
#define CLR0 1
#define MLPFULL 0
#define MLPPF 0
#define TP 1200
#define AP 12
#define GRID 0
#define B2BD 0
#define B2BAUX 16
#define WOB2B 0
#define TOUCHW 1
#define W0 30
#define BARPOS 12
#define NO_WO 0
#define TLBPF 0
#define TMODE 0
#define SPW 4
#define QS 2
#define S1 6
#define S2 1
#define SO 2
#define USL 0
#define JUMP 100
#define DECAY 6
#define HOLD0 10000
#define UEND 420
#define WGAIN2 18
#define WMID2 WMID
#define FOFF 150
#define FMIN 45
#define FMAX 150
#define NRED 64
#define SLOTSH 8
#define WMID 20.5
#define WGAIN 10
#define WMIN 14
#define WMAX 27
#define CLRAUX 16
#define PCONT 1
#define XDEL 20
#define XSPR 0
#define XNT 0
#define MIMIC 1
#define NCLR 2
#define NOFLAG 0
#define CSPR 0
constexpr int UNIT = 4096;
constexpr int NSLOT = 32, RING = NSLOT * UNIT;
constexpr int NKU = (SK / 2) / 16, NUNIT = 2 * NKU;
constexpr int NHC = 6, NHH = 4, NHELD = NHC * NCW + NHH * NHW;
constexpr int FS = 32;
constexpr int L_CTRL = RING;
constexpr int L_X = L_CTRL + 256;
constexpr int L_QP = L_X + D * 2;
constexpr int L_MP = L_QP + NHW * DH * 4;
constexpr int L_REC = L_MP + NHW * 64 * 4;
constexpr int L_H = L_REC + NCW * 272;
constexpr int L_SC = L_H + NHW * 128;
constexpr int LDS_BYTES = L_SC + NKU * 64;
static_assert(LDS_BYTES <= 159 * 1024, "lds");
static_assert(WMIN <= (int)WMID && (int)WMID <= WMAX && WMAX <= 28, "window");
constexpr int WS_EPOCH = 0;
constexpr int WS_REC = 4096;
constexpr int WS_PACE = WS_REC + 256 * 128;
constexpr int WS_Y = WS_PACE + 64 * 2048;
constexpr int WS_BYTES = WS_Y + B * 64 * 2048;
constexpr u64 TAG_STEP = 0x9E3779B97F4A7C15ull;
constexpr unsigned YMASK = 0xFFFu;
constexpr int SPIN_CAP = 1 << 18;
constexpr int NEVER = 1 << 20;
constexpr int C_Q = 4, C_MAX = 5, C_MLP = 6, C_X = 7, C_CF = 9;

__device__ __forceinline__ float bflo(unsigned u) { return __builtin_bit_cast(float, u << 16); }
__device__ __forceinline__ float bfhi(unsigned u) { return __builtin_bit_cast(float, u & 0xffff0000u); }
__device__ __forceinline__ float dot2(unsigned a, unsigned b, float acc) {
  return __builtin_amdgcn_fdot2_f32_bf16(__builtin_bit_cast(bf16x2, a), __builtin_bit_cast(bf16x2, b), acc, false);
}
__device__ __forceinline__ unsigned pack_bf16(float lo, float hi) {
  const bf16x2 v = {(__bf16)lo, (__bf16)hi};
  return __builtin_bit_cast(unsigned, v);
}
__device__ __forceinline__ unsigned perm_lo(unsigned a, unsigned b) { return __builtin_amdgcn_perm(b, a, 0x05040100u); }
__device__ __forceinline__ unsigned perm_hi(unsigned a, unsigned b) { return __builtin_amdgcn_perm(b, a, 0x07060302u); }
template <int CTRL> __device__ __forceinline__ float dpp(float x) {
  return __builtin_bit_cast(float, __builtin_amdgcn_mov_dpp(__builtin_bit_cast(int, x), CTRL, 0xf, 0xf, true));
}
constexpr int XOR1 = 0xB1, ROR1 = 0x121, ROR2 = 0x122, ROR4 = 0x124, ROR8 = 0x128;
__device__ __forceinline__ float row16_sum(float x) { x += dpp<ROR8>(x); x += dpp<ROR4>(x); x += dpp<ROR2>(x); x += dpp<ROR1>(x); return x; }
__device__ __forceinline__ float swadd32(float a, float b) {
  auto t = __builtin_amdgcn_permlane32_swap(__float_as_uint(a), __float_as_uint(b), false, false);
  return __uint_as_float(t[0]) + __uint_as_float(t[1]);
}
__device__ __forceinline__ float swadd16(float a, float b) {
  auto t = __builtin_amdgcn_permlane16_swap(__float_as_uint(a), __float_as_uint(b), false, false);
  return __uint_as_float(t[0]) + __uint_as_float(t[1]);
}
__device__ __forceinline__ float xrow_sum(float x) { x = swadd16(x, x); return swadd32(x, x); }
__device__ __forceinline__ float xrow_max(float x) {
  auto s = __builtin_amdgcn_permlane16_swap(__float_as_uint(x), __float_as_uint(x), false, false);
  x = fmaxf(__uint_as_float(s[0]), __uint_as_float(s[1]));
  auto t = __builtin_amdgcn_permlane32_swap(__float_as_uint(x), __float_as_uint(x), false, false);
  return fmaxf(__uint_as_float(t[0]), __uint_as_float(t[1]));
}
__device__ __forceinline__ __amdgpu_buffer_rsrc_t mkrsrc(const void* p, int bytes) { return __builtin_amdgcn_make_buffer_rsrc((void*)p, 0, bytes, 0x00020000); }
__device__ __forceinline__ u32x4 ldw(__amdgpu_buffer_rsrc_t r, int voff, int soff) { return __builtin_amdgcn_raw_buffer_load_b128(r, voff, soff, 2); }
#define LDS_WAIT() asm volatile("s_waitcnt lgkmcnt(0)" ::: "memory")
#define VM_DRAIN() asm volatile("s_waitcnt vmcnt(0)" ::: "memory")
#define CFENCE() asm volatile("" ::: "memory")
#define BARRIER() asm volatile("s_waitcnt lgkmcnt(0)\n\ts_barrier" ::: "memory")
#define PAUSE(n) do { __builtin_amdgcn_sched_barrier(0); asm volatile("s_sleep %0" :: "i"(n) : "memory"); __builtin_amdgcn_sched_barrier(0); } while (0)

__device__ __forceinline__ void lds_arrive(LAS unsigned char* L, int idx, int lane) {
  LDS_WAIT();
  if (lane == 0) __hip_atomic_fetch_add((LAS int*)(L + L_CTRL) + idx, 1, __ATOMIC_RELAXED, __HIP_MEMORY_SCOPE_WORKGROUP);
}
__device__ __forceinline__ void lds_await(volatile LAS int* ctrl, int idx, int n) {
#pragma unroll 1
  for (int spin = 0; spin < SPIN_CAP; ++spin) { if (__builtin_amdgcn_readfirstlane(ctrl[idx]) >= n) break; __builtin_amdgcn_s_sleep(1); }
  CFENCE();
}

__device__ __forceinline__ void wait_until(unsigned t) {
  __builtin_amdgcn_sched_barrier(0);
#pragma unroll 1
  for (int spin = 0; spin < 4096; ++spin) {
    const int d = (int)(t - (unsigned)__builtin_amdgcn_s_memrealtime());
    if (d <= 0) break;
    if (d > 24) __builtin_amdgcn_s_sleep(6);
  }
  CFENCE();
  __builtin_amdgcn_sched_barrier(0);
}
#define STR2(x) #x
#define STR(x) STR2(x)
constexpr int TAU2 = 2 * (int)(TS * 100.0 * 256.0 / NUNIT);
__device__ __forceinline__ f32x4 mfma(const u32x4& a, const u32x4& b, const f32x4& c) {
  return __builtin_amdgcn_mfma_f32_16x16x32_bf16(__builtin_bit_cast(bf16x8, a), __builtin_bit_cast(bf16x8, b), c, 0, 0, 0);
}
__device__ __forceinline__ u32x2 ldw2(__amdgpu_buffer_rsrc_t r, int voff, int soff) { return __builtin_amdgcn_raw_buffer_load_b64(r, voff, soff, 2); }
template <int CTRL> __device__ __forceinline__ int dppi(int x) { return __builtin_amdgcn_mov_dpp(x, CTRL, 0xf, 0xf, true); }

#define STAMP(i) do { __builtin_amdgcn_sched_barrier(0); ((volatile LAS unsigned*)(L + LDS_BYTES))[wave * 16 + (i)] = (unsigned)__builtin_amdgcn_s_memrealtime(); __builtin_amdgcn_sched_barrier(0); } while (0)
#define STAMP_FLUSH() do { LDS_WAIT(); const __amdgpu_buffer_rsrc_t dr = mkrsrc(ws + (4 << 20), 256 * 1024); \
    __builtin_amdgcn_raw_buffer_store_b128(*(volatile LAS u32x4*)(L + LDS_BYTES + lane * 16), dr, blockIdx.x * 1024 + lane * 16, 0, 16); \
    if (blockIdx.x == 0) { const unsigned t0 = __builtin_amdgcn_readfirstlane(((volatile LAS unsigned*)(L + LDS_BYTES))[0]); \
      while ((unsigned)__builtin_amdgcn_s_memrealtime() - t0 < 13000u) __builtin_amdgcn_s_sleep(8); \
      CFENCE(); float sum = 0.f, mxv = -1e9f; \
      for (int i = 0; i < 4; ++i) { const int wg = lane * 4 + i; \
        const unsigned ref = (RWAVE < 0) ? t0 : __builtin_amdgcn_raw_buffer_load_b32(dr, wg * 1024 + ((RWAVE < 0 ? 0 : RWAVE) * 16 + RIDX) * 4, 0, 16); \
        const float v = (float)(int)(__builtin_amdgcn_raw_buffer_load_b32(dr, wg * 1024 + (MWAVE * 16 + MIDX) * 4, 0, 16) - ref); \
        sum += v; mxv = fmaxf(mxv, v); } \
      sum = xrow_sum(row16_sum(sum)) * (1.f / 256.f); \
      float gm = mxv; gm = fmaxf(gm, dpp<ROR8>(gm)); gm = fmaxf(gm, dpp<ROR4>(gm)); gm = fmaxf(gm, dpp<ROR2>(gm)); gm = fmaxf(gm, dpp<ROR1>(gm)); gm = xrow_max(gm); \
      const float met = (MTYPE == 0 ? sum : MTYPE == 1 ? gm : gm - sum) * MSCALE; \
      const unsigned tgt = __builtin_amdgcn_readfirstlane((unsigned)(15000.f + met)); \
      while ((unsigned)__builtin_amdgcn_s_memrealtime() - t0 < tgt) __builtin_amdgcn_s_sleep(1); } } while (0)
#define STAMP_LDS 1024

__global__ void __launch_bounds__(NTHR) gated_mix_kernel(const u16* x, const u16* kc, const u16* vc, const u16* wq, const u16* wo, const u16* w1,
                                                         const u16* w3, const u16* w2, const u16* gw, u16* out, unsigned char* ws) {
  __shared__ __attribute__((aligned(16))) unsigned char lds_raw[LDS_BYTES + STAMP_LDS];
  LAS unsigned char* L = (LAS unsigned char*)lds_raw;
  volatile LAS int* ctrl = (volatile LAS int*)(L + L_CTRL);
  volatile LAS float* ctrlf = (volatile LAS float*)(L + L_CTRL);
  u64 t_first; asm volatile("s_memrealtime %0\n\ts_waitcnt lgkmcnt(0)" : "=s"(t_first));
  __builtin_amdgcn_sched_barrier(0);
  if (threadIdx.x < 14 * 64) ((volatile LAS unsigned*)(L + LDS_BYTES))[(threadIdx.x >> 6) * 16] = (unsigned)t_first;
#define DBG(x) x
  {
    unsigned va, vb, vo, pa, nxa, t0, t1, t2, t3;
#define CODELD "global_load_lds_dwordx4 %[t0], s[56:57]\n\t"
#define ASTAMP(i) DBG("s_memrealtime s[60:61]\n\ts_waitcnt lgkmcnt(0)\n\tv_mov_b32 %[t1], %c[dbg] + 4 * " #i "\n\tv_mov_b32 %[t2], s60\n\tds_write_b32 %[t1], %[t2]\n\t")
#define WAITW "s_waitcnt (wcnt & 15) | ((wcnt >> 4) << 14) | 0x0F70\n\t"
#define SP(v, sb, wait) wait "global_load_lds_dword " v ", " sb " nt\n\tglobal_load_lds_dword " v ", " sb " sc1\n\t"
#define SW "s_waitcnt vmcnt(%c[spw])\n\t"
#define UNIT4(v) \
        WAITW "global_load_lds_dwordx4 " v ", s[40:41] nt\n\t" \
        WAITW "global_load_lds_dwordx4 " v ", s[40:41] offset:1024 nt\n\t" \
        WAITW "global_load_lds_dwordx4 " v ", s[40:41] offset:2048 nt\n\t" \
        WAITW "global_load_lds_dwordx4 " v ", s[40:41] offset:3072 nt\n\t"
    asm volatile(
        "s_getpc_b64 s[56:57]\n\t"
        "s_load_dwordx8 s[44:51], %[ka], 0x18\n\t"
        "s_load_dwordx2 s[52:53], %[ka], 0x38\n\t"
        "s_load_dwordx4 s[40:43], %[ka], 0x8\n\t"
        "v_readfirstlane_b32 s54, %[tid]\n\t"
        "s_mov_b32 m0, %c[jk]\n\t"
        "s_lshr_b32 s59, %[bid], 3\n\t"
        "s_cmp_ge_u32 s54, 64\n\t"
        "s_cbranch_scc1 90f\n\t"
        "s_min_u32 s54, s59, %c[nch]\n\t"
        "s_lshl_b32 s54, s54, 10\n\t"
        "v_lshlrev_b32 %[vo], 4, %[tid]\n\t"
        "s_and_b32 s56, s56, -128\n\t"
        "v_add_u32 %[t0], s54, %[vo]\n\t"
        "s_and_b32 s58, %[bid], 7\n\t"
        CODELD
        "s_setprio 3\n\t"
        "v_lshlrev_b32 %[t1], 7, %[tid]\n\t"
        "s_lshl_b32 s54, s59, 7\n\t"
        "s_add_u32 s54, s54, s58\n\t"
        "s_lshl_b32 s54, s54, 10\n\t"
        "v_lshrrev_b32 %[va], 3, %[tid]\n\t"
        "v_mul_u32_u24 %[va], 8192 - 1024, %[va]\n\t"
        "v_add3_u32 %[va], %[va], %[t1], s54\n\t"
        "v_add_u32 %[t2], 0x10000, %[va]\n\t"
        "s_lshl_b32 s55, s58, 5\n\t"
        "s_add_u32 s55, s55, s59\n\t"
        "s_mul_i32 s54, s55, 7 * 1024\n\t"
        "v_min_u32 %[t3], 55 * 128, %[t1]\n\t"
        "v_add_u32 %[t3], s54, %[t3]\n\t"
        "s_mul_i32 s54, s58, 448\n\t"
        "s_and_b32 s54, s54, -128\n\t"
        "s_mul_i32 s57, s59, 16 * 3584\n\t"
        "s_add_u32 s54, s54, s57\n\t"
        "v_lshrrev_b32 %[t0], 2, %[tid]\n\t"
        "v_mul_u32_u24 %[t0], 3584 - 512, %[t0]\n\t"
        "v_add3_u32 %[t0], %[t0], %[t1], s54\n\t"
        "v_lshl_add_u32 %[vb], s55, 14, %[t1]\n\t"
        "v_add_u32 %[t1], 0x2000, %[vb]\n\t"
        "s_and_b32 s54, %[bid], 0xc0\n\t"
        "s_lshl3_add_u32 s54, s58, s54\n\t"
        "s_and_b32 s56, s59, 7\n\t"
        "s_add_u32 s54, s54, s56\n\t"
        "s_lshl_b32 s56, s54, 20\n\t"
        "s_waitcnt lgkmcnt(0)\n\t"
        SP("%[va]", "s[44:45]", "") SP("%[t2]", "s[44:45]", "") SP("%[t3]", "s[52:53]", "")
        ".if %c[spw] <= 4\n\ts_barrier\n\t.endif\n\t"
        SP("%[t0]", "s[48:49]", SW)
        ".if %c[spw] == 6\n\ts_barrier\n\t.endif\n\t"
        SP("%[t0]", "s[50:51]", SW)
        ".if %c[spw] == 8\n\ts_barrier\n\t.endif\n\t"
        SP("%[vb]", "s[46:47]", SW) SP("%[t1]", "s[46:47]", SW)
        ".if %c[spw] > 8\n\ts_barrier\n\t.endif\n\t"
        ASTAMP(6)
        "s_add_u32 s40, s40, s56\n\t"
        "s_addc_u32 s41, s41, 0\n\t"
        "s_add_u32 s42, s42, s56\n\t"
        "s_addc_u32 s43, s43, 0\n\t"
        "s_sub_u32 s42, s42, %c[nku] * 4096\n\t"
        "s_subb_u32 s43, s43, 0\n\t"
        ".set so, 0\n\t"
        ".set wcnt, %c[spw]\n\t"
        ".rept 4\n\t"
        "s_movk_i32 m0, so\n\t"
        ".if (so & 4096) == 0\n\t"
        WAITW
        ".set wcnt, wcnt + 6\n\t"
        ".else\n\t"
        "s_nop 0\n\t"
        ".endif\n\t"
        "global_load_lds_dwordx4 %[vo], s[40:41] nt\n\t"
        "global_load_lds_dwordx4 %[vo], s[40:41] offset:1024 nt\n\t"
        "global_load_lds_dwordx4 %[vo], s[40:41] offset:2048 nt\n\t"
        "global_load_lds_dwordx4 %[vo], s[40:41] offset:3072 nt\n\t"
        "v_add_u32 %[vo], 0x1000, %[vo]\n\t"
        ".set so, so + 4096\n\t"
        ".endr\n\t"
        ".if %c[spw] <= 4\n\t"
        "s_waitcnt vmcnt(16)\n\t"
        ".endif\n\t"
        ASTAMP(7)
        "s_getpc_b64 s[50:51]\n"
        "20:\n\t"
        "s_add_u32 s50, s50, 23f-20b-%c[wmin]*128\n\t"
        "s_addc_u32 s51, s51, -1\n\t"
        "v_mov_b32 %[pa], %c[ctl]\n\t"
        "v_and_b32 %[nxa], 0x3c0 >> 2, %[vo]\n\t"
        "v_lshrrev_b32 %[nxa], 2, %[nxa]\n\t"
        "v_add_u32 %[nxa], %c[ctl] + 64, %[nxa]\n\t"
        "s_memrealtime s[58:59]\n\t"
        "s_mov_b32 s62, 0\n\t"
        "s_mov_b32 s63, %c[gain]\n\t"
        "s_mov_b32 s64, %c[wmid]\n\t"
        "s_mov_b32 s44, 4\n\t"
        "s_mov_b32 s45, %c[wmid] >> 8\n\t"
        "s_mov_b32 s46, 0x4000\n\t"
        "s_mov_b32 s47, 2 * %c[tau2]\n\t"
        "s_mov_b32 s48, %c[nslot]\n\t"
        "s_mov_b32 s49, %c[nku]\n\t"
        "s_waitcnt lgkmcnt(0)\n\t"
        "s_mov_b32 s59, 10000\n"
        "22:\n\t"
        "s_memrealtime s[56:57]\n\t"
        "ds_read_b32 %[t3], %[pa] offset:8\n\t"
        "s_lshl_b32 s52, s45, 7\n\t"
        "s_add_u32 s52, s50, s52\n\t"
        "s_addc_u32 s53, s51, 0\n\t"
        "s_add_i32 s54, s45, -1\n\t"
        "s_lshr_b32 s54, s54, 2\n\t"
        "s_sub_i32 s54, s44, s54\n\t"
        "v_mov_b32 %[t1], s54\n\t"
        "v_add_u32 %[t0], 0x1000, %[vo]\n\t"
        "s_add_u32 s55, s46, 0x1000\n\t"
        "s_mov_b32 m0, s46\n\t"
        "s_setpc_b64 s[52:53]\n"
        "24:\n\t"
        "s_waitcnt lgkmcnt(0)\n\t"
        "v_readfirstlane_b32 s57, %[t3]\n\t"
        "v_add_u32 %[t1], 1, %[t1]\n\t"
        "s_lshr_b32 s54, s47, 8\n\t"
        "s_sub_u32 s56, s56, s54\n\t"
        "v_mov_b32 %[t2], s56\n\t"
        "s_sub_i32 s54, s56, s58\n\t"
        "s_mov_b32 s58, s56\n\t"
        "s_sub_i32 s56, s56, s57\n\t"
        "ds_write2_b32 %[pa], %[t1], %[t2] offset1:1\n\t"
        "s_add_i32 s59, s59, s54\n\t"
        "s_add_i32 s59, s59, %c[decay]\n\t"
        "s_cmp_gt_i32 s54, %c[jump]\n\t"
        "s_cselect_b32 s59, s62, s59\n\t"
        "s_min_i32 s62, s56, s59\n\t"
        "s_mul_i32 s56, s62, s63\n\t"
        "s_add_i32 s56, s56, s64\n\t"
        "s_ashr_i32 s56, s56, 8\n\t"
        "s_max_i32 s56, s56, %c[wmin]\n\t"
        "s_min_i32 s45, s56, %c[wmax]\n\t"
        "s_add_u32 s47, s47, %c[tau2]\n\t"
        "s_add_u32 s46, s46, 0x2000\n\t"
        "s_and_b32 s46, s46, %c[nslot] * 4096 - 1\n\t"
        "v_add_u32 %[vo], 0x2000, %[vo]\n\t"
        "s_add_i32 s44, s44, 2\n\t"
        "s_cmp_lt_i32 s44, s48\n\t"
        "s_cbranch_scc1 22b\n\t"
        DBG("s_add_i32 s54, s44, -1\n\ts_lshr_b32 s54, s54, 5\n\ts_lshl_b32 s54, s54, 2\n\ts_add_u32 s54, s54, %c[dbg] + 14 * 64\n\tv_mov_b32 %[t0], s54\n\tv_mov_b32 %[t1], s45\n\t"
            "ds_write2_b32 %[t0], %[t2], %[t1] offset1:16\n\t")
        "s_cmp_lt_i32 s44, s49\n\t"
        "s_cbranch_scc1 25f\n\t"
        "s_cmp_ge_i32 s44, 2 * %c[nku]\n\t"
        "s_cbranch_scc1 30f\n\t"
        "s_mov_b32 s49, 2 * %c[nku]\n\t"
        "s_mov_b64 s[40:41], s[42:43]\n"
        "25:\n\t"
        "s_cmp_ge_i32 s44, %c[uend]\n\t"
        "s_cselect_b32 s63, %c[gain2], s63\n\t"
        "s_cselect_b32 s64, %c[wmid2], s64\n\t"
        "s_mov_b32 s55, %c[cap]\n"
        "26:\n\t"
        "ds_read_b32 %[t0], %[nxa]\n\t"
        "s_waitcnt lgkmcnt(0)\n\t"
        "s_nop 1\n\t"
        "v_min_i32_dpp %[t0], %[t0], %[t0] row_ror:8 row_mask:0xf bank_mask:0xf\n\t"
        "s_nop 1\n\t"
        "v_min_i32_dpp %[t0], %[t0], %[t0] row_ror:4 row_mask:0xf bank_mask:0xf\n\t"
        "s_nop 1\n\t"
        "v_min_i32_dpp %[t0], %[t0], %[t0] row_ror:2 row_mask:0xf bank_mask:0xf\n\t"
        "s_nop 1\n\t"
        "v_min_i32_dpp %[t0], %[t0], %[t0] row_ror:1 row_mask:0xf bank_mask:0xf\n\t"
        "s_nop 1\n\t"
        "v_readfirstlane_b32 s48, %[t0]\n\t"
        "s_nop 1\n\t"
        "s_add_i32 s48, s48, %c[nslot]\n\t"
        "s_and_b32 s48, s48, -2\n\t"
        "s_min_i32 s48, s48, s49\n\t"
        "s_cmp_lt_i32 s44, s48\n\t"
        "s_cbranch_scc1 22b\n\t"
        "v_mov_b32 %[t1], s44\n\t"
        "s_waitcnt vmcnt(0)\n\t"
        "ds_write_b32 %[pa], %[t1]\n\t"
        "s_sleep 1\n\t"
        "s_sub_u32 s55, s55, 1\n\t"
        "s_cmp_lg_u32 s55, 0\n\t"
        "s_cbranch_scc1 26b\n\t"
        "s_branch 22b\n"
        "30:\n\t"
        ".set k, 6\n\t"
        ".rept 7\n\t"
        ".set wcnt, 4 * k\n\t"
        "v_mov_b32 %[t1], 2 * %c[nku] - k\n\t"
        WAITW
        "ds_write_b32 %[pa], %[t1]\n\t"
        ".set k, k - 1\n\t"
        ".endr\n\t"
        DBG("s_memrealtime s[56:57]\n\ts_waitcnt lgkmcnt(0)\n\tv_mov_b32 %[t0], %c[dbg] + 4\n\tv_mov_b32 %[t1], s56\n\tds_write_b32 %[t0], %[t1]\n\t")
        "s_waitcnt lgkmcnt(0)\n\t"
        "s_endpgm\n\t"
        ".p2align 7\n"
        "23:\n\t"
        ".set wcnt, %c[wmin]-1\n\t"
        ".rept %c[wmax] - %c[wmin] + 1\n\t"
        UNIT4("%[vo]")
        "ds_write_b32 %[pa], %[t1]\n\t"
        "s_mov_b32 m0, s55\n\t"
        UNIT4("%[t0]")
        "s_branch 24b\n\t"
        ".p2align 7\n\t"
        ".set wcnt, wcnt+1\n\t"
        ".endr\n"
        "90:\n\t"
        "s_waitcnt lgkmcnt(0)"
        : [va] "=&v"(va), [vb] "=&v"(vb), [vo] "=&v"(vo), [pa] "=&v"(pa), [nxa] "=&v"(nxa), [t0] "=&v"(t0), [t1] "=&v"(t1), [t2] "=&v"(t2), [t3] "=&v"(t3)
        : [tid] "v"(threadIdx.x), [bid] "s"(blockIdx.x), [ka] "s"(__builtin_amdgcn_kernarg_segment_ptr()), [nch] "i"(CODE_KIB - 1), [jk] "i"(RING - UNIT),
          [spw] "i"(SPW), [wmin] "i"(WMIN), [wmax] "i"(WMAX), [gain] "i"(WGAIN), [wmid] "i"((int)(WMID * 256)), [tau2] "i"(TAU2),
          [ctl] "i"(L_CTRL), [nslot] "i"(NSLOT), [nku] "i"(NKU), [cap] "i"(SPIN_CAP), [dbg] "i"(LDS_BYTES), [jump] "i"(JUMP), [decay] "i"(DECAY), [uend] "i"(UEND), [gain2] "i"(WGAIN2), [wmid2] "i"((int)(WMID2 * 256))
        : "memory", "scc", "s40", "s41", "s42", "s43", "s44", "s45", "s46", "s47", "s48", "s49", "s50", "s51", "s52", "s53", "s54", "s55", "s56", "s57", "s58", "s59", "s60", "s61", "s62", "s63", "s64");
  }
  __builtin_amdgcn_sched_barrier(0);
  const int tid = threadIdx.x, lane = tid & 63, wave = __builtin_amdgcn_readfirstlane(tid >> 6);
  const int xg = blockIdx.x & 7, kk = blockIdx.x >> 3;
  const int b = kk >> 3, pj = 8 * xg + (kk & 7), h = pj >> 1, s = pj & 1;
  const int r4 = lane >> 4, c16 = lane & 15;
  asm volatile("" : "+s"(x), "+s"(wq), "+s"(wo), "+s"(w1), "+s"(w3), "+s"(w2), "+s"(gw), "+s"(out), "+s"(ws));
  if (wave == 1) {
    const int wi = lane - 16;
    const int now = (int)(unsigned)__builtin_amdgcn_s_memrealtime() + REF0;
    ctrl[lane] = (lane == 1 || lane == 2) ? now : lane == 8 ? now - REF0 : (lane < 16 || lane >= 32) ? 0 : wi < NCW ? wi : wi < NCW + NHW ? NHC * NCW + wi - NCW : NEVER;
  }
  BARRIER();

  const __amdgpu_buffer_rsrc_t wsr = mkrsrc(ws, WS_BYTES);
  auto wg_max = [&]() __attribute__((always_inline)) {
    float mg = ctrlf[32 + (c16 < NCW + NHW ? c16 : 0)];
    mg = fmaxf(mg, dpp<ROR8>(mg)); mg = fmaxf(mg, dpp<ROR4>(mg)); mg = fmaxf(mg, dpp<ROR2>(mg)); mg = fmaxf(mg, dpp<ROR1>(mg));
    return mg;
  };

  if (wave == XWAVE + 1) {
    unsigned k = ((unsigned)__builtin_amdgcn_s_memrealtime() >> SLOTSH) + 1;
#pragma unroll 1
    for (int it = 0; it < 4096 >> (SLOTSH - 2); ++it, ++k) {
      wait_until(k << SLOTSH);
      if (__builtin_amdgcn_readfirstlane(ctrl[0]) >= NUNIT - 36) break;
      const int own = __builtin_amdgcn_readfirstlane(ctrl[1]);
      constexpr unsigned SM = (8192u >> SLOTSH) - 1u;
      if (!NOPST && blockIdx.x < 16 && lane < 8) __builtin_amdgcn_raw_buffer_store_b128(u32x4{(unsigned)own, (unsigned)own, (unsigned)own, (unsigned)own}, wsr, WS_PACE + (k & SM) * 2048 + blockIdx.x * 128 + lane * 16, 0, PAUX);
      if (it < PLAG || NOPLD) continue;
      int e[16];
      asm volatile("s_load_dword %0, %16, 0x0\n\ts_load_dword %1, %16, 0x80\n\ts_load_dword %2, %16, 0x100\n\ts_load_dword %3, %16, 0x180\n\t"
                   "s_load_dword %4, %16, 0x200\n\ts_load_dword %5, %16, 0x280\n\ts_load_dword %6, %16, 0x300\n\ts_load_dword %7, %16, 0x380\n\t"
                   "s_load_dword %8, %16, 0x400\n\ts_load_dword %9, %16, 0x480\n\ts_load_dword %10, %16, 0x500\n\ts_load_dword %11, %16, 0x580\n\t"
                   "s_load_dword %12, %16, 0x600\n\ts_load_dword %13, %16, 0x680\n\ts_load_dword %14, %16, 0x700\n\ts_load_dword %15, %16, 0x780\n\t"
                   "s_waitcnt lgkmcnt(0)"
                   : "=&s"(e[0]), "=&s"(e[1]), "=&s"(e[2]), "=&s"(e[3]), "=&s"(e[4]), "=&s"(e[5]), "=&s"(e[6]), "=&s"(e[7]),
                     "=&s"(e[8]), "=&s"(e[9]), "=&s"(e[10]), "=&s"(e[11]), "=&s"(e[12]), "=&s"(e[13]), "=&s"(e[14]), "=&s"(e[15])
                   : "s"(ws + WS_PACE + ((k - PLAG) & SM) * 2048) : "memory");
      int sum = 0, cnt = 0;
#pragma unroll
      for (int i = 0; i < 16; ++i) { const int d = e[i] - own; const bool ok = (unsigned)(d + 600) < 1200u; sum += ok ? d : 0; cnt += ok ? 1 : 0; }
      if (cnt >= 8) ctrl[2] = own + (int)((float)sum / (float)cnt);
    }
    return;
  }

  if (wave == XWAVE) {
#pragma unroll 1
    for (int i = 0; i < XDEL + XSPR * (kk & 31); ++i) PAUSE(4);
    const u32x4 xv = *(const GAS u32x4*)(x + b * D + 8 * lane), gv = *(const GAS u32x4*)(gw + 8 * lane);
    *(LAS u32x4*)(L + L_X + lane * 16) = xv;
    float gd = 0.f;
#pragma unroll
    for (int k = 0; k < 4; ++k) gd = dot2(xv[k], gv[k], gd);
    gd = xrow_sum(row16_sum(gd));
    const float g = 1.f / (1.f + __expf(-gd));
    ctrlf[48] = g;
    lds_arrive(L, C_X, lane);
    u64 tag;
    asm volatile("s_load_dwordx2 %0, %1, 0x0\n\ts_waitcnt lgkmcnt(0)" : "=s"(tag) : "s"(ws + WS_EPOCH + b * 256) : "memory");
    tag += TAG_STEP;
#pragma unroll 1
    for (int spin = 0; spin < SPIN_CAP; ++spin) { if (__builtin_amdgcn_readfirstlane(ctrl[C_MAX]) >= NCW + NHW) break; __builtin_amdgcn_s_sleep(16); }
    CFENCE();
    const float mg = wg_max();
    float l = 0.f;
#pragma unroll 4
    for (int i = 0; i < NKU * 64 / 1024; ++i) {
      const f32x4 sc = *(const LAS f32x4*)(L + L_SC + i * 1024 + lane * 16);
      l += (__builtin_amdgcn_exp2f(sc[0] - mg) + __builtin_amdgcn_exp2f(sc[1] - mg)) + (__builtin_amdgcn_exp2f(sc[2] - mg) + __builtin_amdgcn_exp2f(sc[3] - mg));
    }
    l = xrow_sum(row16_sum(l));
    const unsigned tlo = (unsigned)tag, thi = (unsigned)(tag >> 32);
    if (lane < 8) __builtin_amdgcn_raw_buffer_store_b128(u32x4{__float_as_uint(mg), tlo, __float_as_uint(l), thi}, wsr, WS_REC + (b * 64 + pj) * 128 + lane * 16, 0, 16);
#pragma unroll 1
    for (int i = 0; i < 12 + (kk & 31); ++i) PAUSE(12);
    u32x4 rec;
#pragma unroll 1
    for (int spin = 0; spin < SPIN_CAP; ++spin) {
      CFENCE();
      rec = __builtin_amdgcn_raw_buffer_load_b128(wsr, WS_REC + (b * 64 + (pj ^ 1)) * 128, 0, 16);
      if (__builtin_amdgcn_ballot_w64(rec[1] != tlo || rec[3] != thi) == 0ull) break;
      __builtin_amdgcn_s_sleep(16);
    }
    const float mo = __uint_as_float(rec[0]), lo = __uint_as_float(rec[2]);
    const float mh = fmaxf(mg, mo), wt = __builtin_amdgcn_exp2f(mg - mh);
    ctrlf[49] = g * wt / (wt * l + __builtin_amdgcn_exp2f(mo - mh) * lo);
    ctrl[50] = (int)(tlo & YMASK);
    lds_arrive(L, C_CF, lane);
    GAS unsigned* op = (GAS unsigned*)(out + b * D + 8 * pj + 4 * (lane & 1) + 2 * (lane >> 5));
    int off = WS_Y + (b * 64 + (lane >> 1)) * 2048 + pj * 32 + (lane & 1) * 16;
    asm volatile("" : "+v"(op), "+v"(off));
    if ((pj & (64 / NRED - 1)) != 0) return;
    STAMP(4);
    BARRIER();
    {
      const unsigned now = (unsigned)__builtin_amdgcn_s_memrealtime();
      const int d = __builtin_amdgcn_readfirstlane(ctrl[2]) + (int)(TS * 100) + FOFF - (int)now;
      const unsigned t = now + (unsigned)min(max(d, FMIN), FMAX);
#pragma unroll 1
      for (int spin = 0; spin < 4096 && (int)(t - (unsigned)__builtin_amdgcn_s_memrealtime()) > 0; ++spin) {}
    }
    STAMP(9);
    constexpr int NL = 128 / NRED;
    u32x4 yv[NL];
#pragma unroll 1
    for (int spin = 0; spin < SPIN_CAP; ++spin) {
      CFENCE();
      unsigned bad = 0;
#pragma unroll
      for (int i = 0; i < NL; ++i) yv[i] = __builtin_amdgcn_raw_buffer_load_b128(wsr, off, i * (64 / NL) * 2048, 16);
#pragma unroll
      for (int i = 0; i < NL; ++i) bad |= ((yv[i][0] ^ tlo) | (yv[i][1] ^ tlo)) | ((yv[i][2] ^ tlo) | (yv[i][3] ^ tlo));
      bad &= YMASK;
      { const u64 bal = __builtin_amdgcn_ballot_w64(bad != 0u); if (spin < 5) STAMP(10 + spin); ((volatile LAS unsigned*)(L + LDS_BYTES))[wave * 16 + 15] = spin + 1 + (bal == 1234567 ? 1 : 0); }
      if (__builtin_amdgcn_ballot_w64(bad != 0u) == 0ull) break;
      if (PSLP) __builtin_amdgcn_s_sleep(PSLP);
    }
    float o[4];
#pragma unroll
    for (int k = 0; k < 4; ++k) {
      float t[NL];
#pragma unroll
      for (int i = 0; i < NL; ++i) t[i] = __uint_as_float(yv[i][k] & ~YMASK);
#pragma unroll
      for (int st = NL / 2; st >= 1; st /= 2)
#pragma unroll
        for (int i = 0; i < st; ++i) t[i] += t[i + st];
      o[k] = t[0];
    }
    float e0 = swadd32(o[0], o[2]), e1 = swadd32(o[1], o[3]);
    e0 = swadd16(e0, e0); e1 = swadd16(e1, e1);
    e0 += dpp<ROR8>(e0); e1 += dpp<ROR8>(e1); e0 += dpp<ROR4>(e0); e1 += dpp<ROR4>(e1); e0 += dpp<ROR2>(e0); e1 += dpp<ROR2>(e1);
    if ((lane & 30) == 0) OUT_STORE(op, pack_bf16(e0, e1));
    if (pj == 0 && lane < 8) __builtin_amdgcn_raw_buffer_store_b128(u32x4{tlo, thi, tlo, thi}, wsr, WS_EPOCH + b * 256 + lane * 16, 0, 16);
    STAMP(7);
    VM_DRAIN();
    STAMP(8);
    STAMP_FLUSH();
    return;
  }

  const int cw = wave - 1, hw = wave - 1 - NCW;
  int landed = 0;
  auto wait_unit = [&](int u) __attribute__((always_inline)) {
#pragma unroll 1
    for (int spin = 0; landed <= u && spin < SPIN_CAP; ++spin) {
      landed = __builtin_amdgcn_readfirstlane(ctrl[0]);
      if (landed > u) break;
      if (USL) __builtin_amdgcn_s_sleep(USL);
    }
    CFENCE();
  };
  auto take = [&](int u, int next, u32x4& a0, u32x4& a1, u32x4& a2, u32x4& a3) __attribute__((always_inline)) {
    wait_unit(u);
    const LAS u32x4* sp = (const LAS u32x4*)(L + (u & (NSLOT - 1)) * UNIT + lane * 16);
    a0 = sp[0]; a1 = sp[64]; a2 = sp[128]; a3 = sp[192];
    LDS_WAIT();
    ctrl[16 + cw] = next;
    asm volatile("" : "+v"(a0), "+v"(a1), "+v"(a2), "+v"(a3) :: "memory");
  };
  u32x4 q;
  auto build_q = [&]() __attribute__((always_inline)) {
    lds_await(ctrl, C_Q, NHW);
    const LAS f32x4* qp = (const LAS f32x4*)(L + L_QP + (2 * r4 * DH + 8 * c16) * 4);
    const f32x4 lo = qp[0] + qp[DH / 4], hi = qp[1] + qp[DH / 4 + 1];
    q[0] = pack_bf16(xrow_sum(lo[0]), xrow_sum(lo[1])); q[1] = pack_bf16(xrow_sum(lo[2]), xrow_sum(lo[3]));
    q[2] = pack_bf16(xrow_sum(hi[0]), xrow_sum(hi[1])); q[3] = pack_bf16(xrow_sum(hi[2]), xrow_sum(hi[3]));
  };
  constexpr float SCALE = 0.088388347648318440f, LOG2E = 1.4426950408889634f, C2 = SCALE * LOG2E;
  float mx = -INFINITY;
  auto score = [&](int u, const u32x4& k0, const u32x4& k1, const u32x4& k2, const u32x4& k3) __attribute__((always_inline)) {
    float d0 = 0.f, d1 = 0.f, d2 = 0.f, d3 = 0.f;
#pragma unroll
    for (int k = 0; k < 4; ++k) { d0 = dot2(k0[k], q[k], d0); d1 = dot2(k1[k], q[k], d1); d2 = dot2(k2[k], q[k], d2); d3 = dot2(k3[k], q[k], d3); }
    const unsigned s01 = pack_bf16(row16_sum(d0), row16_sum(d1)), s23 = pack_bf16(row16_sum(d2), row16_sum(d3));
    const unsigned t01 = pack_bf16(bflo(s01) * SCALE, bfhi(s01) * SCALE), t23 = pack_bf16(bflo(s23) * SCALE, bfhi(s23) * SCALE);
    const f32x4 sc = {bflo(t01) * LOG2E, bfhi(t01) * LOG2E, bflo(t23) * LOG2E, bfhi(t23) * LOG2E};
    mx = fmaxf(fmaxf(mx, fmaxf(sc[0], sc[1])), fmaxf(sc[2], sc[3]));
    if (c16 == 0) *(LAS f32x4*)(L + L_SC + u * 64 + r4 * 16) = sc;
  };

  if (hw < 0) {
    {
      u32x4 hk[4 * NHC];
#pragma unroll
      for (int i = 0; i < NHC; ++i) take(cw + NCW * i, i + 1 < NHC ? cw + NCW * (i + 1) : NHELD + cw, hk[4 * i], hk[4 * i + 1], hk[4 * i + 2], hk[4 * i + 3]);
      STAMP(10);
      build_q();
      STAMP(3);
#pragma unroll
      for (int i = 0; i < NHC; ++i) { score(cw + NCW * i, hk[4 * i], hk[4 * i + 1], hk[4 * i + 2], hk[4 * i + 3]); __builtin_amdgcn_sched_barrier(0); }
    }
#pragma unroll 1
    for (int u = NHELD + cw; u < NKU; u += NCW) {
      u32x4 k0, k1, k2, k3;
      take(u, u + NCW, k0, k1, k2, k3);
      score(u, k0, k1, k2, k3);
    }
    ctrlf[32 + cw] = xrow_max(mx);
    lds_arrive(L, C_MAX, lane);
    STAMP(11);
    lds_await(ctrl, C_MAX, NCW + NHW);
    const float mg = wg_max();
    STAMP(12);
    float acc[8] = {0.f, 0.f, 0.f, 0.f, 0.f, 0.f, 0.f, 0.f};
    LAS unsigned* rp = (LAS unsigned*)(L + L_REC + cw * 272 + (8 * c16 + 2 * r4) * 2);
    asm volatile("" : "+v"(rp));
    float cf = 0.f;
#pragma unroll 1
    for (int u = NKU + cw; u < NUNIT; u += NCW) {
      const f32x4 sc = *(const LAS f32x4*)(L + L_SC + (u - NKU) * 64 + r4 * 16);
      float p[4];
#pragma unroll
      for (int t = 0; t < 4; ++t) { p[t] = __builtin_amdgcn_exp2f(sc[t] - mg); asm volatile("" : "+v"(p[t])); }
      if ((u & ~(NCW - 1)) == NUNIT - 32) { lds_await(ctrl, C_CF, 1); cf = ctrlf[49]; }
      u32x4 v[4];
      take(u, u + NCW, v[0], v[1], v[2], v[3]);
#pragma unroll
      for (int t = 0; t < 4; ++t)
#pragma unroll
        for (int k = 0; k < 4; ++k) { acc[2 * k] = fmaf(p[t], bflo(v[t][k]), acc[2 * k]); acc[2 * k + 1] = fmaf(p[t], bfhi(v[t][k]), acc[2 * k + 1]); }
    }
    STAMP(5);
    const float e0 = swadd16(swadd32(acc[0], acc[4]), swadd32(acc[2], acc[6])), e1 = swadd16(swadd32(acc[1], acc[5]), swadd32(acc[3], acc[7]));
    *rp = pack_bf16(e0 * cf, e1 * cf);
    BARRIER();
    return;
  }

  u32x4 wb[16];
  const unsigned tstart = (unsigned)__builtin_amdgcn_readfirstlane(ctrl[8]);
  {
    const __amdgpu_buffer_rsrc_t rq = mkrsrc(wq, D * H * DH * 2);
    const int vo = ((64 * hw + 16 * r4) * (H * DH) + h * DH + 8 * c16) * 2;
#pragma unroll
    for (int i = 0; i < 16; ++i) { if (i == 0) wait_until(tstart + TQ + hw * QA); else PAUSE(QS); wb[i] = ldw(rq, vo, i * (H * DH * 2)); }
    lds_await(ctrl, C_X, 1);
    const u32x4 xa = *(const LAS u32x4*)(L + L_X + (64 * hw + 16 * r4) * 2), xc = *(const LAS u32x4*)(L + L_X + (64 * hw + 16 * r4 + 8) * 2);
    float a[8] = {0.f, 0.f, 0.f, 0.f, 0.f, 0.f, 0.f, 0.f};
#pragma unroll
    for (int i = 0; i < 8; ++i) {
      const unsigned xp = i < 4 ? xa[i] : xc[i - 4];
#pragma unroll
      for (int k = 0; k < 4; ++k) {
        a[2 * k] = dot2(perm_lo(wb[2 * i][k], wb[2 * i + 1][k]), xp, a[2 * k]);
        a[2 * k + 1] = dot2(perm_hi(wb[2 * i][k], wb[2 * i + 1][k]), xp, a[2 * k + 1]);
      }
    }
    const float e0 = swadd16(swadd32(a[0], a[4]), swadd32(a[2], a[6])), e1 = swadd16(swadd32(a[1], a[5]), swadd32(a[3], a[7]));
    *(LAS f32x2*)(L + L_QP + (hw * DH + 8 * c16 + 2 * r4) * 4) = f32x2{e0, e1};
    lds_arrive(L, C_Q, lane);
    STAMP(2);
    const int hb = NHC * NCW + hw;
#pragma unroll
    for (int i = 0; i < NHH; ++i) take(hb + NHW * i, i + 1 < NHH ? hb + NHW * (i + 1) : NEVER, wb[4 * i], wb[4 * i + 1], wb[4 * i + 2], wb[4 * i + 3]);
    STAMP(10);
    build_q();
#pragma unroll
    for (int i = 0; i < NHH; ++i) { score(hb + NHW * i, wb[4 * i], wb[4 * i + 1], wb[4 * i + 2], wb[4 * i + 3]); __builtin_amdgcn_sched_barrier(0); }
    ctrlf[32 + cw] = xrow_max(mx);
    lds_arrive(L, C_MAX, lane);
  }
  f32x4 y[4] = {{0.f, 0.f, 0.f, 0.f}, {0.f, 0.f, 0.f, 0.f}, {0.f, 0.f, 0.f, 0.f}, {0.f, 0.f, 0.f, 0.f}};
  if ((pj & 7) != 7) {
    const int sl = 7 * (pj >> 3) + (pj & 7);
    const __amdgpu_buffer_rsrc_t r1 = mkrsrc(w1, D * F * 2), r3 = mkrsrc(w3, D * F * 2), r2 = mkrsrc(w2, F * D * 2);
    const int vo = ((64 * hw + (lane & 60)) * F + FS * sl + 8 * (lane & 3)) * 2;
#pragma unroll
    for (int i = 0; i < 8; ++i) { if (i == 0) wait_until(tstart + T1 + hw * A1); else PAUSE(S1); wb[i] = ldw(i < 4 ? r1 : r3, vo, (i & 3) * (F * 2)); }
    const u32x2 xm = *(const LAS u32x2*)(L + L_X + (64 * hw + (lane & 60)) * 2);
    float a[16];
#pragma unroll
    for (int m = 0; m < 2; ++m)
#pragma unroll
      for (int k = 0; k < 4; ++k) {
        a[8 * m + 2 * k] = dot2(perm_lo(wb[4 * m + 2][k], wb[4 * m + 3][k]), xm[1], dot2(perm_lo(wb[4 * m][k], wb[4 * m + 1][k]), xm[0], 0.f));
        a[8 * m + 2 * k + 1] = dot2(perm_hi(wb[4 * m + 2][k], wb[4 * m + 3][k]), xm[1], dot2(perm_hi(wb[4 * m][k], wb[4 * m + 1][k]), xm[0], 0.f));
      }
    f32x4 e;
#pragma unroll
    for (int k = 0; k < 4; ++k) {
      float t = swadd16(swadd32(a[k], a[8 + k]), swadd32(a[4 + k], a[12 + k]));
      t += dpp<ROR8>(t); e[k] = t + dpp<ROR4>(t);
    }
    if ((lane & 12) == 0) *(LAS f32x4*)(L + L_MP + (hw * 64 + (lane & 32) + 8 * (lane & 3) + 4 * (r4 & 1)) * 4) = e;
    lds_arrive(L, C_MLP, lane);
    u32x2 wr[8];
#pragma unroll
    for (int i = 0; i < 8; ++i) { if (i == 0) wait_until(tstart + T2 + hw * A2); else PAUSE(S2); wr[i] = ldw2(r2, ((FS * sl + 8 * r4) * D + 64 * hw + 4 * c16) * 2, i * (D * 2)); }
    lds_await(ctrl, C_MLP, NHW);
    const LAS float* mp = (const LAS float*)(L + L_MP) + (lane & 31);
    float ga = 0.f, up = 0.f;
#pragma unroll
    for (int w = 0; w < NHW; ++w) { ga += mp[w * 64]; up += mp[w * 64 + 32]; }
    const float hv = ga / (1.f + __expf(-ga)) * up * (1.f - ctrlf[48]);
    const unsigned hh = pack_bf16(hv, 0.f), hl = pack_bf16(hv - bflo(hh), 0.f);
    *(LAS u16*)(L + L_H + hw * 128 + lane * 2) = (u16)(lane < 32 ? hh : hl);
    LDS_WAIT();
    u32x4 ha = *(const LAS u32x4*)(L + L_H + hw * 128 + (c16 & 1) * 64 + r4 * 16);
    if (c16 >= 2) ha = u32x4{0u, 0u, 0u, 0u};
#pragma unroll
    for (int e = 0; e < 4; ++e) {
      u32x4 f;
#pragma unroll
      for (int k = 0; k < 4; ++k) f[k] = (e & 1) ? perm_hi(wr[2 * k][e >> 1], wr[2 * k + 1][e >> 1]) : perm_lo(wr[2 * k][e >> 1], wr[2 * k + 1][e >> 1]);
      y[e] = mfma(ha, f, y[e]);
    }
  }
#pragma unroll
  for (int e = 0; e < 4; ++e) asm volatile("" : "+v"(y[e]));
  {
    const __amdgpu_buffer_rsrc_t ro = mkrsrc(wo, H * DH * D * 2);
#pragma unroll
    for (int t = 0; t < 4; ++t) {
      u32x2 wr[8];
#pragma unroll
      for (int i = 0; i < 8; ++i) { if (t + i == 0) wait_until(tstart + TO + hw * AO); else PAUSE(SO); wr[i] = ldw2(ro, ((h * DH + 32 * r4) * D + 64 * hw + 4 * c16) * 2, (8 * t + i) * (D * 2)); }
#pragma unroll
      for (int e = 0; e < 4; ++e) {
#pragma unroll
        for (int k = 0; k < 4; ++k) wb[4 * t + e][k] = (e & 1) ? perm_hi(wr[2 * k][e >> 1], wr[2 * k + 1][e >> 1]) : perm_lo(wr[2 * k][e >> 1], wr[2 * k + 1][e >> 1]);
        asm volatile("" : "+v"(wb[4 * t + e]));
      }
    }
  }
  lds_await(ctrl, C_CF, 1);
  unsigned yt = (unsigned)ctrl[50];
  int yo = WS_Y + (b * 64 + pj) * 2048 + hw * 256 + c16 * 16;
  const LAS u32x4* rec = (const LAS u32x4*)(L + L_REC + (lane & 3) * 272 + r4 * 64);
  unsigned ym = ~YMASK;
  asm volatile("" : "+v"(yt), "+v"(yo), "+v"(rec), "+s"(ym));
  STAMP(4);
  BARRIER();
  STAMP(9);
  {
    const u32x4 a[4] = {rec[0], rec[1], rec[2], rec[3]};
#pragma unroll
    for (int t = 0; t < 4; ++t)
#pragma unroll
      for (int e = 0; e < 4; ++e) y[e] = mfma(a[t], wb[4 * t + e], y[e]);
    u32x4 v;
#pragma unroll
    for (int e = 0; e < 4; ++e) {
      const f32x2 t = y[e].xy + y[e].zw;
      unsigned r;
      asm("v_add_f32 %0, %1, %2\n\tv_add_u32 %0, %5, %0\n\tv_and_or_b32 %0, %0, %3, %4" : "=&v"(r) : "v"(t[0]), "v"(t[1]), "s"(ym), "v"(yt), "i"((YMASK + 1u) / 2u));
      v[e] = r;
    }
    if (lane < 16) __builtin_amdgcn_raw_buffer_store_b128(v, wsr, yo, 0, 16);
  }
  STAMP(7);
}

extern "C" void kernel_launch(void* const* d_in, const int* in_sizes, int n_in, void* d_out, int out_size, void* d_ws, size_t ws_size,
                              hipStream_t stream) {
  gated_mix_kernel<<<256, NTHR, 0, stream>>>((const u16*)d_in[0], (const u16*)d_in[1], (const u16*)d_in[2], (const u16*)d_in[3], (const u16*)d_in[4],
                                             (const u16*)d_in[6], (const u16*)d_in[7], (const u16*)d_in[8], (const u16*)d_in[9], (u16*)d_out,
                                             (unsigned char*)d_ws);
}
```

```cpp
#include <hip/hip_runtime.h>
#include <hip/hip_bf16.h>
#define STAMPS 1
#define MWAVE 1
#define MIDX 10
#define MTYPE 0
#define RWAVE -1
#define RIDX 0
#define MSCALE 10.f

#define GAS __attribute__((address_space(1)))
#define LAS __attribute__((address_space(3)))
typedef unsigned short u16;
typedef unsigned long long u64;
using u32x4 = __attribute__((ext_vector_type(4))) unsigned;
using u32x2 = __attribute__((ext_vector_type(2))) unsigned;
using f32x4 = __attribute__((ext_vector_type(4))) float;
using f32x2 = __attribute__((ext_vector_type(2))) float;
using bf16x2 = __attribute__((ext_vector_type(2))) __bf16;
using bf16x8 = __attribute__((ext_vector_type(8))) __bf16;

constexpr int B = 4, D = 512, H = 32, SK = 8192, DH = 128, F = 1792;
constexpr int NCW = 4, NHW = 8, XWAVE = 1 + NCW + NHW, NWAVE = XWAVE + 2, NTHR = NWAVE * 64;
#define QA 80
#define QB 5
#define T1 1200
#define A1 150
#define B1 18
#define T2 2500
#define A2 40
#define B2 4
#define TO 2900
#define AO 350
#define BO 8
#define TS 70.6
#define REF0 180
#define PLAG 1
#define NOPST 0
#define NOPLD 0
#define PAUX 16
#define RAUX 16
#define PFW 0
#define TQ 130
#define NINIT 6
#define CODE_KIB 21
#define FDEL 0
#define CODEPF 1
#define YST 16
#define YLD 16
#define RED16 0
#define MASKP 0
#define PSLP 0
#define OUTSC 0
#define OUT_STORE(p, v) *(GAS unsigned*)(p) = v
#define CLR0 1
#define MLPFULL 0
#define MLPPF 0
#define TP 1200
#define AP 12
#define GRID 0
#define B2BD 0
#define B2BAUX 16
#define WOB2B 0
#define TOUCHW 1
#define W0 30
#define BARPOS 12
#define NO_WO 0
#define TLBPF 0
#define TMODE 0
#define SPW 4
#define QS 2
#define S1 6
#define S2 1
#define SO 2
#define USL 0
#define JUMP 100
#define DECAY 6
#define HOLD0 10000
#define UEND 420
#define WGAIN2 18
#define WMID2 WMID
#define FOFF 150
#define FMIN 45
#define FMAX 150
#define NRED 64
#define SLOTSH 8
#define WMID 20.5
#define WGAIN 10
#define WMIN 14
#define WMAX 27
#define CLRAUX 16
#define PCONT 1
#define XDEL 20
#define XSPR 0
#define XNT 0
#define MIMIC 1
#define NCLR 2
#define NOFLAG 0
#define CSPR 0
constexpr int UNIT = 4096;
constexpr int NSLOT = 32, RING = NSLOT * UNIT;
constexpr int NKU = (SK / 2) / 16, NUNIT = 2 * NKU;
constexpr int NHC = 6, NHH = 4, NHELD = NHC * NCW + NHH * NHW;
constexpr int FS = 32;
constexpr int L_CTRL = RING;
constexpr int L_X = L_CTRL + 256;
constexpr int L_QP = L_X + D * 2;
constexpr int L_MP = L_QP + NHW * DH * 4;
constexpr int L_REC = L_MP + NHW * 64 * 4;
constexpr int L_H = L_REC + NCW * 272;
constexpr int L_SC = L_H + NHW * 128;
constexpr int LDS_BYTES = L_SC + NKU * 64;
static_assert(LDS_BYTES <= 159 * 1024, "lds");
static_assert(WMIN <= (int)WMID && (int)WMID <= WMAX && WMAX <= 28, "window");
constexpr int WS_EPOCH = 0;
constexpr int WS_REC = 4096;
constexpr int WS_PACE = WS_REC + 256 * 128;
constexpr int WS_Y = WS_PACE + 64 * 2048;
constexpr int WS_BYTES = WS_Y + B * 64 * 2048;
constexpr u64 TAG_STEP = 0x9E3779B97F4A7C15ull;
constexpr unsigned YMASK = 0xFFFu;
constexpr int SPIN_CAP = 1 << 18;
constexpr int NEVER = 1 << 20;
constexpr int C_Q = 4, C_MAX = 5, C_MLP = 6, C_X = 7, C_CF = 9;

__device__ __forceinline__ float bflo(unsigned u) { return __builtin_bit_cast(float, u << 16); }
__device__ __forceinline__ float bfhi(unsigned u) { return __builtin_bit_cast(float, u & 0xffff0000u); }
__device__ __forceinline__ float dot2(unsigned a, unsigned b, float acc) {
  return __builtin_amdgcn_fdot2_f32_bf16(__builtin_bit_cast(bf16x2, a), __builtin_bit_cast(bf16x2, b), acc, false);
}
__device__ __forceinline__ unsigned pack_bf16(float lo, float hi) {
  const bf16x2 v = {(__bf16)lo, (__bf16)hi};
  return __builtin_bit_cast(unsigned, v);
}
__device__ __forceinline__ unsigned perm_lo(unsigned a, unsigned b) { return __builtin_amdgcn_perm(b, a, 0x05040100u); }
__device__ __forceinline__ unsigned perm_hi(unsigned a, unsigned b) { return __builtin_amdgcn_perm(b, a, 0x07060302u); }
template <int CTRL> __device__ __forceinline__ float dpp(float x) {
  return __builtin_bit_cast(float, __builtin_amdgcn_mov_dpp(__builtin_bit_cast(int, x), CTRL, 0xf, 0xf, true));
}
constexpr int XOR1 = 0xB1, ROR1 = 0x121, ROR2 = 0x122, ROR4 = 0x124, ROR8 = 0x128;
__device__ __forceinline__ float row16_sum(float x) { x += dpp<ROR8>(x); x += dpp<ROR4>(x); x += dpp<ROR2>(x); x += dpp<ROR1>(x); return x; }
__device__ __forceinline__ float swadd32(float a, float b) {
  auto t = __builtin_amdgcn_permlane32_swap(__float_as_uint(a), __float_as_uint(b), false, false);
  return __uint_as_float(t[0]) + __uint_as_float(t[1]);
}
__device__ __forceinline__ float swadd16(float a, float b) {
  auto t = __builtin_amdgcn_permlane16_swap(__float_as_uint(a), __float_as_uint(b), false, false);
  return __uint_as_float(t[0]) + __uint_as_float(t[1]);
}
__device__ __forceinline__ float xrow_sum(float x) { x = swadd16(x, x); return swadd32(x, x); }
__device__ __forceinline__ float xrow_max(float x) {
  auto s = __builtin_amdgcn_permlane16_swap(__float_as_uint(x), __float_as_uint(x), false, false);
  x = fmaxf(__uint_as_float(s[0]), __uint_as_float(s[1]));
  auto t = __builtin_amdgcn_permlane32_swap(__float_as_uint(x), __float_as_uint(x), false, false);
  return fmaxf(__uint_as_float(t[0]), __uint_as_float(t[1]));
}
__device__ __forceinline__ __amdgpu_buffer_rsrc_t mkrsrc(const void* p, int bytes) { return __builtin_amdgcn_make_buffer_rsrc((void*)p, 0, bytes, 0x00020000); }
__device__ __forceinline__ u32x4 ldw(__amdgpu_buffer_rsrc_t r, int voff, int soff) { return __builtin_amdgcn_raw_buffer_load_b128(r, voff, soff, 2); }
#define LDS_WAIT() asm volatile("s_waitcnt lgkmcnt(0)" ::: "memory")
#define VM_DRAIN() asm volatile("s_waitcnt vmcnt(0)" ::: "memory")
#define CFENCE() asm volatile("" ::: "memory")
#define BARRIER() asm volatile("s_waitcnt lgkmcnt(0)\n\ts_barrier" ::: "memory")
#define PAUSE(n) do { __builtin_amdgcn_sched_barrier(0); asm volatile("s_sleep %0" :: "i"(n) : "memory"); __builtin_amdgcn_sched_barrier(0); } while (0)

__device__ __forceinline__ void lds_arrive(LAS unsigned char* L, int idx, int lane) {
  LDS_WAIT();
  if (lane == 0) __hip_atomic_fetch_add((LAS int*)(L + L_CTRL) + idx, 1, __ATOMIC_RELAXED, __HIP_MEMORY_SCOPE_WORKGROUP);
}
__device__ __forceinline__ void lds_await(volatile LAS int* ctrl, int idx, int n) {
#pragma unroll 1
  for (int spin = 0; spin < SPIN_CAP; ++spin) { if (__builtin_amdgcn_readfirstlane(ctrl[idx]) >= n) break; __builtin_amdgcn_s_sleep(1); }
  CFENCE();
}

__device__ __forceinline__ void wait_until(unsigned t) {
  __builtin_amdgcn_sched_barrier(0);
#pragma unroll 1
  for (int spin = 0; spin < 4096; ++spin) {
    const int d = (int)(t - (unsigned)__builtin_amdgcn_s_memrealtime());
    if (d <= 0) break;
    if (d > 24) __builtin_amdgcn_s_sleep(6);
  }
  CFENCE();
  __builtin_amdgcn_sched_barrier(0);
}
#define STR2(x) #x
#define STR(x) STR2(x)
constexpr int TAU2 = 2 * (int)(TS * 100.0 * 256.0 / NUNIT);
__device__ __forceinline__ f32x4 mfma(const u32x4& a, const u32x4& b, const f32x4& c) {
  return __builtin_amdgcn_mfma_f32_16x16x32_bf16(__builtin_bit_cast(bf16x8, a), __builtin_bit_cast(bf16x8, b), c, 0, 0, 0);
}
__device__ __forceinline__ u32x2 ldw2(__amdgpu_buffer_rsrc_t r, int voff, int soff) { return __builtin_amdgcn_raw_buffer_load_b64(r, voff, soff, 2); }
template <int CTRL> __device__ __forceinline__ int dppi(int x) { return __builtin_amdgcn_mov_dpp(x, CTRL, 0xf, 0xf, true); }

#define STAMP(i) do { __builtin_amdgcn_sched_barrier(0); ((volatile LAS unsigned*)(L + LDS_BYTES))[wave * 16 + (i)] = (unsigned)__builtin_amdgcn_s_memrealtime(); __builtin_amdgcn_sched_barrier(0); } while (0)
#define STAMP_FLUSH() do { LDS_WAIT(); const __amdgpu_buffer_rsrc_t dr = mkrsrc(ws + (4 << 20), 256 * 1024); \
    __builtin_amdgcn_raw_buffer_store_b128(*(volatile LAS u32x4*)(L + LDS_BYTES + lane * 16), dr, blockIdx.x * 1024 + lane * 16, 0, 16); \
    if (blockIdx.x == 0) { const unsigned t0 = __builtin_amdgcn_readfirstlane(((volatile LAS unsigned*)(L + LDS_BYTES))[0]); \
      while ((unsigned)__builtin_amdgcn_s_memrealtime() - t0 < 13000u) __builtin_amdgcn_s_sleep(8); \
      CFENCE(); float sum = 0.f, mxv = -1e9f; \
      for (int i = 0; i < 4; ++i) { const int wg = lane * 4 + i; \
        const unsigned ref = (RWAVE < 0) ? t0 : __builtin_amdgcn_raw_buffer_load_b32(dr, wg * 1024 + ((RWAVE < 0 ? 0 : RWAVE) * 16 + RIDX) * 4, 0, 16); \
        const float v = (float)(int)(__builtin_amdgcn_raw_buffer_load_b32(dr, wg * 1024 + (MWAVE * 16 + MIDX) * 4, 0, 16) - ref); \
        sum += v; mxv = fmaxf(mxv, v); } \
      sum = xrow_sum(row16_sum(sum)) * (1.f / 256.f); \
      float gm = mxv; gm = fmaxf(gm, dpp<ROR8>(gm)); gm = fmaxf(gm, dpp<ROR4>(gm)); gm = fmaxf(gm, dpp<ROR2>(gm)); gm = fmaxf(gm, dpp<ROR1>(gm)); gm = xrow_max(gm); \
      const float met = (MTYPE == 0 ? sum : MTYPE == 1 ? gm : gm - sum) * MSCALE; \
      const unsigned tgt = __builtin_amdgcn_readfirstlane((unsigned)(15000.f + met)); \
      while ((unsigned)__builtin_amdgcn_s_memrealtime() - t0 < tgt) __builtin_amdgcn_s_sleep(1); } } while (0)
#define STAMP_LDS 1024

__global__ void __launch_bounds__(NTHR) gated_mix_kernel(const u16* x, const u16* kc, const u16* vc, const u16* wq, const u16* wo, const u16* w1,
                                                         const u16* w3, const u16* w2, const u16* gw, u16* out, unsigned char* ws) {
  __shared__ __attribute__((aligned(16))) unsigned char lds_raw[LDS_BYTES + STAMP_LDS];
  LAS unsigned char* L = (LAS unsigned char*)lds_raw;
  volatile LAS int* ctrl = (volatile LAS int*)(L + L_CTRL);
  volatile LAS float* ctrlf = (volatile LAS float*)(L + L_CTRL);
  u64 t_first; asm volatile("s_memrealtime %0\n\ts_waitcnt lgkmcnt(0)" : "=s"(t_first));
  __builtin_amdgcn_sched_barrier(0);
  if (threadIdx.x < 14 * 64) ((volatile LAS unsigned*)(L + LDS_BYTES))[(threadIdx.x >> 6) * 16] = (unsigned)t_first;
#define DBG(x) x
  {
    unsigned va, vb, vo, pa, nxa, t0, t1, t2, t3;
#define CODELD "global_load_lds_dwordx4 %[t0], s[56:57]\n\t"
#define ASTAMP(i) DBG("s_memrealtime s[60:61]\n\ts_waitcnt lgkmcnt(0)\n\tv_mov_b32 %[t1], %c[dbg] + 4 * " #i "\n\tv_mov_b32 %[t2], s60\n\tds_write_b32 %[t1], %[t2]\n\t")
#define WAITW "s_waitcnt (wcnt & 15) | ((wcnt >> 4) << 14) | 0x0F70\n\t"
#define SP(v, sb, wait) wait "global_load_lds_dword " v ", " sb " nt\n\tglobal_load_lds_dword " v ", " sb " sc1\n\t"
#define SW "s_waitcnt vmcnt(%c[spw])\n\t"
#define UNIT4(v) \
        WAITW "global_load_lds_dwordx4 " v ", s[40:41] nt\n\t" \
        WAITW "global_load_lds_dwordx4 " v ", s[40:41] offset:1024 nt\n\t" \
        WAITW "global_load_lds_dwordx4 " v ", s[40:41] offset:2048 nt\n\t" \
        WAITW "global_load_lds_dwordx4 " v ", s[40:41] offset:3072 nt\n\t"
    asm volatile(
        "s_getpc_b64 s[56:57]\n\t"
        "s_load_dwordx8 s[44:51], %[ka], 0x18\n\t"
        "s_load_dwordx2 s[52:53], %[ka], 0x38\n\t"
        "s_load_dwordx4 s[40:43], %[ka], 0x8\n\t"
        "v_readfirstlane_b32 s54, %[tid]\n\t"
        "s_mov_b32 m0, %c[jk]\n\t"
        "s_lshr_b32 s59, %[bid], 3\n\t"
        "s_cmp_ge_u32 s54, 64\n\t"
        "s_cbranch_scc1 90f\n\t"
        "s_min_u32 s54, s59, %c[nch]\n\t"
        "s_lshl_b32 s54, s54, 10\n\t"
        "v_lshlrev_b32 %[vo], 4, %[tid]\n\t"
        "s_and_b32 s56, s56, -128\n\t"
        "v_add_u32 %[t0], s54, %[vo]\n\t"
        "s_and_b32 s58, %[bid], 7\n\t"
        CODELD
        "s_setprio 3\n\t"
        "v_lshlrev_b32 %[t1], 7, %[tid]\n\t"
        "s_lshl_b32 s54, s59, 7\n\t"
        "s_add_u32 s54, s54, s58\n\t"
        "s_lshl_b32 s54, s54, 10\n\t"
        "v_lshrrev_b32 %[va], 3, %[tid]\n\t"
        "v_mul_u32_u24 %[va], 8192 - 1024, %[va]\n\t"
        "v_add3_u32 %[va], %[va], %[t1], s54\n\t"
        "v_add_u32 %[t2], 0x10000, %[va]\n\t"
        "s_lshl_b32 s55, s58, 5\n\t"
        "s_add_u32 s55, s55, s59\n\t"
        "s_mul_i32 s54, s55, 7 * 1024\n\t"
        "v_min_u32 %[t3], 55 * 128, %[t1]\n\t"
        "v_add_u32 %[t3], s54, %[t3]\n\t"
        "s_mul_i32 s54, s58, 448\n\t"
        "s_and_b32 s54, s54, -128\n\t"
        "s_mul_i32 s57, s59, 16 * 3584\n\t"
        "s_add_u32 s54, s54, s57\n\t"
        "v_lshrrev_b32 %[t0], 2, %[tid]\n\t"
        "v_mul_u32_u24 %[t0], 3584 - 512, %[t0]\n\t"
        "v_add3_u32 %[t0], %[t0], %[t1], s54\n\t"
        "v_lshl_add_u32 %[vb], s55, 14, %[t1]\n\t"
        "v_add_u32 %[t1], 0x2000, %[vb]\n\t"
        "s_and_b32 s54, %[bid], 0xc0\n\t"
        "s_lshl3_add_u32 s54, s58, s54\n\t"
        "s_and_b32 s56, s59, 7\n\t"
        "s_add_u32 s54, s54, s56\n\t"
        "s_lshl_b32 s56, s54, 20\n\t"
        "s_waitcnt lgkmcnt(0)\n\t"
        SP("%[va]", "s[44:45]", "") SP("%[t2]", "s[44:45]", "") SP("%[t3]", "s[52:53]", "")
        ".if %c[spw] <= 4\n\ts_barrier\n\t.endif\n\t"
        SP("%[t0]", "s[48:49]", SW)
        ".if %c[spw] == 6\n\ts_barrier\n\t.endif\n\t"
        SP("%[t0]", "s[50:51]", SW)
        ".if %c[spw] == 8\n\ts_barrier\n\t.endif\n\t"
        SP("%[vb]", "s[46:47]", SW) SP("%[t1]", "s[46:47]", SW)
        ".if %c[spw] > 8\n\ts_barrier\n\t.endif\n\t"
        ASTAMP(6)
        "s_add_u32 s40, s40, s56\n\t"
        "s_addc_u32 s41, s41, 0\n\t"
        "s_add_u32 s42, s42, s56\n\t"
        "s_addc_u32 s43, s43, 0\n\t"
        "s_sub_u32 s42, s42, %c[nku] * 4096\n\t"
        "s_subb_u32 s43, s43, 0\n\t"
        ".set so, 0\n\t"
        ".set wcnt, %c[spw]\n\t"
        ".rept 4\n\t"
        "s_movk_i32 m0, so\n\t"
        ".if (so & 4096) == 0\n\t"
        WAITW
        ".set wcnt, wcnt + 6\n\t"
        ".else\n\t"
        "s_nop 0\n\t"
        ".endif\n\t"
        "global_load_lds_dwordx4 %[vo], s[40:41] nt\n\t"
        "global_load_lds_dwordx4 %[vo], s[40:41] offset:1024 nt\n\t"
        "global_load_lds_dwordx4 %[vo], s[40:41] offset:2048 nt\n\t"
        "global_load_lds_dwordx4 %[vo], s[40:41] offset:3072 nt\n\t"
        "v_add_u32 %[vo], 0x1000, %[vo]\n\t"
        ".set so, so + 4096\n\t"
        ".endr\n\t"
        ".if %c[spw] <= 4\n\t"
        "s_waitcnt vmcnt(16)\n\t"
        ".endif\n\t"
        ASTAMP(7)
        "s_getpc_b64 s[50:51]\n"
        "20:\n\t"
        "s_add_u32 s50, s50, 23f-20b-%c[wmin]*128\n\t"
        "s_addc_u32 s51, s51, -1\n\t"
        "v_mov_b32 %[pa], %c[ctl]\n\t"
        "v_and_b32 %[nxa], 0x3c0 >> 2, %[vo]\n\t"
        "v_lshrrev_b32 %[nxa], 2, %[nxa]\n\t"
        "v_add_u32 %[nxa], %c[ctl] + 64, %[nxa]\n\t"
        "s_memrealtime s[58:59]\n\t"
        "s_mov_b32 s62, 0\n\t"
        "s_mov_b32 s63, %c[gain]\n\t"
        "s_mov_b32 s64, %c[wmid]\n\t"
        "s_mov_b32 s44, 4\n\t"
        "s_mov_b32 s45, %c[wmid] >> 8\n\t"
        "s_mov_b32 s46, 0x4000\n\t"
        "s_mov_b32 s47, 2 * %c[tau2]\n\t"
        "s_mov_b32 s48, %c[nslot]\n\t"
        "s_mov_b32 s49, %c[nku]\n\t"
        "s_waitcnt lgkmcnt(0)\n\t"
        "s_mov_b32 s59, 10000\n"
        "22:\n\t"
        "s_memrealtime s[56:57]\n\t"
        "ds_read_b32 %[t3], %[pa] offset:8\n\t"
        "s_lshl_b32 s52, s45, 7\n\t"
        "s_add_u32 s52, s50, s52\n\t"
        "s_addc_u32 s53, s51, 0\n\t"
        "s_add_i32 s54, s45, -1\n\t"
        "s_lshr_b32 s54, s54, 2\n\t"
        "s_sub_i32 s54, s44, s54\n\t"
        "v_mov_b32 %[t1], s54\n\t"
        "v_add_u32 %[t0], 0x1000, %[vo]\n\t"
        "s_add_u32 s55, s46, 0x1000\n\t"
        "s_mov_b32 m0, s46\n\t"
        "s_setpc_b64 s[52:53]\n"
        "24:\n\t"
        "s_waitcnt lgkmcnt(0)\n\t"
        "v_readfirstlane_b32 s57, %[t3]\n\t"
        "v_add_u32 %[t1], 1, %[t1]\n\t"
        "s_lshr_b32 s54, s47, 8\n\t"
        "s_sub_u32 s56, s56, s54\n\t"
        "v_mov_b32 %[t2], s56\n\t"
        "s_sub_i32 s54, s56, s58\n\t"
        "s_mov_b32 s58, s56\n\t"
        "s_sub_i32 s56, s56, s57\n\t"
        "ds_write2_b32 %[pa], %[t1], %[t2] offset1:1\n\t"
        "s_add_i32 s59, s59, s54\n\t"
        "s_add_i32 s59, s59, %c[decay]\n\t"
        "s_cmp_gt_i32 s54, %c[jump]\n\t"
        "s_cselect_b32 s59, s62, s59\n\t"
        "s_min_i32 s62, s56, s59\n\t"
        "s_mul_i32 s56, s62, s63\n\t"
        "s_add_i32 s56, s56, s64\n\t"
        "s_ashr_i32 s56, s56, 8\n\t"
        "s_max_i32 s56, s56, %c[wmin]\n\t"
        "s_min_i32 s45, s56, %c[wmax]\n\t"
        "s_add_u32 s47, s47, %c[tau2]\n\t"
        "s_add_u32 s46, s46, 0x2000\n\t"
        "s_and_b32 s46, s46, %c[nslot] * 4096 - 1\n\t"
        "v_add_u32 %[vo], 0x2000, %[vo]\n\t"
        "s_add_i32 s44, s44, 2\n\t"
        "s_cmp_lt_i32 s44, s48\n\t"
        "s_cbranch_scc1 22b\n\t"
        DBG("s_add_i32 s54, s44, -1\n\ts_lshr_b32 s54, s54, 5\n\ts_lshl_b32 s54, s54, 2\n\ts_add_u32 s54, s54, %c[dbg] + 14 * 64\n\tv_mov_b32 %[t0], s54\n\tv_mov_b32 %[t1], s45\n\t"
            "ds_write2_b32 %[t0], %[t2], %[t1] offset1:16\n\t")
        "s_cmp_lt_i32 s44, s49\n\t"
        "s_cbranch_scc1 25f\n\t"
        "s_cmp_ge_i32 s44, 2 * %c[nku]\n\t"
        "s_cbranch_scc1 30f\n\t"
        "s_mov_b32 s49, 2 * %c[nku]\n\t"
        "s_mov_b64 s[40:41], s[42:43]\n"
        "25:\n\t"
        "s_cmp_ge_i32 s44, %c[uend]\n\t"
        "s_cselect_b32 s63, %c[gain2], s63\n\t"
        "s_cselect_b32 s64, %c[wmid2], s64\n\t"
        "s_mov_b32 s55, %c[cap]\n"
        "26:\n\t"
        "ds_read_b32 %[t0], %[nxa]\n\t"
        "s_waitcnt lgkmcnt(0)\n\t"
        "s_nop 1\n\t"
        "v_min_i32_dpp %[t0], %[t0], %[t0] row_ror:8 row_mask:0xf bank_mask:0xf\n\t"
        "s_nop 1\n\t"
        "v_min_i32_dpp %[t0], %[t0], %[t0] row_ror:4 row_mask:0xf bank_mask:0xf\n\t"
        "s_nop 1\n\t"
        "v_min_i32_dpp %[t0], %[t0], %[t0] row_ror:2 row_mask:0xf bank_mask:0xf\n\t"
        "s_nop 1\n\t"
        "v_min_i32_dpp %[t0], %[t0], %[t0] row_ror:1 row_mask:0xf bank_mask:0xf\n\t"
        "s_nop 1\n\t"
        "v_readfirstlane_b32 s48, %[t0]\n\t"
        "s_nop 1\n\t"
        "s_add_i32 s48, s48, %c[nslot]\n\t"
        "s_and_b32 s48, s48, -2\n\t"
        "s_min_i32 s48, s48, s49\n\t"
        "s_cmp_lt_i32 s44, s48\n\t"
        "s_cbranch_scc1 22b\n\t"
        "v_mov_b32 %[t1], s44\n\t"
        "s_waitcnt vmcnt(0)\n\t"
        "ds_write_b32 %[pa], %[t1]\n\t"
        "s_sleep 1\n\t"
        "s_sub_u32 s55, s55, 1\n\t"
        "s_cmp_lg_u32 s55, 0\n\t"
        "s_cbranch_scc1 26b\n\t"
        "s_branch 22b\n"
        "30:\n\t"
        ".set k, 6\n\t"
        ".rept 7\n\t"
        ".set wcnt, 4 * k\n\t"
        "v_mov_b32 %[t1], 2 * %c[nku] - k\n\t"
        WAITW
        "ds_write_b32 %[pa], %[t1]\n\t"
        ".set k, k - 1\n\t"
        ".endr\n\t"
        DBG("s_memrealtime s[56:57]\n\ts_waitcnt lgkmcnt(0)\n\tv_mov_b32 %[t0], %c[dbg] + 4\n\tv_mov_b32 %[t1], s56\n\tds_write_b32 %[t0], %[t1]\n\t")
        "s_waitcnt lgkmcnt(0)\n\t"
        "s_endpgm\n\t"
        ".p2align 7\n"
        "23:\n\t"
        ".set wcnt, %c[wmin]-1\n\t"
        ".rept %c[wmax] - %c[wmin] + 1\n\t"
        UNIT4("%[vo]")
        "ds_write_b32 %[pa], %[t1]\n\t"
        "s_mov_b32 m0, s55\n\t"
        UNIT4("%[t0]")
        "s_branch 24b\n\t"
        ".p2align 7\n\t"
        ".set wcnt, wcnt+1\n\t"
        ".endr\n"
        "90:\n\t"
        "s_waitcnt lgkmcnt(0)"
        : [va] "=&v"(va), [vb] "=&v"(vb), [vo] "=&v"(vo), [pa] "=&v"(pa), [nxa] "=&v"(nxa), [t0] "=&v"(t0), [t1] "=&v"(t1), [t2] "=&v"(t2), [t3] "=&v"(t3)
        : [tid] "v"(threadIdx.x), [bid] "s"(blockIdx.x), [ka] "s"(__builtin_amdgcn_kernarg_segment_ptr()), [nch] "i"(CODE_KIB - 1), [jk] "i"(RING - UNIT),
          [spw] "i"(SPW), [wmin] "i"(WMIN), [wmax] "i"(WMAX), [gain] "i"(WGAIN), [wmid] "i"((int)(WMID * 256)), [tau2] "i"(TAU2),
          [ctl] "i"(L_CTRL), [nslot] "i"(NSLOT), [nku] "i"(NKU), [cap] "i"(SPIN_CAP), [dbg] "i"(LDS_BYTES), [jump] "i"(JUMP), [decay] "i"(DECAY), [uend] "i"(UEND), [gain2] "i"(WGAIN2), [wmid2] "i"((int)(WMID2 * 256))
        : "memory", "scc", "s40", "s41", "s42", "s43", "s44", "s45", "s46", "s47", "s48", "s49", "s50", "s51", "s52", "s53", "s54", "s55", "s56", "s57", "s58", "s59", "s60", "s61", "s62", "s63", "s64");
  }
  __builtin_amdgcn_sched_barrier(0);
  const int tid = threadIdx.x, lane = tid & 63, wave = __builtin_amdgcn_readfirstlane(tid >> 6);
  const int xg = blockIdx.x & 7, kk = blockIdx.x >> 3;
  const int b = kk >> 3, pj = 8 * xg + (kk & 7), h = pj >> 1, s = pj & 1;
  const int r4 = lane >> 4, c16 = lane & 15;
  asm volatile("" : "+s"(x), "+s"(wq), "+s"(wo), "+s"(w1), "+s"(w3), "+s"(w2), "+s"(gw), "+s"(out), "+s"(ws));
  if (wave == 1) {
    const int wi = lane - 16;
    const int now = (int)(unsigned)__builtin_amdgcn_s_memrealtime() + REF0;
    ctrl[lane] = (lane == 1 || lane == 2) ? now : lane == 8 ? now - REF0 : (lane < 16 || lane >= 32) ? 0 : wi < NCW ? wi : wi < NCW + NHW ? NHC * NCW + wi - NCW : NEVER;
  }
  BARRIER();

  const __amdgpu_buffer_rsrc_t wsr = mkrsrc(ws, WS_BYTES);
  auto wg_max = [&]() __attribute__((always_inline)) {
    float mg = ctrlf[32 + (c16 < NCW + NHW ? c16 : 0)];
    mg = fmaxf(mg, dpp<ROR8>(mg)); mg = fmaxf(mg, dpp<ROR4>(mg)); mg = fmaxf(mg, dpp<ROR2>(mg)); mg = fmaxf(mg, dpp<ROR1>(mg));
    return mg;
  };

  if (wave == XWAVE + 1) {
    unsigned k = ((unsigned)__builtin_amdgcn_s_memrealtime() >> SLOTSH) + 1;
#pragma unroll 1
    for (int it = 0; it < 4096 >> (SLOTSH - 2); ++it, ++k) {
      wait_until(k << SLOTSH);
      if (__builtin_amdgcn_readfirstlane(ctrl[0]) >= NUNIT - 36) break;
      const int own = __builtin_amdgcn_readfirstlane(ctrl[1]);
      constexpr unsigned SM = (8192u >> SLOTSH) - 1u;
      if (!NOPST && blockIdx.x < 16 && lane < 8) __builtin_amdgcn_raw_buffer_store_b128(u32x4{(unsigned)own, (unsigned)own, (unsigned)own, (unsigned)own}, wsr, WS_PACE + (k & SM) * 2048 + blockIdx.x * 128 + lane * 16, 0, PAUX);
      if (it < PLAG || NOPLD) continue;
      int e[16];
      asm volatile("s_load_dword %0, %16, 0x0\n\ts_load_dword %1, %16, 0x80\n\ts_load_dword %2, %16, 0x100\n\ts_load_dword %3, %16, 0x180\n\t"
                   "s_load_dword %4, %16, 0x200\n\ts_load_dword %5, %16, 0x280\n\ts_load_dword %6, %16, 0x300\n\ts_load_dword %7, %16, 0x380\n\t"
                   "s_load_dword %8, %16, 0x400\n\ts_load_dword %9, %16, 0x480\n\ts_load_dword %10, %16, 0x500\n\ts_load_dword %11, %16, 0x580\n\t"
                   "s_load_dword %12, %16, 0x600\n\ts_load_dword %13, %16, 0x680\n\ts_load_dword %14, %16, 0x700\n\ts_load_dword %15, %16, 0x780\n\t"
                   "s_waitcnt lgkmcnt(0)"
                   : "=&s"(e[0]), "=&s"(e[1]), "=&s"(e[2]), "=&s"(e[3]), "=&s"(e[4]), "=&s"(e[5]), "=&s"(e[6]), "=&s"(e[7]),
                     "=&s"(e[8]), "=&s"(e[9]), "=&s"(e[10]), "=&s"(e[11]), "=&s"(e[12]), "=&s"(e[13]), "=&s"(e[14]), "=&s"(e[15])
                   : "s"(ws + WS_PACE + ((k - PLAG) & SM) * 2048) : "memory");
      int sum = 0, cnt = 0;
#pragma unroll
      for (int i = 0; i < 16; ++i) { const int d = e[i] - own; const bool ok = (unsigned)(d + 600) < 1200u; sum += ok ? d : 0; cnt += ok ? 1 : 0; }
      if (cnt >= 8) ctrl[2] = own + (int)((float)sum / (float)cnt);
    }
    return;
  }

  if (wave == XWAVE) {
#pragma unroll 1
    for (int i = 0; i < XDEL + XSPR * (kk & 31); ++i) PAUSE(4);
    const u32x4 xv = *(const GAS u32x4*)(x + b * D + 8 * lane), gv = *(const GAS u32x4*)(gw + 8 * lane);
    *(LAS u32x4*)(L + L_X + lane * 16) = xv;
    float gd = 0.f;
#pragma unroll
    for (int k = 0; k < 4; ++k) gd = dot2(xv[k], gv[k], gd);
    gd = xrow_sum(row16_sum(gd));
    const float g = 1.f / (1.f + __expf(-gd));
    ctrlf[48] = g;
    lds_arrive(L, C_X, lane);
    u64 tag;
    asm volatile("s_load_dwordx2 %0, %1, 0x0\n\ts_waitcnt lgkmcnt(0)" : "=s"(tag) : "s"(ws + WS_EPOCH + b * 256) : "memory");
    tag += TAG_STEP;
#pragma unroll 1
    for (int spin = 0; spin < SPIN_CAP; ++spin) { if (__builtin_amdgcn_readfirstlane(ctrl[C_MAX]) >= NCW + NHW) break; __builtin_amdgcn_s_sleep(16); }
    CFENCE();
    const float mg = wg_max();
    float l = 0.f;
#pragma unroll 4
    for (int i = 0; i < NKU * 64 / 1024; ++i) {
      const f32x4 sc = *(const LAS f32x4*)(L + L_SC + i * 1024 + lane * 16);
      l += (__builtin_amdgcn_exp2f(sc[0] - mg) + __builtin_amdgcn_exp2f(sc[1] - mg)) + (__builtin_amdgcn_exp2f(sc[2] - mg) + __builtin_amdgcn_exp2f(sc[3] - mg));
    }
    l = xrow_sum(row16_sum(l));
    const unsigned tlo = (unsigned)tag, thi = (unsigned)(tag >> 32);
    if (lane < 8) __builtin_amdgcn_raw_buffer_store_b128(u32x4{__float_as_uint(mg), tlo, __float_as_uint(l), thi}, wsr, WS_REC + (b * 64 + pj) * 128 + lane * 16, 0, 16);
#pragma unroll 1
    for (int i = 0; i < 12 + (kk & 31); ++i) PAUSE(12);
    u32x4 rec;
#pragma unroll 1
    for (int spin = 0; spin < SPIN_CAP; ++spin) {
      CFENCE();
      rec = __builtin_amdgcn_raw_buffer_load_b128(wsr, WS_REC + (b * 64 + (pj ^ 1)) * 128, 0, 16);
      if (__builtin_amdgcn_ballot_w64(rec[1] != tlo || rec[3] != thi) == 0ull) break;
      __builtin_amdgcn_s_sleep(16);
    }
    const float mo = __uint_as_float(rec[0]), lo = __uint_as_float(rec[2]);
    const float mh = fmaxf(mg, mo), wt = __builtin_amdgcn_exp2f(mg - mh);
    ctrlf[49] = g * wt / (wt * l + __builtin_amdgcn_exp2f(mo - mh) * lo);
    ctrl[50] = (int)(tlo & YMASK);
    lds_arrive(L, C_CF, lane);
    GAS unsigned* op = (GAS unsigned*)(out + b * D + 8 * pj + 4 * (lane & 1) + 2 * (lane >> 5));
    int off = WS_Y + (b * 64 + (lane >> 1)) * 2048 + pj * 32 + (lane & 1) * 16;
    asm volatile("" : "+v"(op), "+v"(off));
    if ((pj & (64 / NRED - 1)) != 0) return;
    STAMP(4);
    BARRIER();
    {
      const unsigned now = (unsigned)__builtin_amdgcn_s_memrealtime();
      const int d = __builtin_amdgcn_readfirstlane(ctrl[2]) + (int)(TS * 100) + FOFF - (int)now;
      const unsigned t = now + (unsigned)min(max(d, FMIN), FMAX);
#pragma unroll 1
      for (int spin = 0; spin < 4096 && (int)(t - (unsigned)__builtin_amdgcn_s_memrealtime()) > 0; ++spin) {}
    }
    STAMP(9);
    constexpr int NL = 128 / NRED;
    u32x4 yv[NL];
#pragma unroll 1
    for (int spin = 0; spin < SPIN_CAP; ++spin) {
      CFENCE();
      unsigned bad = 0;
#pragma unroll
      for (int i = 0; i < NL; ++i) yv[i] = __builtin_amdgcn_raw_buffer_load_b128(wsr, off, i * (64 / NL) * 2048, 16);
#pragma unroll
      for (int i = 0; i < NL; ++i) bad |= ((yv[i][0] ^ tlo) | (yv[i][1] ^ tlo)) | ((yv[i][2] ^ tlo) | (yv[i][3] ^ tlo));
      bad &= YMASK;
      { const u64 bal = __builtin_amdgcn_ballot_w64(bad != 0u); if (spin < 5) STAMP(10 + spin); ((volatile LAS unsigned*)(L + LDS_BYTES))[wave * 16 + 15] = spin + 1 + (bal == 1234567 ? 1 : 0); }
      if (__builtin_amdgcn_ballot_w64(bad != 0u) == 0ull) break;
      if (PSLP) __builtin_amdgcn_s_sleep(PSLP);
    }
    float o[4];
#pragma unroll
    for (int k = 0; k < 4; ++k) {
      float t[NL];
#pragma unroll
      for (int i = 0; i < NL; ++i) t[i] = __uint_as_float(yv[i][k] & ~YMASK);
#pragma unroll
      for (int st = NL / 2; st >= 1; st /= 2)
#pragma unroll
        for (int i = 0; i < st; ++i) t[i] += t[i + st];
      o[k] = t[0];
    }
    float e0 = swadd32(o[0], o[2]), e1 = swadd32(o[1], o[3]);
    e0 = swadd16(e0, e0); e1 = swadd16(e1, e1);
    e0 += dpp<ROR8>(e0); e1 += dpp<ROR8>(e1); e0 += dpp<ROR4>(e0); e1 += dpp<ROR4>(e1); e0 += dpp<ROR2>(e0); e1 += dpp<ROR2>(e1);
    if ((lane & 30) == 0) OUT_STORE(op, pack_bf16(e0, e1));
    if (pj == 0 && lane < 8) __builtin_amdgcn_raw_buffer_store_b128(u32x4{tlo, thi, tlo, thi}, wsr, WS_EPOCH + b * 256 + lane * 16, 0, 16);
    STAMP(7);
    VM_DRAIN();
    STAMP(8);
    STAMP_FLUSH();
    return;
  }

  const int cw = wave - 1, hw = wave - 1 - NCW;
  int landed = 0;
  auto wait_unit = [&](int u) __attribute__((always_inline)) {
#pragma unroll 1
    for (int spin = 0; landed <= u && spin < SPIN_CAP; ++spin) {
      landed = __builtin_amdgcn_readfirstlane(ctrl[0]);
      if (landed > u) break;
      if (USL) __builtin_amdgcn_s_sleep(USL);
    }
    CFENCE();
  };
  auto take = [&](int u, int next, u32x4& a0, u32x4& a1, u32x4& a2, u32x4& a3) __attribute__((always_inline)) {
    wait_unit(u);
    const LAS u32x4* sp = (const LAS u32x4*)(L + (u & (NSLOT - 1)) * UNIT + lane * 16);
    a0 = sp[0]; a1 = sp[64]; a2 = sp[128]; a3 = sp[192];
    LDS_WAIT();
    ctrl[16 + cw] = next;
    asm volatile("" : "+v"(a0), "+v"(a1), "+v"(a2), "+v"(a3) :: "memory");
  };
  u32x4 q;
  auto build_q = [&]() __attribute__((always_inline)) {
    lds_await(ctrl, C_Q, NHW);
    const LAS f32x4* qp = (const LAS f32x4*)(L + L_QP + (2 * r4 * DH + 8 * c16) * 4);
    const f32x4 lo = qp[0] + qp[DH / 4], hi = qp[1] + qp[DH / 4 + 1];
    q[0] = pack_bf16(xrow_sum(lo[0]), xrow_sum(lo[1])); q[1] = pack_bf16(xrow_sum(lo[2]), xrow_sum(lo[3]));
    q[2] = pack_bf16(xrow_sum(hi[0]), xrow_sum(hi[1])); q[3] = pack_bf16(xrow_sum(hi[2]), xrow_sum(hi[3]));
  };
  constexpr float SCALE = 0.088388347648318440f, LOG2E = 1.4426950408889634f, C2 = SCALE * LOG2E;
  float mx = -INFINITY;
  auto score = [&](int u, const u32x4& k0, const u32x4& k1, const u32x4& k2, const u32x4& k3) __attribute__((always_inline)) {
    float d0 = 0.f, d1 = 0.f, d2 = 0.f, d3 = 0.f;
#pragma unroll
    for (int k = 0; k < 4; ++k) { d0 = dot2(k0[k], q[k], d0); d1 = dot2(k1[k], q[k], d1); d2 = dot2(k2[k], q[k], d2); d3 = dot2(k3[k], q[k], d3); }
    const unsigned s01 = pack_bf16(row16_sum(d0), row16_sum(d1)), s23 = pack_bf16(row16_sum(d2), row16_sum(d3));
    const unsigned t01 = pack_bf16(bflo(s01) * SCALE, bfhi(s01) * SCALE), t23 = pack_bf16(bflo(s23) * SCALE, bfhi(s23) * SCALE);
    const f32x4 sc = {bflo(t01) * LOG2E, bfhi(t01) * LOG2E, bflo(t23) * LOG2E, bfhi(t23) * LOG2E};
    mx = fmaxf(fmaxf(mx, fmaxf(sc[0], sc[1])), fmaxf(sc[2], sc[3]));
    if (c16 == 0) *(LAS f32x4*)(L + L_SC + u * 64 + r4 * 16) = sc;
  };

  if (hw < 0) {
    {
      u32x4 hk[4 * NHC];
#pragma unroll
      for (int i = 0; i < NHC; ++i) take(cw + NCW * i, i + 1 < NHC ? cw + NCW * (i + 1) : NHELD + cw, hk[4 * i], hk[4 * i + 1], hk[4 * i + 2], hk[4 * i + 3]);
      STAMP(10);
      build_q();
      STAMP(3);
#pragma unroll
      for (int i = 0; i < NHC; ++i) { score(cw + NCW * i, hk[4 * i], hk[4 * i + 1], hk[4 * i + 2], hk[4 * i + 3]); __builtin_amdgcn_sched_barrier(0); }
    }
#pragma unroll 1
    for (int u = NHELD + cw; u < NKU; u += NCW) {
      u32x4 k0, k1, k2, k3;
      take(u, u + NCW, k0, k1, k2, k3);
      score(u, k0, k1, k2, k3);
    }
    ctrlf[32 + cw] = xrow_max(mx);
    lds_arrive(L, C_MAX, lane);
    STAMP(11);
    lds_await(ctrl, C_MAX, NCW + NHW);
    const float mg = wg_max();
    STAMP(12);
    float acc[8] = {0.f, 0.f, 0.f, 0.f, 0.f, 0.f, 0.f, 0.f};
    LAS unsigned* rp = (LAS unsigned*)(L + L_REC + cw * 272 + (8 * c16 + 2 * r4) * 2);
    asm volatile("" : "+v"(rp));
    float cf = 0.f;
#pragma unroll 1
    for (int u = NKU + cw; u < NUNIT; u += NCW) {
      const f32x4 sc = *(const LAS f32x4*)(L + L_SC + (u - NKU) * 64 + r4 * 16);
      float p[4];
#pragma unroll
      for (int t = 0; t < 4; ++t) { p[t] = __builtin_amdgcn_exp2f(sc[t] - mg); asm volatile("" : "+v"(p[t])); }
      if ((u & ~(NCW - 1)) == NUNIT - 32) { lds_await(ctrl, C_CF, 1); cf = ctrlf[49]; }
      u32x4 v[4];
      take(u, u + NCW, v[0], v[1], v[2], v[3]);
#pragma unroll
      for (int t = 0; t < 4; ++t)
#pragma unroll
        for (int k = 0; k < 4; ++k) { acc[2 * k] = fmaf(p[t], bflo(v[t][k]), acc[2 * k]); acc[2 * k + 1] = fmaf(p[t], bfhi(v[t][k]), acc[2 * k + 1]); }
    }
    STAMP(5);
    const float e0 = swadd16(swadd32(acc[0], acc[4]), swadd32(acc[2], acc[6])), e1 = swadd16(swadd32(acc[1], acc[5]), swadd32(acc[3], acc[7]));
    *rp = pack_bf16(e0 * cf, e1 * cf);
    BARRIER();
    return;
  }

  u32x4 wb[16];
  const unsigned tstart = (unsigned)__builtin_amdgcn_readfirstlane(ctrl[8]);
  {
    const __amdgpu_buffer_rsrc_t rq = mkrsrc(wq, D * H * DH * 2);
    const int vo = ((64 * hw + 16 * r4) * (H * DH) + h * DH + 8 * c16) * 2;
#pragma unroll
    for (int i = 0; i < 16; ++i) { if (i == 0) wait_until(tstart + TQ + hw * QA); else PAUSE(QS); wb[i] = ldw(rq, vo, i * (H * DH * 2)); }
    lds_await(ctrl, C_X, 1);
    const u32x4 xa = *(const LAS u32x4*)(L + L_X + (64 * hw + 16 * r4) * 2), xc = *(const LAS u32x4*)(L + L_X + (64 * hw + 16 * r4 + 8) * 2);
    float a[8] = {0.f, 0.f, 0.f, 0.f, 0.f, 0.f, 0.f, 0.f};
#pragma unroll
    for (int i = 0; i < 8; ++i) {
      const unsigned xp = i < 4 ? xa[i] : xc[i - 4];
#pragma unroll
      for (int k = 0; k < 4; ++k) {
        a[2 * k] = dot2(perm_lo(wb[2 * i][k], wb[2 * i + 1][k]), xp, a[2 * k]);
        a[2 * k + 1] = dot2(perm_hi(wb[2 * i][k], wb[2 * i + 1][k]), xp, a[2 * k + 1]);
      }
    }
    const float e0 = swadd16(swadd32(a[0], a[4]), swadd32(a[2], a[6])), e1 = swadd16(swadd32(a[1], a[5]), swadd32(a[3], a[7]));
    *(LAS f32x2*)(L + L_QP + (hw * DH + 8 * c16 + 2 * r4) * 4) = f32x2{e0, e1};
    lds_arrive(L, C_Q, lane);
    STAMP(2);
    const int hb = NHC * NCW + hw;
#pragma unroll
    for (int i = 0; i < NHH; ++i) take(hb + NHW * i, i + 1 < NHH ? hb + NHW * (i + 1) : NEVER, wb[4 * i], wb[4 * i + 1], wb[4 * i + 2], wb[4 * i + 3]);
    STAMP(10);
    build_q();
#pragma unroll
    for (int i = 0; i < NHH; ++i) { score(hb + NHW * i, wb[4 * i], wb[4 * i + 1], wb[4 * i + 2], wb[4 * i + 3]); __builtin_amdgcn_sched_barrier(0); }
    ctrlf[32 + cw] = xrow_max(mx);
    lds_arrive(L, C_MAX, lane);
  }
  f32x4 y[4] = {{0.f, 0.f, 0.f, 0.f}, {0.f, 0.f, 0.f, 0.f}, {0.f, 0.f, 0.f, 0.f}, {0.f, 0.f, 0.f, 0.f}};
  if ((pj & 7) != 7) {
    const int sl = 7 * (pj >> 3) + (pj & 7);
    const __amdgpu_buffer_rsrc_t r1 = mkrsrc(w1, D * F * 2), r3 = mkrsrc(w3, D * F * 2), r2 = mkrsrc(w2, F * D * 2);
    const int vo = ((64 * hw + (lane & 60)) * F + FS * sl + 8 * (lane & 3)) * 2;
#pragma unroll
    for (int i = 0; i < 8; ++i) { if (i == 0) wait_until(tstart + T1 + hw * A1); else PAUSE(S1); wb[i] = ldw(i < 4 ? r1 : r3, vo, (i & 3) * (F * 2)); }
    const u32x2 xm = *(const LAS u32x2*)(L + L_X + (64 * hw + (lane & 60)) * 2);
    float a[16];
#pragma unroll
    for (int m = 0; m < 2; ++m)
#pragma unroll
      for (int k = 0; k < 4; ++k) {
        a[8 * m + 2 * k] = dot2(perm_lo(wb[4 * m + 2][k], wb[4 * m + 3][k]), xm[1], dot2(perm_lo(wb[4 * m][k], wb[4 * m + 1][k]), xm[0], 0.f));
        a[8 * m + 2 * k + 1] = dot2(perm_hi(wb[4 * m + 2][k], wb[4 * m + 3][k]), xm[1], dot2(perm_hi(wb[4 * m][k], wb[4 * m + 1][k]), xm[0], 0.f));
      }
    f32x4 e;
#pragma unroll
    for (int k = 0; k < 4; ++k) {
      float t = swadd16(swadd32(a[k], a[8 + k]), swadd32(a[4 + k], a[12 + k]));
      t += dpp<ROR8>(t); e[k] = t + dpp<ROR4>(t);
    }
    if ((lane & 12) == 0) *(LAS f32x4*)(L + L_MP + (hw * 64 + (lane & 32) + 8 * (lane & 3) + 4 * (r4 & 1)) * 4) = e;
    lds_arrive(L, C_MLP, lane);
    u32x2 wr[8];
#pragma unroll
    for (int i = 0; i < 8; ++i) { if (i == 0) wait_until(tstart + T2 + hw * A2); else PAUSE(S2); wr[i] = ldw2(r2, ((FS * sl + 8 * r4) * D + 64 * hw + 4 * c16) * 2, i * (D * 2)); }
    lds_await(ctrl, C_MLP, NHW);
    const LAS float* mp = (const LAS float*)(L + L_MP) + (lane & 31);
    float ga = 0.f, up = 0.f;
#pragma unroll
    for (int w = 0; w < NHW; ++w) { ga += mp[w * 64]; up += mp[w * 64 + 32]; }
    const float hv = ga / (1.f + __expf(-ga)) * up * (1.f - ctrlf[48]);
    const unsigned hh = pack_bf16(hv, 0.f), hl = pack_bf16(hv - bflo(hh), 0.f);
    *(LAS u16*)(L + L_H + hw * 128 + lane * 2) = (u16)(lane < 32 ? hh : hl);
    LDS_WAIT();
    u32x4 ha = *(const LAS u32x4*)(L + L_H + hw * 128 + (c16 & 1) * 64 + r4 * 16);
    if (c16 >= 2) ha = u32x4{0u, 0u, 0u, 0u};
#pragma unroll
    for (int e = 0; e < 4; ++e) {
      u32x4 f;
#pragma unroll
      for (int k = 0; k < 4; ++k) f[k] = (e & 1) ? perm_hi(wr[2 * k][e >> 1], wr[2 * k + 1][e >> 1]) : perm_lo(wr[2 * k][e >> 1], wr[2 * k + 1][e >> 1]);
      y[e] = mfma(ha, f, y[e]);
    }
  }
#pragma unroll
  for (int e = 0; e < 4; ++e) asm volatile("" : "+v"(y[e]));
  {
    const __amdgpu_buffer_rsrc_t ro = mkrsrc(wo, H * DH * D * 2);
#pragma unroll
    for (int t = 0; t < 4; ++t) {
      u32x2 wr[8];
#pragma unroll
      for (int i = 0; i < 8; ++i) { if (t + i == 0) wait_until(tstart + TO + hw * AO); else PAUSE(SO); wr[i] = ldw2(ro, ((h * DH + 32 * r4) * D + 64 * hw + 4 * c16) * 2, (8 * t + i) * (D * 2)); }
#pragma unroll
      for (int e = 0; e < 4; ++e) {
#pragma unroll
        for (int k = 0; k < 4; ++k) wb[4 * t + e][k] = (e & 1) ? perm_hi(wr[2 * k][e >> 1], wr[2 * k + 1][e >> 1]) : perm_lo(wr[2 * k][e >> 1], wr[2 * k + 1][e >> 1]);
        asm volatile("" : "+v"(wb[4 * t + e]));
      }
    }
  }
  lds_await(ctrl, C_CF, 1);
  unsigned yt = (unsigned)ctrl[50];
  int yo = WS_Y + (b * 64 + pj) * 2048 + hw * 256 + c16 * 16;
  const LAS u32x4* rec = (const LAS u32x4*)(L + L_REC + (lane & 3) * 272 + r4 * 64);
  unsigned ym = ~YMASK;
  asm volatile("" : "+v"(yt), "+v"(yo), "+v"(rec), "+s"(ym));
  STAMP(4);
  BARRIER();
  STAMP(9);
  {
    const u32x4 a[4] = {rec[0], rec[1], rec[2], rec[3]};
#pragma unroll
    for (int t = 0; t < 4; ++t)
#pragma unroll
      for (int e = 0; e < 4; ++e) y[e] = mfma(a[t], wb[4 * t + e], y[e]);
    u32x4 v;
#pragma unroll
    for (int e = 0; e < 4; ++e) {
      const f32x2 t = y[e].xy + y[e].zw;
      unsigned r;
      asm("v_add_f32 %0, %1, %2\n\tv_add_u32 %0, %5, %0\n\tv_and_or_b32 %0, %0, %3, %4" : "=&v"(r) : "v"(t[0]), "v"(t[1]), "s"(ym), "v"(yt), "i"((YMASK + 1u) / 2u));
      v[e] = r;
    }
    if (lane < 16) __builtin_amdgcn_raw_buffer_store_b128(v, wsr, yo, 0, 16);
  }
  STAMP(7);
}

extern "C" void kernel_launch(void* const* d_in, const int* in_sizes, int n_in, void* d_out, int out_size, void* d_ws, size_t ws_size,
                              hipStream_t stream) {
  gated_mix_kernel<<<256, NTHR, 0, stream>>>((const u16*)d_in[0], (const u16*)d_in[1], (const u16*)d_in[2], (const u16*)d_in[3], (const u16*)d_in[4],
                                             (const u16*)d_in[6], (const u16*)d_in[7], (const u16*)d_in[8], (const u16*)d_in[9], (u16*)d_out,
                                             (unsigned char*)d_ws);
}
```

```cpp
#include <hip/hip_runtime.h>
#include <hip/hip_bf16.h>

#define GAS __attribute__((address_space(1)))
#define LAS __attribute__((address_space(3)))
typedef unsigned short u16;
typedef unsigned long long u64;
using u32x4 = __attribute__((ext_vector_type(4))) unsigned;
using u32x2 = __attribute__((ext_vector_type(2))) unsigned;
using f32x4 = __attribute__((ext_vector_type(4))) float;
using f32x2 = __attribute__((ext_vector_type(2))) float;
using bf16x2 = __attribute__((ext_vector_type(2))) __bf16;
using bf16x8 = __attribute__((ext_vector_type(8))) __bf16;

constexpr int B = 4, D = 512, H = 32, SK = 8192, DH = 128, F = 1792;
constexpr int NCW = 4, NHW = 8, XWAVE = 1 + NCW + NHW, NWAVE = XWAVE + 2, NTHR = NWAVE * 64;
constexpr int TQ = 130, QA = 80, QS = 2;
constexpr int T1 = 1200, A1 = 150, S1 = 6;
constexpr int T2 = 2500, A2 = 40, S2 = 1;
constexpr int TO = 2900, AO = 350, SO = 2;
constexpr int TSTREAM = 7060;
constexpr int WMID = 5248, WMIN = 16, WMAX = 27;
constexpr int WGAIN = 10, WGAIN2 = 18, UEND = 420;
constexpr int JUMP = 100, DECAY = 6;
constexpr int REF0 = 180;
constexpr int FOFF = 150, FMIN = 45, FMAX = 150;
constexpr int CODE_KIB = 20;

constexpr int UNIT = 4096;
constexpr int NSLOT = 32, RING = NSLOT * UNIT;
constexpr int NKU = (SK / 2) / 16, NUNIT = 2 * NKU;
constexpr int NHC = 6, NHH = 4, NHELD = NHC * NCW + NHH * NHW;
constexpr int FS = 32;
constexpr int L_CTRL = RING;
constexpr int L_X = L_CTRL + 256;
constexpr int L_QP = L_X + D * 2;
constexpr int L_MP = L_QP + NHW * DH * 4;
constexpr int L_REC = L_MP + NHW * 64 * 4;
constexpr int L_H = L_REC + NCW * 272;
constexpr int L_SC = L_H + NHW * 128;
constexpr int LDS_BYTES = L_SC + NKU * 64;
static_assert(LDS_BYTES <= 159 * 1024, "lds");
static_assert(WMIN <= WMID / 256 && WMID / 256 <= WMAX && WMAX <= 28, "window");
constexpr int WS_EPOCH = 0;
constexpr int WS_REC = 4096;
constexpr int WS_PACE = WS_REC + 256 * 128;
constexpr int WS_Y = WS_PACE + 32 * 2048;
constexpr int WS_BYTES = WS_Y + B * 64 * 2048;
constexpr u64 TAG_STEP = 0x9E3779B97F4A7C15ull;
constexpr unsigned YMASK = 0xFFFu;
constexpr int NT = 2, SC1 = 16;
constexpr int SPIN_CAP = 1 << 22;
constexpr int NEVER = 1 << 20;
constexpr int C_Q = 4, C_MAX = 5, C_MLP = 6, C_X = 7, C_CF = 9;

__device__ __forceinline__ float expd(float d) { return __builtin_amdgcn_exp2f(d * 1.4426950408889634f); }
__device__ __forceinline__ float bflo(unsigned u) { return __builtin_bit_cast(float, u << 16); }
__device__ __forceinline__ float bfhi(unsigned u) { return __builtin_bit_cast(float, u & 0xffff0000u); }
__device__ __forceinline__ float dot2(unsigned a, unsigned b, float acc) {
  return __builtin_amdgcn_fdot2_f32_bf16(__builtin_bit_cast(bf16x2, a), __builtin_bit_cast(bf16x2, b), acc, false);
}
__device__ __forceinline__ unsigned pack_bf16(float lo, float hi) {
  const bf16x2 v = {(__bf16)lo, (__bf16)hi};
  return __builtin_bit_cast(unsigned, v);
}
__device__ __forceinline__ unsigned perm_lo(unsigned a, unsigned b) { return __builtin_amdgcn_perm(b, a, 0x05040100u); }
__device__ __forceinline__ unsigned perm_hi(unsigned a, unsigned b) { return __builtin_amdgcn_perm(b, a, 0x07060302u); }
template <int CTRL> __device__ __forceinline__ float dpp(float x) {
  return __builtin_bit_cast(float, __builtin_amdgcn_mov_dpp(__builtin_bit_cast(int, x), CTRL, 0xf, 0xf, true));
}
constexpr int ROR1 = 0x121, ROR2 = 0x122, ROR4 = 0x124, ROR8 = 0x128;
__device__ __forceinline__ float row16_sum(float x) { x += dpp<ROR8>(x); x += dpp<ROR4>(x); x += dpp<ROR2>(x); x += dpp<ROR1>(x); return x; }
__device__ __forceinline__ float swadd32(float a, float b) {
  auto t = __builtin_amdgcn_permlane32_swap(__float_as_uint(a), __float_as_uint(b), false, false);
  return __uint_as_float(t[0]) + __uint_as_float(t[1]);
}
__device__ __forceinline__ float swadd16(float a, float b) {
  auto t = __builtin_amdgcn_permlane16_swap(__float_as_uint(a), __float_as_uint(b), false, false);
  return __uint_as_float(t[0]) + __uint_as_float(t[1]);
}
__device__ __forceinline__ float xrow_sum(float x) { x = swadd16(x, x); return swadd32(x, x); }
__device__ __forceinline__ float xrow_max(float x) {
  auto s = __builtin_amdgcn_permlane16_swap(__float_as_uint(x), __float_as_uint(x), false, false);
  x = fmaxf(__uint_as_float(s[0]), __uint_as_float(s[1]));
  auto t = __builtin_amdgcn_permlane32_swap(__float_as_uint(x), __float_as_uint(x), false, false);
  return fmaxf(__uint_as_float(t[0]), __uint_as_float(t[1]));
}
__device__ __forceinline__ __amdgpu_buffer_rsrc_t mkrsrc(const void* p, int bytes) { return __builtin_amdgcn_make_buffer_rsrc((void*)p, 0, bytes, 0x00020000); }
__device__ __forceinline__ u32x4 ldw(__amdgpu_buffer_rsrc_t r, int voff, int soff) { return __builtin_amdgcn_raw_buffer_load_b128(r, voff, soff, NT); }
#define LDS_WAIT() asm volatile("s_waitcnt lgkmcnt(0)" ::: "memory")
#define CFENCE() asm volatile("" ::: "memory")
#define BARRIER() asm volatile("s_waitcnt lgkmcnt(0)\n\ts_barrier" ::: "memory")
#define PAUSE(n) do { __builtin_amdgcn_sched_barrier(0); asm volatile("s_sleep %0" :: "i"(n) : "memory"); __builtin_amdgcn_sched_barrier(0); } while (0)

__device__ __forceinline__ void lds_arrive(LAS unsigned char* L, int idx, int lane) {
  LDS_WAIT();
  if (lane == 0) __hip_atomic_fetch_add((LAS int*)(L + L_CTRL) + idx, 1, __ATOMIC_RELAXED, __HIP_MEMORY_SCOPE_WORKGROUP);
}
__device__ __forceinline__ void lds_await(volatile LAS int* ctrl, int idx, int n) {
#pragma unroll 1
  for (int spin = 0; spin < SPIN_CAP; ++spin) { if (__builtin_amdgcn_readfirstlane(ctrl[idx]) >= n) break; __builtin_amdgcn_s_sleep(1); }
  CFENCE();
}

__device__ __forceinline__ void wait_until(unsigned t) {
  __builtin_amdgcn_sched_barrier(0);
#pragma unroll 1
  for (int spin = 0; spin < 4096; ++spin) {
    const int d = (int)(t - (unsigned)__builtin_amdgcn_s_memrealtime());
    if (d <= 0) break;
    if (d > 24) __builtin_amdgcn_s_sleep(6);
  }
  CFENCE();
  __builtin_amdgcn_sched_barrier(0);
}
constexpr int TAU2 = 2 * (TSTREAM * 256 / NUNIT);
__device__ __forceinline__ f32x4 mfma(const u32x4& a, const u32x4& b, const f32x4& c) {
  return __builtin_amdgcn_mfma_f32_16x16x32_bf16(__builtin_bit_cast(bf16x8, a), __builtin_bit_cast(bf16x8, b), c, 0, 0, 0);
}
__device__ __forceinline__ u32x2 ldw2(__amdgpu_buffer_rsrc_t r, int voff, int soff) { return __builtin_amdgcn_raw_buffer_load_b64(r, voff, soff, NT); }

__global__ void __launch_bounds__(NTHR, 4) gated_mix__mha_block_cached__glu_gated_swiglu__decode_kernel(const u16* x, const u16* kc, const u16* vc, const u16* wq, const u16* wo, const u16* w1,
                                                         const u16* w3, const u16* w2, const u16* gw, u16* out, unsigned char* ws) {
  __shared__ __attribute__((aligned(16))) unsigned char lds_raw[LDS_BYTES];
  LAS unsigned char* L = (LAS unsigned char*)lds_raw;
  volatile LAS int* ctrl = (volatile LAS int*)(L + L_CTRL);
  volatile LAS float* ctrlf = (volatile LAS float*)(L + L_CTRL);
  {
    unsigned va, vb, vo, pa, nxa, t0, t1, t2, t3;
#define WAITW "s_waitcnt (wcnt & 15) | ((wcnt >> 4) << 14) | 0x0F70\n\t"
#define SP(v, sb, wait) wait "global_load_lds_dword " v ", " sb " nt\n\tglobal_load_lds_dword " v ", " sb " sc1\n\t"
#define SW "s_waitcnt vmcnt(4)\n\t"
#define UNIT4(v) \
        WAITW "global_load_lds_dwordx4 " v ", s[40:41] nt\n\t" \
        WAITW "global_load_lds_dwordx4 " v ", s[40:41] offset:1024 nt\n\t" \
        WAITW "global_load_lds_dwordx4 " v ", s[40:41] offset:2048 nt\n\t" \
        WAITW "global_load_lds_dwordx4 " v ", s[40:41] offset:3072 nt\n\t"
    asm volatile(
        "s_getpc_b64 s[56:57]\n\t"
        "s_load_dwordx8 s[44:51], %[ka], 0x18\n\t"
        "s_load_dwordx2 s[52:53], %[ka], 0x38\n\t"
        "s_load_dwordx4 s[40:43], %[ka], 0x8\n\t"
        "v_readfirstlane_b32 s54, %[tid]\n\t"
        "s_mov_b32 m0, %c[jk]\n\t"
        "s_lshr_b32 s59, %[bid], 3\n\t"
        "s_cmp_ge_u32 s54, 64\n\t"
        "s_cbranch_scc1 90f\n\t"
        "s_min_u32 s54, s59, %c[nch]\n\t"
        "s_lshl_b32 s54, s54, 10\n\t"
        "v_lshlrev_b32 %[vo], 4, %[tid]\n\t"
        "s_and_b32 s56, s56, -128\n\t"
        "v_add_u32 %[t0], s54, %[vo]\n\t"
        "s_and_b32 s58, %[bid], 7\n\t"
        "global_load_lds_dwordx4 %[t0], s[56:57]\n\t"
        "s_setprio 3\n\t"
        "v_lshlrev_b32 %[t1], 7, %[tid]\n\t"
        "s_lshl_b32 s54, s59, 7\n\t"
        "s_add_u32 s54, s54, s58\n\t"
        "s_lshl_b32 s54, s54, 10\n\t"
        "v_lshrrev_b32 %[va], 3, %[tid]\n\t"
        "v_mul_u32_u24 %[va], 8192 - 1024, %[va]\n\t"
        "v_add3_u32 %[va], %[va], %[t1], s54\n\t"
        "v_add_u32 %[t2], 0x10000, %[va]\n\t"
        "s_lshl_b32 s55, s58, 5\n\t"
        "s_add_u32 s55, s55, s59\n\t"
        "s_mul_i32 s54, s55, 7 * 1024\n\t"
        "v_min_u32 %[t3], 55 * 128, %[t1]\n\t"
        "v_add_u32 %[t3], s54, %[t3]\n\t"
        "s_mul_i32 s54, s58, 448\n\t"
        "s_and_b32 s54, s54, -128\n\t"
        "s_mul_i32 s57, s59, 16 * 3584\n\t"
        "s_add_u32 s54, s54, s57\n\t"
        "v_lshrrev_b32 %[t0], 2, %[tid]\n\t"
        "v_mul_u32_u24 %[t0], 3584 - 512, %[t0]\n\t"
        "v_add3_u32 %[t0], %[t0], %[t1], s54\n\t"
        "v_lshl_add_u32 %[vb], s55, 14, %[t1]\n\t"
        "v_add_u32 %[t1], 0x2000, %[vb]\n\t"
        "s_and_b32 s54, %[bid], 0xc0\n\t"
        "s_lshl3_add_u32 s54, s58, s54\n\t"
        "s_and_b32 s56, s59, 7\n\t"
        "s_add_u32 s54, s54, s56\n\t"
        "s_lshl_b32 s56, s54, 20\n\t"
        "s_waitcnt lgkmcnt(0)\n\t"
        SP("%[va]", "s[44:45]", "") SP("%[t2]", "s[44:45]", "") SP("%[t3]", "s[52:53]", "")
        "s_barrier\n\t"
        SP("%[t0]", "s[48:49]", SW) SP("%[t0]", "s[50:51]", SW) SP("%[vb]", "s[46:47]", SW) SP("%[t1]", "s[46:47]", SW)
        "s_add_u32 s40, s40, s56\n\t"
        "s_addc_u32 s41, s41, 0\n\t"
        "s_add_u32 s42, s42, s56\n\t"
        "s_addc_u32 s43, s43, 0\n\t"
        "s_sub_u32 s42, s42, %c[nku] * 4096\n\t"
        "s_subb_u32 s43, s43, 0\n\t"
        ".set so, 0\n\t"
        ".set wcnt, 4\n\t"
        ".rept 4\n\t"
        "s_movk_i32 m0, so\n\t"
        ".if (so & 4096) == 0\n\t"
        WAITW
        ".set wcnt, wcnt + 6\n\t"
        ".else\n\t"
        "s_nop 0\n\t"
        ".endif\n\t"
        "global_load_lds_dwordx4 %[vo], s[40:41] nt\n\t"
        "global_load_lds_dwordx4 %[vo], s[40:41] offset:1024 nt\n\t"
        "global_load_lds_dwordx4 %[vo], s[40:41] offset:2048 nt\n\t"
        "global_load_lds_dwordx4 %[vo], s[40:41] offset:3072 nt\n\t"
        "v_add_u32 %[vo], 0x1000, %[vo]\n\t"
        ".set so, so + 4096\n\t"
        ".endr\n\t"
        "s_waitcnt vmcnt(16)\n\t"
        "s_getpc_b64 s[50:51]\n"
        "20:\n\t"
        "s_add_u32 s50, s50, 23f-20b-%c[wmin]*128\n\t"
        "s_addc_u32 s51, s51, -1\n\t"
        "v_mov_b32 %[pa], %c[ctl]\n\t"
        "v_and_b32 %[nxa], 0x3c0 >> 2, %[vo]\n\t"
        "v_lshrrev_b32 %[nxa], 2, %[nxa]\n\t"
        "v_add_u32 %[nxa], %c[ctl] + 64, %[nxa]\n\t"
        "s_memrealtime s[58:59]\n\t"
        "s_mov_b32 s62, 0\n\t"
        "s_mov_b32 s63, %c[gain]\n\t"
        "s_mov_b32 s44, 4\n\t"
        "s_mov_b32 s45, %c[wmid] >> 8\n\t"
        "s_mov_b32 s46, 0x4000\n\t"
        "s_mov_b32 s47, 2 * %c[tau2]\n\t"
        "s_mov_b32 s48, %c[nslot]\n\t"
        "s_mov_b32 s49, %c[nku]\n\t"
        "s_waitcnt lgkmcnt(0)\n\t"
        "s_mov_b32 s59, 10000\n"
        "22:\n\t"
        "s_memrealtime s[56:57]\n\t"
        "ds_read_b32 %[t3], %[pa] offset:8\n\t"
        "s_lshl_b32 s52, s45, 7\n\t"
        "s_add_u32 s52, s50, s52\n\t"
        "s_addc_u32 s53, s51, 0\n\t"
        "s_add_i32 s54, s45, -1\n\t"
        "s_lshr_b32 s54, s54, 2\n\t"
        "s_sub_i32 s54, s44, s54\n\t"
        "v_mov_b32 %[t1], s54\n\t"
        "v_add_u32 %[t0], 0x1000, %[vo]\n\t"
        "s_add_u32 s55, s46, 0x1000\n\t"
        "s_mov_b32 m0, s46\n\t"
        "s_setpc_b64 s[52:53]\n"
        "24:\n\t"
        "s_waitcnt lgkmcnt(0)\n\t"
        "v_readfirstlane_b32 s57, %[t3]\n\t"
        "v_add_u32 %[t1], 1, %[t1]\n\t"
        "s_lshr_b32 s54, s47, 8\n\t"
        "s_sub_u32 s56, s56, s54\n\t"
        "v_mov_b32 %[t2], s56\n\t"
        "s_sub_i32 s54, s56, s58\n\t"
        "s_mov_b32 s58, s56\n\t"
        "s_sub_i32 s56, s56, s57\n\t"
        "ds_write2_b32 %[pa], %[t1], %[t2] offset1:1\n\t"
        "s_add_i32 s59, s59, s54\n\t"
        "s_add_i32 s59, s59, %c[decay]\n\t"
        "s_cmp_gt_i32 s54, %c[jump]\n\t"
        "s_cselect_b32 s59, s62, s59\n\t"
        "s_min_i32 s62, s56, s59\n\t"
        "s_mul_i32 s56, s62, s63\n\t"
        "s_add_i32 s56, s56, %c[wmid]\n\t"
        "s_ashr_i32 s56, s56, 8\n\t"
        "s_max_i32 s56, s56, %c[wmin]\n\t"
        "s_min_i32 s45, s56, %c[wmax]\n\t"
        "s_add_u32 s47, s47, %c[tau2]\n\t"
        "s_add_u32 s46, s46, 0x2000\n\t"
        "s_and_b32 s46, s46, %c[nslot] * 4096 - 1\n\t"
        "v_add_u32 %[vo], 0x2000, %[vo]\n\t"
        "s_add_i32 s44, s44, 2\n\t"
        "s_cmp_lt_i32 s44, s48\n\t"
        "s_cbranch_scc1 22b\n\t"
        "s_cmp_lt_i32 s44, s49\n\t"
        "s_cbranch_scc1 25f\n\t"
        "s_cmp_ge_i32 s44, 2 * %c[nku]\n\t"
        "s_cbranch_scc1 30f\n\t"
        "s_mov_b32 s49, 2 * %c[nku]\n\t"
        "s_mov_b64 s[40:41], s[42:43]\n"
        "25:\n\t"
        "s_cmp_ge_i32 s44, %c[uend]\n\t"
        "s_cselect_b32 s63, %c[gain2], s63\n\t"
        "s_mov_b32 s55, %c[cap]\n"
        "26:\n\t"
        "ds_read_b32 %[t0], %[nxa]\n\t"
        "s_waitcnt lgkmcnt(0)\n\t"
        "s_nop 1\n\t"
        "v_min_i32_dpp %[t0], %[t0], %[t0] row_ror:8 row_mask:0xf bank_mask:0xf\n\t"
        "s_nop 1\n\t"
        "v_min_i32_dpp %[t0], %[t0], %[t0] row_ror:4 row_mask:0xf bank_mask:0xf\n\t"
        "s_nop 1\n\t"
        "v_min_i32_dpp %[t0], %[t0], %[t0] row_ror:2 row_mask:0xf bank_mask:0xf\n\t"
        "s_nop 1\n\t"
        "v_min_i32_dpp %[t0], %[t0], %[t0] row_ror:1 row_mask:0xf bank_mask:0xf\n\t"
        "s_nop 1\n\t"
        "v_readfirstlane_b32 s48, %[t0]\n\t"
        "s_nop 1\n\t"
        "s_add_i32 s48, s48, %c[nslot]\n\t"
        "s_and_b32 s48, s48, -2\n\t"
        "s_min_i32 s48, s48, s49\n\t"
        "s_cmp_lt_i32 s44, s48\n\t"
        "s_cbranch_scc1 22b\n\t"
        "v_mov_b32 %[t1], s44\n\t"
        "s_waitcnt vmcnt(0)\n\t"
        "ds_write_b32 %[pa], %[t1]\n\t"
        "s_sleep 1\n\t"
        "s_sub_u32 s55, s55, 1\n\t"
        "s_cmp_lg_u32 s55, 0\n\t"
        "s_cbranch_scc1 26b\n\t"
        "s_branch 22b\n"
        "30:\n\t"
        ".set k, 6\n\t"
        ".rept 7\n\t"
        ".set wcnt, 4 * k\n\t"
        "v_mov_b32 %[t1], 2 * %c[nku] - k\n\t"
        WAITW
        "ds_write_b32 %[pa], %[t1]\n\t"
        ".set k, k - 1\n\t"
        ".endr\n\t"
        "s_waitcnt lgkmcnt(0)\n\t"
        "s_endpgm\n\t"
        ".p2align 7\n"
        "23:\n\t"
        ".set wcnt, %c[wmin]-1\n\t"
        ".rept %c[wmax] - %c[wmin] + 1\n\t"
        UNIT4("%[vo]")
        "ds_write_b32 %[pa], %[t1]\n\t"
        "s_mov_b32 m0, s55\n\t"
        UNIT4("%[t0]")
        "s_branch 24b\n\t"
        ".p2align 7\n\t"
        ".set wcnt, wcnt+1\n\t"
        ".endr\n"
        "90:\n\t"
        "s_waitcnt lgkmcnt(0)"
        : [va] "=&v"(va), [vb] "=&v"(vb), [vo] "=&v"(vo), [pa] "=&v"(pa), [nxa] "=&v"(nxa), [t0] "=&v"(t0), [t1] "=&v"(t1), [t2] "=&v"(t2), [t3] "=&v"(t3)
        : [tid] "v"(threadIdx.x), [bid] "s"(blockIdx.x), [ka] "s"(__builtin_amdgcn_kernarg_segment_ptr()), [nch] "i"(CODE_KIB - 1), [jk] "i"(RING - UNIT),
          [wmin] "i"(WMIN), [wmax] "i"(WMAX), [gain] "i"(WGAIN), [wmid] "i"(WMID), [tau2] "i"(TAU2),
          [ctl] "i"(L_CTRL), [nslot] "i"(NSLOT), [nku] "i"(NKU), [cap] "i"(SPIN_CAP), [jump] "i"(JUMP), [decay] "i"(DECAY), [uend] "i"(UEND), [gain2] "i"(WGAIN2)
        : "memory", "scc", "s40", "s41", "s42", "s43", "s44", "s45", "s46", "s47", "s48", "s49", "s50", "s51", "s52", "s53", "s54", "s55", "s56", "s57", "s58", "s59", "s62", "s63");
  }
  __builtin_amdgcn_sched_barrier(0);
  const int tid = threadIdx.x, lane = tid & 63, wave = __builtin_amdgcn_readfirstlane(tid >> 6);
  const int xg = blockIdx.x & 7, kk = blockIdx.x >> 3;
  const int b = kk >> 3, pj = 8 * xg + (kk & 7), h = pj >> 1;
  const int r4 = lane >> 4, c16 = lane & 15;
  asm volatile("" : "+s"(x), "+s"(wq), "+s"(wo), "+s"(w1), "+s"(w3), "+s"(w2), "+s"(gw), "+s"(out), "+s"(ws));
  if (wave == 1) {
    const int wi = lane - 16;
    const int now = (int)(unsigned)__builtin_amdgcn_s_memrealtime() + REF0;
    ctrl[lane] = (lane == 1 || lane == 2) ? now : lane == 8 ? now - REF0 : (lane < 16 || lane >= 32) ? 0 : wi < NCW ? wi : wi < NCW + NHW ? NHC * NCW + wi - NCW : NEVER;
  }
  BARRIER();

  const __amdgpu_buffer_rsrc_t wsr = mkrsrc(ws, WS_BYTES);
  auto wg_max = [&]() __attribute__((always_inline)) {
    float mg = ctrlf[32 + (c16 < NCW + NHW ? c16 : 0)];
    mg = fmaxf(mg, dpp<ROR8>(mg)); mg = fmaxf(mg, dpp<ROR4>(mg)); mg = fmaxf(mg, dpp<ROR2>(mg)); mg = fmaxf(mg, dpp<ROR1>(mg));
    return mg;
  };

  if (wave == XWAVE + 1) {
    unsigned k = ((unsigned)__builtin_amdgcn_s_memrealtime() >> 8) + 1;
#pragma unroll 1
    for (int it = 0; it < 64; ++it, ++k) {
      wait_until(k << 8);
      if (__builtin_amdgcn_readfirstlane(ctrl[0]) >= NUNIT - 36) break;
      const int own = __builtin_amdgcn_readfirstlane(ctrl[1]);
      if (blockIdx.x < 16 && lane < 8) __builtin_amdgcn_raw_buffer_store_b128(u32x4{(unsigned)own, (unsigned)own, (unsigned)own, (unsigned)own}, wsr, WS_PACE + (k & 31u) * 2048 + blockIdx.x * 128 + lane * 16, 0, SC1);
      if (it == 0) continue;
      int e[16];
      asm volatile("s_load_dword %0, %16, 0x0\n\ts_load_dword %1, %16, 0x80\n\ts_load_dword %2, %16, 0x100\n\ts_load_dword %3, %16, 0x180\n\t"
                   "s_load_dword %4, %16, 0x200\n\ts_load_dword %5, %16, 0x280\n\ts_load_dword %6, %16, 0x300\n\ts_load_dword %7, %16, 0x380\n\t"
                   "s_load_dword %8, %16, 0x400\n\ts_load_dword %9, %16, 0x480\n\ts_load_dword %10, %16, 0x500\n\ts_load_dword %11, %16, 0x580\n\t"
                   "s_load_dword %12, %16, 0x600\n\ts_load_dword %13, %16, 0x680\n\ts_load_dword %14, %16, 0x700\n\ts_load_dword %15, %16, 0x780\n\t"
                   "s_waitcnt lgkmcnt(0)"
                   : "=&s"(e[0]), "=&s"(e[1]), "=&s"(e[2]), "=&s"(e[3]), "=&s"(e[4]), "=&s"(e[5]), "=&s"(e[6]), "=&s"(e[7]),
                     "=&s"(e[8]), "=&s"(e[9]), "=&s"(e[10]), "=&s"(e[11]), "=&s"(e[12]), "=&s"(e[13]), "=&s"(e[14]), "=&s"(e[15])
                   : "s"(ws + WS_PACE + ((k - 1u) & 31u) * 2048) : "memory");
      int sum = 0, cnt = 0;
#pragma unroll
      for (int i = 0; i < 16; ++i) { const int d = e[i] - own; const bool ok = (unsigned)(d + 600) < 1200u; sum += ok ? d : 0; cnt += ok ? 1 : 0; }
      if (cnt >= 8) ctrl[2] = own + (int)((float)sum / (float)cnt);
    }
    return;
  }

  if (wave == XWAVE) {
#pragma unroll 1
    for (int i = 0; i < 20; ++i) PAUSE(4);
    const u32x4 xv = *(const GAS u32x4*)(x + b * D + 8 * lane), gv = *(const GAS u32x4*)(gw + 8 * lane);
    *(LAS u32x4*)(L + L_X + lane * 16) = xv;
    float gd = 0.f;
#pragma unroll
    for (int k = 0; k < 4; ++k) gd = dot2(xv[k], gv[k], gd);
    gd = xrow_sum(row16_sum(gd));
    const float g = 1.f / (1.f + __expf(-gd));
    ctrlf[48] = g;
    lds_arrive(L, C_X, lane);
    u64 tag;
    asm volatile("s_load_dwordx2 %0, %1, 0x0\n\ts_waitcnt lgkmcnt(0)" : "=s"(tag) : "s"(ws + WS_EPOCH + b * 256) : "memory");
    tag += TAG_STEP;
#pragma unroll 1
    for (int spin = 0; spin < SPIN_CAP; ++spin) { if (__builtin_amdgcn_readfirstlane(ctrl[C_MAX]) >= NCW + NHW) break; __builtin_amdgcn_s_sleep(16); }
    CFENCE();
    const float mg = wg_max();
    float l = 0.f;
#pragma unroll 4
    for (int i = 0; i < NKU * 64 / 1024; ++i) {
      const f32x4 sc = *(const LAS f32x4*)(L + L_SC + i * 1024 + lane * 16);
      l += (expd(sc[0] - mg) + expd(sc[1] - mg)) + (expd(sc[2] - mg) + expd(sc[3] - mg));
    }
    l = xrow_sum(row16_sum(l));
    const unsigned tlo = (unsigned)tag, thi = (unsigned)(tag >> 32);
    if (lane < 8) __builtin_amdgcn_raw_buffer_store_b128(u32x4{__float_as_uint(mg), tlo, __float_as_uint(l), thi}, wsr, WS_REC + (b * 64 + pj) * 128 + lane * 16, 0, SC1);
#pragma unroll 1
    for (int i = 0; i < 12 + (kk & 31); ++i) PAUSE(12);
    u32x4 rec;
#pragma unroll 1
    for (int spin = 0; spin < SPIN_CAP; ++spin) {
      CFENCE();
      rec = __builtin_amdgcn_raw_buffer_load_b128(wsr, WS_REC + (b * 64 + (pj ^ 1)) * 128, 0, SC1);
      if (__builtin_amdgcn_ballot_w64(rec[1] != tlo || rec[3] != thi) == 0ull) break;
      __builtin_amdgcn_s_sleep(16);
    }
    const float mo = __uint_as_float(rec[0]), lo = __uint_as_float(rec[2]);
    const float mh = fmaxf(mg, mo), wt = expd(mg - mh);
    ctrlf[49] = g * wt / (wt * l + expd(mo - mh) * lo);
    const u32x4 yt = {tlo & YMASK, (tlo >> 12) & YMASK, (unsigned)(tag >> 24) & YMASK, (thi >> 4) & YMASK};
    *(LAS u32x4*)(L + L_CTRL + 52 * 4) = yt;
    lds_arrive(L, C_CF, lane);
    GAS unsigned* op = (GAS unsigned*)(out + b * D + 8 * pj + 4 * (lane & 1) + 2 * (lane >> 5));
    int off = WS_Y + (b * 64 + (lane >> 1)) * 2048 + pj * 32 + (lane & 1) * 16;
    asm volatile("" : "+v"(op), "+v"(off));
    BARRIER();
    {
      const unsigned now = (unsigned)__builtin_amdgcn_s_memrealtime();
      const int d = __builtin_amdgcn_readfirstlane(ctrl[2]) + TSTREAM + FOFF - (int)now;
      const unsigned t = now + (unsigned)min(max(d, FMIN), FMAX);
#pragma unroll 1
      for (int spin = 0; spin < 4096 && (int)(t - (unsigned)__builtin_amdgcn_s_memrealtime()) > 0; ++spin) {}
    }
    u32x4 ya, yb;
#pragma unroll 1
    for (int spin = 0; spin < SPIN_CAP; ++spin) {
      CFENCE();
      ya = __builtin_amdgcn_raw_buffer_load_b128(wsr, off, 0, SC1);
      yb = __builtin_amdgcn_raw_buffer_load_b128(wsr, off, 32 * 2048, SC1);
      const unsigned bad = (((ya[0] ^ yt[0]) | (ya[1] ^ yt[1])) | ((ya[2] ^ yt[2]) | (ya[3] ^ yt[3])) | ((yb[0] ^ yt[0]) | (yb[1] ^ yt[1])) | ((yb[2] ^ yt[2]) | (yb[3] ^ yt[3]))) & YMASK;
      if (__builtin_amdgcn_ballot_w64(bad != 0u) == 0ull) break;
    }
    float o[4];
#pragma unroll
    for (int k = 0; k < 4; ++k) o[k] = __uint_as_float(ya[k] & ~YMASK) + __uint_as_float(yb[k] & ~YMASK);
    float e0 = swadd32(o[0], o[2]), e1 = swadd32(o[1], o[3]);
    e0 = swadd16(e0, e0); e1 = swadd16(e1, e1);
    e0 += dpp<ROR8>(e0); e1 += dpp<ROR8>(e1); e0 += dpp<ROR4>(e0); e1 += dpp<ROR4>(e1); e0 += dpp<ROR2>(e0); e1 += dpp<ROR2>(e1);
    if ((lane & 30) == 0) *op = pack_bf16(e0, e1);
    if (pj == 0 && lane < 8) __builtin_amdgcn_raw_buffer_store_b128(u32x4{tlo, thi, tlo, thi}, wsr, WS_EPOCH + b * 256 + lane * 16, 0, SC1);
    return;
  }

  const int cw = wave - 1, hw = wave - 1 - NCW;
  int landed = 0;
  auto wait_unit = [&](int u) __attribute__((always_inline)) {
#pragma unroll 1
    for (int spin = 0; landed <= u && spin < SPIN_CAP; ++spin) {
      landed = __builtin_amdgcn_readfirstlane(ctrl[0]);
    }
    CFENCE();
  };
  auto take = [&](int u, int next, u32x4& a0, u32x4& a1, u32x4& a2, u32x4& a3) __attribute__((always_inline)) {
    wait_unit(u);
    const LAS u32x4* sp = (const LAS u32x4*)(L + (u & (NSLOT - 1)) * UNIT + lane * 16);
    a0 = sp[0]; a1 = sp[64]; a2 = sp[128]; a3 = sp[192];
    LDS_WAIT();
    ctrl[16 + cw] = next;
    asm volatile("" : "+v"(a0), "+v"(a1), "+v"(a2), "+v"(a3) :: "memory");
  };
  u32x4 q;
  auto build_q = [&]() __attribute__((always_inline)) {
    lds_await(ctrl, C_Q, NHW);
    const LAS f32x4* qp = (const LAS f32x4*)(L + L_QP + (2 * r4 * DH + 8 * c16) * 4);
    const f32x4 lo = qp[0] + qp[DH / 4], hi = qp[1] + qp[DH / 4 + 1];
    q[0] = pack_bf16(xrow_sum(lo[0]), xrow_sum(lo[1])); q[1] = pack_bf16(xrow_sum(lo[2]), xrow_sum(lo[3]));
    q[2] = pack_bf16(xrow_sum(hi[0]), xrow_sum(hi[1])); q[3] = pack_bf16(xrow_sum(hi[2]), xrow_sum(hi[3]));
  };
  constexpr float SCALE = 0.088388347648318440f;
  float mx = -3.4028234e38f;
  auto score = [&](int u, const u32x4& k0, const u32x4& k1, const u32x4& k2, const u32x4& k3) __attribute__((always_inline)) {
    float d0 = 0.f, d1 = 0.f, d2 = 0.f, d3 = 0.f;
#pragma unroll
    for (int k = 0; k < 4; ++k) { d0 = dot2(k0[k], q[k], d0); d1 = dot2(k1[k], q[k], d1); d2 = dot2(k2[k], q[k], d2); d3 = dot2(k3[k], q[k], d3); }
    const unsigned s01 = pack_bf16(row16_sum(d0), row16_sum(d1)), s23 = pack_bf16(row16_sum(d2), row16_sum(d3));
    const unsigned t01 = pack_bf16(bflo(s01) * SCALE, bfhi(s01) * SCALE), t23 = pack_bf16(bflo(s23) * SCALE, bfhi(s23) * SCALE);
    const f32x4 sc = {bflo(t01), bfhi(t01), bflo(t23), bfhi(t23)};
    mx = fmaxf(fmaxf(mx, fmaxf(sc[0], sc[1])), fmaxf(sc[2], sc[3]));
    if (c16 == 0) *(LAS f32x4*)(L + L_SC + u * 64 + r4 * 16) = sc;
  };

  if (hw < 0) {
    {
      u32x4 hk[4 * NHC];
#pragma unroll
      for (int i = 0; i < NHC; ++i) take(cw + NCW * i, i + 1 < NHC ? cw + NCW * (i + 1) : NHELD + cw, hk[4 * i], hk[4 * i + 1], hk[4 * i + 2], hk[4 * i + 3]);
      build_q();
#pragma unroll
      for (int i = 0; i < NHC; ++i) { score(cw + NCW * i, hk[4 * i], hk[4 * i + 1], hk[4 * i + 2], hk[4 * i + 3]); __builtin_amdgcn_sched_barrier(0); }
    }
#pragma unroll 1
    for (int u = NHELD + cw; u < NKU; u += NCW) {
      u32x4 k0, k1, k2, k3;
      take(u, u + NCW, k0, k1, k2, k3);
      score(u, k0, k1, k2, k3);
    }
    ctrlf[32 + cw] = xrow_max(mx);
    lds_arrive(L, C_MAX, lane);
    lds_await(ctrl, C_MAX, NCW + NHW);
    const float mg = wg_max();
    float acc[8] = {0.f, 0.f, 0.f, 0.f, 0.f, 0.f, 0.f, 0.f};
    LAS unsigned* rp = (LAS unsigned*)(L + L_REC + cw * 272 + (8 * c16 + 2 * r4) * 2);
    asm volatile("" : "+v"(rp));
    float cf = 0.f;
#pragma unroll 1
    for (int u = NKU + cw; u < NUNIT; u += NCW) {
      const f32x4 sc = *(const LAS f32x4*)(L + L_SC + (u - NKU) * 64 + r4 * 16);
      float p[4];
#pragma unroll
      for (int t = 0; t < 4; ++t) { p[t] = expd(sc[t] - mg); asm volatile("" : "+v"(p[t])); }
      if ((u & ~(NCW - 1)) == NUNIT - 32) { lds_await(ctrl, C_CF, 1); cf = ctrlf[49]; }
      u32x4 v[4];
      take(u, u + NCW, v[0], v[1], v[2], v[3]);
#pragma unroll
      for (int t = 0; t < 4; ++t)
#pragma unroll
        for (int k = 0; k < 4; ++k) { acc[2 * k] = fmaf(p[t], bflo(v[t][k]), acc[2 * k]); acc[2 * k + 1] = fmaf(p[t], bfhi(v[t][k]), acc[2 * k + 1]); }
    }
    const float e0 = swadd16(swadd32(acc[0], acc[4]), swadd32(acc[2], acc[6])), e1 = swadd16(swadd32(acc[1], acc[5]), swadd32(acc[3], acc[7]));
    *rp = pack_bf16(e0 * cf, e1 * cf);
    BARRIER();
    return;
  }

  u32x4 wb[16];
  const unsigned tstart = (unsigned)__builtin_amdgcn_readfirstlane(ctrl[8]);
  {
    const __amdgpu_buffer_rsrc_t rq = mkrsrc(wq, D * H * DH * 2);
    const int vo = ((64 * hw + 16 * r4) * (H * DH) + h * DH + 8 * c16) * 2;
#pragma unroll
    for (int i = 0; i < 16; ++i) { if (i == 0) wait_until(tstart + TQ + hw * QA); else PAUSE(QS); wb[i] = ldw(rq, vo, i * (H * DH * 2)); }
    lds_await(ctrl, C_X, 1);
    const u32x4 xa = *(const LAS u32x4*)(L + L_X + (64 * hw + 16 * r4) * 2), xc = *(const LAS u32x4*)(L + L_X + (64 * hw + 16 * r4 + 8) * 2);
    float a[8] = {0.f, 0.f, 0.f, 0.f, 0.f, 0.f, 0.f, 0.f};
#pragma unroll
    for (int i = 0; i < 8; ++i) {
      const unsigned xp = i < 4 ? xa[i] : xc[i - 4];
#pragma unroll
      for (int k = 0; k < 4; ++k) {
        a[2 * k] = dot2(perm_lo(wb[2 * i][k], wb[2 * i + 1][k]), xp, a[2 * k]);
        a[2 * k + 1] = dot2(perm_hi(wb[2 * i][k], wb[2 * i + 1][k]), xp, a[2 * k + 1]);
      }
    }
    const float e0 = swadd16(swadd32(a[0], a[4]), swadd32(a[2], a[6])), e1 = swadd16(swadd32(a[1], a[5]), swadd32(a[3], a[7]));
    *(LAS f32x2*)(L + L_QP + (hw * DH + 8 * c16 + 2 * r4) * 4) = f32x2{e0, e1};
    lds_arrive(L, C_Q, lane);
    const int hb = NHC * NCW + hw;
#pragma unroll
    for (int i = 0; i < NHH; ++i) take(hb + NHW * i, i + 1 < NHH ? hb + NHW * (i + 1) : NEVER, wb[4 * i], wb[4 * i + 1], wb[4 * i + 2], wb[4 * i + 3]);
    build_q();
#pragma unroll
    for (int i = 0; i < NHH; ++i) { score(hb + NHW * i, wb[4 * i], wb[4 * i + 1], wb[4 * i + 2], wb[4 * i + 3]); __builtin_amdgcn_sched_barrier(0); }
    ctrlf[32 + cw] = xrow_max(mx);
    lds_arrive(L, C_MAX, lane);
  }
  f32x4 y[4] = {{0.f, 0.f, 0.f, 0.f}, {0.f, 0.f, 0.f, 0.f}, {0.f, 0.f, 0.f, 0.f}, {0.f, 0.f, 0.f, 0.f}};
  if ((pj & 7) != 7) {
    const int sl = 7 * (pj >> 3) + (pj & 7);
    const __amdgpu_buffer_rsrc_t r1 = mkrsrc(w1, D * F * 2), r3 = mkrsrc(w3, D * F * 2), r2 = mkrsrc(w2, F * D * 2);
    const int vo = ((64 * hw + (lane & 60)) * F + FS * sl + 8 * (lane & 3)) * 2;
#pragma unroll
    for (int i = 0; i < 8; ++i) { if (i == 0) wait_until(tstart + T1 + hw * A1); else PAUSE(S1); wb[i] = ldw(i < 4 ? r1 : r3, vo, (i & 3) * (F * 2)); }
    const u32x2 xm = *(const LAS u32x2*)(L + L_X + (64 * hw + (lane & 60)) * 2);
    float a[16];
#pragma unroll
    for (int m = 0; m < 2; ++m)
#pragma unroll
      for (int k = 0; k < 4; ++k) {
        a[8 * m + 2 * k] = dot2(perm_lo(wb[4 * m + 2][k], wb[4 * m + 3][k]), xm[1], dot2(perm_lo(wb[4 * m][k], wb[4 * m + 1][k]), xm[0], 0.f));
        a[8 * m + 2 * k + 1] = dot2(perm_hi(wb[4 * m + 2][k], wb[4 * m + 3][k]), xm[1], dot2(perm_hi(wb[4 * m][k], wb[4 * m + 1][k]), xm[0], 0.f));
      }
    f32x4 e;
#pragma unroll
    for (int k = 0; k < 4; ++k) {
      float t = swadd16(swadd32(a[k], a[8 + k]), swadd32(a[4 + k], a[12 + k]));
      t += dpp<ROR8>(t); e[k] = t + dpp<ROR4>(t);
    }
    if ((lane & 12) == 0) *(LAS f32x4*)(L + L_MP + (hw * 64 + (lane & 32) + 8 * (lane & 3) + 4 * (r4 & 1)) * 4) = e;
    lds_arrive(L, C_MLP, lane);
    u32x2 wr[8];
#pragma unroll
    for (int i = 0; i < 8; ++i) { if (i == 0) wait_until(tstart + T2 + hw * A2); else PAUSE(S2); wr[i] = ldw2(r2, ((FS * sl + 8 * r4) * D + 64 * hw + 4 * c16) * 2, i * (D * 2)); }
    lds_await(ctrl, C_MLP, NHW);
    const LAS float* mp = (const LAS float*)(L + L_MP) + (lane & 31);
    float ga = 0.f, up = 0.f;
#pragma unroll
    for (int w = 0; w < NHW; ++w) { ga += mp[w * 64]; up += mp[w * 64 + 32]; }
    const float hv = ga / (1.f + __expf(-ga)) * up * (1.f - ctrlf[48]);
    const unsigned hh = pack_bf16(hv, 0.f), hl = pack_bf16(hv - bflo(hh), 0.f);
    *(LAS u16*)(L + L_H + hw * 128 + lane * 2) = (u16)(lane < 32 ? hh : hl);
    LDS_WAIT();
    u32x4 ha = *(const LAS u32x4*)(L + L_H + hw * 128 + (c16 & 1) * 64 + r4 * 16);
    if (c16 >= 2) ha = u32x4{0u, 0u, 0u, 0u};
#pragma unroll
    for (int e = 0; e < 4; ++e) {
      u32x4 f;
#pragma unroll
      for (int k = 0; k < 4; ++k) f[k] = (e & 1) ? perm_hi(wr[2 * k][e >> 1], wr[2 * k + 1][e >> 1]) : perm_lo(wr[2 * k][e >> 1], wr[2 * k + 1][e >> 1]);
      y[e] = mfma(ha, f, y[e]);
    }
  }
#pragma unroll
  for (int e = 0; e < 4; ++e) asm volatile("" : "+v"(y[e]));
  {
    const __amdgpu_buffer_rsrc_t ro = mkrsrc(wo, H * DH * D * 2);
#pragma unroll
    for (int t = 0; t < 4; ++t) {
      u32x2 wr[8];
#pragma unroll
      for (int i = 0; i < 8; ++i) { if (t + i == 0) wait_until(tstart + TO + hw * AO); else PAUSE(SO); wr[i] = ldw2(ro, ((h * DH + 32 * r4) * D + 64 * hw + 4 * c16) * 2, (8 * t + i) * (D * 2)); }
#pragma unroll
      for (int e = 0; e < 4; ++e) {
#pragma unroll
        for (int k = 0; k < 4; ++k) wb[4 * t + e][k] = (e & 1) ? perm_hi(wr[2 * k][e >> 1], wr[2 * k + 1][e >> 1]) : perm_lo(wr[2 * k][e >> 1], wr[2 * k + 1][e >> 1]);
        asm volatile("" : "+v"(wb[4 * t + e]));
      }
    }
  }
  lds_await(ctrl, C_CF, 1);
  u32x4 yt = *(const LAS u32x4*)(L + L_CTRL + 52 * 4);
  int yo = WS_Y + (b * 64 + pj) * 2048 + hw * 256 + c16 * 16;
  const LAS u32x4* rec = (const LAS u32x4*)(L + L_REC + (lane & 3) * 272 + r4 * 64);
  unsigned ym = ~YMASK;
  asm volatile("" : "+v"(yt), "+v"(yo), "+v"(rec), "+s"(ym));
  BARRIER();
  {
    const u32x4 a[4] = {rec[0], rec[1], rec[2], rec[3]};
#pragma unroll
    for (int t = 0; t < 4; ++t)
#pragma unroll
      for (int e = 0; e < 4; ++e) y[e] = mfma(a[t], wb[4 * t + e], y[e]);
    u32x4 v;
#pragma unroll
    for (int e = 0; e < 4; ++e) {
      const f32x2 t = y[e].xy + y[e].zw;
      unsigned r;
      asm("v_add_f32 %0, %1, %2\n\tv_add_u32 %0, %5, %0\n\tv_and_or_b32 %0, %0, %3, %4" : "=&v"(r) : "v"(t[0]), "v"(t[1]), "s"(ym), "v"(yt[e]), "i"((YMASK + 1u) / 2u));
      v[e] = r;
    }
    if (lane < 16) __builtin_amdgcn_raw_buffer_store_b128(v, wsr, yo, 0, SC1);
  }
}

extern "C" void kernel_launch(void* const* d_in, const int* in_sizes, int n_in, void* d_out, int out_size, void* d_ws, size_t ws_size,
                              hipStream_t stream) {
  gated_mix__mha_block_cached__glu_gated_swiglu__decode_kernel<<<256, NTHR, 0, stream>>>((const u16*)d_in[0], (const u16*)d_in[1], (const u16*)d_in[2], (const u16*)d_in[3], (const u16*)d_in[4],
                                             (const u16*)d_in[6], (const u16*)d_in[7], (const u16*)d_in[8], (const u16*)d_in[9], (u16*)d_out,
                                             (unsigned char*)d_ws);
}
```

```cpp
#include <hip/hip_runtime.h>
#include <hip/hip_bf16.h>
#define STAMPS 1
#define MWAVE 0
#define MIDX 0
#define MTYPE 3
#define RWAVE -1
#define RIDX 0
#define MSCALE 40.f

#define GAS __attribute__((address_space(1)))
#define LAS __attribute__((address_space(3)))
typedef unsigned short u16;
typedef unsigned long long u64;
using u32x4 = __attribute__((ext_vector_type(4))) unsigned;
using u32x2 = __attribute__((ext_vector_type(2))) unsigned;
using f32x4 = __attribute__((ext_vector_type(4))) float;
using f32x2 = __attribute__((ext_vector_type(2))) float;
using bf16x2 = __attribute__((ext_vector_type(2))) __bf16;
using bf16x8 = __attribute__((ext_vector_type(8))) __bf16;

constexpr int B = 4, D = 512, H = 32, SK = 8192, DH = 128, F = 1792;
constexpr int NCW = 4, NHW = 8, XWAVE = 1 + NCW + NHW, NWAVE = XWAVE + 2, NTHR = NWAVE * 64;
#define QA 80
#define QB 5
#define T1 1200
#define A1 150
#define B1 18
#define T2 2500
#define A2 40
#define B2 4
#define TO 2900
#define AO 350
#define BO 8
#define TS 70.6
#define REF0 180
#define PLAG 1
#define NOPST 0
#define NOPLD 0
#define PAUX 16
#define RAUX 16
#define PFW 0
#define TQ 130
#define NINIT 6
#define CODE_KIB 21
#define FDEL 0
#define CODEPF 1
#define YST 16
#define YLD 16
#define RED16 0
#define MASKP 0
#define PSLP 0
#define OUTSC 0
#define OUT_STORE(p, v) *(GAS unsigned*)(p) = v
#define CLR0 1
#define MLPFULL 0
#define MLPPF 0
#define TP 1200
#define AP 12
#define GRID 0
#define B2BD 0
#define B2BAUX 16
#define WOB2B 0
#define TOUCHW 1
#define W0 30
#define BARPOS 12
#define NO_WO 0
#define TLBPF 0
#define TMODE 0
#define SPW 4
#define QS 2
#define S1 6
#define S2 1
#define SO 2
#define USL 0
#define JUMP 100
#define DECAY 6
#define HOLD0 10000
#define UEND 420
#define WGAIN2 18
#define WMID2 WMID
#define FOFF 150
#define FMIN 45
#define FMAX 150
#define NRED 64
#define SLOTSH 8
#define WMID 20.5
#define WGAIN 10
#define WMIN 16
#define WMAX 27
#define CLRAUX 16
#define PCONT 1
#define XDEL 20
#define XSPR 0
#define XNT 0
#define MIMIC 1
#define NCLR 2
#define NOFLAG 0
#define CSPR 0
constexpr int UNIT = 4096;
constexpr int NSLOT = 32, RING = NSLOT * UNIT;
constexpr int NKU = (SK / 2) / 16, NUNIT = 2 * NKU;
constexpr int NHC = 6, NHH = 4, NHELD = NHC * NCW + NHH * NHW;
constexpr int FS = 32;
constexpr int L_CTRL = RING;
constexpr int L_X = L_CTRL + 256;
constexpr int L_QP = L_X + D * 2;
constexpr int L_MP = L_QP + NHW * DH * 4;
constexpr int L_REC = L_MP + NHW * 64 * 4;
constexpr int L_H = L_REC + NCW * 272;
constexpr int L_SC = L_H + NHW * 128;
constexpr int LDS_BYTES = L_SC + NKU * 64;
static_assert(LDS_BYTES <= 159 * 1024, "lds");
static_assert(WMIN <= (int)WMID && (int)WMID <= WMAX && WMAX <= 28, "window");
constexpr int WS_EPOCH = 0;
constexpr int WS_REC = 4096;
constexpr int WS_PACE = WS_REC + 256 * 128;
constexpr int WS_Y = WS_PACE + 64 * 2048;
constexpr int WS_BYTES = WS_Y + B * 64 * 2048;
constexpr u64 TAG_STEP = 0x9E3779B97F4A7C15ull;
constexpr unsigned YMASK = 0xFFFu;
constexpr int SPIN_CAP = 1 << 22;
constexpr int NEVER = 1 << 20;
constexpr int C_Q = 4, C_MAX = 5, C_MLP = 6, C_X = 7, C_CF = 9;

__device__ __forceinline__ float expd(float d) { return __builtin_amdgcn_exp2f(d * 1.4426950408889634f); }
__device__ __forceinline__ float bflo(unsigned u) { return __builtin_bit_cast(float, u << 16); }
__device__ __forceinline__ float bfhi(unsigned u) { return __builtin_bit_cast(float, u & 0xffff0000u); }
__device__ __forceinline__ float dot2(unsigned a, unsigned b, float acc) {
  return __builtin_amdgcn_fdot2_f32_bf16(__builtin_bit_cast(bf16x2, a), __builtin_bit_cast(bf16x2, b), acc, false);
}
__device__ __forceinline__ unsigned pack_bf16(float lo, float hi) {
  const bf16x2 v = {(__bf16)lo, (__bf16)hi};
  return __builtin_bit_cast(unsigned, v);
}
__device__ __forceinline__ unsigned perm_lo(unsigned a, unsigned b) { return __builtin_amdgcn_perm(b, a, 0x05040100u); }
__device__ __forceinline__ unsigned perm_hi(unsigned a, unsigned b) { return __builtin_amdgcn_perm(b, a, 0x07060302u); }
template <int CTRL> __device__ __forceinline__ float dpp(float x) {
  return __builtin_bit_cast(float, __builtin_amdgcn_mov_dpp(__builtin_bit_cast(int, x), CTRL, 0xf, 0xf, true));
}
constexpr int XOR1 = 0xB1, ROR1 = 0x121, ROR2 = 0x122, ROR4 = 0x124, ROR8 = 0x128;
__device__ __forceinline__ float row16_sum(float x) { x += dpp<ROR8>(x); x += dpp<ROR4>(x); x += dpp<ROR2>(x); x += dpp<ROR1>(x); return x; }
__device__ __forceinline__ float swadd32(float a, float b) {
  auto t = __builtin_amdgcn_permlane32_swap(__float_as_uint(a), __float_as_uint(b), false, false);
  return __uint_as_float(t[0]) + __uint_as_float(t[1]);
}
__device__ __forceinline__ float swadd16(float a, float b) {
  auto t = __builtin_amdgcn_permlane16_swap(__float_as_uint(a), __float_as_uint(b), false, false);
  return __uint_as_float(t[0]) + __uint_as_float(t[1]);
}
__device__ __forceinline__ float xrow_sum(float x) { x = swadd16(x, x); return swadd32(x, x); }
__device__ __forceinline__ float xrow_max(float x) {
  auto s = __builtin_amdgcn_permlane16_swap(__float_as_uint(x), __float_as_uint(x), false, false);
  x = fmaxf(__uint_as_float(s[0]), __uint_as_float(s[1]));
  auto t = __builtin_amdgcn_permlane32_swap(__float_as_uint(x), __float_as_uint(x), false, false);
  return fmaxf(__uint_as_float(t[0]), __uint_as_float(t[1]));
}
__device__ __forceinline__ __amdgpu_buffer_rsrc_t mkrsrc(const void* p, int bytes) { return __builtin_amdgcn_make_buffer_rsrc((void*)p, 0, bytes, 0x00020000); }
__device__ __forceinline__ u32x4 ldw(__amdgpu_buffer_rsrc_t r, int voff, int soff) { return __builtin_amdgcn_raw_buffer_load_b128(r, voff, soff, 2); }
#define LDS_WAIT() asm volatile("s_waitcnt lgkmcnt(0)" ::: "memory")
#define VM_DRAIN() asm volatile("s_waitcnt vmcnt(0)" ::: "memory")
#define CFENCE() asm volatile("" ::: "memory")
#define BARRIER() asm volatile("s_waitcnt lgkmcnt(0)\n\ts_barrier" ::: "memory")
#define PAUSE(n) do { __builtin_amdgcn_sched_barrier(0); asm volatile("s_sleep %0" :: "i"(n) : "memory"); __builtin_amdgcn_sched_barrier(0); } while (0)

__device__ __forceinline__ void lds_arrive(LAS unsigned char* L, int idx, int lane) {
  LDS_WAIT();
  if (lane == 0) __hip_atomic_fetch_add((LAS int*)(L + L_CTRL) + idx, 1, __ATOMIC_RELAXED, __HIP_MEMORY_SCOPE_WORKGROUP);
}
__device__ __forceinline__ void lds_await(volatile LAS int* ctrl, int idx, int n) {
#pragma unroll 1
  for (int spin = 0; spin < SPIN_CAP; ++spin) { if (__builtin_amdgcn_readfirstlane(ctrl[idx]) >= n) break; __builtin_amdgcn_s_sleep(1); }
  CFENCE();
}

__device__ __forceinline__ void wait_until(unsigned t) {
  __builtin_amdgcn_sched_barrier(0);
#pragma unroll 1
  for (int spin = 0; spin < 4096; ++spin) {
    const int d = (int)(t - (unsigned)__builtin_amdgcn_s_memrealtime());
    if (d <= 0) break;
    if (d > 24) __builtin_amdgcn_s_sleep(6);
  }
  CFENCE();
  __builtin_amdgcn_sched_barrier(0);
}
#define STR2(x) #x
#define STR(x) STR2(x)
constexpr int TAU2 = 2 * (int)(TS * 100.0 * 256.0 / NUNIT);
__device__ __forceinline__ f32x4 mfma(const u32x4& a, const u32x4& b, const f32x4& c) {
  return __builtin_amdgcn_mfma_f32_16x16x32_bf16(__builtin_bit_cast(bf16x8, a), __builtin_bit_cast(bf16x8, b), c, 0, 0, 0);
}
__device__ __forceinline__ u32x2 ldw2(__amdgpu_buffer_rsrc_t r, int voff, int soff) { return __builtin_amdgcn_raw_buffer_load_b64(r, voff, soff, 2); }
template <int CTRL> __device__ __forceinline__ int dppi(int x) { return __builtin_amdgcn_mov_dpp(x, CTRL, 0xf, 0xf, true); }

#define STAMP(i) do { __builtin_amdgcn_sched_barrier(0); ((volatile LAS unsigned*)(L + LDS_BYTES))[wave * 16 + (i)] = (unsigned)__builtin_amdgcn_s_memrealtime(); __builtin_amdgcn_sched_barrier(0); } while (0)
#define STAMP_FLUSH() do { LDS_WAIT(); const __amdgpu_buffer_rsrc_t dr = mkrsrc(ws + (4 << 20), 256 * 1024); \
    __builtin_amdgcn_raw_buffer_store_b128(*(volatile LAS u32x4*)(L + LDS_BYTES + lane * 16), dr, blockIdx.x * 1024 + lane * 16, 0, 16); \
    if (blockIdx.x == 0) { const unsigned t0 = __builtin_amdgcn_readfirstlane(((volatile LAS unsigned*)(L + LDS_BYTES))[0]); \
      while ((unsigned)__builtin_amdgcn_s_memrealtime() - t0 < 13000u) __builtin_amdgcn_s_sleep(8); \
      CFENCE(); float sum = 0.f, mxv = -1e9f, mnv = -1e9f; \
      for (int i = 0; i < 4; ++i) { const int wg = lane * 4 + i; \
        const unsigned ref = (RWAVE < 0) ? t0 : __builtin_amdgcn_raw_buffer_load_b32(dr, wg * 1024 + ((RWAVE < 0 ? 0 : RWAVE) * 16 + RIDX) * 4, 0, 16); \
        const float v = (float)(int)(__builtin_amdgcn_raw_buffer_load_b32(dr, wg * 1024 + (MWAVE * 16 + MIDX) * 4, 0, 16) - ref); \
        sum += v; mxv = fmaxf(mxv, v); mnv = fmaxf(mnv, -v); } \
      sum = xrow_sum(row16_sum(sum)) * (1.f / 256.f); \
      float gm = mxv; gm = fmaxf(gm, dpp<ROR8>(gm)); gm = fmaxf(gm, dpp<ROR4>(gm)); gm = fmaxf(gm, dpp<ROR2>(gm)); gm = fmaxf(gm, dpp<ROR1>(gm)); gm = xrow_max(gm); \
      float gn = mnv; gn = fmaxf(gn, dpp<ROR8>(gn)); gn = fmaxf(gn, dpp<ROR4>(gn)); gn = fmaxf(gn, dpp<ROR2>(gn)); gn = fmaxf(gn, dpp<ROR1>(gn)); gn = xrow_max(gn); \
      const float met = (MTYPE == 0 ? sum : MTYPE == 1 ? gm : MTYPE == 2 ? gm - sum : MTYPE == 3 ? sum + gn : gm + gn) * MSCALE; \
      const unsigned tgt = __builtin_amdgcn_readfirstlane((unsigned)(15000.f + met)); \
      while ((unsigned)__builtin_amdgcn_s_memrealtime() - t0 < tgt) __builtin_amdgcn_s_sleep(1); } } while (0)
#define STAMP_LDS 1024

__global__ void __launch_bounds__(NTHR, 4) gated_mix_kernel(const u16* x, const u16* kc, const u16* vc, const u16* wq, const u16* wo, const u16* w1,
                                                         const u16* w3, const u16* w2, const u16* gw, u16* out, unsigned char* ws) {
  __shared__ __attribute__((aligned(16))) unsigned char lds_raw[LDS_BYTES + STAMP_LDS];
  LAS unsigned char* L = (LAS unsigned char*)lds_raw;
  volatile LAS int* ctrl = (volatile LAS int*)(L + L_CTRL);
  volatile LAS float* ctrlf = (volatile LAS float*)(L + L_CTRL);
  u64 t_first; asm volatile("s_memrealtime %0\n\ts_waitcnt lgkmcnt(0)" : "=s"(t_first));
  __builtin_amdgcn_sched_barrier(0);
  if (threadIdx.x < 14 * 64) ((volatile LAS unsigned*)(L + LDS_BYTES))[(threadIdx.x >> 6) * 16] = (unsigned)t_first;
#define DBG(x) x
  {
    unsigned va, vb, vo, pa, nxa, t0, t1, t2, t3;
#define CODELD "global_load_lds_dwordx4 %[t0], s[56:57]\n\t"
#define ASTAMP(i) DBG("s_memrealtime s[60:61]\n\ts_waitcnt lgkmcnt(0)\n\tv_mov_b32 %[t1], %c[dbg] + 4 * " #i "\n\tv_mov_b32 %[t2], s60\n\tds_write_b32 %[t1], %[t2]\n\t")
#define WAITW "s_waitcnt (wcnt & 15) | ((wcnt >> 4) << 14) | 0x0F70\n\t"
#define SP(v, sb, wait) wait "global_load_lds_dword " v ", " sb " nt\n\tglobal_load_lds_dword " v ", " sb " sc1\n\t"
#define SW "s_waitcnt vmcnt(%c[spw])\n\t"
#define UNIT4(v) \
        WAITW "global_load_lds_dwordx4 " v ", s[40:41] nt\n\t" \
        WAITW "global_load_lds_dwordx4 " v ", s[40:41] offset:1024 nt\n\t" \
        WAITW "global_load_lds_dwordx4 " v ", s[40:41] offset:2048 nt\n\t" \
        WAITW "global_load_lds_dwordx4 " v ", s[40:41] offset:3072 nt\n\t"
    asm volatile(
        "s_getpc_b64 s[56:57]\n\t"
        "s_load_dwordx8 s[44:51], %[ka], 0x18\n\t"
        "s_load_dwordx2 s[52:53], %[ka], 0x38\n\t"
        "s_load_dwordx4 s[40:43], %[ka], 0x8\n\t"
        "v_readfirstlane_b32 s54, %[tid]\n\t"
        "s_mov_b32 m0, %c[jk]\n\t"
        "s_lshr_b32 s59, %[bid], 3\n\t"
        "s_cmp_ge_u32 s54, 64\n\t"
        "s_cbranch_scc1 90f\n\t"
        "s_min_u32 s54, s59, %c[nch]\n\t"
        "s_lshl_b32 s54, s54, 10\n\t"
        "v_lshlrev_b32 %[vo], 4, %[tid]\n\t"
        "s_and_b32 s56, s56, -128\n\t"
        "v_add_u32 %[t0], s54, %[vo]\n\t"
        "s_and_b32 s58, %[bid], 7\n\t"
        CODELD
        "s_setprio 3\n\t"
        "v_lshlrev_b32 %[t1], 7, %[tid]\n\t"
        "s_lshl_b32 s54, s59, 7\n\t"
        "s_add_u32 s54, s54, s58\n\t"
        "s_lshl_b32 s54, s54, 10\n\t"
        "v_lshrrev_b32 %[va], 3, %[tid]\n\t"
        "v_mul_u32_u24 %[va], 8192 - 1024, %[va]\n\t"
        "v_add3_u32 %[va], %[va], %[t1], s54\n\t"
        "v_add_u32 %[t2], 0x10000, %[va]\n\t"
        "s_lshl_b32 s55, s58, 5\n\t"
        "s_add_u32 s55, s55, s59\n\t"
        "s_mul_i32 s54, s55, 7 * 1024\n\t"
        "v_min_u32 %[t3], 55 * 128, %[t1]\n\t"
        "v_add_u32 %[t3], s54, %[t3]\n\t"
        "s_mul_i32 s54, s58, 448\n\t"
        "s_and_b32 s54, s54, -128\n\t"
        "s_mul_i32 s57, s59, 16 * 3584\n\t"
        "s_add_u32 s54, s54, s57\n\t"
        "v_lshrrev_b32 %[t0], 2, %[tid]\n\t"
        "v_mul_u32_u24 %[t0], 3584 - 512, %[t0]\n\t"
        "v_add3_u32 %[t0], %[t0], %[t1], s54\n\t"
        "v_lshl_add_u32 %[vb], s55, 14, %[t1]\n\t"
        "v_add_u32 %[t1], 0x2000, %[vb]\n\t"
        "s_and_b32 s54, %[bid], 0xc0\n\t"
        "s_lshl3_add_u32 s54, s58, s54\n\t"
        "s_and_b32 s56, s59, 7\n\t"
        "s_add_u32 s54, s54, s56\n\t"
        "s_lshl_b32 s56, s54, 20\n\t"
        "s_waitcnt lgkmcnt(0)\n\t"
        SP("%[va]", "s[44:45]", "") SP("%[t2]", "s[44:45]", "") SP("%[t3]", "s[52:53]", "")
        ".if %c[spw] <= 4\n\ts_barrier\n\t.endif\n\t"
        SP("%[t0]", "s[48:49]", SW)
        ".if %c[spw] == 6\n\ts_barrier\n\t.endif\n\t"
        SP("%[t0]", "s[50:51]", SW)
        ".if %c[spw] == 8\n\ts_barrier\n\t.endif\n\t"
        SP("%[vb]", "s[46:47]", SW) SP("%[t1]", "s[46:47]", SW)
        ".if %c[spw] > 8\n\ts_barrier\n\t.endif\n\t"
        ASTAMP(6)
        "s_add_u32 s40, s40, s56\n\t"
        "s_addc_u32 s41, s41, 0\n\t"
        "s_add_u32 s42, s42, s56\n\t"
        "s_addc_u32 s43, s43, 0\n\t"
        "s_sub_u32 s42, s42, %c[nku] * 4096\n\t"
        "s_subb_u32 s43, s43, 0\n\t"
        ".set so, 0\n\t"
        ".set wcnt, %c[spw]\n\t"
        ".rept 4\n\t"
        "s_movk_i32 m0, so\n\t"
        ".if (so & 4096) == 0\n\t"
        WAITW
        ".set wcnt, wcnt + 6\n\t"
        ".else\n\t"
        "s_nop 0\n\t"
        ".endif\n\t"
        "global_load_lds_dwordx4 %[vo], s[40:41] nt\n\t"
        "global_load_lds_dwordx4 %[vo], s[40:41] offset:1024 nt\n\t"
        "global_load_lds_dwordx4 %[vo], s[40:41] offset:2048 nt\n\t"
        "global_load_lds_dwordx4 %[vo], s[40:41] offset:3072 nt\n\t"
        "v_add_u32 %[vo], 0x1000, %[vo]\n\t"
        ".set so, so + 4096\n\t"
        ".endr\n\t"
        ".if %c[spw] <= 4\n\t"
        "s_waitcnt vmcnt(16)\n\t"
        ".endif\n\t"
        ASTAMP(7)
        "s_getpc_b64 s[50:51]\n"
        "20:\n\t"
        "s_add_u32 s50, s50, 23f-20b-%c[wmin]*128\n\t"
        "s_addc_u32 s51, s51, -1\n\t"
        "v_mov_b32 %[pa], %c[ctl]\n\t"
        "v_and_b32 %[nxa], 0x3c0 >> 2, %[vo]\n\t"
        "v_lshrrev_b32 %[nxa], 2, %[nxa]\n\t"
        "v_add_u32 %[nxa], %c[ctl] + 64, %[nxa]\n\t"
        "s_memrealtime s[58:59]\n\t"
        "s_mov_b32 s62, 0\n\t"
        "s_mov_b32 s63, %c[gain]\n\t"
        "s_mov_b32 s64, %c[wmid]\n\t"
        "s_mov_b32 s44, 4\n\t"
        "s_mov_b32 s45, %c[wmid] >> 8\n\t"
        "s_mov_b32 s46, 0x4000\n\t"
        "s_mov_b32 s47, 2 * %c[tau2]\n\t"
        "s_mov_b32 s48, %c[nslot]\n\t"
        "s_mov_b32 s49, %c[nku]\n\t"
        "s_waitcnt lgkmcnt(0)\n\t"
        "s_mov_b32 s59, 10000\n"
        "22:\n\t"
        "s_memrealtime s[56:57]\n\t"
        "ds_read_b32 %[t3], %[pa] offset:8\n\t"
        "s_lshl_b32 s52, s45, 7\n\t"
        "s_add_u32 s52, s50, s52\n\t"
        "s_addc_u32 s53, s51, 0\n\t"
        "s_add_i32 s54, s45, -1\n\t"
        "s_lshr_b32 s54, s54, 2\n\t"
        "s_sub_i32 s54, s44, s54\n\t"
        "v_mov_b32 %[t1], s54\n\t"
        "v_add_u32 %[t0], 0x1000, %[vo]\n\t"
        "s_add_u32 s55, s46, 0x1000\n\t"
        "s_mov_b32 m0, s46\n\t"
        "s_setpc_b64 s[52:53]\n"
        "24:\n\t"
        "s_waitcnt lgkmcnt(0)\n\t"
        "v_readfirstlane_b32 s57, %[t3]\n\t"
        "v_add_u32 %[t1], 1, %[t1]\n\t"
        "s_lshr_b32 s54, s47, 8\n\t"
        "s_sub_u32 s56, s56, s54\n\t"
        "v_mov_b32 %[t2], s56\n\t"
        "s_sub_i32 s54, s56, s58\n\t"
        "s_mov_b32 s58, s56\n\t"
        "s_sub_i32 s56, s56, s57\n\t"
        "ds_write2_b32 %[pa], %[t1], %[t2] offset1:1\n\t"
        "s_add_i32 s59, s59, s54\n\t"
        "s_add_i32 s59, s59, %c[decay]\n\t"
        "s_cmp_gt_i32 s54, %c[jump]\n\t"
        "s_cselect_b32 s59, s62, s59\n\t"
        "s_min_i32 s62, s56, s59\n\t"
        "s_mul_i32 s56, s62, s63\n\t"
        "s_add_i32 s56, s56, s64\n\t"
        "s_ashr_i32 s56, s56, 8\n\t"
        "s_max_i32 s56, s56, %c[wmin]\n\t"
        "s_min_i32 s45, s56, %c[wmax]\n\t"
        "s_add_u32 s47, s47, %c[tau2]\n\t"
        "s_add_u32 s46, s46, 0x2000\n\t"
        "s_and_b32 s46, s46, %c[nslot] * 4096 - 1\n\t"
        "v_add_u32 %[vo], 0x2000, %[vo]\n\t"
        "s_add_i32 s44, s44, 2\n\t"
        "s_cmp_lt_i32 s44, s48\n\t"
        "s_cbranch_scc1 22b\n\t"
        DBG("s_add_i32 s54, s44, -1\n\ts_lshr_b32 s54, s54, 5\n\ts_lshl_b32 s54, s54, 2\n\ts_add_u32 s54, s54, %c[dbg] + 14 * 64\n\tv_mov_b32 %[t0], s54\n\tv_mov_b32 %[t1], s45\n\t"
            "ds_write2_b32 %[t0], %[t2], %[t1] offset1:16\n\t")
        "s_cmp_lt_i32 s44, s49\n\t"
        "s_cbranch_scc1 25f\n\t"
        "s_cmp_ge_i32 s44, 2 * %c[nku]\n\t"
        "s_cbranch_scc1 30f\n\t"
        "s_mov_b32 s49, 2 * %c[nku]\n\t"
        "s_mov_b64 s[40:41], s[42:43]\n"
        "25:\n\t"
        "s_cmp_ge_i32 s44, %c[uend]\n\t"
        "s_cselect_b32 s63, %c[gain2], s63\n\t"
        "s_cselect_b32 s64, %c[wmid2], s64\n\t"
        "s_mov_b32 s55, %c[cap]\n"
        "26:\n\t"
        "ds_read_b32 %[t0], %[nxa]\n\t"
        "s_waitcnt lgkmcnt(0)\n\t"
        "s_nop 1\n\t"
        "v_min_i32_dpp %[t0], %[t0], %[t0] row_ror:8 row_mask:0xf bank_mask:0xf\n\t"
        "s_nop 1\n\t"
        "v_min_i32_dpp %[t0], %[t0], %[t0] row_ror:4 row_mask:0xf bank_mask:0xf\n\t"
        "s_nop 1\n\t"
        "v_min_i32_dpp %[t0], %[t0], %[t0] row_ror:2 row_mask:0xf bank_mask:0xf\n\t"
        "s_nop 1\n\t"
        "v_min_i32_dpp %[t0], %[t0], %[t0] row_ror:1 row_mask:0xf bank_mask:0xf\n\t"
        "s_nop 1\n\t"
        "v_readfirstlane_b32 s48, %[t0]\n\t"
        "s_nop 1\n\t"
        "s_add_i32 s48, s48, %c[nslot]\n\t"
        "s_and_b32 s48, s48, -2\n\t"
        "s_min_i32 s48, s48, s49\n\t"
        "s_cmp_lt_i32 s44, s48\n\t"
        "s_cbranch_scc1 22b\n\t"
        "v_mov_b32 %[t1], s44\n\t"
        "s_waitcnt vmcnt(0)\n\t"
        "ds_write_b32 %[pa], %[t1]\n\t"
        "s_sleep 1\n\t"
        "s_sub_u32 s55, s55, 1\n\t"
        "s_cmp_lg_u32 s55, 0\n\t"
        "s_cbranch_scc1 26b\n\t"
        "s_branch 22b\n"
        "30:\n\t"
        ".set k, 6\n\t"
        ".rept 7\n\t"
        ".set wcnt, 4 * k\n\t"
        "v_mov_b32 %[t1], 2 * %c[nku] - k\n\t"
        WAITW
        "ds_write_b32 %[pa], %[t1]\n\t"
        ".set k, k - 1\n\t"
        ".endr\n\t"
        DBG("s_memrealtime s[56:57]\n\ts_waitcnt lgkmcnt(0)\n\tv_mov_b32 %[t0], %c[dbg] + 4\n\tv_mov_b32 %[t1], s56\n\tds_write_b32 %[t0], %[t1]\n\t")
        "s_waitcnt lgkmcnt(0)\n\t"
        "s_endpgm\n\t"
        ".p2align 7\n"
        "23:\n\t"
        ".set wcnt, %c[wmin]-1\n\t"
        ".rept %c[wmax] - %c[wmin] + 1\n\t"
        UNIT4("%[vo]")
        "ds_write_b32 %[pa], %[t1]\n\t"
        "s_mov_b32 m0, s55\n\t"
        UNIT4("%[t0]")
        "s_branch 24b\n\t"
        ".p2align 7\n\t"
        ".set wcnt, wcnt+1\n\t"
        ".endr\n"
        "90:\n\t"
        "s_waitcnt lgkmcnt(0)"
        : [va] "=&v"(va), [vb] "=&v"(vb), [vo] "=&v"(vo), [pa] "=&v"(pa), [nxa] "=&v"(nxa), [t0] "=&v"(t0), [t1] "=&v"(t1), [t2] "=&v"(t2), [t3] "=&v"(t3)
        : [tid] "v"(threadIdx.x), [bid] "s"(blockIdx.x), [ka] "s"(__builtin_amdgcn_kernarg_segment_ptr()), [nch] "i"(CODE_KIB - 1), [jk] "i"(RING - UNIT),
          [spw] "i"(SPW), [wmin] "i"(WMIN), [wmax] "i"(WMAX), [gain] "i"(WGAIN), [wmid] "i"((int)(WMID * 256)), [tau2] "i"(TAU2),
          [ctl] "i"(L_CTRL), [nslot] "i"(NSLOT), [nku] "i"(NKU), [cap] "i"(SPIN_CAP), [dbg] "i"(LDS_BYTES), [jump] "i"(JUMP), [decay] "i"(DECAY), [uend] "i"(UEND), [gain2] "i"(WGAIN2), [wmid2] "i"((int)(WMID2 * 256))
        : "memory", "scc", "s40", "s41", "s42", "s43", "s44", "s45", "s46", "s47", "s48", "s49", "s50", "s51", "s52", "s53", "s54", "s55", "s56", "s57", "s58", "s59", "s60", "s61", "s62", "s63", "s64");
  }
  __builtin_amdgcn_sched_barrier(0);
  const int tid = threadIdx.x, lane = tid & 63, wave = __builtin_amdgcn_readfirstlane(tid >> 6);
  const int xg = blockIdx.x & 7, kk = blockIdx.x >> 3;
  const int b = kk >> 3, pj = 8 * xg + (kk & 7), h = pj >> 1, s = pj & 1;
  const int r4 = lane >> 4, c16 = lane & 15;
  asm volatile("" : "+s"(x), "+s"(wq), "+s"(wo), "+s"(w1), "+s"(w3), "+s"(w2), "+s"(gw), "+s"(out), "+s"(ws));
  if (wave == 1) {
    const int wi = lane - 16;
    const int now = (int)(unsigned)__builtin_amdgcn_s_memrealtime() + REF0;
    ctrl[lane] = (lane == 1 || lane == 2) ? now : lane == 8 ? now - REF0 : (lane < 16 || lane >= 32) ? 0 : wi < NCW ? wi : wi < NCW + NHW ? NHC * NCW + wi - NCW : NEVER;
  }
  BARRIER();

  const __amdgpu_buffer_rsrc_t wsr = mkrsrc(ws, WS_BYTES);
  auto wg_max = [&]() __attribute__((always_inline)) {
    float mg = ctrlf[32 + (c16 < NCW + NHW ? c16 : 0)];
    mg = fmaxf(mg, dpp<ROR8>(mg)); mg = fmaxf(mg, dpp<ROR4>(mg)); mg = fmaxf(mg, dpp<ROR2>(mg)); mg = fmaxf(mg, dpp<ROR1>(mg));
    return mg;
  };

  if (wave == XWAVE + 1) {
    unsigned k = ((unsigned)__builtin_amdgcn_s_memrealtime() >> SLOTSH) + 1;
#pragma unroll 1
    for (int it = 0; it < 4096 >> (SLOTSH - 2); ++it, ++k) {
      wait_until(k << SLOTSH);
      if (__builtin_amdgcn_readfirstlane(ctrl[0]) >= NUNIT - 36) break;
      const int own = __builtin_amdgcn_readfirstlane(ctrl[1]);
      constexpr unsigned SM = (8192u >> SLOTSH) - 1u;
      if (!NOPST && blockIdx.x < 16 && lane < 8) __builtin_amdgcn_raw_buffer_store_b128(u32x4{(unsigned)own, (unsigned)own, (unsigned)own, (unsigned)own}, wsr, WS_PACE + (k & SM) * 2048 + blockIdx.x * 128 + lane * 16, 0, PAUX);
      if (it < PLAG || NOPLD) continue;
      int e[16];
      asm volatile("s_load_dword %0, %16, 0x0\n\ts_load_dword %1, %16, 0x80\n\ts_load_dword %2, %16, 0x100\n\ts_load_dword %3, %16, 0x180\n\t"
                   "s_load_dword %4, %16, 0x200\n\ts_load_dword %5, %16, 0x280\n\ts_load_dword %6, %16, 0x300\n\ts_load_dword %7, %16, 0x380\n\t"
                   "s_load_dword %8, %16, 0x400\n\ts_load_dword %9, %16, 0x480\n\ts_load_dword %10, %16, 0x500\n\ts_load_dword %11, %16, 0x580\n\t"
                   "s_load_dword %12, %16, 0x600\n\ts_load_dword %13, %16, 0x680\n\ts_load_dword %14, %16, 0x700\n\ts_load_dword %15, %16, 0x780\n\t"
                   "s_waitcnt lgkmcnt(0)"
                   : "=&s"(e[0]), "=&s"(e[1]), "=&s"(e[2]), "=&s"(e[3]), "=&s"(e[4]), "=&s"(e[5]), "=&s"(e[6]), "=&s"(e[7]),
                     "=&s"(e[8]), "=&s"(e[9]), "=&s"(e[10]), "=&s"(e[11]), "=&s"(e[12]), "=&s"(e[13]), "=&s"(e[14]), "=&s"(e[15])
                   : "s"(ws + WS_PACE + ((k - PLAG) & SM) * 2048) : "memory");
      int sum = 0, cnt = 0;
#pragma unroll
      for (int i = 0; i < 16; ++i) { const int d = e[i] - own; const bool ok = (unsigned)(d + 600) < 1200u; sum += ok ? d : 0; cnt += ok ? 1 : 0; }
      if (cnt >= 8) ctrl[2] = own + (int)((float)sum / (float)cnt);
    }
    return;
  }

  if (wave == XWAVE) {
#pragma unroll 1
    for (int i = 0; i < XDEL + XSPR * (kk & 31); ++i) PAUSE(4);
    const u32x4 xv = *(const GAS u32x4*)(x + b * D + 8 * lane), gv = *(const GAS u32x4*)(gw + 8 * lane);
    *(LAS u32x4*)(L + L_X + lane * 16) = xv;
    float gd = 0.f;
#pragma unroll
    for (int k = 0; k < 4; ++k) gd = dot2(xv[k], gv[k], gd);
    gd = xrow_sum(row16_sum(gd));
    const float g = 1.f / (1.f + __expf(-gd));
    ctrlf[48] = g;
    lds_arrive(L, C_X, lane);
    u64 tag;
    asm volatile("s_load_dwordx2 %0, %1, 0x0\n\ts_waitcnt lgkmcnt(0)" : "=s"(tag) : "s"(ws + WS_EPOCH + b * 256) : "memory");
    tag += TAG_STEP;
#pragma unroll 1
    for (int spin = 0; spin < SPIN_CAP; ++spin) { if (__builtin_amdgcn_readfirstlane(ctrl[C_MAX]) >= NCW + NHW) break; __builtin_amdgcn_s_sleep(16); }
    CFENCE();
    const float mg = wg_max();
    float l = 0.f;
#pragma unroll 4
    for (int i = 0; i < NKU * 64 / 1024; ++i) {
      const f32x4 sc = *(const LAS f32x4*)(L + L_SC + i * 1024 + lane * 16);
      l += (expd(sc[0] - mg) + expd(sc[1] - mg)) + (expd(sc[2] - mg) + expd(sc[3] - mg));
    }
    l = xrow_sum(row16_sum(l));
    const unsigned tlo = (unsigned)tag, thi = (unsigned)(tag >> 32);
    if (lane < 8) __builtin_amdgcn_raw_buffer_store_b128(u32x4{__float_as_uint(mg), tlo, __float_as_uint(l), thi}, wsr, WS_REC + (b * 64 + pj) * 128 + lane * 16, 0, 16);
#pragma unroll 1
    for (int i = 0; i < 12 + (kk & 31); ++i) PAUSE(12);
    u32x4 rec;
#pragma unroll 1
    for (int spin = 0; spin < SPIN_CAP; ++spin) {
      CFENCE();
      rec = __builtin_amdgcn_raw_buffer_load_b128(wsr, WS_REC + (b * 64 + (pj ^ 1)) * 128, 0, 16);
      if (__builtin_amdgcn_ballot_w64(rec[1] != tlo || rec[3] != thi) == 0ull) break;
      __builtin_amdgcn_s_sleep(16);
    }
    const float mo = __uint_as_float(rec[0]), lo = __uint_as_float(rec[2]);
    const float mh = fmaxf(mg, mo), wt = expd(mg - mh);
    ctrlf[49] = g * wt / (wt * l + expd(mo - mh) * lo);
    const u32x4 yt = {tlo & YMASK, (tlo >> 12) & YMASK, (unsigned)(tag >> 24) & YMASK, (thi >> 4) & YMASK};
    *(LAS u32x4*)(L + L_CTRL + 52 * 4) = yt;
    lds_arrive(L, C_CF, lane);
    GAS unsigned* op = (GAS unsigned*)(out + b * D + 8 * pj + 4 * (lane & 1) + 2 * (lane >> 5));
    int off = WS_Y + (b * 64 + (lane >> 1)) * 2048 + pj * 32 + (lane & 1) * 16;
    asm volatile("" : "+v"(op), "+v"(off));
    if ((pj & (64 / NRED - 1)) != 0) return;
    STAMP(4);
    BARRIER();
    {
      const unsigned now = (unsigned)__builtin_amdgcn_s_memrealtime();
      const int d = __builtin_amdgcn_readfirstlane(ctrl[2]) + (int)(TS * 100) + FOFF - (int)now;
      const unsigned t = now + (unsigned)min(max(d, FMIN), FMAX);
#pragma unroll 1
      for (int spin = 0; spin < 4096 && (int)(t - (unsigned)__builtin_amdgcn_s_memrealtime()) > 0; ++spin) {}
    }
    STAMP(9);
    constexpr int NL = 128 / NRED;
    u32x4 yv[NL];
#pragma unroll 1
    for (int spin = 0; spin < SPIN_CAP; ++spin) {
      CFENCE();
      unsigned bad = 0;
#pragma unroll
      for (int i = 0; i < NL; ++i) yv[i] = __builtin_amdgcn_raw_buffer_load_b128(wsr, off, i * (64 / NL) * 2048, 16);
#pragma unroll
      for (int i = 0; i < NL; ++i) bad |= ((yv[i][0] ^ yt[0]) | (yv[i][1] ^ yt[1])) | ((yv[i][2] ^ yt[2]) | (yv[i][3] ^ yt[3]));
      bad &= YMASK;
      { const u64 bal = __builtin_amdgcn_ballot_w64(bad != 0u); if (spin < 5) STAMP(10 + spin); ((volatile LAS unsigned*)(L + LDS_BYTES))[wave * 16 + 15] = spin + 1 + (bal == 1234567 ? 1 : 0); }
      if (__builtin_amdgcn_ballot_w64(bad != 0u) == 0ull) break;
      if (PSLP) __builtin_amdgcn_s_sleep(PSLP);
    }
    float o[4];
#pragma unroll
    for (int k = 0; k < 4; ++k) {
      float t[NL];
#pragma unroll
      for (int i = 0; i < NL; ++i) t[i] = __uint_as_float(yv[i][k] & ~YMASK);
#pragma unroll
      for (int st = NL / 2; st >= 1; st /= 2)
#pragma unroll
        for (int i = 0; i < st; ++i) t[i] += t[i + st];
      o[k] = t[0];
    }
    float e0 = swadd32(o[0], o[2]), e1 = swadd32(o[1], o[3]);
    e0 = swadd16(e0, e0); e1 = swadd16(e1, e1);
    e0 += dpp<ROR8>(e0); e1 += dpp<ROR8>(e1); e0 += dpp<ROR4>(e0); e1 += dpp<ROR4>(e1); e0 += dpp<ROR2>(e0); e1 += dpp<ROR2>(e1);
    if ((lane & 30) == 0) OUT_STORE(op, pack_bf16(e0, e1));
    if (pj == 0 && lane < 8) __builtin_amdgcn_raw_buffer_store_b128(u32x4{tlo, thi, tlo, thi}, wsr, WS_EPOCH + b * 256 + lane * 16, 0, 16);
    STAMP(7);
    VM_DRAIN();
    STAMP(8);
    STAMP_FLUSH();
    return;
  }

  const int cw = wave - 1, hw = wave - 1 - NCW;
  int landed = 0;
  auto wait_unit = [&](int u) __attribute__((always_inline)) {
#pragma unroll 1
    for (int spin = 0; landed <= u && spin < SPIN_CAP; ++spin) {
      landed = __builtin_amdgcn_readfirstlane(ctrl[0]);
      if (landed > u) break;
      if (USL) __builtin_amdgcn_s_sleep(USL);
    }
    CFENCE();
  };
  auto take = [&](int u, int next, u32x4& a0, u32x4& a1, u32x4& a2, u32x4& a3) __attribute__((always_inline)) {
    wait_unit(u);
    const LAS u32x4* sp = (const LAS u32x4*)(L + (u & (NSLOT - 1)) * UNIT + lane * 16);
    a0 = sp[0]; a1 = sp[64]; a2 = sp[128]; a3 = sp[192];
    LDS_WAIT();
    ctrl[16 + cw] = next;
    asm volatile("" : "+v"(a0), "+v"(a1), "+v"(a2), "+v"(a3) :: "memory");
  };
  u32x4 q;
  auto build_q = [&]() __attribute__((always_inline)) {
    lds_await(ctrl, C_Q, NHW);
    const LAS f32x4* qp = (const LAS f32x4*)(L + L_QP + (2 * r4 * DH + 8 * c16) * 4);
    const f32x4 lo = qp[0] + qp[DH / 4], hi = qp[1] + qp[DH / 4 + 1];
    q[0] = pack_bf16(xrow_sum(lo[0]), xrow_sum(lo[1])); q[1] = pack_bf16(xrow_sum(lo[2]), xrow_sum(lo[3]));
    q[2] = pack_bf16(xrow_sum(hi[0]), xrow_sum(hi[1])); q[3] = pack_bf16(xrow_sum(hi[2]), xrow_sum(hi[3]));
  };
  constexpr float SCALE = 0.088388347648318440f;
  float mx = -3.4028234e38f;
  auto score = [&](int u, const u32x4& k0, const u32x4& k1, const u32x4& k2, const u32x4& k3) __attribute__((always_inline)) {
    float d0 = 0.f, d1 = 0.f, d2 = 0.f, d3 = 0.f;
#pragma unroll
    for (int k = 0; k < 4; ++k) { d0 = dot2(k0[k], q[k], d0); d1 = dot2(k1[k], q[k], d1); d2 = dot2(k2[k], q[k], d2); d3 = dot2(k3[k], q[k], d3); }
    const unsigned s01 = pack_bf16(row16_sum(d0), row16_sum(d1)), s23 = pack_bf16(row16_sum(d2), row16_sum(d3));
    const unsigned t01 = pack_bf16(bflo(s01) * SCALE, bfhi(s01) * SCALE), t23 = pack_bf16(bflo(s23) * SCALE, bfhi(s23) * SCALE);
    const f32x4 sc = {bflo(t01), bfhi(t01), bflo(t23), bfhi(t23)};
    mx = fmaxf(fmaxf(mx, fmaxf(sc[0], sc[1])), fmaxf(sc[2], sc[3]));
    if (c16 == 0) *(LAS f32x4*)(L + L_SC + u * 64 + r4 * 16) = sc;
  };

  if (hw < 0) {
    {
      u32x4 hk[4 * NHC];
#pragma unroll
      for (int i = 0; i < NHC; ++i) take(cw + NCW * i, i + 1 < NHC ? cw + NCW * (i + 1) : NHELD + cw, hk[4 * i], hk[4 * i + 1], hk[4 * i + 2], hk[4 * i + 3]);
      STAMP(10);
      build_q();
      STAMP(3);
#pragma unroll
      for (int i = 0; i < NHC; ++i) { score(cw + NCW * i, hk[4 * i], hk[4 * i + 1], hk[4 * i + 2], hk[4 * i + 3]); __builtin_amdgcn_sched_barrier(0); }
    }
#pragma unroll 1
    for (int u = NHELD + cw; u < NKU; u += NCW) {
      u32x4 k0, k1, k2, k3;
      take(u, u + NCW, k0, k1, k2, k3);
      score(u, k0, k1, k2, k3);
    }
    ctrlf[32 + cw] = xrow_max(mx);
    lds_arrive(L, C_MAX, lane);
    STAMP(11);
    lds_await(ctrl, C_MAX, NCW + NHW);
    const float mg = wg_max();
    STAMP(12);
    float acc[8] = {0.f, 0.f, 0.f, 0.f, 0.f, 0.f, 0.f, 0.f};
    LAS unsigned* rp = (LAS unsigned*)(L + L_REC + cw * 272 + (8 * c16 + 2 * r4) * 2);
    asm volatile("" : "+v"(rp));
    float cf = 0.f;
#pragma unroll 1
    for (int u = NKU + cw; u < NUNIT; u += NCW) {
      const f32x4 sc = *(const LAS f32x4*)(L + L_SC + (u - NKU) * 64 + r4 * 16);
      float p[4];
#pragma unroll
      for (int t = 0; t < 4; ++t) { p[t] = expd(sc[t] - mg); asm volatile("" : "+v"(p[t])); }
      if ((u & ~(NCW - 1)) == NUNIT - 32) { lds_await(ctrl, C_CF, 1); cf = ctrlf[49]; }
      u32x4 v[4];
      take(u, u + NCW, v[0], v[1], v[2], v[3]);
#pragma unroll
      for (int t = 0; t < 4; ++t)
#pragma unroll
        for (int k = 0; k < 4; ++k) { acc[2 * k] = fmaf(p[t], bflo(v[t][k]), acc[2 * k]); acc[2 * k + 1] = fmaf(p[t], bfhi(v[t][k]), acc[2 * k + 1]); }
    }
    STAMP(5);
    const float e0 = swadd16(swadd32(acc[0], acc[4]), swadd32(acc[2], acc[6])), e1 = swadd16(swadd32(acc[1], acc[5]), swadd32(acc[3], acc[7]));
    *rp = pack_bf16(e0 * cf, e1 * cf);
    BARRIER();
    return;
  }

  u32x4 wb[16];
  const unsigned tstart = (unsigned)__builtin_amdgcn_readfirstlane(ctrl[8]);
  {
    const __amdgpu_buffer_rsrc_t rq = mkrsrc(wq, D * H * DH * 2);
    const int vo = ((64 * hw + 16 * r4) * (H * DH) + h * DH + 8 * c16) * 2;
#pragma unroll
    for (int i = 0; i < 16; ++i) { if (i == 0) wait_until(tstart + TQ + hw * QA); else PAUSE(QS); wb[i] = ldw(rq, vo, i * (H * DH * 2)); }
    lds_await(ctrl, C_X, 1);
    const u32x4 xa = *(const LAS u32x4*)(L + L_X + (64 * hw + 16 * r4) * 2), xc = *(const LAS u32x4*)(L + L_X + (64 * hw + 16 * r4 + 8) * 2);
    float a[8] = {0.f, 0.f, 0.f, 0.f, 0.f, 0.f, 0.f, 0.f};
#pragma unroll
    for (int i = 0; i < 8; ++i) {
      const unsigned xp = i < 4 ? xa[i] : xc[i - 4];
#pragma unroll
      for (int k = 0; k < 4; ++k) {
        a[2 * k] = dot2(perm_lo(wb[2 * i][k], wb[2 * i + 1][k]), xp, a[2 * k]);
        a[2 * k + 1] = dot2(perm_hi(wb[2 * i][k], wb[2 * i + 1][k]), xp, a[2 * k + 1]);
      }
    }
    const float e0 = swadd16(swadd32(a[0], a[4]), swadd32(a[2], a[6])), e1 = swadd16(swadd32(a[1], a[5]), swadd32(a[3], a[7]));
    *(LAS f32x2*)(L + L_QP + (hw * DH + 8 * c16 + 2 * r4) * 4) = f32x2{e0, e1};
    lds_arrive(L, C_Q, lane);
    STAMP(2);
    const int hb = NHC * NCW + hw;
#pragma unroll
    for (int i = 0; i < NHH; ++i) take(hb + NHW * i, i + 1 < NHH ? hb + NHW * (i + 1) : NEVER, wb[4 * i], wb[4 * i + 1], wb[4 * i + 2], wb[4 * i + 3]);
    STAMP(10);
    build_q();
#pragma unroll
    for (int i = 0; i < NHH; ++i) { score(hb + NHW * i, wb[4 * i], wb[4 * i + 1], wb[4 * i + 2], wb[4 * i + 3]); __builtin_amdgcn_sched_barrier(0); }
    ctrlf[32 + cw] = xrow_max(mx);
    lds_arrive(L, C_MAX, lane);
  }
  f32x4 y[4] = {{0.f, 0.f, 0.f, 0.f}, {0.f, 0.f, 0.f, 0.f}, {0.f, 0.f, 0.f, 0.f}, {0.f, 0.f, 0.f, 0.f}};
  if ((pj & 7) != 7) {
    const int sl = 7 * (pj >> 3) + (pj & 7);
    const __amdgpu_buffer_rsrc_t r1 = mkrsrc(w1, D * F * 2), r3 = mkrsrc(w3, D * F * 2), r2 = mkrsrc(w2, F * D * 2);
    const int vo = ((64 * hw + (lane & 60)) * F + FS * sl + 8 * (lane & 3)) * 2;
#pragma unroll
    for (int i = 0; i < 8; ++i) { if (i == 0) wait_until(tstart + T1 + hw * A1); else PAUSE(S1); wb[i] = ldw(i < 4 ? r1 : r3, vo, (i & 3) * (F * 2)); }
    const u32x2 xm = *(const LAS u32x2*)(L + L_X + (64 * hw + (lane & 60)) * 2);
    float a[16];
#pragma unroll
    for (int m = 0; m < 2; ++m)
#pragma unroll
      for (int k = 0; k < 4; ++k) {
        a[8 * m + 2 * k] = dot2(perm_lo(wb[4 * m + 2][k], wb[4 * m + 3][k]), xm[1], dot2(perm_lo(wb[4 * m][k], wb[4 * m + 1][k]), xm[0], 0.f));
        a[8 * m + 2 * k + 1] = dot2(perm_hi(wb[4 * m + 2][k], wb[4 * m + 3][k]), xm[1], dot2(perm_hi(wb[4 * m][k], wb[4 * m + 1][k]), xm[0], 0.f));
      }
    f32x4 e;
#pragma unroll
    for (int k = 0; k < 4; ++k) {
      float t = swadd16(swadd32(a[k], a[8 + k]), swadd32(a[4 + k], a[12 + k]));
      t += dpp<ROR8>(t); e[k] = t + dpp<ROR4>(t);
    }
    if ((lane & 12) == 0) *(LAS f32x4*)(L + L_MP + (hw * 64 + (lane & 32) + 8 * (lane & 3) + 4 * (r4 & 1)) * 4) = e;
    lds_arrive(L, C_MLP, lane);
    u32x2 wr[8];
#pragma unroll
    for (int i = 0; i < 8; ++i) { if (i == 0) wait_until(tstart + T2 + hw * A2); else PAUSE(S2); wr[i] = ldw2(r2, ((FS * sl + 8 * r4) * D + 64 * hw + 4 * c16) * 2, i * (D * 2)); }
    lds_await(ctrl, C_MLP, NHW);
    const LAS float* mp = (const LAS float*)(L + L_MP) + (lane & 31);
    float ga = 0.f, up = 0.f;
#pragma unroll
    for (int w = 0; w < NHW; ++w) { ga += mp[w * 64]; up += mp[w * 64 + 32]; }
    const float hv = ga / (1.f + __expf(-ga)) * up * (1.f - ctrlf[48]);
    const unsigned hh = pack_bf16(hv, 0.f), hl = pack_bf16(hv - bflo(hh), 0.f);
    *(LAS u16*)(L + L_H + hw * 128 + lane * 2) = (u16)(lane < 32 ? hh : hl);
    LDS_WAIT();
    u32x4 ha = *(const LAS u32x4*)(L + L_H + hw * 128 + (c16 & 1) * 64 + r4 * 16);
    if (c16 >= 2) ha = u32x4{0u, 0u, 0u, 0u};
#pragma unroll
    for (int e = 0; e < 4; ++e) {
      u32x4 f;
#pragma unroll
      for (int k = 0; k < 4; ++k) f[k] = (e & 1) ? perm_hi(wr[2 * k][e >> 1], wr[2 * k + 1][e >> 1]) : perm_lo(wr[2 * k][e >> 1], wr[2 * k + 1][e >> 1]);
      y[e] = mfma(ha, f, y[e]);
    }
  }
#pragma unroll
  for (int e = 0; e < 4; ++e) asm volatile("" : "+v"(y[e]));
  {
    const __amdgpu_buffer_rsrc_t ro = mkrsrc(wo, H * DH * D * 2);
#pragma unroll
    for (int t = 0; t < 4; ++t) {
      u32x2 wr[8];
#pragma unroll
      for (int i = 0; i < 8; ++i) { if (t + i == 0) wait_until(tstart + TO + hw * AO); else PAUSE(SO); wr[i] = ldw2(ro, ((h * DH + 32 * r4) * D + 64 * hw + 4 * c16) * 2, (8 * t + i) * (D * 2)); }
#pragma unroll
      for (int e = 0; e < 4; ++e) {
#pragma unroll
        for (int k = 0; k < 4; ++k) wb[4 * t + e][k] = (e & 1) ? perm_hi(wr[2 * k][e >> 1], wr[2 * k + 1][e >> 1]) : perm_lo(wr[2 * k][e >> 1], wr[2 * k + 1][e >> 1]);
        asm volatile("" : "+v"(wb[4 * t + e]));
      }
    }
  }
  lds_await(ctrl, C_CF, 1);
  u32x4 yt = *(const LAS u32x4*)(L + L_CTRL + 52 * 4);
  int yo = WS_Y + (b * 64 + pj) * 2048 + hw * 256 + c16 * 16;
  const LAS u32x4* rec = (const LAS u32x4*)(L + L_REC + (lane & 3) * 272 + r4 * 64);
  unsigned ym = ~YMASK;
  asm volatile("" : "+v"(yt), "+v"(yo), "+v"(rec), "+s"(ym));
  STAMP(4);
  BARRIER();
  STAMP(9);
  {
    const u32x4 a[4] = {rec[0], rec[1], rec[2], rec[3]};
#pragma unroll
    for (int t = 0; t < 4; ++t)
#pragma unroll
      for (int e = 0; e < 4; ++e) y[e] = mfma(a[t], wb[4 * t + e], y[e]);
    u32x4 v;
#pragma unroll
    for (int e = 0; e < 4; ++e) {
      const f32x2 t = y[e].xy + y[e].zw;
      unsigned r;
      asm("v_add_f32 %0, %1, %2\n\tv_add_u32 %0, %5, %0\n\tv_and_or_b32 %0, %0, %3, %4" : "=&v"(r) : "v"(t[0]), "v"(t[1]), "s"(ym), "v"(yt[e]), "i"((YMASK + 1u) / 2u));
      v[e] = r;
    }
    if (lane < 16) __builtin_amdgcn_raw_buffer_store_b128(v, wsr, yo, 0, 16);
  }
  STAMP(7);
}

extern "C" void kernel_launch(void* const* d_in, const int* in_sizes, int n_in, void* d_out, int out_size, void* d_ws, size_t ws_size,
                              hipStream_t stream) {
  gated_mix_kernel<<<256, NTHR, 0, stream>>>((const u16*)d_in[0], (const u16*)d_in[1], (const u16*)d_in[2], (const u16*)d_in[3], (const u16*)d_in[4],
                                             (const u16*)d_in[6], (const u16*)d_in[7], (const u16*)d_in[8], (const u16*)d_in[9], (u16*)d_out,
                                             (unsigned char*)d_ws);
}
```

```cpp
#include <hip/hip_runtime.h>
#include <hip/hip_bf16.h>

#define GAS __attribute__((address_space(1)))
#define LAS __attribute__((address_space(3)))
typedef unsigned short u16;
typedef unsigned long long u64;
using u32x4 = __attribute__((ext_vector_type(4))) unsigned;
using u32x2 = __attribute__((ext_vector_type(2))) unsigned;
using f32x4 = __attribute__((ext_vector_type(4))) float;
using f32x2 = __attribute__((ext_vector_type(2))) float;
using bf16x2 = __attribute__((ext_vector_type(2))) __bf16;
using bf16x8 = __attribute__((ext_vector_type(8))) __bf16;

constexpr int B = 4, D = 512, H = 32, SK = 8192, DH = 128, F = 1792;
constexpr int NCW = 4, NHW = 8, XWAVE = 1 + NCW + NHW, NWAVE = XWAVE + 2, NTHR = NWAVE * 64;
#define QA 112
#define QB 5
#define T1 1400
#define A1 150
#define B1 18
#define T2 2500
#define A2 40
#define B2 4
#define TO 2900
#define AO 350
#define BO 8
#define TS 68.6
#define REF0 180
#define PLAG 1
#define NOPST 0
#define NOPLD 0
#define PAUX 16
#define RAUX 16
#define PFW 0
#define TQ 100
#define NINIT 6
#define CODE_KIB 21
#define FDEL 0
#define CODEPF 1
#define YST 16
#define YLD 16
#define RED16 0
#define MASKP 0
#define PSLP 0
#define OUTSC 0
#define OUT_STORE(p, v) *(GAS unsigned*)(p) = v
#define CLR0 1
#define MLPFULL 0
#define MLPPF 0
#define TP 1200
#define AP 12
#define GRID 0
#define B2BD 0
#define B2BAUX 16
#define WOB2B 0
#define TOUCHW 1
#define W0 30
#define BARPOS 12
#define NO_WO 0
#define TLBPF 0
#define TMODE 0
#define SPW 4
#define QS 2
#define S1 6
#define S2 1
#define SO 2
#define USL 0
#define JUMP 100
#define DECAY 6
#define HOLD0 10000
#define UEND 420
#define WGAIN2 18
#define WMID2 WMID
#define FOFF 162
#define FMIN 45
#define FMAX 150
#define NRED 64
#define KEARLY 0
#define KLAST 16
#define NOTHR 0
#define WMIN0 16
#define QROW 1
#define QAUX 2
#define QXOR 0
#define HX 0
#define QLO 7
#define HAND 31
#define HOR 0
#define HQ (QLO == 7 ? ((h & HAND) | HOR) : QLO == 1 ? xg + 8 * (h & 3) : 2 * xg + (h & 1) + 16 * ((h >> 1) & 1))
#define QPFM 0
#define HBLK 1
#define HM (HBLK ? NHH : 1)
#define HS (HBLK ? 1 : NHW)
#define RMASK 0
#define SBIT 16
#define XB0 6
#define XM0 0x40000
#define XB1 1
#define XM1 0x100000
#define UMID 128
#define PAT 0
#define SLOTSH 8
#define WMID 21.5
#define WGAIN 10
#define WMIN 16
#define WMAX 28
#define CLRAUX 16
#define PCONT 1
#define XDEL 20
#define XSPR 0
#define XNT 0
#define MIMIC 1
#define NCLR 2
#define NOFLAG 0
#define CSPR 0
constexpr int UNIT = 4096;
constexpr int NSLOT = 32, RING = NSLOT * UNIT;
constexpr int NKU = (SK / 2) / 16, NUNIT = 2 * NKU;
constexpr int NHC = 6, NHH = 4, NHELD = NHC * NCW + NHH * NHW;
constexpr int FS = 32;
constexpr int L_CTRL = RING;
constexpr int L_X = L_CTRL + 256;
constexpr int L_QP = L_X + D * 2;
constexpr int L_MP = L_QP + NHW * DH * 4;
constexpr int L_REC = L_MP + NHW * 64 * 4;
constexpr int L_H = L_REC + NCW * 272;
constexpr int L_SC = L_H + NHW * 128;
constexpr int LDS_BYTES = L_SC + NKU * 64;
static_assert(LDS_BYTES <= 159 * 1024, "lds");
static_assert(WMIN <= (int)WMID && (int)WMID <= WMAX && WMAX <= 28, "window");
constexpr int WS_EPOCH = 0;
constexpr int WS_REC = 4096;
constexpr int WS_PACE = WS_REC + 256 * 128;
constexpr int WS_Y = WS_PACE + 64 * 2048;
constexpr int WS_BYTES = WS_Y + B * 64 * 2048;
constexpr u64 TAG_STEP = 0x9E3779B97F4A7C15ull;
constexpr unsigned YMASK = 0xFFFu;
constexpr int SPIN_CAP = 1 << 22;
constexpr int NEVER = 1 << 20;
constexpr int C_Q = 4, C_MAX = 5, C_MLP = 6, C_X = 7, C_CF = 9;

__device__ __forceinline__ float expd(float d) { return __builtin_amdgcn_exp2f(d * 1.4426950408889634f); }
__device__ __forceinline__ float bflo(unsigned u) { return __builtin_bit_cast(float, u << 16); }
__device__ __forceinline__ float bfhi(unsigned u) { return __builtin_bit_cast(float, u & 0xffff0000u); }
__device__ __forceinline__ float dot2(unsigned a, unsigned b, float acc) {
  return __builtin_amdgcn_fdot2_f32_bf16(__builtin_bit_cast(bf16x2, a), __builtin_bit_cast(bf16x2, b), acc, false);
}
__device__ __forceinline__ unsigned pack_bf16(float lo, float hi) {
  const bf16x2 v = {(__bf16)lo, (__bf16)hi};
  return __builtin_bit_cast(unsigned, v);
}
__device__ __forceinline__ unsigned perm_lo(unsigned a, unsigned b) { return __builtin_amdgcn_perm(b, a, 0x05040100u); }
__device__ __forceinline__ unsigned perm_hi(unsigned a, unsigned b) { return __builtin_amdgcn_perm(b, a, 0x07060302u); }
template <int CTRL> __device__ __forceinline__ float dpp(float x) {
  return __builtin_bit_cast(float, __builtin_amdgcn_mov_dpp(__builtin_bit_cast(int, x), CTRL, 0xf, 0xf, true));
}
constexpr int XOR1 = 0xB1, ROR1 = 0x121, ROR2 = 0x122, ROR4 = 0x124, ROR8 = 0x128;
__device__ __forceinline__ float row16_sum(float x) { x += dpp<ROR8>(x); x += dpp<ROR4>(x); x += dpp<ROR2>(x); x += dpp<ROR1>(x); return x; }
__device__ __forceinline__ float swadd32(float a, float b) {
  auto t = __builtin_amdgcn_permlane32_swap(__float_as_uint(a), __float_as_uint(b), false, false);
  return __uint_as_float(t[0]) + __uint_as_float(t[1]);
}
__device__ __forceinline__ float swadd16(float a, float b) {
  auto t = __builtin_amdgcn_permlane16_swap(__float_as_uint(a), __float_as_uint(b), false, false);
  return __uint_as_float(t[0]) + __uint_as_float(t[1]);
}
__device__ __forceinline__ float xrow_sum(float x) { x = swadd16(x, x); return swadd32(x, x); }
__device__ __forceinline__ float xrow_max(float x) {
  auto s = __builtin_amdgcn_permlane16_swap(__float_as_uint(x), __float_as_uint(x), false, false);
  x = fmaxf(__uint_as_float(s[0]), __uint_as_float(s[1]));
  auto t = __builtin_amdgcn_permlane32_swap(__float_as_uint(x), __float_as_uint(x), false, false);
  return fmaxf(__uint_as_float(t[0]), __uint_as_float(t[1]));
}
__device__ __forceinline__ __amdgpu_buffer_rsrc_t mkrsrc(const void* p, int bytes) { return __builtin_amdgcn_make_buffer_rsrc((void*)p, 0, bytes, 0x00020000); }
__device__ __forceinline__ u32x4 ldw(__amdgpu_buffer_rsrc_t r, int voff, int soff) { return __builtin_amdgcn_raw_buffer_load_b128(r, voff, soff, 2); }
#define LDS_WAIT() asm volatile("s_waitcnt lgkmcnt(0)" ::: "memory")
#define VM_DRAIN() asm volatile("s_waitcnt vmcnt(0)" ::: "memory")
#define CFENCE() asm volatile("" ::: "memory")
#define BARRIER() asm volatile("s_waitcnt lgkmcnt(0)\n\ts_barrier" ::: "memory")
#define PAUSE(n) do { __builtin_amdgcn_sched_barrier(0); asm volatile("s_sleep %0" :: "i"(n) : "memory"); __builtin_amdgcn_sched_barrier(0); } while (0)

__device__ __forceinline__ void lds_arrive(LAS unsigned char* L, int idx, int lane) {
  LDS_WAIT();
  if (lane == 0) __hip_atomic_fetch_add((LAS int*)(L + L_CTRL) + idx, 1, __ATOMIC_RELAXED, __HIP_MEMORY_SCOPE_WORKGROUP);
}
__device__ __forceinline__ void lds_await(volatile LAS int* ctrl, int idx, int n) {
#pragma unroll 1
  for (int spin = 0; spin < SPIN_CAP; ++spin) { if (__builtin_amdgcn_readfirstlane(ctrl[idx]) >= n) break; __builtin_amdgcn_s_sleep(1); }
  CFENCE();
}

__device__ __forceinline__ void wait_until(unsigned t) {
  __builtin_amdgcn_sched_barrier(0);
#pragma unroll 1
  for (int spin = 0; spin < 4096; ++spin) {
    const int d = (int)(t - (unsigned)__builtin_amdgcn_s_memrealtime());
    if (d <= 0) break;
    if (d > 24) __builtin_amdgcn_s_sleep(6);
  }
  CFENCE();
  __builtin_amdgcn_sched_barrier(0);
}
#define STR2(x) #x
#define STR(x) STR2(x)
constexpr int TAU2 = 2 * (int)(TS * 100.0 * 256.0 / NUNIT);
__device__ __forceinline__ f32x4 mfma(const u32x4& a, const u32x4& b, const f32x4& c) {
  return __builtin_amdgcn_mfma_f32_16x16x32_bf16(__builtin_bit_cast(bf16x8, a), __builtin_bit_cast(bf16x8, b), c, 0, 0, 0);
}
__device__ __forceinline__ u32x2 ldw2(__amdgpu_buffer_rsrc_t r, int voff, int soff) { return __builtin_amdgcn_raw_buffer_load_b64(r, voff, soff, 2); }
template <int CTRL> __device__ __forceinline__ int dppi(int x) { return __builtin_amdgcn_mov_dpp(x, CTRL, 0xf, 0xf, true); }

#define STAMP(i) do {} while (0)
#define STAMP_FLUSH() do {} while (0)
#define STAMP_LDS 0

__global__ void __launch_bounds__(NTHR, 4) gated_mix_kernel(const u16* x, const u16* kc, const u16* vc, const u16* wq, const u16* wo, const u16* w1,
                                                         const u16* w3, const u16* w2, const u16* gw, u16* out, unsigned char* ws) {
  __shared__ __attribute__((aligned(16))) unsigned char lds_raw[LDS_BYTES + STAMP_LDS];
  LAS unsigned char* L = (LAS unsigned char*)lds_raw;
  volatile LAS int* ctrl = (volatile LAS int*)(L + L_CTRL);
  volatile LAS float* ctrlf = (volatile LAS float*)(L + L_CTRL);
#define DBG(x) ""
  {
    unsigned va, vb, vo, pa, nxa, t0, t1, t2, t3;
#define CODELD "global_load_lds_dwordx4 %[t0], s[56:57]\n\t"
#define ASTAMP(i) DBG("s_memrealtime s[60:61]\n\ts_waitcnt lgkmcnt(0)\n\tv_mov_b32 %[t1], %c[dbg] + 4 * " #i "\n\tv_mov_b32 %[t2], s60\n\tds_write_b32 %[t1], %[t2]\n\t")
#define WAITW "s_waitcnt (wcnt & 15) | ((wcnt >> 4) << 14) | 0x0F70\n\t"
#define SP(v, sb, wait) wait "global_load_lds_dword " v ", " sb " nt\n\tglobal_load_lds_dword " v ", " sb " sc1\n\t"
#define QPF SP("%[va]", "s[44:45]", "") SP("%[t2]", "s[44:45]", "")
#define U0 0x1000
#define U1 0x2000
#define SB 16
#define WALK "s_and_b32 s54, s44, 255\n\ts_and_b32 s55, s54, -16\n\ts_add_u32 s54, s54, s55\n\ts_lshl_b32 s54, s54, 12\n\t"
#define SW "s_waitcnt vmcnt(%c[spw])\n\t"
#define UNIT4(v) \
        WAITW "global_load_lds_dwordx4 " v ", s[40:41] nt\n\t" \
        WAITW "global_load_lds_dwordx4 " v ", s[40:41] offset:1024 nt\n\t" \
        WAITW "global_load_lds_dwordx4 " v ", s[40:41] offset:2048 nt\n\t" \
        WAITW "global_load_lds_dwordx4 " v ", s[40:41] offset:3072 nt\n\t"
    asm volatile(
        "s_getpc_b64 s[56:57]\n\t"
        "s_load_dwordx8 s[44:51], %[ka], 0x18\n\t"
        "s_load_dwordx2 s[52:53], %[ka], 0x38\n\t"
        "s_load_dwordx4 s[40:43], %[ka], 0x8\n\t"
        "v_readfirstlane_b32 s54, %[tid]\n\t"
        "s_mov_b32 m0, %c[jk]\n\t"
        "s_lshr_b32 s59, %[bid], 3\n\t"
        "s_cmp_ge_u32 s54, 64\n\t"
        "s_cbranch_scc1 90f\n\t"
        "s_min_u32 s54, s59, %c[nch]\n\t"
        "s_lshl_b32 s54, s54, 10\n\t"
        "v_lshlrev_b32 %[vo], 4, %[tid]\n\t"
        "s_and_b32 s56, s56, -128\n\t"
        "v_add_u32 %[t0], s54, %[vo]\n\t"
        "s_and_b32 s58, %[bid], 7\n\t"
        CODELD
        "s_setprio 3\n\t"
        "v_lshlrev_b32 %[t1], 7, %[tid]\n\t"
        "v_lshrrev_b32 %[va], 3, %[tid]\n\t"
        "v_lshlrev_b32 %[va], 13, %[va]\n\t"
        "v_and_b32 %[t2], " STR(QLO) ", %[tid]\n\t"
        "v_lshl_add_u32 %[va], %[t2], 7, %[va]\n\t"
        "v_and_b32 %[t2], 7 - " STR(QLO) ", %[tid]\n\t"
        "v_lshl_add_u32 %[va], %[t2], 10, %[va]\n\t"
        "s_lshl_b32 s54, s59, 17\n\t"
        "s_mul_i32 s55, s58, 128 * (" STR(QLO) " + 1)\n\t"
        "s_add_u32 s54, s54, s55\n\t"
        "v_add_u32 %[va], s54, %[va]\n\t"
        "v_add_u32 %[t2], 0x10000, %[va]\n\t"
        "s_lshl_b32 s55, s58, 5\n\t"
        "s_add_u32 s55, s55, s59\n\t"
        "s_mul_i32 s54, s55, 7 * 1024\n\t"
        "v_min_u32 %[t3], 55 * 128, %[t1]\n\t"
        "v_add_u32 %[t3], s54, %[t3]\n\t"
        "s_mul_i32 s54, s58, 448\n\t"
        "s_and_b32 s54, s54, -128\n\t"
        "s_mul_i32 s57, s59, 16 * 3584\n\t"
        "s_add_u32 s54, s54, s57\n\t"
        "v_lshrrev_b32 %[t0], 2, %[tid]\n\t"
        "v_mul_u32_u24 %[t0], 3584 - 512, %[t0]\n\t"
        "v_add3_u32 %[t0], %[t0], %[t1], s54\n\t"
        "v_lshl_add_u32 %[vb], s55, 14, %[t1]\n\t"
        "v_add_u32 %[t1], 0x2000, %[vb]\n\t"
        "s_and_b32 s54, %[bid], 0xc0\n\t"
        "s_lshl3_add_u32 s54, s58, s54\n\t"
        "s_and_b32 s56, s59, 7\n\t"
        "s_add_u32 s54, s54, s56\n\t"
        "s_lshr_b32 s56, s54, 1\n\t"
        "s_lshl_b32 s56, s56, 21\n\t"
        "s_and_b32 s66, s54, 1\n\t"
        "s_lshl_b32 s66, s66, " STR(SB) "\n\t"
        "s_bfe_u32 s57, s54, 0x10000 + 1 + " STR(XB0) "\n\t"
        "s_mul_i32 s57, s57, " STR(XM0) "\n\t"
        "s_xor_b32 s66, s66, s57\n\t"
        "s_bfe_u32 s57, s54, 0x10000 + 1 + " STR(XB1) "\n\t"
        "s_mul_i32 s57, s57, " STR(XM1) "\n\t"
        "s_xor_b32 s66, s66, s57\n\t"
        "s_waitcnt lgkmcnt(0)\n\t"
        QPF SP("%[t3]", "s[52:53]", "")
        ".if %c[spw] <= 4\n\ts_barrier\n\t.endif\n\t"
        SP("%[t0]", "s[48:49]", SW)
        ".if %c[spw] == 6\n\ts_barrier\n\t.endif\n\t"
        SP("%[t0]", "s[50:51]", SW)
        ".if %c[spw] == 8\n\ts_barrier\n\t.endif\n\t"
        SP("%[vb]", "s[46:47]", SW) SP("%[t1]", "s[46:47]", SW)
        ".if %c[spw] > 8\n\ts_barrier\n\t.endif\n\t"
        ASTAMP(6)
        "s_add_u32 s40, s40, s56\n\t"
        "s_addc_u32 s41, s41, 0\n\t"
        "s_add_u32 s42, s42, s56\n\t"
        "s_addc_u32 s43, s43, 0\n\t"
        "v_mov_b32 %[va], %[vo]\n\t"
        "v_or_b32 %[vo], s66, %[vo]\n\t"
        ".set so, 0\n\t"
        ".set wcnt, %c[spw] + " STR(KEARLY) "\n\t"
        ".rept 4\n\t"
        "s_movk_i32 m0, so\n\t"
        ".if (so & 4096) == 0\n\t"
        WAITW
        ".set wcnt, wcnt + 6\n\t"
        ".else\n\t"
        "s_nop 0\n\t"
        ".endif\n\t"
        "global_load_lds_dwordx4 %[vo], s[40:41] nt\n\t"
        "global_load_lds_dwordx4 %[vo], s[40:41] offset:1024 nt\n\t"
        "global_load_lds_dwordx4 %[vo], s[40:41] offset:2048 nt\n\t"
        "global_load_lds_dwordx4 %[vo], s[40:41] offset:3072 nt\n\t"
        ".if (so & 4096) == 0\n\t"
        "v_add_u32 %[vo], " STR(U0) ", %[vo]\n\t"
        ".elseif so == 4096\n\t"
        "v_add_u32 %[vo], " STR(U1) " - " STR(U0) ", %[vo]\n\t"
        ".else\n\t"
        "v_add_u32 %[vo], 0x4000 - " STR(U1) " - " STR(U0) ", %[vo]\n\t"
        ".endif\n\t"
        ".set so, so + 4096\n\t"
        ".endr\n\t"
        "s_waitcnt vmcnt(" STR(KLAST) ")\n\t"
        ASTAMP(7)
        "s_getpc_b64 s[50:51]\n"
        "20:\n\t"
        "s_add_u32 s50, s50, 23f-20b-%c[wmin]*128\n\t"
        "s_addc_u32 s51, s51, -1\n\t"
        "v_mov_b32 %[pa], %c[ctl]\n\t"
        "v_and_b32 %[nxa], 0x3c0 >> 2, %[vo]\n\t"
        "v_lshrrev_b32 %[nxa], 2, %[nxa]\n\t"
        "v_add_u32 %[nxa], %c[ctl] + 64, %[nxa]\n\t"
        "s_memrealtime s[58:59]\n\t"
        "s_mov_b32 s62, 0\n\t"
        "s_mov_b32 s65, " STR(WMIN0) "\n\t"
        "s_mov_b32 s63, %c[gain]\n\t"
        "s_mov_b32 s64, %c[wmid]\n\t"
        "s_mov_b32 s44, 4\n\t"
        "s_mov_b32 s45, %c[wmid] >> 8\n\t"
        "s_mov_b32 s46, 0x4000\n\t"
        "s_mov_b32 s47, 2 * %c[tau2]\n\t"
        "s_mov_b32 s48, %c[nslot]\n\t"
        "s_mov_b32 s49, %c[nku]\n\t"
        "s_waitcnt lgkmcnt(0)\n\t"
        "s_mov_b32 s59, 10000\n"
        "22:\n\t"
        "s_memrealtime s[56:57]\n\t"
        "ds_read_b32 %[t3], %[pa] offset:8\n\t"
        "s_lshl_b32 s52, s45, 7\n\t"
        "s_add_u32 s52, s50, s52\n\t"
        "s_addc_u32 s53, s51, 0\n\t"
        "s_add_i32 s54, s45, -1\n\t"
        "s_lshr_b32 s54, s54, 2\n\t"
        "s_sub_i32 s54, s44, s54\n\t"
        "v_mov_b32 %[t1], s54\n\t"
        "v_add_u32 %[t0], " STR(U0) ", %[vo]\n\t"
        "s_add_u32 s55, s46, 0x1000\n\t"
        "s_mov_b32 m0, s46\n\t"
        "s_setpc_b64 s[52:53]\n"
        "24:\n\t"
        "s_waitcnt lgkmcnt(0)\n\t"
        "v_readfirstlane_b32 s57, %[t3]\n\t"
        "v_add_u32 %[t1], 1, %[t1]\n\t"
        "s_lshr_b32 s54, s47, 8\n\t"
        "s_sub_u32 s56, s56, s54\n\t"
        "v_mov_b32 %[t2], s56\n\t"
        "s_sub_i32 s54, s56, s58\n\t"
        "s_mov_b32 s58, s56\n\t"
        "s_sub_i32 s56, s56, s57\n\t"
        "ds_write2_b32 %[pa], %[t1], %[t2] offset1:1\n\t"
        "s_add_i32 s59, s59, s54\n\t"
        "s_add_i32 s59, s59, %c[decay]\n\t"
        "s_cmp_gt_i32 s54, %c[jump]\n\t"
        "s_cselect_b32 s59, s62, s59\n\t"
        "s_min_i32 s62, s56, s59\n\t"
        "s_mul_i32 s56, s62, s63\n\t"
        "s_add_i32 s56, s56, s64\n\t"
        "s_ashr_i32 s56, s56, 8\n\t"
        "s_max_i32 s56, s56, s65\n\t"
        "s_min_i32 s45, s56, %c[wmax]\n\t"
        "s_add_u32 s47, s47, %c[tau2]\n\t"
        "s_add_u32 s46, s46, 0x2000\n\t"
        "s_and_b32 s46, s46, %c[nslot] * 4096 - 1\n\t"
        "s_add_i32 s44, s44, 2\n\t"
        WALK
        "s_xor_b32 s54, s54, s66\n\t"
        "v_or_b32 %[vo], s54, %[va]\n\t"
        "s_cmp_lt_i32 s44, s48\n\t"
        "s_cbranch_scc1 22b\n\t"
        DBG("s_add_i32 s54, s44, -1\n\ts_lshr_b32 s54, s54, 5\n\ts_lshl_b32 s54, s54, 2\n\ts_add_u32 s54, s54, %c[dbg] + 14 * 64\n\tv_mov_b32 %[t0], s54\n\tv_mov_b32 %[t1], s45\n\t"
            "ds_write2_b32 %[t0], %[t2], %[t1] offset1:16\n\t")
        "s_cmp_lt_i32 s44, s49\n\t"
        "s_cbranch_scc1 25f\n\t"
        "s_cmp_ge_i32 s44, 2 * %c[nku]\n\t"
        "s_cbranch_scc1 30f\n\t"
        "s_mov_b32 s49, 2 * %c[nku]\n\t"
        "s_mov_b64 s[40:41], s[42:43]\n"
        "25:\n\t"
        "s_mov_b32 s65, %c[wmin]\n\t"
        "s_cmp_ge_i32 s44, %c[uend]\n\t"
        "s_cselect_b32 s63, %c[gain2], s63\n\t"
        "s_cmp_ge_i32 s44, " STR(UMID) "\n\t"
        "s_cselect_b32 s64, %c[wmid2], s64\n\t"
        "s_mov_b32 s55, %c[cap]\n"
        "26:\n\t"
        "ds_read_b32 %[t0], %[nxa]\n\t"
        "s_waitcnt lgkmcnt(0)\n\t"
        "s_nop 1\n\t"
        "v_min_i32_dpp %[t0], %[t0], %[t0] row_ror:8 row_mask:0xf bank_mask:0xf\n\t"
        "s_nop 1\n\t"
        "v_min_i32_dpp %[t0], %[t0], %[t0] row_ror:4 row_mask:0xf bank_mask:0xf\n\t"
        "s_nop 1\n\t"
        "v_min_i32_dpp %[t0], %[t0], %[t0] row_ror:2 row_mask:0xf bank_mask:0xf\n\t"
        "s_nop 1\n\t"
        "v_min_i32_dpp %[t0], %[t0], %[t0] row_ror:1 row_mask:0xf bank_mask:0xf\n\t"
        "s_nop 1\n\t"
        "v_readfirstlane_b32 s48, %[t0]\n\t"
        "s_nop 1\n\t"
        "s_add_i32 s48, s48, %c[nslot]\n\t"
        "s_and_b32 s48, s48, -2\n\t"
        "s_min_i32 s48, s48, s49\n\t"
        "s_cmp_lt_i32 s44, s48\n\t"
        "s_cbranch_scc1 22b\n\t"
        "v_mov_b32 %[t1], s44\n\t"
        "s_waitcnt vmcnt(0)\n\t"
        "ds_write_b32 %[pa], %[t1]\n\t"
        "s_sleep 1\n\t"
        "s_sub_u32 s55, s55, 1\n\t"
        "s_cmp_lg_u32 s55, 0\n\t"
        "s_cbranch_scc1 26b\n\t"
        "s_branch 22b\n"
        "30:\n\t"
        ".set k, 6\n\t"
        ".rept 7\n\t"
        ".set wcnt, 4 * k\n\t"
        "v_mov_b32 %[t1], 2 * %c[nku] - k\n\t"
        WAITW
        "ds_write_b32 %[pa], %[t1]\n\t"
        ".set k, k - 1\n\t"
        ".endr\n\t"
        DBG("s_memrealtime s[56:57]\n\ts_waitcnt lgkmcnt(0)\n\tv_mov_b32 %[t0], %c[dbg] + 4\n\tv_mov_b32 %[t1], s56\n\tds_write_b32 %[t0], %[t1]\n\t")
        "s_waitcnt lgkmcnt(0)\n\t"
        "s_endpgm\n\t"
        ".p2align 7\n"
        "23:\n\t"
        ".set wcnt, %c[wmin]-1\n\t"
        ".rept %c[wmax] - %c[wmin] + 1\n\t"
        UNIT4("%[vo]")
        "ds_write_b32 %[pa], %[t1]\n\t"
        "s_mov_b32 m0, s55\n\t"
        UNIT4("%[t0]")
        "s_branch 24b\n\t"
        ".p2align 7\n\t"
        ".set wcnt, wcnt+1\n\t"
        ".endr\n"
        "90:\n\t"
        "s_waitcnt lgkmcnt(0)"
        : [va] "=&v"(va), [vb] "=&v"(vb), [vo] "=&v"(vo), [pa] "=&v"(pa), [nxa] "=&v"(nxa), [t0] "=&v"(t0), [t1] "=&v"(t1), [t2] "=&v"(t2), [t3] "=&v"(t3)
        : [tid] "v"(threadIdx.x), [bid] "s"(blockIdx.x), [ka] "s"(__builtin_amdgcn_kernarg_segment_ptr()), [nch] "i"(CODE_KIB - 1), [jk] "i"(RING - UNIT),
          [spw] "i"(SPW), [wmin] "i"(WMIN), [wmax] "i"(WMAX), [gain] "i"(WGAIN), [wmid] "i"((int)(WMID * 256)), [tau2] "i"(TAU2),
          [ctl] "i"(L_CTRL), [nslot] "i"(NSLOT), [nku] "i"(NKU), [cap] "i"(SPIN_CAP), [dbg] "i"(LDS_BYTES), [jump] "i"(JUMP), [decay] "i"(DECAY), [uend] "i"(UEND), [gain2] "i"(WGAIN2), [wmid2] "i"((int)(WMID2 * 256))
        : "memory", "scc", "s40", "s41", "s42", "s43", "s44", "s45", "s46", "s47", "s48", "s49", "s50", "s51", "s52", "s53", "s54", "s55", "s56", "s57", "s58", "s59", "s60", "s61", "s62", "s63", "s64", "s65", "s66");
  }
  __builtin_amdgcn_sched_barrier(0);
  const int tid = threadIdx.x, lane = tid & 63, wave = __builtin_amdgcn_readfirstlane(tid >> 6);
  const int xg = blockIdx.x & 7, kk = blockIdx.x >> 3;
  const int b = kk >> 3, pj = 8 * xg + (kk & 7), h = pj >> 1, s = pj & 1;
  const int r4 = lane >> 4, c16 = lane & 15;
  asm volatile("" : "+s"(x), "+s"(wq), "+s"(wo), "+s"(w1), "+s"(w3), "+s"(w2), "+s"(gw), "+s"(out), "+s"(ws));
  if (wave == 1) {
    const int wi = lane - 16;
    const int now = (int)(unsigned)__builtin_amdgcn_s_memrealtime() + REF0;
    ctrl[lane] = (lane == 1 || lane == 2) ? now : lane == 8 ? now - REF0 : (lane < 16 || lane >= 32) ? 0 : wi < NCW ? wi : wi < NCW + NHW ? NHC * NCW + HM * (wi - NCW) : NEVER;
  }
  BARRIER();

  const __amdgpu_buffer_rsrc_t wsr = mkrsrc(ws, WS_BYTES);
  auto wg_max = [&]() __attribute__((always_inline)) {
    float mg = ctrlf[32 + (c16 < NCW + NHW ? c16 : 0)];
    mg = fmaxf(mg, dpp<ROR8>(mg)); mg = fmaxf(mg, dpp<ROR4>(mg)); mg = fmaxf(mg, dpp<ROR2>(mg)); mg = fmaxf(mg, dpp<ROR1>(mg));
    return mg;
  };

  if (wave == XWAVE + 1) {
    unsigned k = ((unsigned)__builtin_amdgcn_s_memrealtime() >> SLOTSH) + 1;
#pragma unroll 1
    for (int it = 0; it < 4096 >> (SLOTSH - 2); ++it, ++k) {
      wait_until(k << SLOTSH);
      if (__builtin_amdgcn_readfirstlane(ctrl[0]) >= NUNIT - 36) break;
      const int own = __builtin_amdgcn_readfirstlane(ctrl[1]);
      constexpr unsigned SM = (8192u >> SLOTSH) - 1u;
      if (!NOPST && blockIdx.x < 16 && lane < 8) __builtin_amdgcn_raw_buffer_store_b128(u32x4{(unsigned)own, (unsigned)own, (unsigned)own, (unsigned)own}, wsr, WS_PACE + (k & SM) * 2048 + blockIdx.x * 128 + lane * 16, 0, PAUX);
      if (it < PLAG || NOPLD) continue;
      int e[16];
      asm volatile("s_load_dword %0, %16, 0x0\n\ts_load_dword %1, %16, 0x80\n\ts_load_dword %2, %16, 0x100\n\ts_load_dword %3, %16, 0x180\n\t"
                   "s_load_dword %4, %16, 0x200\n\ts_load_dword %5, %16, 0x280\n\ts_load_dword %6, %16, 0x300\n\ts_load_dword %7, %16, 0x380\n\t"
                   "s_load_dword %8, %16, 0x400\n\ts_load_dword %9, %16, 0x480\n\ts_load_dword %10, %16, 0x500\n\ts_load_dword %11, %16, 0x580\n\t"
                   "s_load_dword %12, %16, 0x600\n\ts_load_dword %13, %16, 0x680\n\ts_load_dword %14, %16, 0x700\n\ts_load_dword %15, %16, 0x780\n\t"
                   "s_waitcnt lgkmcnt(0)"
                   : "=&s"(e[0]), "=&s"(e[1]), "=&s"(e[2]), "=&s"(e[3]), "=&s"(e[4]), "=&s"(e[5]), "=&s"(e[6]), "=&s"(e[7]),
                     "=&s"(e[8]), "=&s"(e[9]), "=&s"(e[10]), "=&s"(e[11]), "=&s"(e[12]), "=&s"(e[13]), "=&s"(e[14]), "=&s"(e[15])
                   : "s"(ws + WS_PACE + ((k - PLAG) & SM) * 2048) : "memory");
      int sum = 0, cnt = 0;
#pragma unroll
      for (int i = 0; i < 16; ++i) { const int d = e[i] - own; const bool ok = (unsigned)(d + 600) < 1200u; sum += ok ? d : 0; cnt += ok ? 1 : 0; }
      if (cnt >= 8) ctrl[2] = own + (int)((float)sum / (float)cnt);
    }
    return;
  }

  if (wave == XWAVE) {
#pragma unroll 1
    for (int i = 0; i < XDEL + XSPR * (kk & 31); ++i) PAUSE(4);
    const u32x4 xv = *(const GAS u32x4*)(x + b * D + 8 * lane), gv = *(const GAS u32x4*)(gw + 8 * lane);
    *(LAS u32x4*)(L + L_X + lane * 16) = xv;
    float gd = 0.f;
#pragma unroll
    for (int k = 0; k < 4; ++k) gd = dot2(xv[k], gv[k], gd);
    gd = xrow_sum(row16_sum(gd));
    const float g = 1.f / (1.f + __expf(-gd));
    ctrlf[48] = g;
    lds_arrive(L, C_X, lane);
    u64 tag;
    asm volatile("s_load_dwordx2 %0, %1, 0x0\n\ts_waitcnt lgkmcnt(0)" : "=s"(tag) : "s"(ws + WS_EPOCH + b * 256) : "memory");
    tag += TAG_STEP;
#pragma unroll 1
    for (int spin = 0; spin < SPIN_CAP; ++spin) { if (__builtin_amdgcn_readfirstlane(ctrl[C_MAX]) >= NCW + NHW) break; __builtin_amdgcn_s_sleep(16); }
    CFENCE();
    const float mg = wg_max();
    float l = 0.f;
#pragma unroll 4
    for (int i = 0; i < NKU * 64 / 1024; ++i) {
      const f32x4 sc = *(const LAS f32x4*)(L + L_SC + i * 1024 + lane * 16);
      l += (expd(sc[0] - mg) + expd(sc[1] - mg)) + (expd(sc[2] - mg) + expd(sc[3] - mg));
    }
    l = xrow_sum(row16_sum(l));
    const unsigned tlo = (unsigned)tag, thi = (unsigned)(tag >> 32);
    if (lane < 8) __builtin_amdgcn_raw_buffer_store_b128(u32x4{__float_as_uint(mg), tlo, __float_as_uint(l), thi}, wsr, WS_REC + (b * 64 + pj) * 128 + lane * 16, 0, 16);
#pragma unroll 1
    for (int i = 0; i < 12 + (kk & 31); ++i) PAUSE(12);
    u32x4 rec;
#pragma unroll 1
    for (int spin = 0; spin < SPIN_CAP; ++spin) {
      CFENCE();
      rec = __builtin_amdgcn_raw_buffer_load_b128(wsr, WS_REC + (b * 64 + (pj ^ 1)) * 128, 0, 16);
      if (__builtin_amdgcn_ballot_w64(rec[1] != tlo || rec[3] != thi) == 0ull) break;
      __builtin_amdgcn_s_sleep(16);
    }
    const float mo = __uint_as_float(rec[0]), lo = __uint_as_float(rec[2]);
    const float mh = fmaxf(mg, mo), wt = expd(mg - mh);
    ctrlf[49] = g * wt / (wt * l + expd(mo - mh) * lo);
    const u32x4 yt = {tlo & YMASK, (tlo >> 12) & YMASK, (unsigned)(tag >> 24) & YMASK, (thi >> 4) & YMASK};
    *(LAS u32x4*)(L + L_CTRL + 52 * 4) = yt;
    lds_arrive(L, C_CF, lane);
    GAS unsigned* op = (GAS unsigned*)(out + b * D + 8 * pj + 4 * (lane & 1) + 2 * (lane >> 5));
    int off = WS_Y + (b * 64 + (lane >> 1)) * 2048 + pj * 32 + (lane & 1) * 16;
    asm volatile("" : "+v"(op), "+v"(off));
    if ((pj & (64 / NRED - 1)) != 0) return;
    STAMP(4);
    BARRIER();
    {
      const unsigned now = (unsigned)__builtin_amdgcn_s_memrealtime();
      const int d = __builtin_amdgcn_readfirstlane(ctrl[2]) + (int)(TS * 100) + FOFF - (int)now;
      const unsigned t = now + (unsigned)min(max(d, FMIN), FMAX);
#pragma unroll 1
      for (int spin = 0; spin < 4096 && (int)(t - (unsigned)__builtin_amdgcn_s_memrealtime()) > 0; ++spin) {}
    }
    STAMP(9);
    constexpr int NL = 128 / NRED;
    u32x4 yv[NL];
    int offs[NL];
#pragma unroll
    for (int i = 0; i < NL; ++i) { offs[i] = off; yv[i] = u32x4{0u, 0u, 0u, 0u}; }
#pragma unroll 1
    for (int spin = 0; spin < SPIN_CAP; ++spin) {
      CFENCE();
      unsigned bad = 0;
#pragma unroll
      for (int i = 0; i < NL; ++i) yv[i] = __builtin_amdgcn_raw_buffer_load_b128(wsr, off, i * (64 / NL) * 2048, 16);
#pragma unroll
      for (int i = 0; i < NL; ++i) bad |= ((yv[i][0] ^ yt[0]) | (yv[i][1] ^ yt[1])) | ((yv[i][2] ^ yt[2]) | (yv[i][3] ^ yt[3]));
      bad &= YMASK;
      if (__builtin_amdgcn_ballot_w64(bad != 0u) == 0ull) break;
      if (PSLP) __builtin_amdgcn_s_sleep(PSLP);
    }
    float o[4];
#pragma unroll
    for (int k = 0; k < 4; ++k) {
      float t[NL];
#pragma unroll
      for (int i = 0; i < NL; ++i) t[i] = __uint_as_float(yv[i][k] & ~YMASK);
#pragma unroll
      for (int st = NL / 2; st >= 1; st /= 2)
#pragma unroll
        for (int i = 0; i < st; ++i) t[i] += t[i + st];
      o[k] = t[0];
    }
    float e0 = swadd32(o[0], o[2]), e1 = swadd32(o[1], o[3]);
    e0 = swadd16(e0, e0); e1 = swadd16(e1, e1);
    e0 += dpp<ROR8>(e0); e1 += dpp<ROR8>(e1); e0 += dpp<ROR4>(e0); e1 += dpp<ROR4>(e1); e0 += dpp<ROR2>(e0); e1 += dpp<ROR2>(e1);
    if ((lane & 30) == 0) OUT_STORE(op, pack_bf16(e0, e1));
    if (pj == 0 && lane < 8) __builtin_amdgcn_raw_buffer_store_b128(u32x4{tlo, thi, tlo, thi}, wsr, WS_EPOCH + b * 256 + lane * 16, 0, 16);
    STAMP(8);
    STAMP_FLUSH();
    return;
  }

  const int cw = wave - 1, hw = wave - 1 - NCW;
  int landed = 0;
  auto wait_unit = [&](int u) __attribute__((always_inline)) {
#pragma unroll 1
    for (int spin = 0; landed <= u && spin < SPIN_CAP; ++spin) {
      landed = __builtin_amdgcn_readfirstlane(ctrl[0]);
      if (landed > u) break;
      if (USL) __builtin_amdgcn_s_sleep(USL);
    }
    CFENCE();
  };
  auto take = [&](int u, int next, u32x4& a0, u32x4& a1, u32x4& a2, u32x4& a3) __attribute__((always_inline)) {
    wait_unit(u);
    const LAS u32x4* sp = (const LAS u32x4*)(L + (u & (NSLOT - 1)) * UNIT + lane * 16);
    a0 = sp[0]; a1 = sp[64]; a2 = sp[128]; a3 = sp[192];
    LDS_WAIT();
    ctrl[16 + cw] = next;
    asm volatile("" : "+v"(a0), "+v"(a1), "+v"(a2), "+v"(a3) :: "memory");
  };
  u32x4 q;
  auto build_q = [&]() __attribute__((always_inline)) {
    lds_await(ctrl, C_Q, NHW);
    const LAS f32x4* qp = (const LAS f32x4*)(L + L_QP + (2 * r4 * DH + 8 * c16) * 4);
    const f32x4 lo = qp[0] + qp[DH / 4], hi = qp[1] + qp[DH / 4 + 1];
    q[0] = pack_bf16(xrow_sum(lo[0]), xrow_sum(lo[1])); q[1] = pack_bf16(xrow_sum(lo[2]), xrow_sum(lo[3]));
    q[2] = pack_bf16(xrow_sum(hi[0]), xrow_sum(hi[1])); q[3] = pack_bf16(xrow_sum(hi[2]), xrow_sum(hi[3]));
  };
  constexpr float SCALE = 0.088388347648318440f;
  float mx = -3.4028234e38f;
  auto score = [&](int u, const u32x4& k0, const u32x4& k1, const u32x4& k2, const u32x4& k3) __attribute__((always_inline)) {
    float d0 = 0.f, d1 = 0.f, d2 = 0.f, d3 = 0.f;
#pragma unroll
    for (int k = 0; k < 4; ++k) { d0 = dot2(k0[k], q[k], d0); d1 = dot2(k1[k], q[k], d1); d2 = dot2(k2[k], q[k], d2); d3 = dot2(k3[k], q[k], d3); }
    const unsigned s01 = pack_bf16(row16_sum(d0), row16_sum(d1)), s23 = pack_bf16(row16_sum(d2), row16_sum(d3));
    const unsigned t01 = pack_bf16(bflo(s01) * SCALE, bfhi(s01) * SCALE), t23 = pack_bf16(bflo(s23) * SCALE, bfhi(s23) * SCALE);
    const f32x4 sc = {bflo(t01), bfhi(t01), bflo(t23), bfhi(t23)};
    mx = fmaxf(fmaxf(mx, fmaxf(sc[0], sc[1])), fmaxf(sc[2], sc[3]));
    if (c16 == 0) *(LAS f32x4*)(L + L_SC + u * 64 + r4 * 16) = sc;
  };

  if (hw < 0) {
    {
      u32x4 hk[4 * NHC];
#pragma unroll
      for (int i = 0; i < NHC; ++i) take(cw + NCW * i, i + 1 < NHC ? cw + NCW * (i + 1) : NHELD + cw, hk[4 * i], hk[4 * i + 1], hk[4 * i + 2], hk[4 * i + 3]);
      STAMP(10);
      build_q();
      STAMP(3);
#pragma unroll
      for (int i = 0; i < NHC; ++i) { score(cw + NCW * i, hk[4 * i], hk[4 * i + 1], hk[4 * i + 2], hk[4 * i + 3]); __builtin_amdgcn_sched_barrier(0); }
    }
#pragma unroll 1
    for (int u = NHELD + cw; u < NKU; u += NCW) {
      u32x4 k0, k1, k2, k3;
      take(u, u + NCW, k0, k1, k2, k3);
      score(u, k0, k1, k2, k3);
    }
    ctrlf[32 + cw] = xrow_max(mx);
    lds_arrive(L, C_MAX, lane);
    STAMP(11);
    lds_await(ctrl, C_MAX, NCW + NHW);
    const float mg = wg_max();
    STAMP(12);
    float acc[8] = {0.f, 0.f, 0.f, 0.f, 0.f, 0.f, 0.f, 0.f};
    LAS unsigned* rp = (LAS unsigned*)(L + L_REC + cw * 272 + (8 * c16 + 2 * r4) * 2);
    asm volatile("" : "+v"(rp));
    float cf = 0.f;
#pragma unroll 1
    for (int u = NKU + cw; u < NUNIT; u += NCW) {
      const f32x4 sc = *(const LAS f32x4*)(L + L_SC + (u - NKU) * 64 + r4 * 16);
      float p[4];
#pragma unroll
      for (int t = 0; t < 4; ++t) { p[t] = expd(sc[t] - mg); asm volatile("" : "+v"(p[t])); }
      if ((u & ~(NCW - 1)) == NUNIT - 32) { lds_await(ctrl, C_CF, 1); cf = ctrlf[49]; }
      u32x4 v[4];
      take(u, u + NCW, v[0], v[1], v[2], v[3]);
#pragma unroll
      for (int t = 0; t < 4; ++t)
#pragma unroll
        for (int k = 0; k < 4; ++k) { acc[2 * k] = fmaf(p[t], bflo(v[t][k]), acc[2 * k]); acc[2 * k + 1] = fmaf(p[t], bfhi(v[t][k]), acc[2 * k + 1]); }
    }
    STAMP(5);
    const float e0 = swadd16(swadd32(acc[0], acc[4]), swadd32(acc[2], acc[6])), e1 = swadd16(swadd32(acc[1], acc[5]), swadd32(acc[3], acc[7]));
    *rp = pack_bf16(e0 * cf, e1 * cf);
    BARRIER();
    return;
  }

  u32x4 wb[16];
  const unsigned tstart = (unsigned)__builtin_amdgcn_readfirstlane(ctrl[8]);
  {
    const __amdgpu_buffer_rsrc_t rq = mkrsrc(wq, D * H * DH * 2);
    const int vo = ((64 * hw + 2 * r4) * (H * DH) + h * DH + 8 * c16) * 2;
#define QR(i) (8 * ((i) >> 1) + ((i) & 1))
#pragma unroll
    for (int i = 0; i < 16; ++i) { if (i == 0) wait_until(tstart + TQ + hw * QA); else PAUSE(QS); wb[i] = __builtin_amdgcn_raw_buffer_load_b128(rq, vo, QR(i) * (H * DH * 2), QAUX); }
    lds_await(ctrl, C_X, 1);
    const LAS unsigned* xl = (const LAS unsigned*)(L + L_X + (QROW == 1 ? 64 * hw + 2 * r4 : QROW == 2 ? 64 * hw + 4 * r4 : QROW == 3 ? 16 * hw + 128 * r4 : 64 * hw + 16 * r4) * 2);
    float a[8] = {0.f, 0.f, 0.f, 0.f, 0.f, 0.f, 0.f, 0.f};
#pragma unroll
    for (int i = 0; i < 8; ++i) {
      const unsigned xp = xl[QR(2 * i) / 2];
#pragma unroll
      for (int k = 0; k < 4; ++k) {
        a[2 * k] = dot2(perm_lo(wb[2 * i][k], wb[2 * i + 1][k]), xp, a[2 * k]);
        a[2 * k + 1] = dot2(perm_hi(wb[2 * i][k], wb[2 * i + 1][k]), xp, a[2 * k + 1]);
      }
    }
    const float e0 = swadd16(swadd32(a[0], a[4]), swadd32(a[2], a[6])), e1 = swadd16(swadd32(a[1], a[5]), swadd32(a[3], a[7]));
    *(LAS f32x2*)(L + L_QP + (hw * DH + 8 * c16 + 2 * r4) * 4) = f32x2{e0, e1};
    lds_arrive(L, C_Q, lane);
    STAMP(2);
    const int hb = NHC * NCW + HM * hw;
#pragma unroll
    for (int i = 0; i < NHH; ++i) take(hb + HS * i, i + 1 < NHH ? hb + HS * (i + 1) : NEVER, wb[4 * i], wb[4 * i + 1], wb[4 * i + 2], wb[4 * i + 3]);
    STAMP(10);
    build_q();
#pragma unroll
    for (int i = 0; i < NHH; ++i) { score(hb + HS * i, wb[4 * i], wb[4 * i + 1], wb[4 * i + 2], wb[4 * i + 3]); __builtin_amdgcn_sched_barrier(0); }
    ctrlf[32 + cw] = xrow_max(mx);
    lds_arrive(L, C_MAX, lane);
  }
  f32x4 y[4] = {{0.f, 0.f, 0.f, 0.f}, {0.f, 0.f, 0.f, 0.f}, {0.f, 0.f, 0.f, 0.f}, {0.f, 0.f, 0.f, 0.f}};
  if ((pj & 7) != 7) {
    const int sl = 7 * (pj >> 3) + (pj & 7);
    const __amdgpu_buffer_rsrc_t r1 = mkrsrc(w1, D * F * 2), r3 = mkrsrc(w3, D * F * 2), r2 = mkrsrc(w2, F * D * 2);
    const int vo = ((64 * hw + (lane & 60)) * F + FS * sl + 8 * (lane & 3)) * 2;
#pragma unroll
    for (int i = 0; i < 8; ++i) { if (i == 0) wait_until(tstart + T1 + hw * A1); else PAUSE(S1); wb[i] = ldw(i < 4 ? r1 : r3, vo, (i & 3) * (F * 2)); }
    const u32x2 xm = *(const LAS u32x2*)(L + L_X + (64 * hw + (lane & 60)) * 2);
    float a[16];
#pragma unroll
    for (int m = 0; m < 2; ++m)
#pragma unroll
      for (int k = 0; k < 4; ++k) {
        a[8 * m + 2 * k] = dot2(perm_lo(wb[4 * m + 2][k], wb[4 * m + 3][k]), xm[1], dot2(perm_lo(wb[4 * m][k], wb[4 * m + 1][k]), xm[0], 0.f));
        a[8 * m + 2 * k + 1] = dot2(perm_hi(wb[4 * m + 2][k], wb[4 * m + 3][k]), xm[1], dot2(perm_hi(wb[4 * m][k], wb[4 * m + 1][k]), xm[0], 0.f));
      }
    f32x4 e;
#pragma unroll
    for (int k = 0; k < 4; ++k) {
      float t = swadd16(swadd32(a[k], a[8 + k]), swadd32(a[4 + k], a[12 + k]));
      t += dpp<ROR8>(t); e[k] = t + dpp<ROR4>(t);
    }
    if ((lane & 12) == 0) *(LAS f32x4*)(L + L_MP + (hw * 64 + (lane & 32) + 8 * (lane & 3) + 4 * (r4 & 1)) * 4) = e;
    lds_arrive(L, C_MLP, lane);
    u32x2 wr[8];
#pragma unroll
    for (int i = 0; i < 8; ++i) { if (i == 0) wait_until(tstart + T2 + hw * A2); else PAUSE(S2); wr[i] = ldw2(r2, ((FS * sl + 8 * r4) * D + 64 * hw + 4 * c16) * 2, i * (D * 2)); }
    lds_await(ctrl, C_MLP, NHW);
    const LAS float* mp = (const LAS float*)(L + L_MP) + (lane & 31);
    float ga = 0.f, up = 0.f;
#pragma unroll
    for (int w = 0; w < NHW; ++w) { ga += mp[w * 64]; up += mp[w * 64 + 32]; }
    const float hv = ga / (1.f + __expf(-ga)) * up * (1.f - ctrlf[48]);
    const unsigned hh = pack_bf16(hv, 0.f), hl = pack_bf16(hv - bflo(hh), 0.f);
    *(LAS u16*)(L + L_H + hw * 128 + lane * 2) = (u16)(lane < 32 ? hh : hl);
    LDS_WAIT();
    u32x4 ha = *(const LAS u32x4*)(L + L_H + hw * 128 + (c16 & 1) * 64 + r4 * 16);
    if (c16 >= 2) ha = u32x4{0u, 0u, 0u, 0u};
#pragma unroll
    for (int e = 0; e < 4; ++e) {
      u32x4 f;
#pragma unroll
      for (int k = 0; k < 4; ++k) f[k] = (e & 1) ? perm_hi(wr[2 * k][e >> 1], wr[2 * k + 1][e >> 1]) : perm_lo(wr[2 * k][e >> 1], wr[2 * k + 1][e >> 1]);
      y[e] = mfma(ha, f, y[e]);
    }
  }
#pragma unroll
  for (int e = 0; e < 4; ++e) asm volatile("" : "+v"(y[e]));
  {
    const __amdgpu_buffer_rsrc_t ro = mkrsrc(wo, H * DH * D * 2);
#pragma unroll
    for (int t = 0; t < 4; ++t) {
      u32x2 wr[8];
#pragma unroll
      for (int i = 0; i < 8; ++i) { if (t + i == 0) wait_until(tstart + TO + hw * AO); else PAUSE(SO); wr[i] = ldw2(ro, ((h * DH + 32 * r4) * D + 64 * hw + 4 * c16) * 2, (8 * t + i) * (D * 2)); }
#pragma unroll
      for (int e = 0; e < 4; ++e) {
#pragma unroll
        for (int k = 0; k < 4; ++k) wb[4 * t + e][k] = (e & 1) ? perm_hi(wr[2 * k][e >> 1], wr[2 * k + 1][e >> 1]) : perm_lo(wr[2 * k][e >> 1], wr[2 * k + 1][e >> 1]);
        asm volatile("" : "+v"(wb[4 * t + e]));
      }
    }
  }
  lds_await(ctrl, C_CF, 1);
  u32x4 yt = *(const LAS u32x4*)(L + L_CTRL + 52 * 4);
  int yo = WS_Y + (b * 64 + pj) * 2048 + hw * 256 + c16 * 16;
  const LAS u32x4* rec = (const LAS u32x4*)(L + L_REC + (lane & 3) * 272 + r4 * 64);
  unsigned ym = ~YMASK;
  asm volatile("" : "+v"(yt), "+v"(yo), "+v"(rec), "+s"(ym));
  STAMP(4);
  BARRIER();
  STAMP(9);
  {
    const u32x4 a[4] = {rec[0], rec[1], rec[2], rec[3]};
#pragma unroll
    for (int t = 0; t < 4; ++t)
#pragma unroll
      for (int e = 0; e < 4; ++e) y[e] = mfma(a[t], wb[4 * t + e], y[e]);
    u32x4 v;
#pragma unroll
    for (int e = 0; e < 4; ++e) {
      const f32x2 t = y[e].xy + y[e].zw;
      unsigned r;
      asm("v_add_f32 %0, %1, %2\n\tv_add_u32 %0, %5, %0\n\tv_and_or_b32 %0, %0, %3, %4" : "=&v"(r) : "v"(t[0]), "v"(t[1]), "s"(ym), "v"(yt[e]), "i"((YMASK + 1u) / 2u));
      v[e] = r;
    }
    if (lane < 16) __builtin_amdgcn_raw_buffer_store_b128(v, wsr, yo, 0, 16);
  }
  STAMP(7);
}

extern "C" void kernel_launch(void* const* d_in, const int* in_sizes, int n_in, void* d_out, int out_size, void* d_ws, size_t ws_size,
                              hipStream_t stream) {
  gated_mix_kernel<<<256, NTHR, 0, stream>>>((const u16*)d_in[0], (const u16*)d_in[1], (const u16*)d_in[2], (const u16*)d_in[3], (const u16*)d_in[4],
                                             (const u16*)d_in[6], (const u16*)d_in[7], (const u16*)d_in[8], (const u16*)d_in[9], (u16*)d_out,
                                             (unsigned char*)d_ws);
}
```

```cpp
#include <hip/hip_runtime.h>
#include <hip/hip_bf16.h>

#define GAS __attribute__((address_space(1)))
#define LAS __attribute__((address_space(3)))
typedef unsigned short u16;
typedef unsigned long long u64;
using u32x4 = __attribute__((ext_vector_type(4))) unsigned;
using u32x2 = __attribute__((ext_vector_type(2))) unsigned;
using f32x4 = __attribute__((ext_vector_type(4))) float;
using f32x2 = __attribute__((ext_vector_type(2))) float;
using bf16x2 = __attribute__((ext_vector_type(2))) __bf16;
using bf16x8 = __attribute__((ext_vector_type(8))) __bf16;

constexpr int B = 4, D = 512, H = 32, SK = 8192, DH = 128, F = 1792;
constexpr int NCW = 4, NHW = 8, XWAVE = 1 + NCW + NHW, NWAVE = XWAVE + 2, NTHR = NWAVE * 64;
constexpr int TQ = 100, QA = 112, QS = 2;
constexpr int T1 = 1400, A1 = 150, S1 = 6;
constexpr int T2 = 2500, A2 = 40, S2 = 1;
constexpr int TO = 2900, AO = 350, SO = 2;
constexpr int TSTREAM = 6860;
constexpr int WMID = 5536, WMIN = 16, WMAX = 28;
constexpr int WGAIN = 10, WGAIN2 = 18, UEND = 420;
constexpr int JUMP = 100, DECAY = 6;
constexpr int REF0 = 180;
constexpr int FOFF = 156, FMIN = 45, FMAX = 150;
constexpr int CODE_KIB = 21;

constexpr int UNIT = 4096;
constexpr int NSLOT = 32, RING = NSLOT * UNIT;
constexpr int NKU = (SK / 2) / 16, NUNIT = 2 * NKU;
constexpr int NHC = 6, NHH = 4, NHELD = NHC * NCW + NHH * NHW;
constexpr int FS = 32;
constexpr int L_CTRL = RING;
constexpr int L_X = L_CTRL + 256;
constexpr int L_QP = L_X + D * 2;
constexpr int L_MP = L_QP + NHW * DH * 4;
constexpr int L_REC = L_MP + NHW * 64 * 4;
constexpr int L_H = L_REC + NCW * 272;
constexpr int L_SC = L_H + NHW * 128;
constexpr int LDS_BYTES = L_SC + NKU * 64;
static_assert(LDS_BYTES <= 159 * 1024, "lds");
static_assert(WMIN <= WMID / 256 && WMID / 256 <= WMAX && WMAX <= 28, "window");
constexpr int WS_EPOCH = 0;
constexpr int WS_REC = 4096;
constexpr int WS_PACE = WS_REC + 256 * 128;
constexpr int WS_Y = WS_PACE + 32 * 2048;
constexpr int WS_BYTES = WS_Y + B * 64 * 2048;
constexpr u64 TAG_STEP = 0x9E3779B97F4A7C15ull;
constexpr unsigned YMASK = 0xFFFu;
constexpr int NT = 2, SC1 = 16;
constexpr int SPIN_CAP = 1 << 22;
constexpr int NEVER = 1 << 20;
constexpr int C_Q = 4, C_MAX = 5, C_MLP = 6, C_X = 7, C_CF = 9;

__device__ __forceinline__ float expd(float d) { return __builtin_amdgcn_exp2f(d * 1.4426950408889634f); }
__device__ __forceinline__ float bflo(unsigned u) { return __builtin_bit_cast(float, u << 16); }
__device__ __forceinline__ float bfhi(unsigned u) { return __builtin_bit_cast(float, u & 0xffff0000u); }
__device__ __forceinline__ float dot2(unsigned a, unsigned b, float acc) {
  return __builtin_amdgcn_fdot2_f32_bf16(__builtin_bit_cast(bf16x2, a), __builtin_bit_cast(bf16x2, b), acc, false);
}
__device__ __forceinline__ unsigned pack_bf16(float lo, float hi) {
  const bf16x2 v = {(__bf16)lo, (__bf16)hi};
  return __builtin_bit_cast(unsigned, v);
}
__device__ __forceinline__ unsigned perm_lo(unsigned a, unsigned b) { return __builtin_amdgcn_perm(b, a, 0x05040100u); }
__device__ __forceinline__ unsigned perm_hi(unsigned a, unsigned b) { return __builtin_amdgcn_perm(b, a, 0x07060302u); }
template <int CTRL> __device__ __forceinline__ float dpp(float x) {
  return __builtin_bit_cast(float, __builtin_amdgcn_mov_dpp(__builtin_bit_cast(int, x), CTRL, 0xf, 0xf, true));
}
constexpr int ROR1 = 0x121, ROR2 = 0x122, ROR4 = 0x124, ROR8 = 0x128;
__device__ __forceinline__ float row16_sum(float x) { x += dpp<ROR8>(x); x += dpp<ROR4>(x); x += dpp<ROR2>(x); x += dpp<ROR1>(x); return x; }
__device__ __forceinline__ float swadd32(float a, float b) {
  auto t = __builtin_amdgcn_permlane32_swap(__float_as_uint(a), __float_as_uint(b), false, false);
  return __uint_as_float(t[0]) + __uint_as_float(t[1]);
}
__device__ __forceinline__ float swadd16(float a, float b) {
  auto t = __builtin_amdgcn_permlane16_swap(__float_as_uint(a), __float_as_uint(b), false, false);
  return __uint_as_float(t[0]) + __uint_as_float(t[1]);
}
__device__ __forceinline__ float xrow_sum(float x) { x = swadd16(x, x); return swadd32(x, x); }
__device__ __forceinline__ float xrow_max(float x) {
  auto s = __builtin_amdgcn_permlane16_swap(__float_as_uint(x), __float_as_uint(x), false, false);
  x = fmaxf(__uint_as_float(s[0]), __uint_as_float(s[1]));
  auto t = __builtin_amdgcn_permlane32_swap(__float_as_uint(x), __float_as_uint(x), false, false);
  return fmaxf(__uint_as_float(t[0]), __uint_as_float(t[1]));
}
__device__ __forceinline__ __amdgpu_buffer_rsrc_t mkrsrc(const void* p, int bytes) { return __builtin_amdgcn_make_buffer_rsrc((void*)p, 0, bytes, 0x00020000); }
__device__ __forceinline__ u32x4 ldw(__amdgpu_buffer_rsrc_t r, int voff, int soff) { return __builtin_amdgcn_raw_buffer_load_b128(r, voff, soff, NT); }
#define LDS_WAIT() asm volatile("s_waitcnt lgkmcnt(0)" ::: "memory")
#define CFENCE() asm volatile("" ::: "memory")
#define BARRIER() asm volatile("s_waitcnt lgkmcnt(0)\n\ts_barrier" ::: "memory")
#define PAUSE(n) do { __builtin_amdgcn_sched_barrier(0); asm volatile("s_sleep %0" :: "i"(n) : "memory"); __builtin_amdgcn_sched_barrier(0); } while (0)

__device__ __forceinline__ void lds_arrive(LAS unsigned char* L, int idx, int lane) {
  LDS_WAIT();
  if (lane == 0) __hip_atomic_fetch_add((LAS int*)(L + L_CTRL) + idx, 1, __ATOMIC_RELAXED, __HIP_MEMORY_SCOPE_WORKGROUP);
}
__device__ __forceinline__ void lds_await(volatile LAS int* ctrl, int idx, int n) {
#pragma unroll 1
  for (int spin = 0; spin < SPIN_CAP; ++spin) { if (__builtin_amdgcn_readfirstlane(ctrl[idx]) >= n) break; __builtin_amdgcn_s_sleep(1); }
  CFENCE();
}

__device__ __forceinline__ void wait_until(unsigned t) {
  __builtin_amdgcn_sched_barrier(0);
#pragma unroll 1
  for (int spin = 0; spin < 4096; ++spin) {
    const int d = (int)(t - (unsigned)__builtin_amdgcn_s_memrealtime());
    if (d <= 0) break;
    if (d > 24) __builtin_amdgcn_s_sleep(6);
  }
  CFENCE();
  __builtin_amdgcn_sched_barrier(0);
}
constexpr int TAU2 = 2 * (TSTREAM * 256 / NUNIT);
__device__ __forceinline__ f32x4 mfma(const u32x4& a, const u32x4& b, const f32x4& c) {
  return __builtin_amdgcn_mfma_f32_16x16x32_bf16(__builtin_bit_cast(bf16x8, a), __builtin_bit_cast(bf16x8, b), c, 0, 0, 0);
}
__device__ __forceinline__ u32x2 ldw2(__amdgpu_buffer_rsrc_t r, int voff, int soff) { return __builtin_amdgcn_raw_buffer_load_b64(r, voff, soff, NT); }

__global__ void __launch_bounds__(NTHR, 4) gated_mix__mha_block_cached__glu_gated_swiglu__decode_kernel(const u16* x, const u16* kc, const u16* vc, const u16* wq, const u16* wo, const u16* w1,
                                                         const u16* w3, const u16* w2, const u16* gw, u16* out, unsigned char* ws) {
  __shared__ __attribute__((aligned(16))) unsigned char lds_raw[LDS_BYTES];
  LAS unsigned char* L = (LAS unsigned char*)lds_raw;
  volatile LAS int* ctrl = (volatile LAS int*)(L + L_CTRL);
  volatile LAS float* ctrlf = (volatile LAS float*)(L + L_CTRL);
  {
    unsigned va, vb, vo, pa, nxa, t0, t1, t2, t3;
#define WAITW "s_waitcnt (wcnt & 15) | ((wcnt >> 4) << 14) | 0x0F70\n\t"
#define SP(v, sb, wait) wait "global_load_lds_dword " v ", " sb " nt\n\tglobal_load_lds_dword " v ", " sb " sc1\n\t"
#define SW "s_waitcnt vmcnt(4)\n\t"
#define UNIT4(v) \
        WAITW "global_load_lds_dwordx4 " v ", s[40:41] nt\n\t" \
        WAITW "global_load_lds_dwordx4 " v ", s[40:41] offset:1024 nt\n\t" \
        WAITW "global_load_lds_dwordx4 " v ", s[40:41] offset:2048 nt\n\t" \
        WAITW "global_load_lds_dwordx4 " v ", s[40:41] offset:3072 nt\n\t"
    asm volatile(
        "s_getpc_b64 s[56:57]\n\t"
        "s_load_dwordx8 s[44:51], %[ka], 0x18\n\t"
        "s_load_dwordx2 s[52:53], %[ka], 0x38\n\t"
        "s_load_dwordx4 s[40:43], %[ka], 0x8\n\t"
        "v_readfirstlane_b32 s54, %[tid]\n\t"
        "s_mov_b32 m0, %c[jk]\n\t"
        "s_lshr_b32 s59, %[bid], 3\n\t"
        "s_cmp_ge_u32 s54, 64\n\t"
        "s_cbranch_scc1 90f\n\t"
        "s_min_u32 s54, s59, %c[nch]\n\t"
        "s_lshl_b32 s54, s54, 10\n\t"
        "v_lshlrev_b32 %[vo], 4, %[tid]\n\t"
        "s_and_b32 s56, s56, -128\n\t"
        "v_add_u32 %[t0], s54, %[vo]\n\t"
        "s_and_b32 s58, %[bid], 7\n\t"
        "global_load_lds_dwordx4 %[t0], s[56:57]\n\t"
        "s_setprio 3\n\t"
        "v_lshlrev_b32 %[t1], 7, %[tid]\n\t"
        "s_lshl_b32 s54, s59, 7\n\t"
        "s_add_u32 s54, s54, s58\n\t"
        "s_lshl_b32 s54, s54, 10\n\t"
        "v_lshrrev_b32 %[va], 3, %[tid]\n\t"
        "v_mul_u32_u24 %[va], 8192 - 1024, %[va]\n\t"
        "v_add3_u32 %[va], %[va], %[t1], s54\n\t"
        "v_add_u32 %[t2], 0x10000, %[va]\n\t"
        "s_lshl_b32 s55, s58, 5\n\t"
        "s_add_u32 s55, s55, s59\n\t"
        "s_mul_i32 s54, s55, 7 * 1024\n\t"
        "v_min_u32 %[t3], 55 * 128, %[t1]\n\t"
        "v_add_u32 %[t3], s54, %[t3]\n\t"
        "s_mul_i32 s54, s58, 448\n\t"
        "s_and_b32 s54, s54, -128\n\t"
        "s_mul_i32 s57, s59, 16 * 3584\n\t"
        "s_add_u32 s54, s54, s57\n\t"
        "v_lshrrev_b32 %[t0], 2, %[tid]\n\t"
        "v_mul_u32_u24 %[t0], 3584 - 512, %[t0]\n\t"
        "v_add3_u32 %[t0], %[t0], %[t1], s54\n\t"
        "v_lshl_add_u32 %[vb], s55, 14, %[t1]\n\t"
        "v_add_u32 %[t1], 0x2000, %[vb]\n\t"
        "s_and_b32 s54, %[bid], 0xc0\n\t"
        "s_lshl3_add_u32 s54, s58, s54\n\t"
        "s_and_b32 s56, s59, 7\n\t"
        "s_add_u32 s54, s54, s56\n\t"
        "s_lshr_b32 s56, s54, 1\n\t"
        "s_lshl_b32 s56, s56, 21\n\t"
        "s_and_b32 s64, s54, 1\n\t"
        "s_bfe_u32 s57, s54, 0x10007\n\t"
        "s_lshl2_add_u32 s64, s57, s64\n\t"
        "s_bfe_u32 s57, s54, 0x10002\n\t"
        "s_lshl4_add_u32 s64, s57, s64\n\t"
        "s_lshl_b32 s64, s64, 16\n\t"
        "s_waitcnt lgkmcnt(0)\n\t"
        SP("%[va]", "s[44:45]", "") SP("%[t2]", "s[44:45]", "") SP("%[t3]", "s[52:53]", "")
        "s_barrier\n\t"
        SP("%[t0]", "s[48:49]", SW) SP("%[t0]", "s[50:51]", SW) SP("%[vb]", "s[46:47]", SW) SP("%[t1]", "s[46:47]", SW)
        "s_add_u32 s40, s40, s56\n\t"
        "s_addc_u32 s41, s41, 0\n\t"
        "s_add_u32 s42, s42, s56\n\t"
        "s_addc_u32 s43, s43, 0\n\t"
        "v_mov_b32 %[va], %[vo]\n\t"
        "v_or_b32 %[vo], s64, %[vo]\n\t"
        ".set so, 0\n\t"
        ".set wcnt, 4\n\t"
        ".rept 4\n\t"
        "s_movk_i32 m0, so\n\t"
        ".if (so & 4096) == 0\n\t"
        WAITW
        ".set wcnt, wcnt + 6\n\t"
        ".else\n\t"
        "s_nop 0\n\t"
        ".endif\n\t"
        "global_load_lds_dwordx4 %[vo], s[40:41] nt\n\t"
        "global_load_lds_dwordx4 %[vo], s[40:41] offset:1024 nt\n\t"
        "global_load_lds_dwordx4 %[vo], s[40:41] offset:2048 nt\n\t"
        "global_load_lds_dwordx4 %[vo], s[40:41] offset:3072 nt\n\t"
        "v_add_u32 %[vo], 0x1000, %[vo]\n\t"
        ".set so, so + 4096\n\t"
        ".endr\n\t"
        "s_waitcnt vmcnt(16)\n\t"
        "s_getpc_b64 s[50:51]\n"
        "20:\n\t"
        "s_add_u32 s50, s50, 23f-20b-%c[wmin]*128\n\t"
        "s_addc_u32 s51, s51, -1\n\t"
        "v_mov_b32 %[pa], %c[ctl]\n\t"
        "v_and_b32 %[nxa], 0x3c0 >> 2, %[vo]\n\t"
        "v_lshrrev_b32 %[nxa], 2, %[nxa]\n\t"
        "v_add_u32 %[nxa], %c[ctl] + 64, %[nxa]\n\t"
        "s_memrealtime s[58:59]\n\t"
        "s_mov_b32 s62, 0\n\t"
        "s_mov_b32 s63, %c[gain]\n\t"
        "s_mov_b32 s44, 4\n\t"
        "s_mov_b32 s45, %c[wmid] >> 8\n\t"
        "s_mov_b32 s46, 0x4000\n\t"
        "s_mov_b32 s47, 2 * %c[tau2]\n\t"
        "s_mov_b32 s48, %c[nslot]\n\t"
        "s_mov_b32 s49, %c[nku]\n\t"
        "s_waitcnt lgkmcnt(0)\n\t"
        "s_mov_b32 s59, 10000\n"
        "22:\n\t"
        "s_memrealtime s[56:57]\n\t"
        "ds_read_b32 %[t3], %[pa] offset:8\n\t"
        "s_lshl_b32 s52, s45, 7\n\t"
        "s_add_u32 s52, s50, s52\n\t"
        "s_addc_u32 s53, s51, 0\n\t"
        "s_add_i32 s54, s45, -1\n\t"
        "s_lshr_b32 s54, s54, 2\n\t"
        "s_sub_i32 s54, s44, s54\n\t"
        "v_mov_b32 %[t1], s54\n\t"
        "v_add_u32 %[t0], 0x1000, %[vo]\n\t"
        "s_add_u32 s55, s46, 0x1000\n\t"
        "s_mov_b32 m0, s46\n\t"
        "s_setpc_b64 s[52:53]\n"
        "24:\n\t"
        "s_waitcnt lgkmcnt(0)\n\t"
        "v_readfirstlane_b32 s57, %[t3]\n\t"
        "v_add_u32 %[t1], 1, %[t1]\n\t"
        "s_lshr_b32 s54, s47, 8\n\t"
        "s_sub_u32 s56, s56, s54\n\t"
        "v_mov_b32 %[t2], s56\n\t"
        "s_sub_i32 s54, s56, s58\n\t"
        "s_mov_b32 s58, s56\n\t"
        "s_sub_i32 s56, s56, s57\n\t"
        "ds_write2_b32 %[pa], %[t1], %[t2] offset1:1\n\t"
        "s_add_i32 s59, s59, s54\n\t"
        "s_add_i32 s59, s59, %c[decay]\n\t"
        "s_cmp_gt_i32 s54, %c[jump]\n\t"
        "s_cselect_b32 s59, s62, s59\n\t"
        "s_min_i32 s62, s56, s59\n\t"
        "s_mul_i32 s56, s62, s63\n\t"
        "s_add_i32 s56, s56, %c[wmid]\n\t"
        "s_ashr_i32 s56, s56, 8\n\t"
        "s_max_i32 s56, s56, %c[wmin]\n\t"
        "s_min_i32 s45, s56, %c[wmax]\n\t"
        "s_add_u32 s47, s47, %c[tau2]\n\t"
        "s_add_u32 s46, s46, 0x2000\n\t"
        "s_and_b32 s46, s46, %c[nslot] * 4096 - 1\n\t"
        "s_add_i32 s44, s44, 2\n\t"
        "s_and_b32 s54, s44, 0xff\n\t"
        "s_and_b32 s55, s44, 0xf0\n\t"
        "s_add_u32 s54, s54, s55\n\t"
        "s_lshl_b32 s54, s54, 12\n\t"
        "s_xor_b32 s54, s54, s64\n\t"
        "v_or_b32 %[vo], s54, %[va]\n\t"
        "s_cmp_lt_i32 s44, s48\n\t"
        "s_cbranch_scc1 22b\n\t"
        "s_cmp_lt_i32 s44, s49\n\t"
        "s_cbranch_scc1 25f\n\t"
        "s_cmp_ge_i32 s44, 2 * %c[nku]\n\t"
        "s_cbranch_scc1 30f\n\t"
        "s_mov_b32 s49, 2 * %c[nku]\n\t"
        "s_mov_b64 s[40:41], s[42:43]\n"
        "25:\n\t"
        "s_cmp_ge_i32 s44, %c[uend]\n\t"
        "s_cselect_b32 s63, %c[gain2], s63\n\t"
        "s_mov_b32 s55, %c[cap]\n"
        "26:\n\t"
        "ds_read_b32 %[t0], %[nxa]\n\t"
        "s_waitcnt lgkmcnt(0)\n\t"
        "s_nop 1\n\t"
        "v_min_i32_dpp %[t0], %[t0], %[t0] row_ror:8 row_mask:0xf bank_mask:0xf\n\t"
        "s_nop 1\n\t"
        "v_min_i32_dpp %[t0], %[t0], %[t0] row_ror:4 row_mask:0xf bank_mask:0xf\n\t"
        "s_nop 1\n\t"
        "v_min_i32_dpp %[t0], %[t0], %[t0] row_ror:2 row_mask:0xf bank_mask:0xf\n\t"
        "s_nop 1\n\t"
        "v_min_i32_dpp %[t0], %[t0], %[t0] row_ror:1 row_mask:0xf bank_mask:0xf\n\t"
        "s_nop 1\n\t"
        "v_readfirstlane_b32 s48, %[t0]\n\t"
        "s_nop 1\n\t"
        "s_add_i32 s48, s48, %c[nslot]\n\t"
        "s_and_b32 s48, s48, -2\n\t"
        "s_min_i32 s48, s48, s49\n\t"
        "s_cmp_lt_i32 s44, s48\n\t"
        "s_cbranch_scc1 22b\n\t"
        "v_mov_b32 %[t1], s44\n\t"
        "s_waitcnt vmcnt(0)\n\t"
        "ds_write_b32 %[pa], %[t1]\n\t"
        "s_sleep 1\n\t"
        "s_sub_u32 s55, s55, 1\n\t"
        "s_cmp_lg_u32 s55, 0\n\t"
        "s_cbranch_scc1 26b\n\t"
        "s_branch 22b\n"
        "30:\n\t"
        ".set k, 6\n\t"
        ".rept 7\n\t"
        ".set wcnt, 4 * k\n\t"
        "v_mov_b32 %[t1], 2 * %c[nku] - k\n\t"
        WAITW
        "ds_write_b32 %[pa], %[t1]\n\t"
        ".set k, k - 1\n\t"
        ".endr\n\t"
        "s_waitcnt lgkmcnt(0)\n\t"
        "s_endpgm\n\t"
        ".p2align 7\n"
        "23:\n\t"
        ".set wcnt, %c[wmin]-1\n\t"
        ".rept %c[wmax] - %c[wmin] + 1\n\t"
        UNIT4("%[vo]")
        "ds_write_b32 %[pa], %[t1]\n\t"
        "s_mov_b32 m0, s55\n\t"
        UNIT4("%[t0]")
        "s_branch 24b\n\t"
        ".p2align 7\n\t"
        ".set wcnt, wcnt+1\n\t"
        ".endr\n"
        "90:\n\t"
        "s_waitcnt lgkmcnt(0)"
        : [va] "=&v"(va), [vb] "=&v"(vb), [vo] "=&v"(vo), [pa] "=&v"(pa), [nxa] "=&v"(nxa), [t0] "=&v"(t0), [t1] "=&v"(t1), [t2] "=&v"(t2), [t3] "=&v"(t3)
        : [tid] "v"(threadIdx.x), [bid] "s"(blockIdx.x), [ka] "s"(__builtin_amdgcn_kernarg_segment_ptr()), [nch] "i"(CODE_KIB - 1), [jk] "i"(RING - UNIT),
          [wmin] "i"(WMIN), [wmax] "i"(WMAX), [gain] "i"(WGAIN), [wmid] "i"(WMID), [tau2] "i"(TAU2),
          [ctl] "i"(L_CTRL), [nslot] "i"(NSLOT), [nku] "i"(NKU), [cap] "i"(SPIN_CAP), [jump] "i"(JUMP), [decay] "i"(DECAY), [uend] "i"(UEND), [gain2] "i"(WGAIN2)
        : "memory", "scc", "s40", "s41", "s42", "s43", "s44", "s45", "s46", "s47", "s48", "s49", "s50", "s51", "s52", "s53", "s54", "s55", "s56", "s57", "s58", "s59", "s62", "s63", "s64");
  }
  __builtin_amdgcn_sched_barrier(0);
  const int tid = threadIdx.x, lane = tid & 63, wave = __builtin_amdgcn_readfirstlane(tid >> 6);
  const int xg = blockIdx.x & 7, kk = blockIdx.x >> 3;
  const int b = kk >> 3, pj = 8 * xg + (kk & 7), h = pj >> 1;
  const int r4 = lane >> 4, c16 = lane & 15;
  asm volatile("" : "+s"(x), "+s"(wq), "+s"(wo), "+s"(w1), "+s"(w3), "+s"(w2), "+s"(gw), "+s"(out), "+s"(ws));
  if (wave == 1) {
    const int wi = lane - 16;
    const int now = (int)(unsigned)__builtin_amdgcn_s_memrealtime() + REF0;
    ctrl[lane] = (lane == 1 || lane == 2) ? now : lane == 8 ? now - REF0 : (lane < 16 || lane >= 32) ? 0 : wi < NCW ? wi : wi < NCW + NHW ? NHC * NCW + NHH * (wi - NCW) : NEVER;
  }
  BARRIER();

  const __amdgpu_buffer_rsrc_t wsr = mkrsrc(ws, WS_BYTES);
  auto wg_max = [&]() __attribute__((always_inline)) {
    float mg = ctrlf[32 + (c16 < NCW + NHW ? c16 : 0)];
    mg = fmaxf(mg, dpp<ROR8>(mg)); mg = fmaxf(mg, dpp<ROR4>(mg)); mg = fmaxf(mg, dpp<ROR2>(mg)); mg = fmaxf(mg, dpp<ROR1>(mg));
    return mg;
  };

  if (wave == XWAVE + 1) {
    unsigned k = ((unsigned)__builtin_amdgcn_s_memrealtime() >> 8) + 1;
#pragma unroll 1
    for (int it = 0; it < 64; ++it, ++k) {
      wait_until(k << 8);
      if (__builtin_amdgcn_readfirstlane(ctrl[0]) >= NUNIT - 36) break;
      const int own = __builtin_amdgcn_readfirstlane(ctrl[1]);
      if (blockIdx.x < 16 && lane < 8) __builtin_amdgcn_raw_buffer_store_b128(u32x4{(unsigned)own, (unsigned)own, (unsigned)own, (unsigned)own}, wsr, WS_PACE + (k & 31u) * 2048 + blockIdx.x * 128 + lane * 16, 0, SC1);
      if (it == 0) continue;
      int e[16];
      asm volatile("s_load_dword %0, %16, 0x0\n\ts_load_dword %1, %16, 0x80\n\ts_load_dword %2, %16, 0x100\n\ts_load_dword %3, %16, 0x180\n\t"
                   "s_load_dword %4, %16, 0x200\n\ts_load_dword %5, %16, 0x280\n\ts_load_dword %6, %16, 0x300\n\ts_load_dword %7, %16, 0x380\n\t"
                   "s_load_dword %8, %16, 0x400\n\ts_load_dword %9, %16, 0x480\n\ts_load_dword %10, %16, 0x500\n\ts_load_dword %11, %16, 0x580\n\t"
                   "s_load_dword %12, %16, 0x600\n\ts_load_dword %13, %16, 0x680\n\ts_load_dword %14, %16, 0x700\n\ts_load_dword %15, %16, 0x780\n\t"
                   "s_waitcnt lgkmcnt(0)"
                   : "=&s"(e[0]), "=&s"(e[1]), "=&s"(e[2]), "=&s"(e[3]), "=&s"(e[4]), "=&s"(e[5]), "=&s"(e[6]), "=&s"(e[7]),
                     "=&s"(e[8]), "=&s"(e[9]), "=&s"(e[10]), "=&s"(e[11]), "=&s"(e[12]), "=&s"(e[13]), "=&s"(e[14]), "=&s"(e[15])
                   : "s"(ws + WS_PACE + ((k - 1u) & 31u) * 2048) : "memory");
      int sum = 0, cnt = 0;
#pragma unroll
      for (int i = 0; i < 16; ++i) { const int d = e[i] - own; const bool ok = (unsigned)(d + 600) < 1200u; sum += ok ? d : 0; cnt += ok ? 1 : 0; }
      if (cnt >= 8) ctrl[2] = own + (int)((float)sum / (float)cnt);
    }
    return;
  }

  if (wave == XWAVE) {
#pragma unroll 1
    for (int i = 0; i < 20; ++i) PAUSE(4);
    const u32x4 xv = *(const GAS u32x4*)(x + b * D + 8 * lane), gv = *(const GAS u32x4*)(gw + 8 * lane);
    *(LAS u32x4*)(L + L_X + lane * 16) = xv;
    float gd = 0.f;
#pragma unroll
    for (int k = 0; k < 4; ++k) gd = dot2(xv[k], gv[k], gd);
    gd = xrow_sum(row16_sum(gd));
    const float g = 1.f / (1.f + __expf(-gd));
    ctrlf[48] = g;
    lds_arrive(L, C_X, lane);
    u64 tag;
    asm volatile("s_load_dwordx2 %0, %1, 0x0\n\ts_waitcnt lgkmcnt(0)" : "=s"(tag) : "s"(ws + WS_EPOCH + b * 256) : "memory");
    tag += TAG_STEP;
#pragma unroll 1
    for (int spin = 0; spin < SPIN_CAP; ++spin) { if (__builtin_amdgcn_readfirstlane(ctrl[C_MAX]) >= NCW + NHW) break; __builtin_amdgcn_s_sleep(16); }
    CFENCE();
    const float mg = wg_max();
    float l = 0.f;
#pragma unroll 4
    for (int i = 0; i < NKU * 64 / 1024; ++i) {
      const f32x4 sc = *(const LAS f32x4*)(L + L_SC + i * 1024 + lane * 16);
      l += (expd(sc[0] - mg) + expd(sc[1] - mg)) + (expd(sc[2] - mg) + expd(sc[3] - mg));
    }
    l = xrow_sum(row16_sum(l));
    const unsigned tlo = (unsigned)tag, thi = (unsigned)(tag >> 32);
    if (lane < 8) __builtin_amdgcn_raw_buffer_store_b128(u32x4{__float_as_uint(mg), tlo, __float_as_uint(l), thi}, wsr, WS_REC + (b * 64 + pj) * 128 + lane * 16, 0, SC1);
#pragma unroll 1
    for (int i = 0; i < 12 + (kk & 31); ++i) PAUSE(12);
    u32x4 rec;
#pragma unroll 1
    for (int spin = 0; spin < SPIN_CAP; ++spin) {
      CFENCE();
      rec = __builtin_amdgcn_raw_buffer_load_b128(wsr, WS_REC + (b * 64 + (pj ^ 1)) * 128, 0, SC1);
      if (__builtin_amdgcn_ballot_w64(rec[1] != tlo || rec[3] != thi) == 0ull) break;
      __builtin_amdgcn_s_sleep(16);
    }
    const float mo = __uint_as_float(rec[0]), lo = __uint_as_float(rec[2]);
    const float mh = fmaxf(mg, mo), wt = expd(mg - mh);
    ctrlf[49] = g * wt / (wt * l + expd(mo - mh) * lo);
    const u32x4 yt = {tlo & YMASK, (tlo >> 12) & YMASK, (unsigned)(tag >> 24) & YMASK, (thi >> 4) & YMASK};
    *(LAS u32x4*)(L + L_CTRL + 52 * 4) = yt;
    lds_arrive(L, C_CF, lane);
    GAS unsigned* op = (GAS unsigned*)(out + b * D + 8 * pj + 4 * (lane & 1) + 2 * (lane >> 5));
    int off = WS_Y + (b * 64 + (lane >> 1)) * 2048 + pj * 32 + (lane & 1) * 16;
    asm volatile("" : "+v"(op), "+v"(off));
    BARRIER();
    {
      const unsigned now = (unsigned)__builtin_amdgcn_s_memrealtime();
      const int d = __builtin_amdgcn_readfirstlane(ctrl[2]) + TSTREAM + FOFF - (int)now;
      const unsigned t = now + (unsigned)min(max(d, FMIN), FMAX);
#pragma unroll 1
      for (int spin = 0; spin < 4096 && (int)(t - (unsigned)__builtin_amdgcn_s_memrealtime()) > 0; ++spin) {}
    }
    u32x4 ya, yb;
#pragma unroll 1
    for (int spin = 0; spin < SPIN_CAP; ++spin) {
      CFENCE();
      ya = __builtin_amdgcn_raw_buffer_load_b128(wsr, off, 0, SC1);
      yb = __builtin_amdgcn_raw_buffer_load_b128(wsr, off, 32 * 2048, SC1);
      const unsigned bad = (((ya[0] ^ yt[0]) | (ya[1] ^ yt[1])) | ((ya[2] ^ yt[2]) | (ya[3] ^ yt[3])) | ((yb[0] ^ yt[0]) | (yb[1] ^ yt[1])) | ((yb[2] ^ yt[2]) | (yb[3] ^ yt[3]))) & YMASK;
      if (__builtin_amdgcn_ballot_w64(bad != 0u) == 0ull) break;
    }
    float o[4];
#pragma unroll
    for (int k = 0; k < 4; ++k) o[k] = __uint_as_float(ya[k] & ~YMASK) + __uint_as_float(yb[k] & ~YMASK);
    float e0 = swadd32(o[0], o[2]), e1 = swadd32(o[1], o[3]);
    e0 = swadd16(e0, e0); e1 = swadd16(e1, e1);
    e0 += dpp<ROR8>(e0); e1 += dpp<ROR8>(e1); e0 += dpp<ROR4>(e0); e1 += dpp<ROR4>(e1); e0 += dpp<ROR2>(e0); e1 += dpp<ROR2>(e1);
    if ((lane & 30) == 0) *op = pack_bf16(e0, e1);
    if (pj == 0 && lane < 8) __builtin_amdgcn_raw_buffer_store_b128(u32x4{tlo, thi, tlo, thi}, wsr, WS_EPOCH + b * 256 + lane * 16, 0, SC1);
    return;
  }

  const int cw = wave - 1, hw = wave - 1 - NCW;
  int landed = 0;
  auto wait_unit = [&](int u) __attribute__((always_inline)) {
#pragma unroll 1
    for (int spin = 0; landed <= u && spin < SPIN_CAP; ++spin) {
      landed = __builtin_amdgcn_readfirstlane(ctrl[0]);
    }
    CFENCE();
  };
  auto take = [&](int u, int next, u32x4& a0, u32x4& a1, u32x4& a2, u32x4& a3) __attribute__((always_inline)) {
    wait_unit(u);
    const LAS u32x4* sp = (const LAS u32x4*)(L + (u & (NSLOT - 1)) * UNIT + lane * 16);
    a0 = sp[0]; a1 = sp[64]; a2 = sp[128]; a3 = sp[192];
    LDS_WAIT();
    ctrl[16 + cw] = next;
    asm volatile("" : "+v"(a0), "+v"(a1), "+v"(a2), "+v"(a3) :: "memory");
  };
  u32x4 q;
  auto build_q = [&]() __attribute__((always_inline)) {
    lds_await(ctrl, C_Q, NHW);
    const LAS f32x4* qp = (const LAS f32x4*)(L + L_QP + (2 * r4 * DH + 8 * c16) * 4);
    const f32x4 lo = qp[0] + qp[DH / 4], hi = qp[1] + qp[DH / 4 + 1];
    q[0] = pack_bf16(xrow_sum(lo[0]), xrow_sum(lo[1])); q[1] = pack_bf16(xrow_sum(lo[2]), xrow_sum(lo[3]));
    q[2] = pack_bf16(xrow_sum(hi[0]), xrow_sum(hi[1])); q[3] = pack_bf16(xrow_sum(hi[2]), xrow_sum(hi[3]));
  };
  constexpr float SCALE = 0.088388347648318440f;
  float mx = -3.4028234e38f;
  auto score = [&](int u, const u32x4& k0, const u32x4& k1, const u32x4& k2, const u32x4& k3) __attribute__((always_inline)) {
    float d0 = 0.f, d1 = 0.f, d2 = 0.f, d3 = 0.f;
#pragma unroll
    for (int k = 0; k < 4; ++k) { d0 = dot2(k0[k], q[k], d0); d1 = dot2(k1[k], q[k], d1); d2 = dot2(k2[k], q[k], d2); d3 = dot2(k3[k], q[k], d3); }
    const unsigned s01 = pack_bf16(row16_sum(d0), row16_sum(d1)), s23 = pack_bf16(row16_sum(d2), row16_sum(d3));
    const unsigned t01 = pack_bf16(bflo(s01) * SCALE, bfhi(s01) * SCALE), t23 = pack_bf16(bflo(s23) * SCALE, bfhi(s23) * SCALE);
    const f32x4 sc = {bflo(t01), bfhi(t01), bflo(t23), bfhi(t23)};
    mx = fmaxf(fmaxf(mx, fmaxf(sc[0], sc[1])), fmaxf(sc[2], sc[3]));
    if (c16 == 0) *(LAS f32x4*)(L + L_SC + u * 64 + r4 * 16) = sc;
  };

  if (hw < 0) {
    {
      u32x4 hk[4 * NHC];
#pragma unroll
      for (int i = 0; i < NHC; ++i) take(cw + NCW * i, i + 1 < NHC ? cw + NCW * (i + 1) : NHELD + cw, hk[4 * i], hk[4 * i + 1], hk[4 * i + 2], hk[4 * i + 3]);
      build_q();
#pragma unroll
      for (int i = 0; i < NHC; ++i) { score(cw + NCW * i, hk[4 * i], hk[4 * i + 1], hk[4 * i + 2], hk[4 * i + 3]); __builtin_amdgcn_sched_barrier(0); }
    }
#pragma unroll 1
    for (int u = NHELD + cw; u < NKU; u += NCW) {
      u32x4 k0, k1, k2, k3;
      take(u, u + NCW, k0, k1, k2, k3);
      score(u, k0, k1, k2, k3);
    }
    ctrlf[32 + cw] = xrow_max(mx);
    lds_arrive(L, C_MAX, lane);
    lds_await(ctrl, C_MAX, NCW + NHW);
    const float mg = wg_max();
    float acc[8] = {0.f, 0.f, 0.f, 0.f, 0.f, 0.f, 0.f, 0.f};
    LAS unsigned* rp = (LAS unsigned*)(L + L_REC + cw * 272 + (8 * c16 + 2 * r4) * 2);
    asm volatile("" : "+v"(rp));
    float cf = 0.f;
#pragma unroll 1
    for (int u = NKU + cw; u < NUNIT; u += NCW) {
      const f32x4 sc = *(const LAS f32x4*)(L + L_SC + (u - NKU) * 64 + r4 * 16);
      float p[4];
#pragma unroll
      for (int t = 0; t < 4; ++t) { p[t] = expd(sc[t] - mg); asm volatile("" : "+v"(p[t])); }
      if ((u & ~(NCW - 1)) == NUNIT - 32) { lds_await(ctrl, C_CF, 1); cf = ctrlf[49]; }
      u32x4 v[4];
      take(u, u + NCW, v[0], v[1], v[2], v[3]);
#pragma unroll
      for (int t = 0; t < 4; ++t)
#pragma unroll
        for (int k = 0; k < 4; ++k) { acc[2 * k] = fmaf(p[t], bflo(v[t][k]), acc[2 * k]); acc[2 * k + 1] = fmaf(p[t], bfhi(v[t][k]), acc[2 * k + 1]); }
    }
    const float e0 = swadd16(swadd32(acc[0], acc[4]), swadd32(acc[2], acc[6])), e1 = swadd16(swadd32(acc[1], acc[5]), swadd32(acc[3], acc[7]));
    *rp = pack_bf16(e0 * cf, e1 * cf);
    BARRIER();
    return;
  }

  u32x4 wb[16];
  const unsigned tstart = (unsigned)__builtin_amdgcn_readfirstlane(ctrl[8]);
  {
    const __amdgpu_buffer_rsrc_t rq = mkrsrc(wq, D * H * DH * 2);
    const int vo = ((64 * hw + 2 * r4) * (H * DH) + h * DH + 8 * c16) * 2;
#pragma unroll
    for (int i = 0; i < 16; ++i) { if (i == 0) wait_until(tstart + TQ + hw * QA); else PAUSE(QS); wb[i] = ldw(rq, vo, (8 * (i >> 1) + (i & 1)) * (H * DH * 2)); }
    lds_await(ctrl, C_X, 1);
    const LAS unsigned* xl = (const LAS unsigned*)(L + L_X + (64 * hw + 2 * r4) * 2);
    float a[8] = {0.f, 0.f, 0.f, 0.f, 0.f, 0.f, 0.f, 0.f};
#pragma unroll
    for (int i = 0; i < 8; ++i) {
      const unsigned xp = xl[4 * i];
#pragma unroll
      for (int k = 0; k < 4; ++k) {
        a[2 * k] = dot2(perm_lo(wb[2 * i][k], wb[2 * i + 1][k]), xp, a[2 * k]);
        a[2 * k + 1] = dot2(perm_hi(wb[2 * i][k], wb[2 * i + 1][k]), xp, a[2 * k + 1]);
      }
    }
    const float e0 = swadd16(swadd32(a[0], a[4]), swadd32(a[2], a[6])), e1 = swadd16(swadd32(a[1], a[5]), swadd32(a[3], a[7]));
    *(LAS f32x2*)(L + L_QP + (hw * DH + 8 * c16 + 2 * r4) * 4) = f32x2{e0, e1};
    lds_arrive(L, C_Q, lane);
    const int hb = NHC * NCW + NHH * hw;
#pragma unroll
    for (int i = 0; i < NHH; ++i) take(hb + i, i + 1 < NHH ? hb + i + 1 : NEVER, wb[4 * i], wb[4 * i + 1], wb[4 * i + 2], wb[4 * i + 3]);
    build_q();
#pragma unroll
    for (int i = 0; i < NHH; ++i) { score(hb + i, wb[4 * i], wb[4 * i + 1], wb[4 * i + 2], wb[4 * i + 3]); __builtin_amdgcn_sched_barrier(0); }
    ctrlf[32 + cw] = xrow_max(mx);
    lds_arrive(L, C_MAX, lane);
  }
  f32x4 y[4] = {{0.f, 0.f, 0.f, 0.f}, {0.f, 0.f, 0.f, 0.f}, {0.f, 0.f, 0.f, 0.f}, {0.f, 0.f, 0.f, 0.f}};
  if ((pj & 7) != 7) {
    const int sl = 7 * (pj >> 3) + (pj & 7);
    const __amdgpu_buffer_rsrc_t r1 = mkrsrc(w1, D * F * 2), r3 = mkrsrc(w3, D * F * 2), r2 = mkrsrc(w2, F * D * 2);
    const int vo = ((64 * hw + (lane & 60)) * F + FS * sl + 8 * (lane & 3)) * 2;
#pragma unroll
    for (int i = 0; i < 8; ++i) { if (i == 0) wait_until(tstart + T1 + hw * A1); else PAUSE(S1); wb[i] = ldw(i < 4 ? r1 : r3, vo, (i & 3) * (F * 2)); }
    const u32x2 xm = *(const LAS u32x2*)(L + L_X + (64 * hw + (lane & 60)) * 2);
    float a[16];
#pragma unroll
    for (int m = 0; m < 2; ++m)
#pragma unroll
      for (int k = 0; k < 4; ++k) {
        a[8 * m + 2 * k] = dot2(perm_lo(wb[4 * m + 2][k], wb[4 * m + 3][k]), xm[1], dot2(perm_lo(wb[4 * m][k], wb[4 * m + 1][k]), xm[0], 0.f));
        a[8 * m + 2 * k + 1] = dot2(perm_hi(wb[4 * m + 2][k], wb[4 * m + 3][k]), xm[1], dot2(perm_hi(wb[4 * m][k], wb[4 * m + 1][k]), xm[0], 0.f));
      }
    f32x4 e;
#pragma unroll
    for (int k = 0; k < 4; ++k) {
      float t = swadd16(swadd32(a[k], a[8 + k]), swadd32(a[4 + k], a[12 + k]));
      t += dpp<ROR8>(t); e[k] = t + dpp<ROR4>(t);
    }
    if ((lane & 12) == 0) *(LAS f32x4*)(L + L_MP + (hw * 64 + (lane & 32) + 8 * (lane & 3) + 4 * (r4 & 1)) * 4) = e;
    lds_arrive(L, C_MLP, lane);
    u32x2 wr[8];
#pragma unroll
    for (int i = 0; i < 8; ++i) { if (i == 0) wait_until(tstart + T2 + hw * A2); else PAUSE(S2); wr[i] = ldw2(r2, ((FS * sl + 8 * r4) * D + 64 * hw + 4 * c16) * 2, i * (D * 2)); }
    lds_await(ctrl, C_MLP, NHW);
    const LAS float* mp = (const LAS float*)(L + L_MP) + (lane & 31);
    float ga = 0.f, up = 0.f;
#pragma unroll
    for (int w = 0; w < NHW; ++w) { ga += mp[w * 64]; up += mp[w * 64 + 32]; }
    const float hv = ga / (1.f + __expf(-ga)) * up * (1.f - ctrlf[48]);
    const unsigned hh = pack_bf16(hv, 0.f), hl = pack_bf16(hv - bflo(hh), 0.f);
    *(LAS u16*)(L + L_H + hw * 128 + lane * 2) = (u16)(lane < 32 ? hh : hl);
    LDS_WAIT();
    u32x4 ha = *(const LAS u32x4*)(L + L_H + hw * 128 + (c16 & 1) * 64 + r4 * 16);
    if (c16 >= 2) ha = u32x4{0u, 0u, 0u, 0u};
#pragma unroll
    for (int e = 0; e < 4; ++e) {
      u32x4 f;
#pragma unroll
      for (int k = 0; k < 4; ++k) f[k] = (e & 1) ? perm_hi(wr[2 * k][e >> 1], wr[2 * k + 1][e >> 1]) : perm_lo(wr[2 * k][e >> 1], wr[2 * k + 1][e >> 1]);
      y[e] = mfma(ha, f, y[e]);
    }
  }
#pragma unroll
  for (int e = 0; e < 4; ++e) asm volatile("" : "+v"(y[e]));
  {
    const __amdgpu_buffer_rsrc_t ro = mkrsrc(wo, H * DH * D * 2);
#pragma unroll
    for (int t = 0; t < 4; ++t) {
      u32x2 wr[8];
#pragma unroll
      for (int i = 0; i < 8; ++i) { if (t + i == 0) wait_until(tstart + TO + hw * AO); else PAUSE(SO); wr[i] = ldw2(ro, ((h * DH + 32 * r4) * D + 64 * hw + 4 * c16) * 2, (8 * t + i) * (D * 2)); }
#pragma unroll
      for (int e = 0; e < 4; ++e) {
#pragma unroll
        for (int k = 0; k < 4; ++k) wb[4 * t + e][k] = (e & 1) ? perm_hi(wr[2 * k][e >> 1], wr[2 * k + 1][e >> 1]) : perm_lo(wr[2 * k][e >> 1], wr[2 * k + 1][e >> 1]);
        asm volatile("" : "+v"(wb[4 * t + e]));
      }
    }
  }
  lds_await(ctrl, C_CF, 1);
  u32x4 yt = *(const LAS u32x4*)(L + L_CTRL + 52 * 4);
  int yo = WS_Y + (b * 64 + pj) * 2048 + hw * 256 + c16 * 16;
  const LAS u32x4* rec = (const LAS u32x4*)(L + L_REC + (lane & 3) * 272 + r4 * 64);
  unsigned ym = ~YMASK;
  asm volatile("" : "+v"(yt), "+v"(yo), "+v"(rec), "+s"(ym));
  BARRIER();
  {
    const u32x4 a[4] = {rec[0], rec[1], rec[2], rec[3]};
#pragma unroll
    for (int t = 0; t < 4; ++t)
#pragma unroll
      for (int e = 0; e < 4; ++e) y[e] = mfma(a[t], wb[4 * t + e], y[e]);
    u32x4 v;
#pragma unroll
    for (int e = 0; e < 4; ++e) {
      const f32x2 t = y[e].xy + y[e].zw;
      unsigned r;
      asm("v_add_f32 %0, %1, %2\n\tv_add_u32 %0, %5, %0\n\tv_and_or_b32 %0, %0, %3, %4" : "=&v"(r) : "v"(t[0]), "v"(t[1]), "s"(ym), "v"(yt[e]), "i"((YMASK + 1u) / 2u));
      v[e] = r;
    }
    if (lane < 16) __builtin_amdgcn_raw_buffer_store_b128(v, wsr, yo, 0, SC1);
  }
}

extern "C" void kernel_launch(void* const* d_in, const int* in_sizes, int n_in, void* d_out, int out_size, void* d_ws, size_t ws_size,
                              hipStream_t stream) {
  gated_mix__mha_block_cached__glu_gated_swiglu__decode_kernel<<<256, NTHR, 0, stream>>>((const u16*)d_in[0], (const u16*)d_in[1], (const u16*)d_in[2], (const u16*)d_in[3], (const u16*)d_in[4],
                                             (const u16*)d_in[6], (const u16*)d_in[7], (const u16*)d_in[8], (const u16*)d_in[9], (u16*)d_out,
                                             (unsigned char*)d_ws);
}
```

```cpp
#include <hip/hip_runtime.h>
#include <hip/hip_bf16.h>

#define GAS __attribute__((address_space(1)))
#define LAS __attribute__((address_space(3)))
typedef unsigned short u16;
typedef unsigned long long u64;
using u32x4 = __attribute__((ext_vector_type(4))) unsigned;
using u32x2 = __attribute__((ext_vector_type(2))) unsigned;
using f32x4 = __attribute__((ext_vector_type(4))) float;
using f32x2 = __attribute__((ext_vector_type(2))) float;
using bf16x2 = __attribute__((ext_vector_type(2))) __bf16;
using bf16x8 = __attribute__((ext_vector_type(8))) __bf16;

constexpr int B = 4, D = 512, H = 32, SK = 8192, DH = 128, F = 1792;
constexpr int NCW = 4, NHW = 8, XWAVE = 1 + NCW + NHW, NWAVE = XWAVE + 2, NTHR = NWAVE * 64;
#define QA 112
#define QB 5
#define T1 1400
#define A1 150
#define B1 18
#define T2 2500
#define A2 40
#define B2 4
#define TO 2900
#define AO 350
#define BO 8
#define TS 68.6
#define REF0 180
#define PLAG 1
#define NOPST 0
#define NOPLD 0
#define PAUX 16
#define RAUX 16
#define PFW 0
#define TQ 100
#define NINIT 6
#define CODE_KIB 21
#define FDEL 0
#define CODEPF 1
#define YST 16
#define YLD 16
#define RED16 0
#define MASKP 0
#define PSLP 0
#define OUTSC 0
#define OUT_STORE(p, v) *(GAS unsigned*)(p) = v
#define CLR0 1
#define MLPFULL 0
#define MLPPF 0
#define TP 1200
#define AP 12
#define GRID 0
#define B2BD 0
#define B2BAUX 16
#define WOB2B 0
#define TOUCHW 1
#define W0 30
#define BARPOS 12
#define NO_WO 0
#define TLBPF 0
#define TMODE 0
#define SPW 4
#define QS 2
#define S1 6
#define S2 1
#define SO 2
#define USL 0
#define JUMP 100
#define DECAY 6
#define HOLD0 10000
#define UEND 420
#define WGAIN2 18
#define WMID2 WMID
#define FOFF 156
#define FMIN 45
#define FMAX 150
#define NRED 64
#define KEARLY 0
#define KLAST 16
#define NOTHR 0
#define WMIN0 16
#define QROW 1
#define QAUX 2
#define QXOR 0
#define HX 0
#define QLO 7
#define HAND 31
#define HOR 0
#define HQ (QLO == 7 ? ((h & HAND) | HOR) : QLO == 1 ? xg + 8 * (h & 3) : 2 * xg + (h & 1) + 16 * ((h >> 1) & 1))
#define QPFM 0
#define HBLK 1
#define HM (HBLK ? NHH : 1)
#define HS (HBLK ? 1 : NHW)
#define RMASK 0
#define SBIT 16
#define XB0 1
#define XM0 0x40000
#define XB1 1
#define XM1 0
#define UMID 128
#define PAT 0
#define SLOTSH 8
#define WMID 21.25
#define WGAIN 10
#define WMIN 16
#define WMAX 28
#define CLRAUX 16
#define PCONT 1
#define XDEL 20
#define XSPR 0
#define XNT 0
#define MIMIC 1
#define NCLR 2
#define NOFLAG 0
#define CSPR 0
constexpr int UNIT = 4096;
constexpr int NSLOT = 32, RING = NSLOT * UNIT;
constexpr int NKU = (SK / 2) / 16, NUNIT = 2 * NKU;
constexpr int NHC = 6, NHH = 4, NHELD = NHC * NCW + NHH * NHW;
constexpr int FS = 32;
constexpr int L_CTRL = RING;
constexpr int L_X = L_CTRL + 256;
constexpr int L_QP = L_X + D * 2;
constexpr int L_MP = L_QP + NHW * DH * 4;
constexpr int L_REC = L_MP + NHW * 64 * 4;
constexpr int L_H = L_REC + NCW * 272;
constexpr int L_SC = L_H + NHW * 128;
constexpr int LDS_BYTES = L_SC + NKU * 64;
static_assert(LDS_BYTES <= 159 * 1024, "lds");
static_assert(WMIN <= (int)WMID && (int)WMID <= WMAX && WMAX <= 28, "window");
constexpr int WS_EPOCH = 0;
constexpr int WS_REC = 4096;
constexpr int WS_PACE = WS_REC + 256 * 128;
constexpr int WS_Y = WS_PACE + 64 * 2048;
constexpr int WS_BYTES = WS_Y + B * 64 * 2048;
constexpr u64 TAG_STEP = 0x9E3779B97F4A7C15ull;
constexpr unsigned YMASK = 0xFFFu;
constexpr int SPIN_CAP = 1 << 22;
constexpr int NEVER = 1 << 20;
constexpr int C_Q = 4, C_MAX = 5, C_MLP = 6, C_X = 7, C_CF = 9;

__device__ __forceinline__ float expd(float d) { return __builtin_amdgcn_exp2f(d * 1.4426950408889634f); }
__device__ __forceinline__ float bflo(unsigned u) { return __builtin_bit_cast(float, u << 16); }
__device__ __forceinline__ float bfhi(unsigned u) { return __builtin_bit_cast(float, u & 0xffff0000u); }
__device__ __forceinline__ float dot2(unsigned a, unsigned b, float acc) {
  return __builtin_amdgcn_fdot2_f32_bf16(__builtin_bit_cast(bf16x2, a), __builtin_bit_cast(bf16x2, b), acc, false);
}
__device__ __forceinline__ unsigned pack_bf16(float lo, float hi) {
  const bf16x2 v = {(__bf16)lo, (__bf16)hi};
  return __builtin_bit_cast(unsigned, v);
}
__device__ __forceinline__ unsigned perm_lo(unsigned a, unsigned b) { return __builtin_amdgcn_perm(b, a, 0x05040100u); }
__device__ __forceinline__ unsigned perm_hi(unsigned a, unsigned b) { return __builtin_amdgcn_perm(b, a, 0x07060302u); }
template <int CTRL> __device__ __forceinline__ float dpp(float x) {
  return __builtin_bit_cast(float, __builtin_amdgcn_mov_dpp(__builtin_bit_cast(int, x), CTRL, 0xf, 0xf, true));
}
constexpr int XOR1 = 0xB1, ROR1 = 0x121, ROR2 = 0x122, ROR4 = 0x124, ROR8 = 0x128;
__device__ __forceinline__ float row16_sum(float x) { x += dpp<ROR8>(x); x += dpp<ROR4>(x); x += dpp<ROR2>(x); x += dpp<ROR1>(x); return x; }
__device__ __forceinline__ float swadd32(float a, float b) {
  auto t = __builtin_amdgcn_permlane32_swap(__float_as_uint(a), __float_as_uint(b), false, false);
  return __uint_as_float(t[0]) + __uint_as_float(t[1]);
}
__device__ __forceinline__ float swadd16(float a, float b) {
  auto t = __builtin_amdgcn_permlane16_swap(__float_as_uint(a), __float_as_uint(b), false, false);
  return __uint_as_float(t[0]) + __uint_as_float(t[1]);
}
__device__ __forceinline__ float xrow_sum(float x) { x = swadd16(x, x); return swadd32(x, x); }
__device__ __forceinline__ float xrow_max(float x) {
  auto s = __builtin_amdgcn_permlane16_swap(__float_as_uint(x), __float_as_uint(x), false, false);
  x = fmaxf(__uint_as_float(s[0]), __uint_as_float(s[1]));
  auto t = __builtin_amdgcn_permlane32_swap(__float_as_uint(x), __float_as_uint(x), false, false);
  return fmaxf(__uint_as_float(t[0]), __uint_as_float(t[1]));
}
__device__ __forceinline__ __amdgpu_buffer_rsrc_t mkrsrc(const void* p, int bytes) { return __builtin_amdgcn_make_buffer_rsrc((void*)p, 0, bytes, 0x00020000); }
__device__ __forceinline__ u32x4 ldw(__amdgpu_buffer_rsrc_t r, int voff, int soff) { return __builtin_amdgcn_raw_buffer_load_b128(r, voff, soff, 2); }
#define LDS_WAIT() asm volatile("s_waitcnt lgkmcnt(0)" ::: "memory")
#define VM_DRAIN() asm volatile("s_waitcnt vmcnt(0)" ::: "memory")
#define CFENCE() asm volatile("" ::: "memory")
#define BARRIER() asm volatile("s_waitcnt lgkmcnt(0)\n\ts_barrier" ::: "memory")
#define PAUSE(n) do { __builtin_amdgcn_sched_barrier(0); asm volatile("s_sleep %0" :: "i"(n) : "memory"); __builtin_amdgcn_sched_barrier(0); } while (0)

__device__ __forceinline__ void lds_arrive(LAS unsigned char* L, int idx, int lane) {
  LDS_WAIT();
  if (lane == 0) __hip_atomic_fetch_add((LAS int*)(L + L_CTRL) + idx, 1, __ATOMIC_RELAXED, __HIP_MEMORY_SCOPE_WORKGROUP);
}
__device__ __forceinline__ void lds_await(volatile LAS int* ctrl, int idx, int n) {
#pragma unroll 1
  for (int spin = 0; spin < SPIN_CAP; ++spin) { if (__builtin_amdgcn_readfirstlane(ctrl[idx]) >= n) break; __builtin_amdgcn_s_sleep(1); }
  CFENCE();
}

__device__ __forceinline__ void wait_until(unsigned t) {
  __builtin_amdgcn_sched_barrier(0);
#pragma unroll 1
  for (int spin = 0; spin < 4096; ++spin) {
    const int d = (int)(t - (unsigned)__builtin_amdgcn_s_memrealtime());
    if (d <= 0) break;
    if (d > 24) __builtin_amdgcn_s_sleep(6);
  }
  CFENCE();
  __builtin_amdgcn_sched_barrier(0);
}
#define STR2(x) #x
#define STR(x) STR2(x)
constexpr int TAU2 = 2 * (int)(TS * 100.0 * 256.0 / NUNIT);
__device__ __forceinline__ f32x4 mfma(const u32x4& a, const u32x4& b, const f32x4& c) {
  return __builtin_amdgcn_mfma_f32_16x16x32_bf16(__builtin_bit_cast(bf16x8, a), __builtin_bit_cast(bf16x8, b), c, 0, 0, 0);
}
__device__ __forceinline__ u32x2 ldw2(__amdgpu_buffer_rsrc_t r, int voff, int soff) { return __builtin_amdgcn_raw_buffer_load_b64(r, voff, soff, 2); }
template <int CTRL> __device__ __forceinline__ int dppi(int x) { return __builtin_amdgcn_mov_dpp(x, CTRL, 0xf, 0xf, true); }

#define STAMP(i) do {} while (0)
#define STAMP_FLUSH() do {} while (0)
#define STAMP_LDS 0

__global__ void __launch_bounds__(NTHR, 4) gated_mix_kernel(const u16* x, const u16* kc, const u16* vc, const u16* wq, const u16* wo, const u16* w1,
                                                         const u16* w3, const u16* w2, const u16* gw, u16* out, unsigned char* ws) {
  __shared__ __attribute__((aligned(16))) unsigned char lds_raw[LDS_BYTES + STAMP_LDS];
  LAS unsigned char* L = (LAS unsigned char*)lds_raw;
  volatile LAS int* ctrl = (volatile LAS int*)(L + L_CTRL);
  volatile LAS float* ctrlf = (volatile LAS float*)(L + L_CTRL);
#define DBG(x) ""
  {
    unsigned va, vb, vc2, vo, pa, nxa, t0, t1, t2, t3;
#define CODELD "global_load_lds_dwordx4 %[t0], s[56:57]\n\t"
#define ASTAMP(i) DBG("s_memrealtime s[60:61]\n\ts_waitcnt lgkmcnt(0)\n\tv_mov_b32 %[t1], %c[dbg] + 4 * " #i "\n\tv_mov_b32 %[t2], s60\n\tds_write_b32 %[t1], %[t2]\n\t")
#define WAITW "s_waitcnt (wcnt & 15) | ((wcnt >> 4) << 14) | 0x0F70\n\t"
#define SP(v, sb, wait) wait "global_load_lds_dword " v ", " sb " nt\n\tglobal_load_lds_dword " v ", " sb " sc1\n\t"
#define QPF SP("%[va]", "s[44:45]", "") SP("%[t2]", "s[44:45]", "")
#define U0 0x1000
#define U1 0x2000
#define U2 0x4000
#define UH 0
#define SB 16
#define WALK "s_and_b32 s54, s44, 255\n\ts_and_b32 s55, s54, -16\n\ts_add_u32 s54, s54, s55\n\ts_lshl_b32 s54, s54, 12\n\t"
#define SW "s_waitcnt vmcnt(%c[spw])\n\t"
#define UNIT4(v, v2) \
        WAITW "global_load_lds_dwordx4 " v ", s[40:41] nt\n\t" \
        WAITW "global_load_lds_dwordx4 " v ", s[40:41] offset:1024 nt\n\t" \
        WAITW "global_load_lds_dwordx4 " v2 ", s[40:41] offset:2048 nt\n\t" \
        WAITW "global_load_lds_dwordx4 " v2 ", s[40:41] offset:3072 nt\n\t"
    asm volatile(
        "s_getpc_b64 s[56:57]\n\t"
        "s_load_dwordx8 s[44:51], %[ka], 0x18\n\t"
        "s_load_dwordx2 s[52:53], %[ka], 0x38\n\t"
        "s_load_dwordx4 s[40:43], %[ka], 0x8\n\t"
        "v_readfirstlane_b32 s54, %[tid]\n\t"
        "s_mov_b32 m0, %c[jk]\n\t"
        "s_lshr_b32 s59, %[bid], 3\n\t"
        "s_cmp_ge_u32 s54, 64\n\t"
        "s_cbranch_scc1 90f\n\t"
        "s_min_u32 s54, s59, %c[nch]\n\t"
        "s_lshl_b32 s54, s54, 10\n\t"
        "v_lshlrev_b32 %[vo], 4, %[tid]\n\t"
        "s_and_b32 s56, s56, -128\n\t"
        "v_add_u32 %[t0], s54, %[vo]\n\t"
        "s_and_b32 s58, %[bid], 7\n\t"
        CODELD
        "s_setprio 3\n\t"
        "v_lshlrev_b32 %[t1], 7, %[tid]\n\t"
        "v_lshrrev_b32 %[va], 3, %[tid]\n\t"
        "v_lshlrev_b32 %[va], 13, %[va]\n\t"
        "v_and_b32 %[t2], " STR(QLO) ", %[tid]\n\t"
        "v_lshl_add_u32 %[va], %[t2], 7, %[va]\n\t"
        "v_and_b32 %[t2], 7 - " STR(QLO) ", %[tid]\n\t"
        "v_lshl_add_u32 %[va], %[t2], 10, %[va]\n\t"
        "s_lshl_b32 s54, s59, 17\n\t"
        "s_mul_i32 s55, s58, 128 * (" STR(QLO) " + 1)\n\t"
        "s_add_u32 s54, s54, s55\n\t"
        "v_add_u32 %[va], s54, %[va]\n\t"
        "v_add_u32 %[t2], 0x10000, %[va]\n\t"
        "s_lshl_b32 s55, s58, 5\n\t"
        "s_add_u32 s55, s55, s59\n\t"
        "s_mul_i32 s54, s55, 7 * 1024\n\t"
        "v_min_u32 %[t3], 55 * 128, %[t1]\n\t"
        "v_add_u32 %[t3], s54, %[t3]\n\t"
        "s_mul_i32 s54, s58, 448\n\t"
        "s_and_b32 s54, s54, -128\n\t"
        "s_mul_i32 s57, s59, 16 * 3584\n\t"
        "s_add_u32 s54, s54, s57\n\t"
        "v_lshrrev_b32 %[t0], 2, %[tid]\n\t"
        "v_mul_u32_u24 %[t0], 3584 - 512, %[t0]\n\t"
        "v_add3_u32 %[t0], %[t0], %[t1], s54\n\t"
        "v_lshl_add_u32 %[vb], s55, 14, %[t1]\n\t"
        "v_add_u32 %[t1], 0x2000, %[vb]\n\t"
        "s_and_b32 s54, %[bid], 0xc0\n\t"
        "s_lshl3_add_u32 s54, s58, s54\n\t"
        "s_and_b32 s56, s59, 7\n\t"
        "s_add_u32 s54, s54, s56\n\t"
        "s_lshr_b32 s56, s54, 1\n\t"
        "s_lshl_b32 s56, s56, 21\n\t"
        "s_and_b32 s66, s54, 1\n\t"
        "s_lshl_b32 s66, s66, " STR(SB) "\n\t"
        "s_bfe_u32 s57, s54, 0x10000 + 1 + " STR(XB0) "\n\t"
        "s_mul_i32 s57, s57, " STR(XM0) "\n\t"
        "s_xor_b32 s66, s66, s57\n\t"
        "s_bfe_u32 s57, s54, 0x10000 + 1 + " STR(XB1) "\n\t"
        "s_mul_i32 s57, s57, " STR(XM1) "\n\t"
        "s_xor_b32 s66, s66, s57\n\t"
        "s_waitcnt lgkmcnt(0)\n\t"
        QPF SP("%[t3]", "s[52:53]", "")
        ".if %c[spw] <= 4\n\ts_barrier\n\t.endif\n\t"
        SP("%[t0]", "s[48:49]", SW)
        ".if %c[spw] == 6\n\ts_barrier\n\t.endif\n\t"
        SP("%[t0]", "s[50:51]", SW)
        ".if %c[spw] == 8\n\ts_barrier\n\t.endif\n\t"
        SP("%[vb]", "s[46:47]", SW) SP("%[t1]", "s[46:47]", SW)
        ".if %c[spw] > 8\n\ts_barrier\n\t.endif\n\t"
        ASTAMP(6)
        "s_add_u32 s40, s40, s56\n\t"
        "s_addc_u32 s41, s41, 0\n\t"
        "s_add_u32 s42, s42, s56\n\t"
        "s_addc_u32 s43, s43, 0\n\t"
        "v_mov_b32 %[va], %[vo]\n\t"
        "v_or_b32 %[vo], s66, %[vo]\n\t"
        ".set so, 0\n\t"
        ".set wcnt, %c[spw] + " STR(KEARLY) "\n\t"
        ".rept 4\n\t"
        "s_movk_i32 m0, so\n\t"
        ".if (so & 4096) == 0\n\t"
        WAITW
        ".set wcnt, wcnt + 6\n\t"
        ".else\n\t"
        "s_nop 0\n\t"
        ".endif\n\t"
        "v_add_u32 %[vb], " STR(UH) ", %[vo]\n\t"
        "global_load_lds_dwordx4 %[vo], s[40:41] nt\n\t"
        "global_load_lds_dwordx4 %[vo], s[40:41] offset:1024 nt\n\t"
        "global_load_lds_dwordx4 %[vb], s[40:41] offset:2048 nt\n\t"
        "global_load_lds_dwordx4 %[vb], s[40:41] offset:3072 nt\n\t"
        ".if (so & 4096) == 0\n\t"
        "v_add_u32 %[vo], " STR(U0) ", %[vo]\n\t"
        ".elseif so == 4096\n\t"
        "v_add_u32 %[vo], " STR(U1) " - " STR(U0) ", %[vo]\n\t"
        ".else\n\t"
        "v_add_u32 %[vo], " STR(U2) " - " STR(U1) " - " STR(U0) ", %[vo]\n\t"
        ".endif\n\t"
        ".set so, so + 4096\n\t"
        ".endr\n\t"
        "s_waitcnt vmcnt(" STR(KLAST) ")\n\t"
        ASTAMP(7)
        "s_getpc_b64 s[50:51]\n"
        "20:\n\t"
        "s_add_u32 s50, s50, 23f-20b-%c[wmin]*128\n\t"
        "s_addc_u32 s51, s51, -1\n\t"
        "v_mov_b32 %[pa], %c[ctl]\n\t"
        "v_and_b32 %[nxa], 0x3c0 >> 2, %[vo]\n\t"
        "v_lshrrev_b32 %[nxa], 2, %[nxa]\n\t"
        "v_add_u32 %[nxa], %c[ctl] + 64, %[nxa]\n\t"
        "s_memrealtime s[58:59]\n\t"
        "s_mov_b32 s62, 0\n\t"
        "s_mov_b32 s65, " STR(WMIN0) "\n\t"
        "s_mov_b32 s63, %c[gain]\n\t"
        "s_mov_b32 s64, %c[wmid]\n\t"
        "s_mov_b32 s44, 4\n\t"
        "s_mov_b32 s45, %c[wmid] >> 8\n\t"
        "s_mov_b32 s46, 0x4000\n\t"
        "s_mov_b32 s47, 2 * %c[tau2]\n\t"
        "s_mov_b32 s48, %c[nslot]\n\t"
        "s_mov_b32 s49, %c[nku]\n\t"
        "s_waitcnt lgkmcnt(0)\n\t"
        "s_mov_b32 s59, 10000\n"
        "22:\n\t"
        "s_memrealtime s[56:57]\n\t"
        "ds_read_b32 %[t3], %[pa] offset:8\n\t"
        "s_lshl_b32 s52, s45, 7\n\t"
        "s_add_u32 s52, s50, s52\n\t"
        "s_addc_u32 s53, s51, 0\n\t"
        "s_add_i32 s54, s45, -1\n\t"
        "s_lshr_b32 s54, s54, 2\n\t"
        "s_sub_i32 s54, s44, s54\n\t"
        "v_mov_b32 %[t1], s54\n\t"
        "v_add_u32 %[t0], " STR(U0) ", %[vo]\n\t"
        "v_add_u32 %[vb], " STR(UH) ", %[vo]\n\t"
        "v_add_u32 %[vc], " STR(U0) " + " STR(UH) ", %[vo]\n\t"
        "s_add_u32 s55, s46, 0x1000\n\t"
        "s_mov_b32 m0, s46\n\t"
        "s_setpc_b64 s[52:53]\n"
        "24:\n\t"
        "s_waitcnt lgkmcnt(0)\n\t"
        "v_readfirstlane_b32 s57, %[t3]\n\t"
        "v_add_u32 %[t1], 1, %[t1]\n\t"
        "s_lshr_b32 s54, s47, 8\n\t"
        "s_sub_u32 s56, s56, s54\n\t"
        "v_mov_b32 %[t2], s56\n\t"
        "s_sub_i32 s54, s56, s58\n\t"
        "s_mov_b32 s58, s56\n\t"
        "s_sub_i32 s56, s56, s57\n\t"
        "ds_write2_b32 %[pa], %[t1], %[t2] offset1:1\n\t"
        "s_add_i32 s59, s59, s54\n\t"
        "s_add_i32 s59, s59, " STR(DECAY) "\n\t"
        "s_cmp_gt_i32 s54, %c[jump]\n\t"
        "s_cselect_b32 s59, s62, s59\n\t"
        "s_min_i32 s62, s56, s59\n\t"
        "s_mul_i32 s56, s62, s63\n\t"
        "s_add_i32 s56, s56, s64\n\t"
        "s_ashr_i32 s56, s56, 8\n\t"
        "s_max_i32 s56, s56, s65\n\t"
        "s_min_i32 s45, s56, %c[wmax]\n\t"
        "s_add_u32 s47, s47, %c[tau2]\n\t"
        "s_add_u32 s46, s46, 0x2000\n\t"
        "s_and_b32 s46, s46, %c[nslot] * 4096 - 1\n\t"
        "s_add_i32 s44, s44, 2\n\t"
        WALK
        "s_xor_b32 s54, s54, s66\n\t"
        "v_or_b32 %[vo], s54, %[va]\n\t"
        "s_cmp_lt_i32 s44, s48\n\t"
        "s_cbranch_scc1 22b\n\t"
        DBG("s_add_i32 s54, s44, -1\n\ts_lshr_b32 s54, s54, 5\n\ts_lshl_b32 s54, s54, 2\n\ts_add_u32 s54, s54, %c[dbg] + 14 * 64\n\tv_mov_b32 %[t0], s54\n\tv_mov_b32 %[t1], s45\n\t"
            "ds_write2_b32 %[t0], %[t2], %[t1] offset1:16\n\t")
        "s_cmp_lt_i32 s44, s49\n\t"
        "s_cbranch_scc1 25f\n\t"
        "s_cmp_ge_i32 s44, 2 * %c[nku]\n\t"
        "s_cbranch_scc1 30f\n\t"
        "s_mov_b32 s49, 2 * %c[nku]\n\t"
        "s_mov_b64 s[40:41], s[42:43]\n"
        "25:\n\t"
        "s_mov_b32 s65, %c[wmin]\n\t"
        "s_cmp_ge_i32 s44, %c[uend]\n\t"
        "s_cselect_b32 s63, %c[gain2], s63\n\t"
        "s_cmp_ge_i32 s44, " STR(UMID) "\n\t"
        "s_cselect_b32 s64, %c[wmid2], s64\n\t"
        "s_mov_b32 s55, %c[cap]\n"
        "26:\n\t"
        "ds_read_b32 %[t0], %[nxa]\n\t"
        "s_waitcnt lgkmcnt(0)\n\t"
        "s_nop 1\n\t"
        "v_min_i32_dpp %[t0], %[t0], %[t0] row_ror:8 row_mask:0xf bank_mask:0xf\n\t"
        "s_nop 1\n\t"
        "v_min_i32_dpp %[t0], %[t0], %[t0] row_ror:4 row_mask:0xf bank_mask:0xf\n\t"
        "s_nop 1\n\t"
        "v_min_i32_dpp %[t0], %[t0], %[t0] row_ror:2 row_mask:0xf bank_mask:0xf\n\t"
        "s_nop 1\n\t"
        "v_min_i32_dpp %[t0], %[t0], %[t0] row_ror:1 row_mask:0xf bank_mask:0xf\n\t"
        "s_nop 1\n\t"
        "v_readfirstlane_b32 s48, %[t0]\n\t"
        "s_nop 1\n\t"
        "s_add_i32 s48, s48, %c[nslot]\n\t"
        "s_and_b32 s48, s48, -2\n\t"
        "s_min_i32 s48, s48, s49\n\t"
        "s_cmp_lt_i32 s44, s48\n\t"
        "s_cbranch_scc1 22b\n\t"
        "v_mov_b32 %[t1], s44\n\t"
        "s_waitcnt vmcnt(0)\n\t"
        "ds_write_b32 %[pa], %[t1]\n\t"
        "s_sleep 1\n\t"
        "s_sub_u32 s55, s55, 1\n\t"
        "s_cmp_lg_u32 s55, 0\n\t"
        "s_cbranch_scc1 26b\n\t"
        "s_branch 22b\n"
        "30:\n\t"
        ".set k, 6\n\t"
        ".rept 7\n\t"
        ".set wcnt, 4 * k\n\t"
        "v_mov_b32 %[t1], 2 * %c[nku] - k\n\t"
        WAITW
        "ds_write_b32 %[pa], %[t1]\n\t"
        ".set k, k - 1\n\t"
        ".endr\n\t"
        DBG("s_memrealtime s[56:57]\n\ts_waitcnt lgkmcnt(0)\n\tv_mov_b32 %[t0], %c[dbg] + 4\n\tv_mov_b32 %[t1], s56\n\tds_write_b32 %[t0], %[t1]\n\t")
        "s_waitcnt lgkmcnt(0)\n\t"
        "s_endpgm\n\t"
        ".p2align 7\n"
        "23:\n\t"
        ".set wcnt, %c[wmin]-1\n\t"
        ".rept %c[wmax] - %c[wmin] + 1\n\t"
        UNIT4("%[vo]", "%[vb]")
        "ds_write_b32 %[pa], %[t1]\n\t"
        "s_mov_b32 m0, s55\n\t"
        UNIT4("%[t0]", "%[vc]")
        "s_branch 24b\n\t"
        ".p2align 7\n\t"
        ".set wcnt, wcnt+1\n\t"
        ".endr\n"
        "90:\n\t"
        "s_waitcnt lgkmcnt(0)"
        : [va] "=&v"(va), [vb] "=&v"(vb), [vc] "=&v"(vc2), [vo] "=&v"(vo), [pa] "=&v"(pa), [nxa] "=&v"(nxa), [t0] "=&v"(t0), [t1] "=&v"(t1), [t2] "=&v"(t2), [t3] "=&v"(t3)
        : [tid] "v"(threadIdx.x), [bid] "s"(blockIdx.x), [ka] "s"(__builtin_amdgcn_kernarg_segment_ptr()), [nch] "i"(CODE_KIB - 1), [jk] "i"(RING - UNIT),
          [spw] "i"(SPW), [wmin] "i"(WMIN), [wmax] "i"(WMAX), [gain] "i"(WGAIN), [wmid] "i"((int)(WMID * 256)), [tau2] "i"(TAU2),
          [ctl] "i"(L_CTRL), [nslot] "i"(NSLOT), [nku] "i"(NKU), [cap] "i"(SPIN_CAP), [dbg] "i"(LDS_BYTES), [jump] "i"(JUMP), [uend] "i"(UEND), [gain2] "i"(WGAIN2), [wmid2] "i"((int)(WMID2 * 256))
        : "memory", "scc", "s40", "s41", "s42", "s43", "s44", "s45", "s46", "s47", "s48", "s49", "s50", "s51", "s52", "s53", "s54", "s55", "s56", "s57", "s58", "s59", "s60", "s61", "s62", "s63", "s64", "s65", "s66");
  }
  __builtin_amdgcn_sched_barrier(0);
  const int tid = threadIdx.x, lane = tid & 63, wave = __builtin_amdgcn_readfirstlane(tid >> 6);
  const int xg = blockIdx.x & 7, kk = blockIdx.x >> 3;
  const int b = kk >> 3, pj = 8 * xg + (kk & 7), h = pj >> 1, s = pj & 1;
  const int r4 = lane >> 4, c16 = lane & 15;
  asm volatile("" : "+s"(x), "+s"(wq), "+s"(wo), "+s"(w1), "+s"(w3), "+s"(w2), "+s"(gw), "+s"(out), "+s"(ws));
  if (wave == 1) {
    const int wi = lane - 16;
    const int now = (int)(unsigned)__builtin_amdgcn_s_memrealtime() + REF0;
    ctrl[lane] = (lane == 1 || lane == 2) ? now : lane == 8 ? now - REF0 : (lane < 16 || lane >= 32) ? 0 : wi < NCW ? wi : wi < NCW + NHW ? NHC * NCW + HM * (wi - NCW) : NEVER;
  }
  BARRIER();

  const __amdgpu_buffer_rsrc_t wsr = mkrsrc(ws, WS_BYTES);
  auto wg_max = [&]() __attribute__((always_inline)) {
    float mg = ctrlf[32 + (c16 < NCW + NHW ? c16 : 0)];
    mg = fmaxf(mg, dpp<ROR8>(mg)); mg = fmaxf(mg, dpp<ROR4>(mg)); mg = fmaxf(mg, dpp<ROR2>(mg)); mg = fmaxf(mg, dpp<ROR1>(mg));
    return mg;
  };

  if (wave == XWAVE + 1) {
    unsigned k = ((unsigned)__builtin_amdgcn_s_memrealtime() >> SLOTSH) + 1;
#pragma unroll 1
    for (int it = 0; it < 4096 >> (SLOTSH - 2); ++it, ++k) {
      wait_until(k << SLOTSH);
      if (__builtin_amdgcn_readfirstlane(ctrl[0]) >= NUNIT - 36) break;
      const int own = __builtin_amdgcn_readfirstlane(ctrl[1]);
      constexpr unsigned SM = (8192u >> SLOTSH) - 1u;
      if (!NOPST && blockIdx.x < 16 && lane < 8) __builtin_amdgcn_raw_buffer_store_b128(u32x4{(unsigned)own, (unsigned)own, (unsigned)own, (unsigned)own}, wsr, WS_PACE + (k & SM) * 2048 + blockIdx.x * 128 + lane * 16, 0, PAUX);
      if (it < PLAG || NOPLD) continue;
      int e[16];
      asm volatile("s_load_dword %0, %16, 0x0\n\ts_load_dword %1, %16, 0x80\n\ts_load_dword %2, %16, 0x100\n\ts_load_dword %3, %16, 0x180\n\t"
                   "s_load_dword %4, %16, 0x200\n\ts_load_dword %5, %16, 0x280\n\ts_load_dword %6, %16, 0x300\n\ts_load_dword %7, %16, 0x380\n\t"
                   "s_load_dword %8, %16, 0x400\n\ts_load_dword %9, %16, 0x480\n\ts_load_dword %10, %16, 0x500\n\ts_load_dword %11, %16, 0x580\n\t"
                   "s_load_dword %12, %16, 0x600\n\ts_load_dword %13, %16, 0x680\n\ts_load_dword %14, %16, 0x700\n\ts_load_dword %15, %16, 0x780\n\t"
                   "s_waitcnt lgkmcnt(0)"
                   : "=&s"(e[0]), "=&s"(e[1]), "=&s"(e[2]), "=&s"(e[3]), "=&s"(e[4]), "=&s"(e[5]), "=&s"(e[6]), "=&s"(e[7]),
                     "=&s"(e[8]), "=&s"(e[9]), "=&s"(e[10]), "=&s"(e[11]), "=&s"(e[12]), "=&s"(e[13]), "=&s"(e[14]), "=&s"(e[15])
                   : "s"(ws + WS_PACE + ((k - PLAG) & SM) * 2048) : "memory");
      int sum = 0, cnt = 0;
#pragma unroll
      for (int i = 0; i < 16; ++i) { const int d = e[i] - own; const bool ok = (unsigned)(d + 600) < 1200u; sum += ok ? d : 0; cnt += ok ? 1 : 0; }
      if (cnt >= 8) ctrl[2] = own + (int)((float)sum / (float)cnt);
    }
    return;
  }

  if (wave == XWAVE) {
#pragma unroll 1
    for (int i = 0; i < XDEL + XSPR * (kk & 31); ++i) PAUSE(4);
    const u32x4 xv = *(const GAS u32x4*)(x + b * D + 8 * lane), gv = *(const GAS u32x4*)(gw + 8 * lane);
    *(LAS u32x4*)(L + L_X + lane * 16) = xv;
    float gd = 0.f;
#pragma unroll
    for (int k = 0; k < 4; ++k) gd = dot2(xv[k], gv[k], gd);
    gd = xrow_sum(row16_sum(gd));
    const float g = 1.f / (1.f + __expf(-gd));
    ctrlf[48] = g;
    lds_arrive(L, C_X, lane);
    u64 tag;
    asm volatile("s_load_dwordx2 %0, %1, 0x0\n\ts_waitcnt lgkmcnt(0)" : "=s"(tag) : "s"(ws + WS_EPOCH + b * 256) : "memory");
    tag += TAG_STEP;
#pragma unroll 1
    for (int spin = 0; spin < SPIN_CAP; ++spin) { if (__builtin_amdgcn_readfirstlane(ctrl[C_MAX]) >= NCW + NHW) break; __builtin_amdgcn_s_sleep(16); }
    CFENCE();
    const float mg = wg_max();
    float l = 0.f;
#pragma unroll 4
    for (int i = 0; i < NKU * 64 / 1024; ++i) {
      const f32x4 sc = *(const LAS f32x4*)(L + L_SC + i * 1024 + lane * 16);
      l += (expd(sc[0] - mg) + expd(sc[1] - mg)) + (expd(sc[2] - mg) + expd(sc[3] - mg));
    }
    l = xrow_sum(row16_sum(l));
    const unsigned tlo = (unsigned)tag, thi = (unsigned)(tag >> 32);
    if (lane < 8) __builtin_amdgcn_raw_buffer_store_b128(u32x4{__float_as_uint(mg), tlo, __float_as_uint(l), thi}, wsr, WS_REC + (b * 64 + pj) * 128 + lane * 16, 0, 16);
#pragma unroll 1
    for (int i = 0; i < 12 + (kk & 31); ++i) PAUSE(12);
    u32x4 rec;
#pragma unroll 1
    for (int spin = 0; spin < SPIN_CAP; ++spin) {
      CFENCE();
      rec = __builtin_amdgcn_raw_buffer_load_b128(wsr, WS_REC + (b * 64 + (pj ^ 1)) * 128, 0, 16);
      if (__builtin_amdgcn_ballot_w64(rec[1] != tlo || rec[3] != thi) == 0ull) break;
      __builtin_amdgcn_s_sleep(16);
    }
    const float mo = __uint_as_float(rec[0]), lo = __uint_as_float(rec[2]);
    const float mh = fmaxf(mg, mo), wt = expd(mg - mh);
    ctrlf[49] = g * wt / (wt * l + expd(mo - mh) * lo);
    const u32x4 yt = {tlo & YMASK, (tlo >> 12) & YMASK, (unsigned)(tag >> 24) & YMASK, (thi >> 4) & YMASK};
    *(LAS u32x4*)(L + L_CTRL + 52 * 4) = yt;
    lds_arrive(L, C_CF, lane);
    GAS unsigned* op = (GAS unsigned*)(out + b * D + 8 * pj + 4 * (lane & 1) + 2 * (lane >> 5));
    int off = WS_Y + (b * 64 + (lane >> 1)) * 2048 + pj * 32 + (lane & 1) * 16;
    asm volatile("" : "+v"(op), "+v"(off));
    if ((pj & (64 / NRED - 1)) != 0) return;
    STAMP(4);
    BARRIER();
    {
      const unsigned now = (unsigned)__builtin_amdgcn_s_memrealtime();
      const int d = __builtin_amdgcn_readfirstlane(ctrl[2]) + (int)(TS * 100) + FOFF - (int)now;
      const unsigned t = now + (unsigned)min(max(d, FMIN), FMAX);
#pragma unroll 1
      for (int spin = 0; spin < 4096 && (int)(t - (unsigned)__builtin_amdgcn_s_memrealtime()) > 0; ++spin) {}
    }
    STAMP(9);
    constexpr int NL = 128 / NRED;
    u32x4 yv[NL];
    int offs[NL];
#pragma unroll
    for (int i = 0; i < NL; ++i) { offs[i] = off; yv[i] = u32x4{0u, 0u, 0u, 0u}; }
#pragma unroll 1
    for (int spin = 0; spin < SPIN_CAP; ++spin) {
      CFENCE();
      unsigned bad = 0;
#pragma unroll
      for (int i = 0; i < NL; ++i) yv[i] = __builtin_amdgcn_raw_buffer_load_b128(wsr, off, i * (64 / NL) * 2048, 16);
#pragma unroll
      for (int i = 0; i < NL; ++i) bad |= ((yv[i][0] ^ yt[0]) | (yv[i][1] ^ yt[1])) | ((yv[i][2] ^ yt[2]) | (yv[i][3] ^ yt[3]));
      bad &= YMASK;
      if (__builtin_amdgcn_ballot_w64(bad != 0u) == 0ull) break;
      if (PSLP) __builtin_amdgcn_s_sleep(PSLP);
    }
    float o[4];
#pragma unroll
    for (int k = 0; k < 4; ++k) {
      float t[NL];
#pragma unroll
      for (int i = 0; i < NL; ++i) t[i] = __uint_as_float(yv[i][k] & ~YMASK);
#pragma unroll
      for (int st = NL / 2; st >= 1; st /= 2)
#pragma unroll
        for (int i = 0; i < st; ++i) t[i] += t[i + st];
      o[k] = t[0];
    }
    float e0 = swadd32(o[0], o[2]), e1 = swadd32(o[1], o[3]);
    e0 = swadd16(e0, e0); e1 = swadd16(e1, e1);
    e0 += dpp<ROR8>(e0); e1 += dpp<ROR8>(e1); e0 += dpp<ROR4>(e0); e1 += dpp<ROR4>(e1); e0 += dpp<ROR2>(e0); e1 += dpp<ROR2>(e1);
    if ((lane & 30) == 0) OUT_STORE(op, pack_bf16(e0, e1));
    if (pj == 0 && lane < 8) __builtin_amdgcn_raw_buffer_store_b128(u32x4{tlo, thi, tlo, thi}, wsr, WS_EPOCH + b * 256 + lane * 16, 0, 16);
    STAMP(8);
    STAMP_FLUSH();
    return;
  }

  const int cw = wave - 1, hw = wave - 1 - NCW;
  int landed = 0;
  auto wait_unit = [&](int u) __attribute__((always_inline)) {
#pragma unroll 1
    for (int spin = 0; landed <= u && spin < SPIN_CAP; ++spin) {
      landed = __builtin_amdgcn_readfirstlane(ctrl[0]);
      if (landed > u) break;
      if (USL) __builtin_amdgcn_s_sleep(USL);
    }
    CFENCE();
  };
  auto take = [&](int u, int next, u32x4& a0, u32x4& a1, u32x4& a2, u32x4& a3) __attribute__((always_inline)) {
    wait_unit(u);
    const LAS u32x4* sp = (const LAS u32x4*)(L + (u & (NSLOT - 1)) * UNIT + lane * 16);
    a0 = sp[0]; a1 = sp[64]; a2 = sp[128]; a3 = sp[192];
    LDS_WAIT();
    ctrl[16 + cw] = next;
    asm volatile("" : "+v"(a0), "+v"(a1), "+v"(a2), "+v"(a3) :: "memory");
  };
  u32x4 q;
  auto build_q = [&]() __attribute__((always_inline)) {
    lds_await(ctrl, C_Q, NHW);
    const LAS f32x4* qp = (const LAS f32x4*)(L + L_QP + (2 * r4 * DH + 8 * c16) * 4);
    const f32x4 lo = qp[0] + qp[DH / 4], hi = qp[1] + qp[DH / 4 + 1];
    q[0] = pack_bf16(xrow_sum(lo[0]), xrow_sum(lo[1])); q[1] = pack_bf16(xrow_sum(lo[2]), xrow_sum(lo[3]));
    q[2] = pack_bf16(xrow_sum(hi[0]), xrow_sum(hi[1])); q[3] = pack_bf16(xrow_sum(hi[2]), xrow_sum(hi[3]));
  };
  constexpr float SCALE = 0.088388347648318440f;
  float mx = -3.4028234e38f;
  auto score = [&](int u, const u32x4& k0, const u32x4& k1, const u32x4& k2, const u32x4& k3) __attribute__((always_inline)) {
    float d0 = 0.f, d1 = 0.f, d2 = 0.f, d3 = 0.f;
#pragma unroll
    for (int k = 0; k < 4; ++k) { d0 = dot2(k0[k], q[k], d0); d1 = dot2(k1[k], q[k], d1); d2 = dot2(k2[k], q[k], d2); d3 = dot2(k3[k], q[k], d3); }
    const unsigned s01 = pack_bf16(row16_sum(d0), row16_sum(d1)), s23 = pack_bf16(row16_sum(d2), row16_sum(d3));
    const unsigned t01 = pack_bf16(bflo(s01) * SCALE, bfhi(s01) * SCALE), t23 = pack_bf16(bflo(s23) * SCALE, bfhi(s23) * SCALE);
    const f32x4 sc = {bflo(t01), bfhi(t01), bflo(t23), bfhi(t23)};
    mx = fmaxf(fmaxf(mx, fmaxf(sc[0], sc[1])), fmaxf(sc[2], sc[3]));
    if (c16 == 0) *(LAS f32x4*)(L + L_SC + u * 64 + r4 * 16) = sc;
  };

  if (hw < 0) {
    {
      u32x4 hk[4 * NHC];
#pragma unroll
      for (int i = 0; i < NHC; ++i) take(cw + NCW * i, i + 1 < NHC ? cw + NCW * (i + 1) : NHELD + cw, hk[4 * i], hk[4 * i + 1], hk[4 * i + 2], hk[4 * i + 3]);
      STAMP(10);
      build_q();
      STAMP(3);
#pragma unroll
      for (int i = 0; i < NHC; ++i) { score(cw + NCW * i, hk[4 * i], hk[4 * i + 1], hk[4 * i + 2], hk[4 * i + 3]); __builtin_amdgcn_sched_barrier(0); }
    }
#pragma unroll 1
    for (int u = NHELD + cw; u < NKU; u += NCW) {
      u32x4 k0, k1, k2, k3;
      take(u, u + NCW, k0, k1, k2, k3);
      score(u, k0, k1, k2, k3);
    }
    ctrlf[32 + cw] = xrow_max(mx);
    lds_arrive(L, C_MAX, lane);
    STAMP(11);
    lds_await(ctrl, C_MAX, NCW + NHW);
    const float mg = wg_max();
    STAMP(12);
    float acc[8] = {0.f, 0.f, 0.f, 0.f, 0.f, 0.f, 0.f, 0.f};
    LAS unsigned* rp = (LAS unsigned*)(L + L_REC + cw * 272 + (8 * c16 + 2 * r4) * 2);
    asm volatile("" : "+v"(rp));
    float cf = 0.f;
#pragma unroll 1
    for (int u = NKU + cw; u < NUNIT; u += NCW) {
      const f32x4 sc = *(const LAS f32x4*)(L + L_SC + (u - NKU) * 64 + r4 * 16);
      float p[4];
#pragma unroll
      for (int t = 0; t < 4; ++t) { p[t] = expd(sc[t] - mg); asm volatile("" : "+v"(p[t])); }
      if ((u & ~(NCW - 1)) == NUNIT - 32) { lds_await(ctrl, C_CF, 1); cf = ctrlf[49]; }
      u32x4 v[4];
      take(u, u + NCW, v[0], v[1], v[2], v[3]);
#pragma unroll
      for (int t = 0; t < 4; ++t)
#pragma unroll
        for (int k = 0; k < 4; ++k) { acc[2 * k] = fmaf(p[t], bflo(v[t][k]), acc[2 * k]); acc[2 * k + 1] = fmaf(p[t], bfhi(v[t][k]), acc[2 * k + 1]); }
    }
    STAMP(5);
    const float e0 = swadd16(swadd32(acc[0], acc[4]), swadd32(acc[2], acc[6])), e1 = swadd16(swadd32(acc[1], acc[5]), swadd32(acc[3], acc[7]));
    *rp = pack_bf16(e0 * cf, e1 * cf);
    BARRIER();
    return;
  }

  u32x4 wb[16];
  const unsigned tstart = (unsigned)__builtin_amdgcn_readfirstlane(ctrl[8]);
  {
    const __amdgpu_buffer_rsrc_t rq = mkrsrc(wq, D * H * DH * 2);
    const int vo = ((64 * hw + 2 * r4) * (H * DH) + h * DH + 8 * c16) * 2;
#define QR(i) (8 * ((i) >> 1) + ((i) & 1))
#pragma unroll
    for (int i = 0; i < 16; ++i) { if (i == 0) wait_until(tstart + TQ + hw * QA); else PAUSE(QS); wb[i] = __builtin_amdgcn_raw_buffer_load_b128(rq, vo, QR(i) * (H * DH * 2), QAUX); }
    lds_await(ctrl, C_X, 1);
    const LAS unsigned* xl = (const LAS unsigned*)(L + L_X + (QROW == 1 ? 64 * hw + 2 * r4 : QROW == 2 ? 64 * hw + 4 * r4 : QROW == 3 ? 16 * hw + 128 * r4 : 64 * hw + 16 * r4) * 2);
    float a[8] = {0.f, 0.f, 0.f, 0.f, 0.f, 0.f, 0.f, 0.f};
#pragma unroll
    for (int i = 0; i < 8; ++i) {
      const unsigned xp = xl[QR(2 * i) / 2];
#pragma unroll
      for (int k = 0; k < 4; ++k) {
        a[2 * k] = dot2(perm_lo(wb[2 * i][k], wb[2 * i + 1][k]), xp, a[2 * k]);
        a[2 * k + 1] = dot2(perm_hi(wb[2 * i][k], wb[2 * i + 1][k]), xp, a[2 * k + 1]);
      }
    }
    const float e0 = swadd16(swadd32(a[0], a[4]), swadd32(a[2], a[6])), e1 = swadd16(swadd32(a[1], a[5]), swadd32(a[3], a[7]));
    *(LAS f32x2*)(L + L_QP + (hw * DH + 8 * c16 + 2 * r4) * 4) = f32x2{e0, e1};
    lds_arrive(L, C_Q, lane);
    STAMP(2);
    const int hb = NHC * NCW + HM * hw;
#pragma unroll
    for (int i = 0; i < NHH; ++i) take(hb + HS * i, i + 1 < NHH ? hb + HS * (i + 1) : NEVER, wb[4 * i], wb[4 * i + 1], wb[4 * i + 2], wb[4 * i + 3]);
    STAMP(10);
    build_q();
#pragma unroll
    for (int i = 0; i < NHH; ++i) { score(hb + HS * i, wb[4 * i], wb[4 * i + 1], wb[4 * i + 2], wb[4 * i + 3]); __builtin_amdgcn_sched_barrier(0); }
    ctrlf[32 + cw] = xrow_max(mx);
    lds_arrive(L, C_MAX, lane);
  }
  f32x4 y[4] = {{0.f, 0.f, 0.f, 0.f}, {0.f, 0.f, 0.f, 0.f}, {0.f, 0.f, 0.f, 0.f}, {0.f, 0.f, 0.f, 0.f}};
  if ((pj & 7) != 7) {
    const int sl = 7 * (pj >> 3) + (pj & 7);
    const __amdgpu_buffer_rsrc_t r1 = mkrsrc(w1, D * F * 2), r3 = mkrsrc(w3, D * F * 2), r2 = mkrsrc(w2, F * D * 2);
    const int vo = ((64 * hw + (lane & 60)) * F + FS * sl + 8 * (lane & 3)) * 2;
#pragma unroll
    for (int i = 0; i < 8; ++i) { if (i == 0) wait_until(tstart + T1 + hw * A1); else PAUSE(S1); wb[i] = ldw(i < 4 ? r1 : r3, vo, (i & 3) * (F * 2)); }
    const u32x2 xm = *(const LAS u32x2*)(L + L_X + (64 * hw + (lane & 60)) * 2);
    float a[16];
#pragma unroll
    for (int m = 0; m < 2; ++m)
#pragma unroll
      for (int k = 0; k < 4; ++k) {
        a[8 * m + 2 * k] = dot2(perm_lo(wb[4 * m + 2][k], wb[4 * m + 3][k]), xm[1], dot2(perm_lo(wb[4 * m][k], wb[4 * m + 1][k]), xm[0], 0.f));
        a[8 * m + 2 * k + 1] = dot2(perm_hi(wb[4 * m + 2][k], wb[4 * m + 3][k]), xm[1], dot2(perm_hi(wb[4 * m][k], wb[4 * m + 1][k]), xm[0], 0.f));
      }
    f32x4 e;
#pragma unroll
    for (int k = 0; k < 4; ++k) {
      float t = swadd16(swadd32(a[k], a[8 + k]), swadd32(a[4 + k], a[12 + k]));
      t += dpp<ROR8>(t); e[k] = t + dpp<ROR4>(t);
    }
    if ((lane & 12) == 0) *(LAS f32x4*)(L + L_MP + (hw * 64 + (lane & 32) + 8 * (lane & 3) + 4 * (r4 & 1)) * 4) = e;
    lds_arrive(L, C_MLP, lane);
    u32x2 wr[8];
#pragma unroll
    for (int i = 0; i < 8; ++i) { if (i == 0) wait_until(tstart + T2 + hw * A2); else PAUSE(S2); wr[i] = ldw2(r2, ((FS * sl + 8 * r4) * D + 64 * hw + 4 * c16) * 2, i * (D * 2)); }
    lds_await(ctrl, C_MLP, NHW);
    const LAS float* mp = (const LAS float*)(L + L_MP) + (lane & 31);
    float ga = 0.f, up = 0.f;
#pragma unroll
    for (int w = 0; w < NHW; ++w) { ga += mp[w * 64]; up += mp[w * 64 + 32]; }
    const float hv = ga / (1.f + __expf(-ga)) * up * (1.f - ctrlf[48]);
    const unsigned hh = pack_bf16(hv, 0.f), hl = pack_bf16(hv - bflo(hh), 0.f);
    *(LAS u16*)(L + L_H + hw * 128 + lane * 2) = (u16)(lane < 32 ? hh : hl);
    LDS_WAIT();
    u32x4 ha = *(const LAS u32x4*)(L + L_H + hw * 128 + (c16 & 1) * 64 + r4 * 16);
    if (c16 >= 2) ha = u32x4{0u, 0u, 0u, 0u};
#pragma unroll
    for (int e = 0; e < 4; ++e) {
      u32x4 f;
#pragma unroll
      for (int k = 0; k < 4; ++k) f[k] = (e & 1) ? perm_hi(wr[2 * k][e >> 1], wr[2 * k + 1][e >> 1]) : perm_lo(wr[2 * k][e >> 1], wr[2 * k + 1][e >> 1]);
      y[e] = mfma(ha, f, y[e]);
    }
  }
#pragma unroll
  for (int e = 0; e < 4; ++e) asm volatile("" : "+v"(y[e]));
  {
    const __amdgpu_buffer_rsrc_t ro = mkrsrc(wo, H * DH * D * 2);
#pragma unroll
    for (int t = 0; t < 4; ++t) {
      u32x2 wr[8];
#pragma unroll
      for (int i = 0; i < 8; ++i) { if (t + i == 0) wait_until(tstart + TO + hw * AO); else PAUSE(SO); wr[i] = ldw2(ro, ((h * DH + 32 * r4) * D + 64 * hw + 4 * c16) * 2, (8 * t + i) * (D * 2)); }
#pragma unroll
      for (int e = 0; e < 4; ++e) {
#pragma unroll
        for (int k = 0; k < 4; ++k) wb[4 * t + e][k] = (e & 1) ? perm_hi(wr[2 * k][e >> 1], wr[2 * k + 1][e >> 1]) : perm_lo(wr[2 * k][e >> 1], wr[2 * k + 1][e >> 1]);
        asm volatile("" : "+v"(wb[4 * t + e]));
      }
    }
  }
  lds_await(ctrl, C_CF, 1);
  u32x4 yt = *(const LAS u32x4*)(L + L_CTRL + 52 * 4);
  int yo = WS_Y + (b * 64 + pj) * 2048 + hw * 256 + c16 * 16;
  const LAS u32x4* rec = (const LAS u32x4*)(L + L_REC + (lane & 3) * 272 + r4 * 64);
  unsigned ym = ~YMASK;
  asm volatile("" : "+v"(yt), "+v"(yo), "+v"(rec), "+s"(ym));
  STAMP(4);
  BARRIER();
  STAMP(9);
  {
    const u32x4 a[4] = {rec[0], rec[1], rec[2], rec[3]};
#pragma unroll
    for (int t = 0; t < 4; ++t)
#pragma unroll
      for (int e = 0; e < 4; ++e) y[e] = mfma(a[t], wb[4 * t + e], y[e]);
    u32x4 v;
#pragma unroll
    for (int e = 0; e < 4; ++e) {
      const f32x2 t = y[e].xy + y[e].zw;
      unsigned r;
      asm("v_add_f32 %0, %1, %2\n\tv_add_u32 %0, %5, %0\n\tv_and_or_b32 %0, %0, %3, %4" : "=&v"(r) : "v"(t[0]), "v"(t[1]), "s"(ym), "v"(yt[e]), "i"((YMASK + 1u) / 2u));
      v[e] = r;
    }
    if (lane < 16) __builtin_amdgcn_raw_buffer_store_b128(v, wsr, yo, 0, 16);
  }
  STAMP(7);
}

extern "C" void kernel_launch(void* const* d_in, const int* in_sizes, int n_in, void* d_out, int out_size, void* d_ws, size_t ws_size,
                              hipStream_t stream) {
  gated_mix_kernel<<<256, NTHR, 0, stream>>>((const u16*)d_in[0], (const u16*)d_in[1], (const u16*)d_in[2], (const u16*)d_in[3], (const u16*)d_in[4],
                                             (const u16*)d_in[6], (const u16*)d_in[7], (const u16*)d_in[8], (const u16*)d_in[9], (u16*)d_out,
                                             (unsigned char*)d_ws);
}
```

```cpp
#include <hip/hip_runtime.h>
#include <hip/hip_bf16.h>

#define GAS __attribute__((address_space(1)))
#define LAS __attribute__((address_space(3)))
typedef unsigned short u16;
typedef unsigned long long u64;
using u32x4 = __attribute__((ext_vector_type(4))) unsigned;
using u32x2 = __attribute__((ext_vector_type(2))) unsigned;
using f32x4 = __attribute__((ext_vector_type(4))) float;
using f32x2 = __attribute__((ext_vector_type(2))) float;
using bf16x2 = __attribute__((ext_vector_type(2))) __bf16;
using bf16x8 = __attribute__((ext_vector_type(8))) __bf16;

constexpr int B = 4, D = 512, H = 32, SK = 8192, DH = 128, F = 1792;
constexpr int NCW = 4, NHW = 8, XWAVE = 1 + NCW + NHW, NWAVE = XWAVE + 2, NTHR = NWAVE * 64;
constexpr int TQ = 100, QA = 112, QS = 2;
constexpr int T1 = 1400, A1 = 150, S1 = 6;
constexpr int T2 = 2500, A2 = 40, S2 = 1;
constexpr int TO = 2900, AO = 350, SO = 2;
constexpr int TSTREAM = 6860;
constexpr int WMID = 5472, WMIN = 16, WMAX = 28;
constexpr int WGAIN = 10, WGAIN2 = 18, UEND = 420;
constexpr int JUMP = 100, DECAY = 6;
constexpr int REF0 = 180;
constexpr int FOFF = 156, FMIN = 45, FMAX = 150;
constexpr int CODE_KIB = 21;

constexpr int UNIT = 4096;
constexpr int NSLOT = 32, RING = NSLOT * UNIT;
constexpr int NKU = (SK / 2) / 16, NUNIT = 2 * NKU;
constexpr int NHC = 6, NHH = 4, NHELD = NHC * NCW + NHH * NHW;
constexpr int FS = 32;
constexpr int L_CTRL = RING;
constexpr int L_X = L_CTRL + 256;
constexpr int L_QP = L_X + D * 2;
constexpr int L_MP = L_QP + NHW * DH * 4;
constexpr int L_REC = L_MP + NHW * 64 * 4;
constexpr int L_H = L_REC + NCW * 272;
constexpr int L_SC = L_H + NHW * 128;
constexpr int LDS_BYTES = L_SC + NKU * 64;
static_assert(LDS_BYTES <= 159 * 1024, "lds");
static_assert(WMIN <= WMID / 256 && WMID / 256 <= WMAX && WMAX <= 28, "window");
constexpr int WS_EPOCH = 0;
constexpr int WS_REC = 4096;
constexpr int WS_PACE = WS_REC + 256 * 128;
constexpr int WS_Y = WS_PACE + 32 * 2048;
constexpr int WS_BYTES = WS_Y + B * 64 * 2048;
constexpr u64 TAG_STEP = 0x9E3779B97F4A7C15ull;
constexpr unsigned YMASK = 0xFFFu;
constexpr int NT = 2, SC1 = 16;
constexpr int SPIN_CAP = 1 << 22;
constexpr int NEVER = 1 << 20;
constexpr int C_Q = 4, C_MAX = 5, C_MLP = 6, C_X = 7, C_CF = 9;

__device__ __forceinline__ float expd(float d) { return __builtin_amdgcn_exp2f(d * 1.4426950408889634f); }
__device__ __forceinline__ float bflo(unsigned u) { return __builtin_bit_cast(float, u << 16); }
__device__ __forceinline__ float bfhi(unsigned u) { return __builtin_bit_cast(float, u & 0xffff0000u); }
__device__ __forceinline__ float dot2(unsigned a, unsigned b, float acc) {
  return __builtin_amdgcn_fdot2_f32_bf16(__builtin_bit_cast(bf16x2, a), __builtin_bit_cast(bf16x2, b), acc, false);
}
__device__ __forceinline__ unsigned pack_bf16(float lo, float hi) {
  const bf16x2 v = {(__bf16)lo, (__bf16)hi};
  return __builtin_bit_cast(unsigned, v);
}
__device__ __forceinline__ unsigned perm_lo(unsigned a, unsigned b) { return __builtin_amdgcn_perm(b, a, 0x05040100u); }
__device__ __forceinline__ unsigned perm_hi(unsigned a, unsigned b) { return __builtin_amdgcn_perm(b, a, 0x07060302u); }
template <int CTRL> __device__ __forceinline__ float dpp(float x) {
  return __builtin_bit_cast(float, __builtin_amdgcn_mov_dpp(__builtin_bit_cast(int, x), CTRL, 0xf, 0xf, true));
}
constexpr int ROR1 = 0x121, ROR2 = 0x122, ROR4 = 0x124, ROR8 = 0x128;
__device__ __forceinline__ float row16_sum(float x) { x += dpp<ROR8>(x); x += dpp<ROR4>(x); x += dpp<ROR2>(x); x += dpp<ROR1>(x); return x; }
__device__ __forceinline__ float swadd32(float a, float b) {
  auto t = __builtin_amdgcn_permlane32_swap(__float_as_uint(a), __float_as_uint(b), false, false);
  return __uint_as_float(t[0]) + __uint_as_float(t[1]);
}
__device__ __forceinline__ float swadd16(float a, float b) {
  auto t = __builtin_amdgcn_permlane16_swap(__float_as_uint(a), __float_as_uint(b), false, false);
  return __uint_as_float(t[0]) + __uint_as_float(t[1]);
}
__device__ __forceinline__ float xrow_sum(float x) { x = swadd16(x, x); return swadd32(x, x); }
__device__ __forceinline__ float xrow_max(float x) {
  auto s = __builtin_amdgcn_permlane16_swap(__float_as_uint(x), __float_as_uint(x), false, false);
  x = fmaxf(__uint_as_float(s[0]), __uint_as_float(s[1]));
  auto t = __builtin_amdgcn_permlane32_swap(__float_as_uint(x), __float_as_uint(x), false, false);
  return fmaxf(__uint_as_float(t[0]), __uint_as_float(t[1]));
}
__device__ __forceinline__ __amdgpu_buffer_rsrc_t mkrsrc(const void* p, int bytes) { return __builtin_amdgcn_make_buffer_rsrc((void*)p, 0, bytes, 0x00020000); }
__device__ __forceinline__ u32x4 ldw(__amdgpu_buffer_rsrc_t r, int voff, int soff) { return __builtin_amdgcn_raw_buffer_load_b128(r, voff, soff, NT); }
#define LDS_WAIT() asm volatile("s_waitcnt lgkmcnt(0)" ::: "memory")
#define CFENCE() asm volatile("" ::: "memory")
#define BARRIER() asm volatile("s_waitcnt lgkmcnt(0)\n\ts_barrier" ::: "memory")
#define PAUSE(n) do { __builtin_amdgcn_sched_barrier(0); asm volatile("s_sleep %0" :: "i"(n) : "memory"); __builtin_amdgcn_sched_barrier(0); } while (0)

__device__ __forceinline__ void lds_arrive(LAS unsigned char* L, int idx, int lane) {
  LDS_WAIT();
  if (lane == 0) __hip_atomic_fetch_add((LAS int*)(L + L_CTRL) + idx, 1, __ATOMIC_RELAXED, __HIP_MEMORY_SCOPE_WORKGROUP);
}
__device__ __forceinline__ void lds_await(volatile LAS int* ctrl, int idx, int n) {
#pragma unroll 1
  for (int spin = 0; spin < SPIN_CAP; ++spin) { if (__builtin_amdgcn_readfirstlane(ctrl[idx]) >= n) break; __builtin_amdgcn_s_sleep(1); }
  CFENCE();
}

__device__ __forceinline__ void wait_until(unsigned t) {
  __builtin_amdgcn_sched_barrier(0);
#pragma unroll 1
  for (int spin = 0; spin < 4096; ++spin) {
    const int d = (int)(t - (unsigned)__builtin_amdgcn_s_memrealtime());
    if (d <= 0) break;
    if (d > 24) __builtin_amdgcn_s_sleep(6);
  }
  CFENCE();
  __builtin_amdgcn_sched_barrier(0);
}
constexpr int TAU2 = 2 * (TSTREAM * 256 / NUNIT);
__device__ __forceinline__ f32x4 mfma(const u32x4& a, const u32x4& b, const f32x4& c) {
  return __builtin_amdgcn_mfma_f32_16x16x32_bf16(__builtin_bit_cast(bf16x8, a), __builtin_bit_cast(bf16x8, b), c, 0, 0, 0);
}
__device__ __forceinline__ u32x2 ldw2(__amdgpu_buffer_rsrc_t r, int voff, int soff) { return __builtin_amdgcn_raw_buffer_load_b64(r, voff, soff, NT); }

__global__ void __launch_bounds__(NTHR, 4) gated_mix__mha_block_cached__glu_gated_swiglu__decode_kernel(const u16* x, const u16* kc, const u16* vc, const u16* wq, const u16* wo, const u16* w1,
                                                         const u16* w3, const u16* w2, const u16* gw, u16* out, unsigned char* ws) {
  __shared__ __attribute__((aligned(16))) unsigned char lds_raw[LDS_BYTES];
  LAS unsigned char* L = (LAS unsigned char*)lds_raw;
  volatile LAS int* ctrl = (volatile LAS int*)(L + L_CTRL);
  volatile LAS float* ctrlf = (volatile LAS float*)(L + L_CTRL);
  {
    unsigned va, vb, vo, pa, nxa, t0, t1, t2, t3;
#define WAITW "s_waitcnt (wcnt & 15) | ((wcnt >> 4) << 14) | 0x0F70\n\t"
#define SP(v, sb, wait) wait "global_load_lds_dword " v ", " sb " nt\n\tglobal_load_lds_dword " v ", " sb " sc1\n\t"
#define SW "s_waitcnt vmcnt(4)\n\t"
#define UNIT4(v) \
        WAITW "global_load_lds_dwordx4 " v ", s[40:41] nt\n\t" \
        WAITW "global_load_lds_dwordx4 " v ", s[40:41] offset:1024 nt\n\t" \
        WAITW "global_load_lds_dwordx4 " v ", s[40:41] offset:2048 nt\n\t" \
        WAITW "global_load_lds_dwordx4 " v ", s[40:41] offset:3072 nt\n\t"
    asm volatile(
        "s_getpc_b64 s[56:57]\n\t"
        "s_load_dwordx8 s[44:51], %[ka], 0x18\n\t"
        "s_load_dwordx2 s[52:53], %[ka], 0x38\n\t"
        "s_load_dwordx4 s[40:43], %[ka], 0x8\n\t"
        "v_readfirstlane_b32 s54, %[tid]\n\t"
        "s_mov_b32 m0, %c[jk]\n\t"
        "s_lshr_b32 s59, %[bid], 3\n\t"
        "s_cmp_ge_u32 s54, 64\n\t"
        "s_cbranch_scc1 90f\n\t"
        "s_min_u32 s54, s59, %c[nch]\n\t"
        "s_lshl_b32 s54, s54, 10\n\t"
        "v_lshlrev_b32 %[vo], 4, %[tid]\n\t"
        "s_and_b32 s56, s56, -128\n\t"
        "v_add_u32 %[t0], s54, %[vo]\n\t"
        "s_and_b32 s58, %[bid], 7\n\t"
        "global_load_lds_dwordx4 %[t0], s[56:57]\n\t"
        "s_setprio 3\n\t"
        "v_lshlrev_b32 %[t1], 7, %[tid]\n\t"
        "s_lshl_b32 s54, s59, 7\n\t"
        "s_add_u32 s54, s54, s58\n\t"
        "s_lshl_b32 s54, s54, 10\n\t"
        "v_lshrrev_b32 %[va], 3, %[tid]\n\t"
        "v_mul_u32_u24 %[va], 8192 - 1024, %[va]\n\t"
        "v_add3_u32 %[va], %[va], %[t1], s54\n\t"
        "v_add_u32 %[t2], 0x10000, %[va]\n\t"
        "s_lshl_b32 s55, s58, 5\n\t"
        "s_add_u32 s55, s55, s59\n\t"
        "s_mul_i32 s54, s55, 7 * 1024\n\t"
        "v_min_u32 %[t3], 55 * 128, %[t1]\n\t"
        "v_add_u32 %[t3], s54, %[t3]\n\t"
        "s_mul_i32 s54, s58, 448\n\t"
        "s_and_b32 s54, s54, -128\n\t"
        "s_mul_i32 s57, s59, 16 * 3584\n\t"
        "s_add_u32 s54, s54, s57\n\t"
        "v_lshrrev_b32 %[t0], 2, %[tid]\n\t"
        "v_mul_u32_u24 %[t0], 3584 - 512, %[t0]\n\t"
        "v_add3_u32 %[t0], %[t0], %[t1], s54\n\t"
        "v_lshl_add_u32 %[vb], s55, 14, %[t1]\n\t"
        "v_add_u32 %[t1], 0x2000, %[vb]\n\t"
        "s_and_b32 s54, %[bid], 0xc0\n\t"
        "s_lshl3_add_u32 s54, s58, s54\n\t"
        "s_and_b32 s56, s59, 7\n\t"
        "s_add_u32 s54, s54, s56\n\t"
        "s_lshr_b32 s56, s54, 1\n\t"
        "s_lshl_b32 s56, s56, 21\n\t"
        "s_and_b32 s64, s54, 1\n\t"
        "s_bfe_u32 s57, s54, 0x10007\n\t"
        "s_lshl2_add_u32 s64, s57, s64\n\t"
        "s_bfe_u32 s57, s54, 0x10002\n\t"
        "s_lshl4_add_u32 s64, s57, s64\n\t"
        "s_lshl_b32 s64, s64, 16\n\t"
        "s_waitcnt lgkmcnt(0)\n\t"
        SP("%[va]", "s[44:45]", "") SP("%[t2]", "s[44:45]", "") SP("%[t3]", "s[52:53]", "")
        "s_barrier\n\t"
        SP("%[t0]", "s[48:49]", SW) SP("%[t0]", "s[50:51]", SW) SP("%[vb]", "s[46:47]", SW) SP("%[t1]", "s[46:47]", SW)
        "s_add_u32 s40, s40, s56\n\t"
        "s_addc_u32 s41, s41, 0\n\t"
        "s_add_u32 s42, s42, s56\n\t"
        "s_addc_u32 s43, s43, 0\n\t"
        "v_mov_b32 %[va], %[vo]\n\t"
        "v_or_b32 %[vo], s64, %[vo]\n\t"
        ".set so, 0\n\t"
        ".set wcnt, 4\n\t"
        ".rept 4\n\t"
        "s_movk_i32 m0, so\n\t"
        ".if (so & 4096) == 0\n\t"
        WAITW
        ".set wcnt, wcnt + 6\n\t"
        ".else\n\t"
        "s_nop 0\n\t"
        ".endif\n\t"
        "global_load_lds_dwordx4 %[vo], s[40:41] nt\n\t"
        "global_load_lds_dwordx4 %[vo], s[40:41] offset:1024 nt\n\t"
        "global_load_lds_dwordx4 %[vo], s[40:41] offset:2048 nt\n\t"
        "global_load_lds_dwordx4 %[vo], s[40:41] offset:3072 nt\n\t"
        "v_add_u32 %[vo], 0x1000, %[vo]\n\t"
        ".set so, so + 4096\n\t"
        ".endr\n\t"
        "s_waitcnt vmcnt(16)\n\t"
        "s_getpc_b64 s[50:51]\n"
        "20:\n\t"
        "s_add_u32 s50, s50, 23f-20b-%c[wmin]*128\n\t"
        "s_addc_u32 s51, s51, -1\n\t"
        "v_mov_b32 %[pa], %c[ctl]\n\t"
        "v_and_b32 %[nxa], 0x3c0 >> 2, %[vo]\n\t"
        "v_lshrrev_b32 %[nxa], 2, %[nxa]\n\t"
        "v_add_u32 %[nxa], %c[ctl] + 64, %[nxa]\n\t"
        "s_memrealtime s[58:59]\n\t"
        "s_mov_b32 s62, 0\n\t"
        "s_mov_b32 s63, %c[gain]\n\t"
        "s_mov_b32 s44, 4\n\t"
        "s_mov_b32 s45, %c[wmid] >> 8\n\t"
        "s_mov_b32 s46, 0x4000\n\t"
        "s_mov_b32 s47, 2 * %c[tau2]\n\t"
        "s_mov_b32 s48, %c[nslot]\n\t"
        "s_mov_b32 s49, %c[nku]\n\t"
        "s_waitcnt lgkmcnt(0)\n\t"
        "s_mov_b32 s59, 10000\n"
        "22:\n\t"
        "s_memrealtime s[56:57]\n\t"
        "ds_read_b32 %[t3], %[pa] offset:8\n\t"
        "s_lshl_b32 s52, s45, 7\n\t"
        "s_add_u32 s52, s50, s52\n\t"
        "s_addc_u32 s53, s51, 0\n\t"
        "s_add_i32 s54, s45, -1\n\t"
        "s_lshr_b32 s54, s54, 2\n\t"
        "s_sub_i32 s54, s44, s54\n\t"
        "v_mov_b32 %[t1], s54\n\t"
        "v_add_u32 %[t0], 0x1000, %[vo]\n\t"
        "s_add_u32 s55, s46, 0x1000\n\t"
        "s_mov_b32 m0, s46\n\t"
        "s_setpc_b64 s[52:53]\n"
        "24:\n\t"
        "s_waitcnt lgkmcnt(0)\n\t"
        "v_readfirstlane_b32 s57, %[t3]\n\t"
        "v_add_u32 %[t1], 1, %[t1]\n\t"
        "s_lshr_b32 s54, s47, 8\n\t"
        "s_sub_u32 s56, s56, s54\n\t"
        "v_mov_b32 %[t2], s56\n\t"
        "s_sub_i32 s54, s56, s58\n\t"
        "s_mov_b32 s58, s56\n\t"
        "s_sub_i32 s56, s56, s57\n\t"
        "ds_write2_b32 %[pa], %[t1], %[t2] offset1:1\n\t"
        "s_add_i32 s59, s59, s54\n\t"
        "s_add_i32 s59, s59, %c[decay]\n\t"
        "s_cmp_gt_i32 s54, %c[jump]\n\t"
        "s_cselect_b32 s59, s62, s59\n\t"
        "s_min_i32 s62, s56, s59\n\t"
        "s_mul_i32 s56, s62, s63\n\t"
        "s_add_i32 s56, s56, %c[wmid]\n\t"
        "s_ashr_i32 s56, s56, 8\n\t"
        "s_max_i32 s56, s56, %c[wmin]\n\t"
        "s_min_i32 s45, s56, %c[wmax]\n\t"
        "s_add_u32 s47, s47, %c[tau2]\n\t"
        "s_add_u32 s46, s46, 0x2000\n\t"
        "s_and_b32 s46, s46, %c[nslot] * 4096 - 1\n\t"
        "s_add_i32 s44, s44, 2\n\t"
        "s_and_b32 s54, s44, 0xff\n\t"
        "s_and_b32 s55, s44, 0xf0\n\t"
        "s_add_u32 s54, s54, s55\n\t"
        "s_lshl_b32 s54, s54, 12\n\t"
        "s_xor_b32 s54, s54, s64\n\t"
        "v_or_b32 %[vo], s54, %[va]\n\t"
        "s_cmp_lt_i32 s44, s48\n\t"
        "s_cbranch_scc1 22b\n\t"
        "s_cmp_lt_i32 s44, s49\n\t"
        "s_cbranch_scc1 25f\n\t"
        "s_cmp_ge_i32 s44, 2 * %c[nku]\n\t"
        "s_cbranch_scc1 30f\n\t"
        "s_mov_b32 s49, 2 * %c[nku]\n\t"
        "s_mov_b64 s[40:41], s[42:43]\n"
        "25:\n\t"
        "s_cmp_ge_i32 s44, %c[uend]\n\t"
        "s_cselect_b32 s63, %c[gain2], s63\n\t"
        "s_mov_b32 s55, %c[cap]\n"
        "26:\n\t"
        "ds_read_b32 %[t0], %[nxa]\n\t"
        "s_waitcnt lgkmcnt(0)\n\t"
        "s_nop 1\n\t"
        "v_min_i32_dpp %[t0], %[t0], %[t0] row_ror:8 row_mask:0xf bank_mask:0xf\n\t"
        "s_nop 1\n\t"
        "v_min_i32_dpp %[t0], %[t0], %[t0] row_ror:4 row_mask:0xf bank_mask:0xf\n\t"
        "s_nop 1\n\t"
        "v_min_i32_dpp %[t0], %[t0], %[t0] row_ror:2 row_mask:0xf bank_mask:0xf\n\t"
        "s_nop 1\n\t"
        "v_min_i32_dpp %[t0], %[t0], %[t0] row_ror:1 row_mask:0xf bank_mask:0xf\n\t"
        "s_nop 1\n\t"
        "v_readfirstlane_b32 s48, %[t0]\n\t"
        "s_nop 1\n\t"
        "s_add_i32 s48, s48, %c[nslot]\n\t"
        "s_and_b32 s48, s48, -2\n\t"
        "s_min_i32 s48, s48, s49\n\t"
        "s_cmp_lt_i32 s44, s48\n\t"
        "s_cbranch_scc1 22b\n\t"
        "v_mov_b32 %[t1], s44\n\t"
        "s_waitcnt vmcnt(0)\n\t"
        "ds_write_b32 %[pa], %[t1]\n\t"
        "s_sleep 1\n\t"
        "s_sub_u32 s55, s55, 1\n\t"
        "s_cmp_lg_u32 s55, 0\n\t"
        "s_cbranch_scc1 26b\n\t"
        "s_branch 22b\n"
        "30:\n\t"
        ".set k, 6\n\t"
        ".rept 7\n\t"
        ".set wcnt, 4 * k\n\t"
        "v_mov_b32 %[t1], 2 * %c[nku] - k\n\t"
        WAITW
        "ds_write_b32 %[pa], %[t1]\n\t"
        ".set k, k - 1\n\t"
        ".endr\n\t"
        "s_waitcnt lgkmcnt(0)\n\t"
        "s_endpgm\n\t"
        ".p2align 7\n"
        "23:\n\t"
        ".set wcnt, %c[wmin]-1\n\t"
        ".rept %c[wmax] - %c[wmin] + 1\n\t"
        UNIT4("%[vo]")
        "ds_write_b32 %[pa], %[t1]\n\t"
        "s_mov_b32 m0, s55\n\t"
        UNIT4("%[t0]")
        "s_branch 24b\n\t"
        ".p2align 7\n\t"
        ".set wcnt, wcnt+1\n\t"
        ".endr\n"
        "90:\n\t"
        "s_waitcnt lgkmcnt(0)"
        : [va] "=&v"(va), [vb] "=&v"(vb), [vo] "=&v"(vo), [pa] "=&v"(pa), [nxa] "=&v"(nxa), [t0] "=&v"(t0), [t1] "=&v"(t1), [t2] "=&v"(t2), [t3] "=&v"(t3)
        : [tid] "v"(threadIdx.x), [bid] "s"(blockIdx.x), [ka] "s"(__builtin_amdgcn_kernarg_segment_ptr()), [nch] "i"(CODE_KIB - 1), [jk] "i"(RING - UNIT),
          [wmin] "i"(WMIN), [wmax] "i"(WMAX), [gain] "i"(WGAIN), [wmid] "i"(WMID), [tau2] "i"(TAU2),
          [ctl] "i"(L_CTRL), [nslot] "i"(NSLOT), [nku] "i"(NKU), [cap] "i"(SPIN_CAP), [jump] "i"(JUMP), [decay] "i"(DECAY), [uend] "i"(UEND), [gain2] "i"(WGAIN2)
        : "memory", "scc", "s40", "s41", "s42", "s43", "s44", "s45", "s46", "s47", "s48", "s49", "s50", "s51", "s52", "s53", "s54", "s55", "s56", "s57", "s58", "s59", "s62", "s63", "s64");
  }
  __builtin_amdgcn_sched_barrier(0);
  const int tid = threadIdx.x, lane = tid & 63, wave = __builtin_amdgcn_readfirstlane(tid >> 6);
  const int xg = blockIdx.x & 7, kk = blockIdx.x >> 3;
  const int b = kk >> 3, pj = 8 * xg + (kk & 7), h = pj >> 1;
  const int r4 = lane >> 4, c16 = lane & 15;
  asm volatile("" : "+s"(x), "+s"(wq), "+s"(wo), "+s"(w1), "+s"(w3), "+s"(w2), "+s"(gw), "+s"(out), "+s"(ws));
  if (wave == 1) {
    const int wi = lane - 16;
    const int now = (int)(unsigned)__builtin_amdgcn_s_memrealtime() + REF0;
    ctrl[lane] = (lane == 1 || lane == 2) ? now : lane == 8 ? now - REF0 : (lane < 16 || lane >= 32) ? 0 : wi < NCW ? wi : wi < NCW + NHW ? NHC * NCW + NHH * (wi - NCW) : NEVER;
  }
  BARRIER();

  const __amdgpu_buffer_rsrc_t wsr = mkrsrc(ws, WS_BYTES);
  auto wg_max = [&]() __attribute__((always_inline)) {
    float mg = ctrlf[32 + (c16 < NCW + NHW ? c16 : 0)];
    mg = fmaxf(mg, dpp<ROR8>(mg)); mg = fmaxf(mg, dpp<ROR4>(mg)); mg = fmaxf(mg, dpp<ROR2>(mg)); mg = fmaxf(mg, dpp<ROR1>(mg));
    return mg;
  };

  if (wave == XWAVE + 1) {
    unsigned k = ((unsigned)__builtin_amdgcn_s_memrealtime() >> 8) + 1;
#pragma unroll 1
    for (int it = 0; it < 64; ++it, ++k) {
      wait_until(k << 8);
      if (__builtin_amdgcn_readfirstlane(ctrl[0]) >= NUNIT - 36) break;
      const int own = __builtin_amdgcn_readfirstlane(ctrl[1]);
      if (blockIdx.x < 16 && lane < 8) __builtin_amdgcn_raw_buffer_store_b128(u32x4{(unsigned)own, (unsigned)own, (unsigned)own, (unsigned)own}, wsr, WS_PACE + (k & 31u) * 2048 + blockIdx.x * 128 + lane * 16, 0, SC1);
      if (it == 0) continue;
      int e[16];
      asm volatile("s_load_dword %0, %16, 0x0\n\ts_load_dword %1, %16, 0x80\n\ts_load_dword %2, %16, 0x100\n\ts_load_dword %3, %16, 0x180\n\t"
                   "s_load_dword %4, %16, 0x200\n\ts_load_dword %5, %16, 0x280\n\ts_load_dword %6, %16, 0x300\n\ts_load_dword %7, %16, 0x380\n\t"
                   "s_load_dword %8, %16, 0x400\n\ts_load_dword %9, %16, 0x480\n\ts_load_dword %10, %16, 0x500\n\ts_load_dword %11, %16, 0x580\n\t"
                   "s_load_dword %12, %16, 0x600\n\ts_load_dword %13, %16, 0x680\n\ts_load_dword %14, %16, 0x700\n\ts_load_dword %15, %16, 0x780\n\t"
                   "s_waitcnt lgkmcnt(0)"
                   : "=&s"(e[0]), "=&s"(e[1]), "=&s"(e[2]), "=&s"(e[3]), "=&s"(e[4]), "=&s"(e[5]), "=&s"(e[6]), "=&s"(e[7]),
                     "=&s"(e[8]), "=&s"(e[9]), "=&s"(e[10]), "=&s"(e[11]), "=&s"(e[12]), "=&s"(e[13]), "=&s"(e[14]), "=&s"(e[15])
                   : "s"(ws + WS_PACE + ((k - 1u) & 31u) * 2048) : "memory");
      int sum = 0, cnt = 0;
#pragma unroll
      for (int i = 0; i < 16; ++i) { const int d = e[i] - own; const bool ok = (unsigned)(d + 600) < 1200u; sum += ok ? d : 0; cnt += ok ? 1 : 0; }
      if (cnt >= 8) ctrl[2] = own + (int)((float)sum / (float)cnt);
    }
    return;
  }

  if (wave == XWAVE) {
#pragma unroll 1
    for (int i = 0; i < 20; ++i) PAUSE(4);
    const u32x4 xv = *(const GAS u32x4*)(x + b * D + 8 * lane), gv = *(const GAS u32x4*)(gw + 8 * lane);
    *(LAS u32x4*)(L + L_X + lane * 16) = xv;
    float gd = 0.f;
#pragma unroll
    for (int k = 0; k < 4; ++k) gd = dot2(xv[k], gv[k], gd);
    gd = xrow_sum(row16_sum(gd));
    const float g = 1.f / (1.f + __expf(-gd));
    ctrlf[48] = g;
    lds_arrive(L, C_X, lane);
    u64 tag;
    asm volatile("s_load_dwordx2 %0, %1, 0x0\n\ts_waitcnt lgkmcnt(0)" : "=s"(tag) : "s"(ws + WS_EPOCH + b * 256) : "memory");
    tag += TAG_STEP;
#pragma unroll 1
    for (int spin = 0; spin < SPIN_CAP; ++spin) { if (__builtin_amdgcn_readfirstlane(ctrl[C_MAX]) >= NCW + NHW) break; __builtin_amdgcn_s_sleep(16); }
    CFENCE();
    const float mg = wg_max();
    float l = 0.f;
#pragma unroll 4
    for (int i = 0; i < NKU * 64 / 1024; ++i) {
      const f32x4 sc = *(const LAS f32x4*)(L + L_SC + i * 1024 + lane * 16);
      l += (expd(sc[0] - mg) + expd(sc[1] - mg)) + (expd(sc[2] - mg) + expd(sc[3] - mg));
    }
    l = xrow_sum(row16_sum(l));
    const unsigned tlo = (unsigned)tag, thi = (unsigned)(tag >> 32);
    if (lane < 8) __builtin_amdgcn_raw_buffer_store_b128(u32x4{__float_as_uint(mg), tlo, __float_as_uint(l), thi}, wsr, WS_REC + (b * 64 + pj) * 128 + lane * 16, 0, SC1);
#pragma unroll 1
    for (int i = 0; i < 12 + (kk & 31); ++i) PAUSE(12);
    u32x4 rec;
#pragma unroll 1
    for (int spin = 0; spin < SPIN_CAP; ++spin) {
      CFENCE();
      rec = __builtin_amdgcn_raw_buffer_load_b128(wsr, WS_REC + (b * 64 + (pj ^ 1)) * 128, 0, SC1);
      if (__builtin_amdgcn_ballot_w64(rec[1] != tlo || rec[3] != thi) == 0ull) break;
      __builtin_amdgcn_s_sleep(16);
    }
    const float mo = __uint_as_float(rec[0]), lo = __uint_as_float(rec[2]);
    const float mh = fmaxf(mg, mo), wt = expd(mg - mh);
    ctrlf[49] = g * wt / (wt * l + expd(mo - mh) * lo);
    const u32x4 yt = {tlo & YMASK, (tlo >> 12) & YMASK, (unsigned)(tag >> 24) & YMASK, (thi >> 4) & YMASK};
    *(LAS u32x4*)(L + L_CTRL + 52 * 4) = yt;
    lds_arrive(L, C_CF, lane);
    GAS unsigned* op = (GAS unsigned*)(out + b * D + 8 * pj + 4 * (lane & 1) + 2 * (lane >> 5));
    int off = WS_Y + (b * 64 + (lane >> 1)) * 2048 + pj * 32 + (lane & 1) * 16;
    asm volatile("" : "+v"(op), "+v"(off));
    BARRIER();
    {
      const unsigned now = (unsigned)__builtin_amdgcn_s_memrealtime();
      const int d = __builtin_amdgcn_readfirstlane(ctrl[2]) + TSTREAM + FOFF - (int)now;
      const unsigned t = now + (unsigned)min(max(d, FMIN), FMAX);
#pragma unroll 1
      for (int spin = 0; spin < 4096 && (int)(t - (unsigned)__builtin_amdgcn_s_memrealtime()) > 0; ++spin) {}
    }
    u32x4 ya, yb;
#pragma unroll 1
    for (int spin = 0; spin < SPIN_CAP; ++spin) {
      CFENCE();
      ya = __builtin_amdgcn_raw_buffer_load_b128(wsr, off, 0, SC1);
      yb = __builtin_amdgcn_raw_buffer_load_b128(wsr, off, 32 * 2048, SC1);
      const unsigned bad = (((ya[0] ^ yt[0]) | (ya[1] ^ yt[1])) | ((ya[2] ^ yt[2]) | (ya[3] ^ yt[3])) | ((yb[0] ^ yt[0]) | (yb[1] ^ yt[1])) | ((yb[2] ^ yt[2]) | (yb[3] ^ yt[3]))) & YMASK;
      if (__builtin_amdgcn_ballot_w64(bad != 0u) == 0ull) break;
    }
    float o[4];
#pragma unroll
    for (int k = 0; k < 4; ++k) o[k] = __uint_as_float(ya[k] & ~YMASK) + __uint_as_float(yb[k] & ~YMASK);
    float e0 = swadd32(o[0], o[2]), e1 = swadd32(o[1], o[3]);
    e0 = swadd16(e0, e0); e1 = swadd16(e1, e1);
    e0 += dpp<ROR8>(e0); e1 += dpp<ROR8>(e1); e0 += dpp<ROR4>(e0); e1 += dpp<ROR4>(e1); e0 += dpp<ROR2>(e0); e1 += dpp<ROR2>(e1);
    if ((lane & 30) == 0) *op = pack_bf16(e0, e1);
    if (pj == 0 && lane < 8) __builtin_amdgcn_raw_buffer_store_b128(u32x4{tlo, thi, tlo, thi}, wsr, WS_EPOCH + b * 256 + lane * 16, 0, SC1);
    return;
  }

  const int cw = wave - 1, hw = wave - 1 - NCW;
  int landed = 0;
  auto wait_unit = [&](int u) __attribute__((always_inline)) {
#pragma unroll 1
    for (int spin = 0; landed <= u && spin < SPIN_CAP; ++spin) {
      landed = __builtin_amdgcn_readfirstlane(ctrl[0]);
    }
    CFENCE();
  };
  auto take = [&](int u, int next, u32x4& a0, u32x4& a1, u32x4& a2, u32x4& a3) __attribute__((always_inline)) {
    wait_unit(u);
    const LAS u32x4* sp = (const LAS u32x4*)(L + (u & (NSLOT - 1)) * UNIT + lane * 16);
    a0 = sp[0]; a1 = sp[64]; a2 = sp[128]; a3 = sp[192];
    LDS_WAIT();
    ctrl[16 + cw] = next;
    asm volatile("" : "+v"(a0), "+v"(a1), "+v"(a2), "+v"(a3) :: "memory");
  };
  u32x4 q;
  auto build_q = [&]() __attribute__((always_inline)) {
    lds_await(ctrl, C_Q, NHW);
    const LAS f32x4* qp = (const LAS f32x4*)(L + L_QP + (2 * r4 * DH + 8 * c16) * 4);
    const f32x4 lo = qp[0] + qp[DH / 4], hi = qp[1] + qp[DH / 4 + 1];
    q[0] = pack_bf16(xrow_sum(lo[0]), xrow_sum(lo[1])); q[1] = pack_bf16(xrow_sum(lo[2]), xrow_sum(lo[3]));
    q[2] = pack_bf16(xrow_sum(hi[0]), xrow_sum(hi[1])); q[3] = pack_bf16(xrow_sum(hi[2]), xrow_sum(hi[3]));
  };
  constexpr float SCALE = 0.088388347648318440f;
  float mx = -3.4028234e38f;
  auto score = [&](int u, const u32x4& k0, const u32x4& k1, const u32x4& k2, const u32x4& k3) __attribute__((always_inline)) {
    float d0 = 0.f, d1 = 0.f, d2 = 0.f, d3 = 0.f;
#pragma unroll
    for (int k = 0; k < 4; ++k) { d0 = dot2(k0[k], q[k], d0); d1 = dot2(k1[k], q[k], d1); d2 = dot2(k2[k], q[k], d2); d3 = dot2(k3[k], q[k], d3); }
    const unsigned s01 = pack_bf16(row16_sum(d0), row16_sum(d1)), s23 = pack_bf16(row16_sum(d2), row16_sum(d3));
    const unsigned t01 = pack_bf16(bflo(s01) * SCALE, bfhi(s01) * SCALE), t23 = pack_bf16(bflo(s23) * SCALE, bfhi(s23) * SCALE);
    const f32x4 sc = {bflo(t01), bfhi(t01), bflo(t23), bfhi(t23)};
    mx = fmaxf(fmaxf(mx, fmaxf(sc[0], sc[1])), fmaxf(sc[2], sc[3]));
    if (c16 == 0) *(LAS f32x4*)(L + L_SC + u * 64 + r4 * 16) = sc;
  };

  if (hw < 0) {
    {
      u32x4 hk[4 * NHC];
#pragma unroll
      for (int i = 0; i < NHC; ++i) take(cw + NCW * i, i + 1 < NHC ? cw + NCW * (i + 1) : NHELD + cw, hk[4 * i], hk[4 * i + 1], hk[4 * i + 2], hk[4 * i + 3]);
      build_q();
#pragma unroll
      for (int i = 0; i < NHC; ++i) { score(cw + NCW * i, hk[4 * i], hk[4 * i + 1], hk[4 * i + 2], hk[4 * i + 3]); __builtin_amdgcn_sched_barrier(0); }
    }
#pragma unroll 1
    for (int u = NHELD + cw; u < NKU; u += NCW) {
      u32x4 k0, k1, k2, k3;
      take(u, u + NCW, k0, k1, k2, k3);
      score(u, k0, k1, k2, k3);
    }
    ctrlf[32 + cw] = xrow_max(mx);
    lds_arrive(L, C_MAX, lane);
    lds_await(ctrl, C_MAX, NCW + NHW);
    const float mg = wg_max();
    float acc[8] = {0.f, 0.f, 0.f, 0.f, 0.f, 0.f, 0.f, 0.f};
    LAS unsigned* rp = (LAS unsigned*)(L + L_REC + cw * 272 + (8 * c16 + 2 * r4) * 2);
    asm volatile("" : "+v"(rp));
    float cf = 0.f;
#pragma unroll 1
    for (int u = NKU + cw; u < NUNIT; u += NCW) {
      const f32x4 sc = *(const LAS f32x4*)(L + L_SC + (u - NKU) * 64 + r4 * 16);
      float p[4];
#pragma unroll
      for (int t = 0; t < 4; ++t) { p[t] = expd(sc[t] - mg); asm volatile("" : "+v"(p[t])); }
      if ((u & ~(NCW - 1)) == NUNIT - 32) { lds_await(ctrl, C_CF, 1); cf = ctrlf[49]; }
      u32x4 v[4];
      take(u, u + NCW, v[0], v[1], v[2], v[3]);
#pragma unroll
      for (int t = 0; t < 4; ++t)
#pragma unroll
        for (int k = 0; k < 4; ++k) { acc[2 * k] = fmaf(p[t], bflo(v[t][k]), acc[2 * k]); acc[2 * k + 1] = fmaf(p[t], bfhi(v[t][k]), acc[2 * k + 1]); }
    }
    const float e0 = swadd16(swadd32(acc[0], acc[4]), swadd32(acc[2], acc[6])), e1 = swadd16(swadd32(acc[1], acc[5]), swadd32(acc[3], acc[7]));
    *rp = pack_bf16(e0 * cf, e1 * cf);
    BARRIER();
    return;
  }

  u32x4 wb[16];
  const unsigned tstart = (unsigned)__builtin_amdgcn_readfirstlane(ctrl[8]);
  {
    const __amdgpu_buffer_rsrc_t rq = mkrsrc(wq, D * H * DH * 2);
    const int vo = ((64 * hw + 2 * r4) * (H * DH) + h * DH + 8 * c16) * 2;
#pragma unroll
    for (int i = 0; i < 16; ++i) { if (i == 0) wait_until(tstart + TQ + hw * QA); else PAUSE(QS); wb[i] = ldw(rq, vo, (8 * (i >> 1) + (i & 1)) * (H * DH * 2)); }
    lds_await(ctrl, C_X, 1);
    const LAS unsigned* xl = (const LAS unsigned*)(L + L_X + (64 * hw + 2 * r4) * 2);
    float a[8] = {0.f, 0.f, 0.f, 0.f, 0.f, 0.f, 0.f, 0.f};
#pragma unroll
    for (int i = 0; i < 8; ++i) {
      const unsigned xp = xl[4 * i];
#pragma unroll
      for (int k = 0; k < 4; ++k) {
        a[2 * k] = dot2(perm_lo(wb[2 * i][k], wb[2 * i + 1][k]), xp, a[2 * k]);
        a[2 * k + 1] = dot2(perm_hi(wb[2 * i][k], wb[2 * i + 1][k]), xp, a[2 * k + 1]);
      }
    }
    const float e0 = swadd16(swadd32(a[0], a[4]), swadd32(a[2], a[6])), e1 = swadd16(swadd32(a[1], a[5]), swadd32(a[3], a[7]));
    *(LAS f32x2*)(L + L_QP + (hw * DH + 8 * c16 + 2 * r4) * 4) = f32x2{e0, e1};
    lds_arrive(L, C_Q, lane);
    const int hb = NHC * NCW + NHH * hw;
#pragma unroll
    for (int i = 0; i < NHH; ++i) take(hb + i, i + 1 < NHH ? hb + i + 1 : NEVER, wb[4 * i], wb[4 * i + 1], wb[4 * i + 2], wb[4 * i + 3]);
    build_q();
#pragma unroll
    for (int i = 0; i < NHH; ++i) { score(hb + i, wb[4 * i], wb[4 * i + 1], wb[4 * i + 2], wb[4 * i + 3]); __builtin_amdgcn_sched_barrier(0); }
    ctrlf[32 + cw] = xrow_max(mx);
    lds_arrive(L, C_MAX, lane);
  }
  f32x4 y[4] = {{0.f, 0.f, 0.f, 0.f}, {0.f, 0.f, 0.f, 0.f}, {0.f, 0.f, 0.f, 0.f}, {0.f, 0.f, 0.f, 0.f}};
  if ((pj & 7) != 7) {
    const int sl = 7 * (pj >> 3) + (pj & 7);
    const __amdgpu_buffer_rsrc_t r1 = mkrsrc(w1, D * F * 2), r3 = mkrsrc(w3, D * F * 2), r2 = mkrsrc(w2, F * D * 2);
    const int vo = ((64 * hw + (lane & 60)) * F + FS * sl + 8 * (lane & 3)) * 2;
#pragma unroll
    for (int i = 0; i < 8; ++i) { if (i == 0) wait_until(tstart + T1 + hw * A1); else PAUSE(S1); wb[i] = ldw(i < 4 ? r1 : r3, vo, (i & 3) * (F * 2)); }
    const u32x2 xm = *(const LAS u32x2*)(L + L_X + (64 * hw + (lane & 60)) * 2);
    float a[16];
#pragma unroll
    for (int m = 0; m < 2; ++m)
#pragma unroll
      for (int k = 0; k < 4; ++k) {
        a[8 * m + 2 * k] = dot2(perm_lo(wb[4 * m + 2][k], wb[4 * m + 3][k]), xm[1], dot2(perm_lo(wb[4 * m][k], wb[4 * m + 1][k]), xm[0], 0.f));
        a[8 * m + 2 * k + 1] = dot2(perm_hi(wb[4 * m + 2][k], wb[4 * m + 3][k]), xm[1], dot2(perm_hi(wb[4 * m][k], wb[4 * m + 1][k]), xm[0], 0.f));
      }
    f32x4 e;
#pragma unroll
    for (int k = 0; k < 4; ++k) {
      float t = swadd16(swadd32(a[k], a[8 + k]), swadd32(a[4 + k], a[12 + k]));
      t += dpp<ROR8>(t); e[k] = t + dpp<ROR4>(t);
    }
    if ((lane & 12) == 0) *(LAS f32x4*)(L + L_MP + (hw * 64 + (lane & 32) + 8 * (lane & 3) + 4 * (r4 & 1)) * 4) = e;
    lds_arrive(L, C_MLP, lane);
    u32x2 wr[8];
#pragma unroll
    for (int i = 0; i < 8; ++i) { if (i == 0) wait_until(tstart + T2 + hw * A2); else PAUSE(S2); wr[i] = ldw2(r2, ((FS * sl + 8 * r4) * D + 64 * hw + 4 * c16) * 2, i * (D * 2)); }
    lds_await(ctrl, C_MLP, NHW);
    const LAS float* mp = (const LAS float*)(L + L_MP) + (lane & 31);
    float ga = 0.f, up = 0.f;
#pragma unroll
    for (int w = 0; w < NHW; ++w) { ga += mp[w * 64]; up += mp[w * 64 + 32]; }
    const float hv = ga / (1.f + __expf(-ga)) * up * (1.f - ctrlf[48]);
    const unsigned hh = pack_bf16(hv, 0.f), hl = pack_bf16(hv - bflo(hh), 0.f);
    *(LAS u16*)(L + L_H + hw * 128 + lane * 2) = (u16)(lane < 32 ? hh : hl);
    LDS_WAIT();
    u32x4 ha = *(const LAS u32x4*)(L + L_H + hw * 128 + (c16 & 1) * 64 + r4 * 16);
    if (c16 >= 2) ha = u32x4{0u, 0u, 0u, 0u};
#pragma unroll
    for (int e = 0; e < 4; ++e) {
      u32x4 f;
#pragma unroll
      for (int k = 0; k < 4; ++k) f[k] = (e & 1) ? perm_hi(wr[2 * k][e >> 1], wr[2 * k + 1][e >> 1]) : perm_lo(wr[2 * k][e >> 1], wr[2 * k + 1][e >> 1]);
      y[e] = mfma(ha, f, y[e]);
    }
  }
#pragma unroll
  for (int e = 0; e < 4; ++e) asm volatile("" : "+v"(y[e]));
  {
    const __amdgpu_buffer_rsrc_t ro = mkrsrc(wo, H * DH * D * 2);
#pragma unroll
    for (int t = 0; t < 4; ++t) {
      u32x2 wr[8];
#pragma unroll
      for (int i = 0; i < 8; ++i) { if (t + i == 0) wait_until(tstart + TO + hw * AO); else PAUSE(SO); wr[i] = ldw2(ro, ((h * DH + 32 * r4) * D + 64 * hw + 4 * c16) * 2, (8 * t + i) * (D * 2)); }
#pragma unroll
      for (int e = 0; e < 4; ++e) {
#pragma unroll
        for (int k = 0; k < 4; ++k) wb[4 * t + e][k] = (e & 1) ? perm_hi(wr[2 * k][e >> 1], wr[2 * k + 1][e >> 1]) : perm_lo(wr[2 * k][e >> 1], wr[2 * k + 1][e >> 1]);
        asm volatile("" : "+v"(wb[4 * t + e]));
      }
    }
  }
  lds_await(ctrl, C_CF, 1);
  u32x4 yt = *(const LAS u32x4*)(L + L_CTRL + 52 * 4);
  int yo = WS_Y + (b * 64 + pj) * 2048 + hw * 256 + c16 * 16;
  const LAS u32x4* rec = (const LAS u32x4*)(L + L_REC + (lane & 3) * 272 + r4 * 64);
  unsigned ym = ~YMASK;
  asm volatile("" : "+v"(yt), "+v"(yo), "+v"(rec), "+s"(ym));
  BARRIER();
  {
    const u32x4 a[4] = {rec[0], rec[1], rec[2], rec[3]};
#pragma unroll
    for (int t = 0; t < 4; ++t)
#pragma unroll
      for (int e = 0; e < 4; ++e) y[e] = mfma(a[t], wb[4 * t + e], y[e]);
    u32x4 v;
#pragma unroll
    for (int e = 0; e < 4; ++e) {
      const f32x2 t = y[e].xy + y[e].zw;
      unsigned r;
      asm("v_add_f32 %0, %1, %2\n\tv_add_u32 %0, %5, %0\n\tv_and_or_b32 %0, %0, %3, %4" : "=&v"(r) : "v"(t[0]), "v"(t[1]), "s"(ym), "v"(yt[e]), "i"((YMASK + 1u) / 2u));
      v[e] = r;
    }
    if (lane < 16) __builtin_amdgcn_raw_buffer_store_b128(v, wsr, yo, 0, SC1);
  }
}

extern "C" void kernel_launch(void* const* d_in, const int* in_sizes, int n_in, void* d_out, int out_size, void* d_ws, size_t ws_size,
                              hipStream_t stream) {
  gated_mix__mha_block_cached__glu_gated_swiglu__decode_kernel<<<256, NTHR, 0, stream>>>((const u16*)d_in[0], (const u16*)d_in[1], (const u16*)d_in[2], (const u16*)d_in[3], (const u16*)d_in[4],
                                             (const u16*)d_in[6], (const u16*)d_in[7], (const u16*)d_in[8], (const u16*)d_in[9], (u16*)d_out,
                                             (unsigned char*)d_ws);
}
```

```cpp
#include <hip/hip_runtime.h>
#include <hip/hip_bf16.h>

#define GAS __attribute__((address_space(1)))
#define LAS __attribute__((address_space(3)))
typedef unsigned short u16;
typedef unsigned long long u64;
using u32x4 = __attribute__((ext_vector_type(4))) unsigned;
using u32x2 = __attribute__((ext_vector_type(2))) unsigned;
using f32x4 = __attribute__((ext_vector_type(4))) float;
using f32x2 = __attribute__((ext_vector_type(2))) float;
using bf16x2 = __attribute__((ext_vector_type(2))) __bf16;
using bf16x8 = __attribute__((ext_vector_type(8))) __bf16;

constexpr int B = 4, D = 512, H = 32, SK = 8192, DH = 128, F = 1792;
constexpr int NCW = 4, NHW = 8, XWAVE = 1 + NCW + NHW, NWAVE = XWAVE + 2, NTHR = NWAVE * 64;
constexpr int TQ = 100, QA = 112, QS = 2;
constexpr int T1 = 1400, A1 = 150, S1 = 6;
constexpr int T2 = 2500, A2 = 40, S2 = 1;
constexpr int TO = 2900, AO = 350, SO = 2;
constexpr int TSTREAM = 6860;
constexpr int WMID = 5472, WMIN = 16, WMAX = 28;
constexpr int WGAIN = 10, WGAIN2 = 18, UEND = 420;
constexpr int JUMP = 100, DECAY = 6;
constexpr int REF0 = 180;
constexpr int FOFF = 156, FMIN = 45, FMAX = 150;
constexpr int CODE_KIB = 21;

constexpr int UNIT = 4096;
constexpr int NSLOT = 32, RING = NSLOT * UNIT;
constexpr int NKU = (SK / 2) / 16, NUNIT = 2 * NKU;
constexpr int NHC = 6, NHH = 4, NHELD = NHC * NCW + NHH * NHW;
constexpr int FS = 32;
constexpr int L_CTRL = RING;
constexpr int L_X = L_CTRL + 256;
constexpr int L_QP = L_X + D * 2;
constexpr int L_MP = L_QP + NHW * DH * 4;
constexpr int L_REC = L_MP + NHW * 64 * 4;
constexpr int L_H = L_REC + NCW * 272;
constexpr int L_SC = L_H + NHW * 128;
constexpr int LDS_BYTES = L_SC + NKU * 64;
static_assert(LDS_BYTES <= 159 * 1024, "lds");
static_assert(WMIN <= WMID / 256 && WMID / 256 <= WMAX && WMAX <= 28, "window");
constexpr int WS_EPOCH = 0;
constexpr int WS_REC = 4096;
constexpr int WS_PACE = WS_REC + 256 * 128;
constexpr int WS_Y = WS_PACE + 32 * 2048;
constexpr int WS_BYTES = WS_Y + B * 64 * 2048;
constexpr u64 TAG_STEP = 0x9E3779B97F4A7C15ull;
constexpr unsigned YMASK = 0xFFFu;
constexpr int NT = 2, SC1 = 16;
constexpr int SPIN_CAP = 1 << 22;
constexpr int NEVER = 1 << 20;
constexpr int C_Q = 4, C_MAX = 5, C_MLP = 6, C_X = 7, C_CF = 9;

__device__ __forceinline__ float expd(float d) { return __builtin_amdgcn_exp2f(d * 1.4426950408889634f); }
__device__ __forceinline__ float bflo(unsigned u) { return __builtin_bit_cast(float, u << 16); }
__device__ __forceinline__ float bfhi(unsigned u) { return __builtin_bit_cast(float, u & 0xffff0000u); }
__device__ __forceinline__ float dot2(unsigned a, unsigned b, float acc) {
  return __builtin_amdgcn_fdot2_f32_bf16(__builtin_bit_cast(bf16x2, a), __builtin_bit_cast(bf16x2, b), acc, false);
}
__device__ __forceinline__ unsigned pack_bf16(float lo, float hi) {
  const bf16x2 v = {(__bf16)lo, (__bf16)hi};
  return __builtin_bit_cast(unsigned, v);
}
__device__ __forceinline__ unsigned perm_lo(unsigned a, unsigned b) { return __builtin_amdgcn_perm(b, a, 0x05040100u); }
__device__ __forceinline__ unsigned perm_hi(unsigned a, unsigned b) { return __builtin_amdgcn_perm(b, a, 0x07060302u); }
template <int CTRL> __device__ __forceinline__ float dpp(float x) {
  return __builtin_bit_cast(float, __builtin_amdgcn_mov_dpp(__builtin_bit_cast(int, x), CTRL, 0xf, 0xf, true));
}
constexpr int ROR1 = 0x121, ROR2 = 0x122, ROR4 = 0x124, ROR8 = 0x128;
__device__ __forceinline__ float row16_sum(float x) { x += dpp<ROR8>(x); x += dpp<ROR4>(x); x += dpp<ROR2>(x); x += dpp<ROR1>(x); return x; }
__device__ __forceinline__ float swadd32(float a, float b) {
  auto t = __builtin_amdgcn_permlane32_swap(__float_as_uint(a), __float_as_uint(b), false, false);
  return __uint_as_float(t[0]) + __uint_as_float(t[1]);
}
__device__ __forceinline__ float swadd16(float a, float b) {
  auto t = __builtin_amdgcn_permlane16_swap(__float_as_uint(a), __float_as_uint(b), false, false);
  return __uint_as_float(t[0]) + __uint_as_float(t[1]);
}
__device__ __forceinline__ float xrow_sum(float x) { x = swadd16(x, x); return swadd32(x, x); }
__device__ __forceinline__ float xrow_max(float x) {
  auto s = __builtin_amdgcn_permlane16_swap(__float_as_uint(x), __float_as_uint(x), false, false);
  x = fmaxf(__uint_as_float(s[0]), __uint_as_float(s[1]));
  auto t = __builtin_amdgcn_permlane32_swap(__float_as_uint(x), __float_as_uint(x), false, false);
  return fmaxf(__uint_as_float(t[0]), __uint_as_float(t[1]));
}
__device__ __forceinline__ __amdgpu_buffer_rsrc_t mkrsrc(const void* p, int bytes) { return __builtin_amdgcn_make_buffer_rsrc((void*)p, 0, bytes, 0x00020000); }
__device__ __forceinline__ u32x4 ldw(__amdgpu_buffer_rsrc_t r, int voff, int soff) { return __builtin_amdgcn_raw_buffer_load_b128(r, voff, soff, NT); }
#define LDS_WAIT() asm volatile("s_waitcnt lgkmcnt(0)" ::: "memory")
#define CFENCE() asm volatile("" ::: "memory")
#define BARRIER() asm volatile("s_waitcnt lgkmcnt(0)\n\ts_barrier" ::: "memory")
#define PAUSE(n) do { __builtin_amdgcn_sched_barrier(0); asm volatile("s_sleep %0" :: "i"(n) : "memory"); __builtin_amdgcn_sched_barrier(0); } while (0)

__device__ __forceinline__ void lds_arrive(LAS unsigned char* L, int idx, int lane) {
  LDS_WAIT();
  if (lane == 0) __hip_atomic_fetch_add((LAS int*)(L + L_CTRL) + idx, 1, __ATOMIC_RELAXED, __HIP_MEMORY_SCOPE_WORKGROUP);
}
__device__ __forceinline__ void lds_await(volatile LAS int* ctrl, int idx, int n) {
#pragma unroll 1
  for (int spin = 0; spin < SPIN_CAP; ++spin) { if (__builtin_amdgcn_readfirstlane(ctrl[idx]) >= n) break; __builtin_amdgcn_s_sleep(1); }
  CFENCE();
}

__device__ __forceinline__ void wait_until(unsigned t) {
  __builtin_amdgcn_sched_barrier(0);
#pragma unroll 1
  for (int spin = 0; spin < 4096; ++spin) {
    const int d = (int)(t - (unsigned)__builtin_amdgcn_s_memrealtime());
    if (d <= 0) break;
    if (d > 24) __builtin_amdgcn_s_sleep(6);
  }
  CFENCE();
  __builtin_amdgcn_sched_barrier(0);
}
constexpr int TAU2 = 2 * (TSTREAM * 256 / NUNIT);
__device__ __forceinline__ f32x4 mfma(const u32x4& a, const u32x4& b, const f32x4& c) {
  return __builtin_amdgcn_mfma_f32_16x16x32_bf16(__builtin_bit_cast(bf16x8, a), __builtin_bit_cast(bf16x8, b), c, 0, 0, 0);
}
__device__ __forceinline__ u32x2 ldw2(__amdgpu_buffer_rsrc_t r, int voff, int soff) { return __builtin_amdgcn_raw_buffer_load_b64(r, voff, soff, NT); }

__global__ void __launch_bounds__(NTHR, 4) gated_mix__mha_block_cached__glu_gated_swiglu__decode_kernel(const u16* x, const u16* kc, const u16* vc, const u16* wq, const u16* wo, const u16* w1,
                                                         const u16* w3, const u16* w2, const u16* gw, u16* out, unsigned char* ws) {
  __shared__ __attribute__((aligned(16))) unsigned char lds_raw[LDS_BYTES];
  LAS unsigned char* L = (LAS unsigned char*)lds_raw;
  volatile LAS int* ctrl = (volatile LAS int*)(L + L_CTRL);
  volatile LAS float* ctrlf = (volatile LAS float*)(L + L_CTRL);
  {
    unsigned va, vb, vo, pa, nxa, t0, t1, t2, t3;
#define WAITW "s_waitcnt (wcnt & 15) | ((wcnt >> 4) << 14) | 0x0F70\n\t"
#define SP(v, sb, wait) wait "global_load_lds_dword " v ", " sb " nt\n\tglobal_load_lds_dword " v ", " sb " sc1\n\t"
#define SW "s_waitcnt vmcnt(4)\n\t"
#define UNIT4(v) \
        WAITW "global_load_lds_dwordx4 " v ", s[40:41] nt\n\t" \
        WAITW "global_load_lds_dwordx4 " v ", s[40:41] offset:1024 nt\n\t" \
        WAITW "global_load_lds_dwordx4 " v ", s[40:41] offset:2048 nt\n\t" \
        WAITW "global_load_lds_dwordx4 " v ", s[40:41] offset:3072 nt\n\t"
    asm volatile(
        "s_getpc_b64 s[56:57]\n\t"
        "s_load_dwordx8 s[44:51], %[ka], 0x18\n\t"
        "s_load_dwordx2 s[52:53], %[ka], 0x38\n\t"
        "s_load_dwordx4 s[40:43], %[ka], 0x8\n\t"
        "v_readfirstlane_b32 s54, %[tid]\n\t"
        "s_mov_b32 m0, %c[jk]\n\t"
        "s_lshr_b32 s59, %[bid], 3\n\t"
        "s_cmp_ge_u32 s54, 64\n\t"
        "s_cbranch_scc1 90f\n\t"
        "s_min_u32 s54, s59, %c[nch]\n\t"
        "s_lshl_b32 s54, s54, 10\n\t"
        "v_lshlrev_b32 %[vo], 4, %[tid]\n\t"
        "s_and_b32 s56, s56, -128\n\t"
        "v_add_u32 %[t0], s54, %[vo]\n\t"
        "s_and_b32 s58, %[bid], 7\n\t"
        "global_load_lds_dwordx4 %[t0], s[56:57]\n\t"
        "s_setprio 3\n\t"
        "v_lshlrev_b32 %[t1], 7, %[tid]\n\t"
        "s_lshl_b32 s54, s59, 7\n\t"
        "s_add_u32 s54, s54, s58\n\t"
        "s_lshl_b32 s54, s54, 10\n\t"
        "v_lshrrev_b32 %[va], 3, %[tid]\n\t"
        "v_mul_u32_u24 %[va], 8192 - 1024, %[va]\n\t"
        "v_add3_u32 %[va], %[va], %[t1], s54\n\t"
        "v_add_u32 %[t2], 0x10000, %[va]\n\t"
        "s_lshl_b32 s55, s58, 5\n\t"
        "s_add_u32 s55, s55, s59\n\t"
        "s_mul_i32 s54, s55, 7 * 1024\n\t"
        "v_min_u32 %[t3], 55 * 128, %[t1]\n\t"
        "v_add_u32 %[t3], s54, %[t3]\n\t"
        "s_mul_i32 s54, s58, 448\n\t"
        "s_and_b32 s54, s54, -128\n\t"
        "s_mul_i32 s57, s59, 16 * 3584\n\t"
        "s_add_u32 s54, s54, s57\n\t"
        "v_lshrrev_b32 %[t0], 2, %[tid]\n\t"
        "v_mul_u32_u24 %[t0], 3584 - 512, %[t0]\n\t"
        "v_add3_u32 %[t0], %[t0], %[t1], s54\n\t"
        "v_lshl_add_u32 %[vb], s55, 14, %[t1]\n\t"
        "v_add_u32 %[t1], 0x2000, %[vb]\n\t"
        "s_and_b32 s54, %[bid], 0xc0\n\t"
        "s_lshl3_add_u32 s54, s58, s54\n\t"
        "s_and_b32 s56, s59, 7\n\t"
        "s_add_u32 s54, s54, s56\n\t"
        "s_lshr_b32 s56, s54, 1\n\t"
        "s_lshl_b32 s56, s56, 21\n\t"
        "s_and_b32 s64, s54, 1\n\t"
        "s_bfe_u32 s57, s54, 0x10007\n\t"
        "s_lshl2_add_u32 s64, s57, s64\n\t"
        "s_bfe_u32 s57, s54, 0x10002\n\t"
        "s_lshl4_add_u32 s64, s57, s64\n\t"
        "s_lshl_b32 s64, s64, 16\n\t"
        "s_waitcnt lgkmcnt(0)\n\t"
        SP("%[va]", "s[44:45]", "") SP("%[t2]", "s[44:45]", "") SP("%[t3]", "s[52:53]", "")
        "s_barrier\n\t"
        SP("%[t0]", "s[48:49]", SW) SP("%[t0]", "s[50:51]", SW) SP("%[vb]", "s[46:47]", SW) SP("%[t1]", "s[46:47]", SW)
        "s_add_u32 s40, s40, s56\n\t"
        "s_addc_u32 s41, s41, 0\n\t"
        "s_add_u32 s42, s42, s56\n\t"
        "s_addc_u32 s43, s43, 0\n\t"
        "v_mov_b32 %[va], %[vo]\n\t"
        "v_or_b32 %[vo], s64, %[vo]\n\t"
        ".set so, 0\n\t"
        ".set wcnt, 4\n\t"
        ".rept 4\n\t"
        "s_movk_i32 m0, so\n\t"
        ".if (so & 4096) == 0\n\t"
        WAITW
        ".set wcnt, wcnt + 6\n\t"
        ".else\n\t"
        "s_nop 0\n\t"
        ".endif\n\t"
        "global_load_lds_dwordx4 %[vo], s[40:41] nt\n\t"
        "global_load_lds_dwordx4 %[vo], s[40:41] offset:1024 nt\n\t"
        "global_load_lds_dwordx4 %[vo], s[40:41] offset:2048 nt\n\t"
        "global_load_lds_dwordx4 %[vo], s[40:41] offset:3072 nt\n\t"
        "v_add_u32 %[vo], 0x1000, %[vo]\n\t"
        ".set so, so + 4096\n\t"
        ".endr\n\t"
        "s_waitcnt vmcnt(16)\n\t"
        "s_getpc_b64 s[50:51]\n"
        "20:\n\t"
        "s_add_u32 s50, s50, 23f-20b-%c[wmin]*128\n\t"
        "s_addc_u32 s51, s51, -1\n\t"
        "v_mov_b32 %[pa], %c[ctl]\n\t"
        "v_and_b32 %[nxa], 0x3c0 >> 2, %[vo]\n\t"
        "v_lshrrev_b32 %[nxa], 2, %[nxa]\n\t"
        "v_add_u32 %[nxa], %c[ctl] + 64, %[nxa]\n\t"
        "s_memrealtime s[58:59]\n\t"
        "s_mov_b32 s62, 0\n\t"
        "s_mov_b32 s63, %c[gain]\n\t"
        "s_mov_b32 s44, 4\n\t"
        "s_mov_b32 s45, %c[wmid] >> 8\n\t"
        "s_mov_b32 s46, 0x4000\n\t"
        "s_mov_b32 s47, 2 * %c[tau2]\n\t"
        "s_mov_b32 s48, %c[nslot]\n\t"
        "s_mov_b32 s49, %c[nku]\n\t"
        "s_waitcnt lgkmcnt(0)\n\t"
        "s_mov_b32 s59, 10000\n"
        "22:\n\t"
        "s_memrealtime s[56:57]\n\t"
        "ds_read_b32 %[t3], %[pa] offset:8\n\t"
        "s_lshl_b32 s52, s45, 7\n\t"
        "s_add_u32 s52, s50, s52\n\t"
        "s_addc_u32 s53, s51, 0\n\t"
        "s_add_i32 s54, s45, -1\n\t"
        "s_lshr_b32 s54, s54, 2\n\t"
        "s_sub_i32 s54, s44, s54\n\t"
        "v_mov_b32 %[t1], s54\n\t"
        "v_add_u32 %[t0], 0x1000, %[vo]\n\t"
        "s_add_u32 s55, s46, 0x1000\n\t"
        "s_mov_b32 m0, s46\n\t"
        "s_setpc_b64 s[52:53]\n"
        "24:\n\t"
        "s_waitcnt lgkmcnt(0)\n\t"
        "v_readfirstlane_b32 s57, %[t3]\n\t"
        "v_add_u32 %[t1], 1, %[t1]\n\t"
        "s_lshr_b32 s54, s47, 8\n\t"
        "s_sub_u32 s56, s56, s54\n\t"
        "v_mov_b32 %[t2], s56\n\t"
        "s_sub_i32 s54, s56, s58\n\t"
        "s_mov_b32 s58, s56\n\t"
        "s_sub_i32 s56, s56, s57\n\t"
        "ds_write2_b32 %[pa], %[t1], %[t2] offset1:1\n\t"
        "s_add_i32 s59, s59, s54\n\t"
        "s_add_i32 s59, s59, %c[decay]\n\t"
        "s_cmp_gt_i32 s54, %c[jump]\n\t"
        "s_cselect_b32 s59, s62, s59\n\t"
        "s_min_i32 s62, s56, s59\n\t"
        "s_mul_i32 s56, s62, s63\n\t"
        "s_add_i32 s56, s56, %c[wmid]\n\t"
        "s_ashr_i32 s56, s56, 8\n\t"
        "s_max_i32 s56, s56, %c[wmin]\n\t"
        "s_min_i32 s45, s56, %c[wmax]\n\t"
        "s_add_u32 s47, s47, %c[tau2]\n\t"
        "s_add_u32 s46, s46, 0x2000\n\t"
        "s_and_b32 s46, s46, %c[nslot] * 4096 - 1\n\t"
        "s_add_i32 s44, s44, 2\n\t"
        "s_and_b32 s54, s44, 0xff\n\t"
        "s_and_b32 s55, s44, 0xf0\n\t"
        "s_add_u32 s54, s54, s55\n\t"
        "s_lshl_b32 s54, s54, 12\n\t"
        "s_xor_b32 s54, s54, s64\n\t"
        "v_or_b32 %[vo], s54, %[va]\n\t"
        "s_cmp_lt_i32 s44, s48\n\t"
        "s_cbranch_scc1 22b\n\t"
        "s_cmp_lt_i32 s44, s49\n\t"
        "s_cbranch_scc1 25f\n\t"
        "s_cmp_ge_i32 s44, 2 * %c[nku]\n\t"
        "s_cbranch_scc1 30f\n\t"
        "s_mov_b32 s49, 2 * %c[nku]\n\t"
        "s_mov_b64 s[40:41], s[42:43]\n"
        "25:\n\t"
        "s_cmp_ge_i32 s44, %c[uend]\n\t"
        "s_cselect_b32 s63, %c[gain2], s63\n\t"
        "s_mov_b32 s55, %c[cap]\n"
        "26:\n\t"
        "ds_read_b32 %[t0], %[nxa]\n\t"
        "s_waitcnt lgkmcnt(0)\n\t"
        "s_nop 1\n\t"
        "v_min_i32_dpp %[t0], %[t0], %[t0] row_ror:8 row_mask:0xf bank_mask:0xf\n\t"
        "s_nop 1\n\t"
        "v_min_i32_dpp %[t0], %[t0], %[t0] row_ror:4 row_mask:0xf bank_mask:0xf\n\t"
        "s_nop 1\n\t"
        "v_min_i32_dpp %[t0], %[t0], %[t0] row_ror:2 row_mask:0xf bank_mask:0xf\n\t"
        "s_nop 1\n\t"
        "v_min_i32_dpp %[t0], %[t0], %[t0] row_ror:1 row_mask:0xf bank_mask:0xf\n\t"
        "s_nop 1\n\t"
        "v_readfirstlane_b32 s48, %[t0]\n\t"
        "s_nop 1\n\t"
        "s_add_i32 s48, s48, %c[nslot]\n\t"
        "s_and_b32 s48, s48, -2\n\t"
        "s_min_i32 s48, s48, s49\n\t"
        "s_cmp_lt_i32 s44, s48\n\t"
        "s_cbranch_scc1 22b\n\t"
        "v_mov_b32 %[t1], s44\n\t"
        "s_waitcnt vmcnt(0)\n\t"
        "ds_write_b32 %[pa], %[t1]\n\t"
        "s_sleep 1\n\t"
        "s_sub_u32 s55, s55, 1\n\t"
        "s_cmp_lg_u32 s55, 0\n\t"
        "s_cbranch_scc1 26b\n\t"
        "s_branch 22b\n"
        "30:\n\t"
        ".set k, 6\n\t"
        ".rept 7\n\t"
        ".set wcnt, 4 * k\n\t"
        "v_mov_b32 %[t1], 2 * %c[nku] - k\n\t"
        WAITW
        "ds_write_b32 %[pa], %[t1]\n\t"
        ".set k, k - 1\n\t"
        ".endr\n\t"
        "s_waitcnt lgkmcnt(0)\n\t"
        "s_endpgm\n\t"
        ".p2align 7\n"
        "23:\n\t"
        ".set wcnt, %c[wmin]-1\n\t"
        ".rept %c[wmax] - %c[wmin] + 1\n\t"
        UNIT4("%[vo]")
        "ds_write_b32 %[pa], %[t1]\n\t"
        "s_mov_b32 m0, s55\n\t"
        UNIT4("%[t0]")
        "s_branch 24b\n\t"
        ".p2align 7\n\t"
        ".set wcnt, wcnt+1\n\t"
        ".endr\n"
        "90:\n\t"
        "s_waitcnt lgkmcnt(0)"
        : [va] "=&v"(va), [vb] "=&v"(vb), [vo] "=&v"(vo), [pa] "=&v"(pa), [nxa] "=&v"(nxa), [t0] "=&v"(t0), [t1] "=&v"(t1), [t2] "=&v"(t2), [t3] "=&v"(t3)
        : [tid] "v"(threadIdx.x), [bid] "s"(blockIdx.x), [ka] "s"(__builtin_amdgcn_kernarg_segment_ptr()), [nch] "i"(CODE_KIB - 1), [jk] "i"(RING - UNIT),
          [wmin] "i"(WMIN), [wmax] "i"(WMAX), [gain] "i"(WGAIN), [wmid] "i"(WMID), [tau2] "i"(TAU2),
          [ctl] "i"(L_CTRL), [nslot] "i"(NSLOT), [nku] "i"(NKU), [cap] "i"(SPIN_CAP), [jump] "i"(JUMP), [decay] "i"(DECAY), [uend] "i"(UEND), [gain2] "i"(WGAIN2)
        : "memory", "scc", "s40", "s41", "s42", "s43", "s44", "s45", "s46", "s47", "s48", "s49", "s50", "s51", "s52", "s53", "s54", "s55", "s56", "s57", "s58", "s59", "s62", "s63", "s64");
  }
  __builtin_amdgcn_sched_barrier(0);
  const int tid = threadIdx.x, lane = tid & 63, wave = __builtin_amdgcn_readfirstlane(tid >> 6);
  const int xg = blockIdx.x & 7, kk = blockIdx.x >> 3;
  const int b = kk >> 3, pj = 8 * xg + (kk & 7), h = pj >> 1;
  const int r4 = lane >> 4, c16 = lane & 15;
  asm volatile("" : "+s"(x), "+s"(wq), "+s"(wo), "+s"(w1), "+s"(w3), "+s"(w2), "+s"(gw), "+s"(out), "+s"(ws));
  if (wave == 1) {
    const int wi = lane - 16;
    const int now = (int)(unsigned)__builtin_amdgcn_s_memrealtime() + REF0;
    ctrl[lane] = (lane == 1 || lane == 2) ? now : lane == 8 ? now - REF0 : (lane < 16 || lane >= 32) ? 0 : wi < NCW ? wi : wi < NCW + NHW ? NHC * NCW + NHH * (wi - NCW) : NEVER;
  }
  BARRIER();

  const __amdgpu_buffer_rsrc_t wsr = mkrsrc(ws, WS_BYTES);
  auto wg_max = [&]() __attribute__((always_inline)) {
    float mg = ctrlf[32 + (c16 < NCW + NHW ? c16 : 0)];
    mg = fmaxf(mg, dpp<ROR8>(mg)); mg = fmaxf(mg, dpp<ROR4>(mg)); mg = fmaxf(mg, dpp<ROR2>(mg)); mg = fmaxf(mg, dpp<ROR1>(mg));
    return mg;
  };

  if (wave == XWAVE + 1) {
    unsigned k = ((unsigned)__builtin_amdgcn_s_memrealtime() >> 8) + 1;
#pragma unroll 1
    for (int it = 0; it < 64; ++it, ++k) {
      wait_until(k << 8);
      if (__builtin_amdgcn_readfirstlane(ctrl[0]) >= NUNIT - 36) break;
      const int own = __builtin_amdgcn_readfirstlane(ctrl[1]);
      if (blockIdx.x < 8 && lane < 8) __builtin_amdgcn_raw_buffer_store_b128(u32x4{(unsigned)own, (unsigned)own, (unsigned)own, (unsigned)own}, wsr, WS_PACE + (k & 31u) * 2048 + blockIdx.x * 128 + lane * 16, 0, SC1);
      if (it == 0) continue;
      int e[8];
      asm volatile("s_load_dword %0, %8, 0x0\n\ts_load_dword %1, %8, 0x80\n\ts_load_dword %2, %8, 0x100\n\ts_load_dword %3, %8, 0x180\n\t"
                   "s_load_dword %4, %8, 0x200\n\ts_load_dword %5, %8, 0x280\n\ts_load_dword %6, %8, 0x300\n\ts_load_dword %7, %8, 0x380\n\t"
                   "s_waitcnt lgkmcnt(0)"
                   : "=&s"(e[0]), "=&s"(e[1]), "=&s"(e[2]), "=&s"(e[3]), "=&s"(e[4]), "=&s"(e[5]), "=&s"(e[6]), "=&s"(e[7])
                   : "s"(ws + WS_PACE + ((k - 1u) & 31u) * 2048) : "memory");
      int sum = 0, cnt = 0;
#pragma unroll
      for (int i = 0; i < 8; ++i) { const int d = e[i] - own; const bool ok = (unsigned)(d + 600) < 1200u; sum += ok ? d : 0; cnt += ok ? 1 : 0; }
      if (cnt >= 4) ctrl[2] = own + (int)((float)sum / (float)cnt);
    }
    return;
  }

  if (wave == XWAVE) {
#pragma unroll 1
    for (int i = 0; i < 20; ++i) PAUSE(4);
    const u32x4 xv = *(const GAS u32x4*)(x + b * D + 8 * lane), gv = *(const GAS u32x4*)(gw + 8 * lane);
    *(LAS u32x4*)(L + L_X + lane * 16) = xv;
    float gd = 0.f;
#pragma unroll
    for (int k = 0; k < 4; ++k) gd = dot2(xv[k], gv[k], gd);
    gd = xrow_sum(row16_sum(gd));
    const float g = 1.f / (1.f + __expf(-gd));
    ctrlf[48] = g;
    lds_arrive(L, C_X, lane);
    u64 tag;
    asm volatile("s_load_dwordx2 %0, %1, 0x0\n\ts_waitcnt lgkmcnt(0)" : "=s"(tag) : "s"(ws + WS_EPOCH + b * 256) : "memory");
    tag += TAG_STEP;
#pragma unroll 1
    for (int spin = 0; spin < SPIN_CAP; ++spin) { if (__builtin_amdgcn_readfirstlane(ctrl[C_MAX]) >= NCW + NHW) break; __builtin_amdgcn_s_sleep(16); }
    CFENCE();
    const float mg = wg_max();
    float l = 0.f;
#pragma unroll 4
    for (int i = 0; i < NKU * 64 / 1024; ++i) {
      const f32x4 sc = *(const LAS f32x4*)(L + L_SC + i * 1024 + lane * 16);
      l += (expd(sc[0] - mg) + expd(sc[1] - mg)) + (expd(sc[2] - mg) + expd(sc[3] - mg));
    }
    l = xrow_sum(row16_sum(l));
    const unsigned tlo = (unsigned)tag, thi = (unsigned)(tag >> 32);
    if (lane < 8) __builtin_amdgcn_raw_buffer_store_b128(u32x4{__float_as_uint(mg), tlo, __float_as_uint(l), thi}, wsr, WS_REC + (b * 64 + pj) * 128 + lane * 16, 0, SC1);
#pragma unroll 1
    for (int i = 0; i < 12 + (kk & 31); ++i) PAUSE(12);
    u32x4 rec;
#pragma unroll 1
    for (int spin = 0; spin < SPIN_CAP; ++spin) {
      CFENCE();
      rec = __builtin_amdgcn_raw_buffer_load_b128(wsr, WS_REC + (b * 64 + (pj ^ 1)) * 128, 0, SC1);
      if (__builtin_amdgcn_ballot_w64(rec[1] != tlo || rec[3] != thi) == 0ull) break;
      __builtin_amdgcn_s_sleep(16);
    }
    const float mo = __uint_as_float(rec[0]), lo = __uint_as_float(rec[2]);
    const float mh = fmaxf(mg, mo), wt = expd(mg - mh);
    ctrlf[49] = g * wt / (wt * l + expd(mo - mh) * lo);
    const u32x4 yt = {tlo & YMASK, (tlo >> 12) & YMASK, (unsigned)(tag >> 24) & YMASK, (thi >> 4) & YMASK};
    *(LAS u32x4*)(L + L_CTRL + 52 * 4) = yt;
    lds_arrive(L, C_CF, lane);
    GAS unsigned* op = (GAS unsigned*)(out + b * D + 8 * pj + 4 * (lane & 1) + 2 * (lane >> 5));
    int off = WS_Y + (b * 64 + (lane >> 1)) * 2048 + pj * 32 + (lane & 1) * 16;
    asm volatile("" : "+v"(op), "+v"(off));
    BARRIER();
    {
      const unsigned now = (unsigned)__builtin_amdgcn_s_memrealtime();
      const int d = __builtin_amdgcn_readfirstlane(ctrl[2]) + TSTREAM + FOFF - (int)now;
      const unsigned t = now + (unsigned)min(max(d, FMIN), FMAX);
#pragma unroll 1
      for (int spin = 0; spin < 4096 && (int)(t - (unsigned)__builtin_amdgcn_s_memrealtime()) > 0; ++spin) {}
    }
    u32x4 ya, yb;
#pragma unroll 1
    for (int spin = 0; spin < SPIN_CAP; ++spin) {
      CFENCE();
      ya = __builtin_amdgcn_raw_buffer_load_b128(wsr, off, 0, SC1);
      yb = __builtin_amdgcn_raw_buffer_load_b128(wsr, off, 32 * 2048, SC1);
      const unsigned bad = (((ya[0] ^ yt[0]) | (ya[1] ^ yt[1])) | ((ya[2] ^ yt[2]) | (ya[3] ^ yt[3])) | ((yb[0] ^ yt[0]) | (yb[1] ^ yt[1])) | ((yb[2] ^ yt[2]) | (yb[3] ^ yt[3]))) & YMASK;
      if (__builtin_amdgcn_ballot_w64(bad != 0u) == 0ull) break;
    }
    float o[4];
#pragma unroll
    for (int k = 0; k < 4; ++k) o[k] = __uint_as_float(ya[k] & ~YMASK) + __uint_as_float(yb[k] & ~YMASK);
    float e0 = swadd32(o[0], o[2]), e1 = swadd32(o[1], o[3]);
    e0 = swadd16(e0, e0); e1 = swadd16(e1, e1);
    e0 += dpp<ROR8>(e0); e1 += dpp<ROR8>(e1); e0 += dpp<ROR4>(e0); e1 += dpp<ROR4>(e1); e0 += dpp<ROR2>(e0); e1 += dpp<ROR2>(e1);
    if ((lane & 30) == 0) *op = pack_bf16(e0, e1);
    if (pj == 0 && lane < 8) __builtin_amdgcn_raw_buffer_store_b128(u32x4{tlo, thi, tlo, thi}, wsr, WS_EPOCH + b * 256 + lane * 16, 0, SC1);
    return;
  }

  const int cw = wave - 1, hw = wave - 1 - NCW;
  int landed = 0;
  auto wait_unit = [&](int u) __attribute__((always_inline)) {
#pragma unroll 1
    for (int spin = 0; landed <= u && spin < SPIN_CAP; ++spin) {
      landed = __builtin_amdgcn_readfirstlane(ctrl[0]);
    }
    CFENCE();
  };
  auto take = [&](int u, int next, u32x4& a0, u32x4& a1, u32x4& a2, u32x4& a3) __attribute__((always_inline)) {
    wait_unit(u);
    const LAS u32x4* sp = (const LAS u32x4*)(L + (u & (NSLOT - 1)) * UNIT + lane * 16);
    a0 = sp[0]; a1 = sp[64]; a2 = sp[128]; a3 = sp[192];
    LDS_WAIT();
    ctrl[16 + cw] = next;
    asm volatile("" : "+v"(a0), "+v"(a1), "+v"(a2), "+v"(a3) :: "memory");
  };
  u32x4 q;
  auto build_q = [&]() __attribute__((always_inline)) {
    lds_await(ctrl, C_Q, NHW);
    const LAS f32x4* qp = (const LAS f32x4*)(L + L_QP + (2 * r4 * DH + 8 * c16) * 4);
    const f32x4 lo = qp[0] + qp[DH / 4], hi = qp[1] + qp[DH / 4 + 1];
    q[0] = pack_bf16(xrow_sum(lo[0]), xrow_sum(lo[1])); q[1] = pack_bf16(xrow_sum(lo[2]), xrow_sum(lo[3]));
    q[2] = pack_bf16(xrow_sum(hi[0]), xrow_sum(hi[1])); q[3] = pack_bf16(xrow_sum(hi[2]), xrow_sum(hi[3]));
  };
  constexpr float SCALE = 0.088388347648318440f;
  float mx = -3.4028234e38f;
  auto score = [&](int u, const u32x4& k0, const u32x4& k1, const u32x4& k2, const u32x4& k3) __attribute__((always_inline)) {
    float d0 = 0.f, d1 = 0.f, d2 = 0.f, d3 = 0.f;
#pragma unroll
    for (int k = 0; k < 4; ++k) { d0 = dot2(k0[k], q[k], d0); d1 = dot2(k1[k], q[k], d1); d2 = dot2(k2[k], q[k], d2); d3 = dot2(k3[k], q[k], d3); }
    const unsigned s01 = pack_bf16(row16_sum(d0), row16_sum(d1)), s23 = pack_bf16(row16_sum(d2), row16_sum(d3));
    const unsigned t01 = pack_bf16(bflo(s01) * SCALE, bfhi(s01) * SCALE), t23 = pack_bf16(bflo(s23) * SCALE, bfhi(s23) * SCALE);
    const f32x4 sc = {bflo(t01), bfhi(t01), bflo(t23), bfhi(t23)};
    mx = fmaxf(fmaxf(mx, fmaxf(sc[0], sc[1])), fmaxf(sc[2], sc[3]));
    if (c16 == 0) *(LAS f32x4*)(L + L_SC + u * 64 + r4 * 16) = sc;
  };

  if (hw < 0) {
    {
      u32x4 hk[4 * NHC];
#pragma unroll
      for (int i = 0; i < NHC; ++i) take(cw + NCW * i, i + 1 < NHC ? cw + NCW * (i + 1) : NHELD + cw, hk[4 * i], hk[4 * i + 1], hk[4 * i + 2], hk[4 * i + 3]);
      build_q();
#pragma unroll
      for (int i = 0; i < NHC; ++i) { score(cw + NCW * i, hk[4 * i], hk[4 * i + 1], hk[4 * i + 2], hk[4 * i + 3]); __builtin_amdgcn_sched_barrier(0); }
    }
#pragma unroll 1
    for (int u = NHELD + cw; u < NKU; u += NCW) {
      u32x4 k0, k1, k2, k3;
      take(u, u + NCW, k0, k1, k2, k3);
      score(u, k0, k1, k2, k3);
    }
    ctrlf[32 + cw] = xrow_max(mx);
    lds_arrive(L, C_MAX, lane);
    lds_await(ctrl, C_MAX, NCW + NHW);
    const float mg = wg_max();
    float acc[8] = {0.f, 0.f, 0.f, 0.f, 0.f, 0.f, 0.f, 0.f};
    LAS unsigned* rp = (LAS unsigned*)(L + L_REC + cw * 272 + (8 * c16 + 2 * r4) * 2);
    asm volatile("" : "+v"(rp));
    float cf = 0.f;
#pragma unroll 1
    for (int u = NKU + cw; u < NUNIT; u += NCW) {
      const f32x4 sc = *(const LAS f32x4*)(L + L_SC + (u - NKU) * 64 + r4 * 16);
      float p[4];
#pragma unroll
      for (int t = 0; t < 4; ++t) { p[t] = expd(sc[t] - mg); asm volatile("" : "+v"(p[t])); }
      if ((u & ~(NCW - 1)) == NUNIT - 32) { lds_await(ctrl, C_CF, 1); cf = ctrlf[49]; }
      u32x4 v[4];
      take(u, u + NCW, v[0], v[1], v[2], v[3]);
#pragma unroll
      for (int t = 0; t < 4; ++t)
#pragma unroll
        for (int k = 0; k < 4; ++k) { acc[2 * k] = fmaf(p[t], bflo(v[t][k]), acc[2 * k]); acc[2 * k + 1] = fmaf(p[t], bfhi(v[t][k]), acc[2 * k + 1]); }
    }
    const float e0 = swadd16(swadd32(acc[0], acc[4]), swadd32(acc[2], acc[6])), e1 = swadd16(swadd32(acc[1], acc[5]), swadd32(acc[3], acc[7]));
    *rp = pack_bf16(e0 * cf, e1 * cf);
    BARRIER();
    return;
  }

  u32x4 wb[16];
  const unsigned tstart = (unsigned)__builtin_amdgcn_readfirstlane(ctrl[8]);
  {
    const __amdgpu_buffer_rsrc_t rq = mkrsrc(wq, D * H * DH * 2);
    const int vo = ((64 * hw + 2 * r4) * (H * DH) + h * DH + 8 * c16) * 2;
#pragma unroll
    for (int i = 0; i < 16; ++i) { if (i == 0) wait_until(tstart + TQ + hw * QA); else PAUSE(QS); wb[i] = ldw(rq, vo, (8 * (i >> 1) + (i & 1)) * (H * DH * 2)); }
    lds_await(ctrl, C_X, 1);
    const LAS unsigned* xl = (const LAS unsigned*)(L + L_X + (64 * hw + 2 * r4) * 2);
    float a[8] = {0.f, 0.f, 0.f, 0.f, 0.f, 0.f, 0.f, 0.f};
#pragma unroll
    for (int i = 0; i < 8; ++i) {
      const unsigned xp = xl[4 * i];
#pragma unroll
      for (int k = 0; k < 4; ++k) {
        a[2 * k] = dot2(perm_lo(wb[2 * i][k], wb[2 * i + 1][k]), xp, a[2 * k]);
        a[2 * k + 1] = dot2(perm_hi(wb[2 * i][k], wb[2 * i + 1][k]), xp, a[2 * k + 1]);
      }
    }
    const float e0 = swadd16(swadd32(a[0], a[4]), swadd32(a[2], a[6])), e1 = swadd16(swadd32(a[1], a[5]), swadd32(a[3], a[7]));
    *(LAS f32x2*)(L + L_QP + (hw * DH + 8 * c16 + 2 * r4) * 4) = f32x2{e0, e1};
    lds_arrive(L, C_Q, lane);
    const int hb = NHC * NCW + NHH * hw;
#pragma unroll
    for (int i = 0; i < NHH; ++i) take(hb + i, i + 1 < NHH ? hb + i + 1 : NEVER, wb[4 * i], wb[4 * i + 1], wb[4 * i + 2], wb[4 * i + 3]);
    build_q();
#pragma unroll
    for (int i = 0; i < NHH; ++i) { score(hb + i, wb[4 * i], wb[4 * i + 1], wb[4 * i + 2], wb[4 * i + 3]); __builtin_amdgcn_sched_barrier(0); }
    ctrlf[32 + cw] = xrow_max(mx);
    lds_arrive(L, C_MAX, lane);
  }
  f32x4 y[4] = {{0.f, 0.f, 0.f, 0.f}, {0.f, 0.f, 0.f, 0.f}, {0.f, 0.f, 0.f, 0.f}, {0.f, 0.f, 0.f, 0.f}};
  if ((pj & 7) != 7) {
    const int sl = 7 * (pj >> 3) + (pj & 7);
    const __amdgpu_buffer_rsrc_t r1 = mkrsrc(w1, D * F * 2), r3 = mkrsrc(w3, D * F * 2), r2 = mkrsrc(w2, F * D * 2);
    const int vo = ((64 * hw + (lane & 60)) * F + FS * sl + 8 * (lane & 3)) * 2;
#pragma unroll
    for (int i = 0; i < 8; ++i) { if (i == 0) wait_until(tstart + T1 + hw * A1); else PAUSE(S1); wb[i] = ldw(i < 4 ? r1 : r3, vo, (i & 3) * (F * 2)); }
    const u32x2 xm = *(const LAS u32x2*)(L + L_X + (64 * hw + (lane & 60)) * 2);
    float a[16];
#pragma unroll
    for (int m = 0; m < 2; ++m)
#pragma unroll
      for (int k = 0; k < 4; ++k) {
        a[8 * m + 2 * k] = dot2(perm_lo(wb[4 * m + 2][k], wb[4 * m + 3][k]), xm[1], dot2(perm_lo(wb[4 * m][k], wb[4 * m + 1][k]), xm[0], 0.f));
        a[8 * m + 2 * k + 1] = dot2(perm_hi(wb[4 * m + 2][k], wb[4 * m + 3][k]), xm[1], dot2(perm_hi(wb[4 * m][k], wb[4 * m + 1][k]), xm[0], 0.f));
      }
    f32x4 e;
#pragma unroll
    for (int k = 0; k < 4; ++k) {
      float t = swadd16(swadd32(a[k], a[8 + k]), swadd32(a[4 + k], a[12 + k]));
      t += dpp<ROR8>(t); e[k] = t + dpp<ROR4>(t);
    }
    if ((lane & 12) == 0) *(LAS f32x4*)(L + L_MP + (hw * 64 + (lane & 32) + 8 * (lane & 3) + 4 * (r4 & 1)) * 4) = e;
    lds_arrive(L, C_MLP, lane);
    u32x2 wr[8];
#pragma unroll
    for (int i = 0; i < 8; ++i) { if (i == 0) wait_until(tstart + T2 + hw * A2); else PAUSE(S2); wr[i] = ldw2(r2, ((FS * sl + 8 * r4) * D + 64 * hw + 4 * c16) * 2, i * (D * 2)); }
    lds_await(ctrl, C_MLP, NHW);
    const LAS float* mp = (const LAS float*)(L + L_MP) + (lane & 31);
    float ga = 0.f, up = 0.f;
#pragma unroll
    for (int w = 0; w < NHW; ++w) { ga += mp[w * 64]; up += mp[w * 64 + 32]; }
    const float hv = ga / (1.f + __expf(-ga)) * up * (1.f - ctrlf[48]);
    const unsigned hh = pack_bf16(hv, 0.f), hl = pack_bf16(hv - bflo(hh), 0.f);
    *(LAS u16*)(L + L_H + hw * 128 + lane * 2) = (u16)(lane < 32 ? hh : hl);
    LDS_WAIT();
    u32x4 ha = *(const LAS u32x4*)(L + L_H + hw * 128 + (c16 & 1) * 64 + r4 * 16);
    if (c16 >= 2) ha = u32x4{0u, 0u, 0u, 0u};
#pragma unroll
    for (int e = 0; e < 4; ++e) {
      u32x4 f;
#pragma unroll
      for (int k = 0; k < 4; ++k) f[k] = (e & 1) ? perm_hi(wr[2 * k][e >> 1], wr[2 * k + 1][e >> 1]) : perm_lo(wr[2 * k][e >> 1], wr[2 * k + 1][e >> 1]);
      y[e] = mfma(ha, f, y[e]);
    }
  }
#pragma unroll
  for (int e = 0; e < 4; ++e) asm volatile("" : "+v"(y[e]));
  {
    const __amdgpu_buffer_rsrc_t ro = mkrsrc(wo, H * DH * D * 2);
#pragma unroll
    for (int t = 0; t < 4; ++t) {
      u32x2 wr[8];
#pragma unroll
      for (int i = 0; i < 8; ++i) { if (t + i == 0) wait_until(tstart + TO + hw * AO); else PAUSE(SO); wr[i] = ldw2(ro, ((h * DH + 32 * r4) * D + 64 * hw + 4 * c16) * 2, (8 * t + i) * (D * 2)); }
#pragma unroll
      for (int e = 0; e < 4; ++e) {
#pragma unroll
        for (int k = 0; k < 4; ++k) wb[4 * t + e][k] = (e & 1) ? perm_hi(wr[2 * k][e >> 1], wr[2 * k + 1][e >> 1]) : perm_lo(wr[2 * k][e >> 1], wr[2 * k + 1][e >> 1]);
        asm volatile("" : "+v"(wb[4 * t + e]));
      }
    }
  }
  lds_await(ctrl, C_CF, 1);
  u32x4 yt = *(const LAS u32x4*)(L + L_CTRL + 52 * 4);
  int yo = WS_Y + (b * 64 + pj) * 2048 + hw * 256 + c16 * 16;
  const LAS u32x4* rec = (const LAS u32x4*)(L + L_REC + (lane & 3) * 272 + r4 * 64);
  unsigned ym = ~YMASK;
  asm volatile("" : "+v"(yt), "+v"(yo), "+v"(rec), "+s"(ym));
  BARRIER();
  {
    const u32x4 a[4] = {rec[0], rec[1], rec[2], rec[3]};
#pragma unroll
    for (int t = 0; t < 4; ++t)
#pragma unroll
      for (int e = 0; e < 4; ++e) y[e] = mfma(a[t], wb[4 * t + e], y[e]);
    u32x4 v;
#pragma unroll
    for (int e = 0; e < 4; ++e) {
      const f32x2 t = y[e].xy + y[e].zw;
      unsigned r;
      asm("v_add_f32 %0, %1, %2\n\tv_add_u32 %0, %5, %0\n\tv_and_or_b32 %0, %0, %3, %4" : "=&v"(r) : "v"(t[0]), "v"(t[1]), "s"(ym), "v"(yt[e]), "i"((YMASK + 1u) / 2u));
      v[e] = r;
    }
    if (lane < 16) __builtin_amdgcn_raw_buffer_store_b128(v, wsr, yo, 0, SC1);
  }
}

extern "C" void kernel_launch(void* const* d_in, const int* in_sizes, int n_in, void* d_out, int out_size, void* d_ws, size_t ws_size,
                              hipStream_t stream) {
  gated_mix__mha_block_cached__glu_gated_swiglu__decode_kernel<<<256, NTHR, 0, stream>>>((const u16*)d_in[0], (const u16*)d_in[1], (const u16*)d_in[2], (const u16*)d_in[3], (const u16*)d_in[4],
                                             (const u16*)d_in[6], (const u16*)d_in[7], (const u16*)d_in[8], (const u16*)d_in[9], (u16*)d_out,
                                             (unsigned char*)d_ws);
}
```

```cpp
#include <hip/hip_runtime.h>
#include <hip/hip_bf16.h>

#define GAS __attribute__((address_space(1)))
#define LAS __attribute__((address_space(3)))
typedef unsigned short u16;
typedef unsigned long long u64;
using u32x4 = __attribute__((ext_vector_type(4))) unsigned;
using u32x2 = __attribute__((ext_vector_type(2))) unsigned;
using f32x4 = __attribute__((ext_vector_type(4))) float;
using f32x2 = __attribute__((ext_vector_type(2))) float;
using bf16x2 = __attribute__((ext_vector_type(2))) __bf16;
using bf16x8 = __attribute__((ext_vector_type(8))) __bf16;

constexpr int B = 4, D = 512, H = 32, SK = 8192, DH = 128, F = 1792;
constexpr int NCW = 4, NHW = 8, XWAVE = 1 + NCW + NHW, NWAVE = XWAVE + 2, NTHR = NWAVE * 64;
constexpr int TQ = 100, QA = 112, QS = 2;
constexpr int T1 = 1400, A1 = 150, S1 = 6;
constexpr int T2 = 2500, A2 = 40, S2 = 1;
constexpr int TO = 2900, AO = 350, SO = 2;
constexpr int TSTREAM = 6860;
constexpr int WMID = 5472, WMIN = 16, WMAX = 28;
constexpr int WGAIN = 10, WGAIN2 = 18, UEND = 420;
constexpr int JUMP = 100, DECAY = 3;
constexpr int REF0 = 180;
constexpr int FOFF = 156, FMIN = 45, FMAX = 150;
constexpr int CODE_KIB = 21;

constexpr int UNIT = 4096;
constexpr int NSLOT = 32, RING = NSLOT * UNIT;
constexpr int NKU = (SK / 2) / 16, NUNIT = 2 * NKU;
constexpr int NHC = 6, NHH = 4, NHELD = NHC * NCW + NHH * NHW;
constexpr int FS = 32;
constexpr int L_CTRL = RING;
constexpr int L_X = L_CTRL + 256;
constexpr int L_QP = L_X + D * 2;
constexpr int L_MP = L_QP + NHW * DH * 4;
constexpr int L_REC = L_MP + NHW * 64 * 4;
constexpr int L_H = L_REC + NCW * 272;
constexpr int L_SC = L_H + NHW * 128;
constexpr int LDS_BYTES = L_SC + NKU * 64;
static_assert(LDS_BYTES <= 159 * 1024, "lds");
static_assert(WMIN <= WMID / 256 && WMID / 256 <= WMAX && WMAX <= 28, "window");
constexpr int WS_EPOCH = 0;
constexpr int WS_REC = 4096;
constexpr int WS_PACE = WS_REC + 256 * 128;
constexpr int WS_Y = WS_PACE + 32 * 2048;
constexpr int WS_BYTES = WS_Y + B * 64 * 2048;
constexpr u64 TAG_STEP = 0x9E3779B97F4A7C15ull;
constexpr unsigned YMASK = 0xFFFu;
constexpr int NT = 2, SC1 = 16;
constexpr int SPIN_CAP = 1 << 22;
constexpr int NEVER = 1 << 20;
constexpr int C_Q = 4, C_MAX = 5, C_MLP = 6, C_X = 7, C_CF = 9;

__device__ __forceinline__ float expd(float d) { return __builtin_amdgcn_exp2f(d * 1.4426950408889634f); }
__device__ __forceinline__ float bflo(unsigned u) { return __builtin_bit_cast(float, u << 16); }
__device__ __forceinline__ float bfhi(unsigned u) { return __builtin_bit_cast(float, u & 0xffff0000u); }
__device__ __forceinline__ float dot2(unsigned a, unsigned b, float acc) {
  return __builtin_amdgcn_fdot2_f32_bf16(__builtin_bit_cast(bf16x2, a), __builtin_bit_cast(bf16x2, b), acc, false);
}
__device__ __forceinline__ unsigned pack_bf16(float lo, float hi) {
  const bf16x2 v = {(__bf16)lo, (__bf16)hi};
  return __builtin_bit_cast(unsigned, v);
}
__device__ __forceinline__ unsigned perm_lo(unsigned a, unsigned b) { return __builtin_amdgcn_perm(b, a, 0x05040100u); }
__device__ __forceinline__ unsigned perm_hi(unsigned a, unsigned b) { return __builtin_amdgcn_perm(b, a, 0x07060302u); }
template <int CTRL> __device__ __forceinline__ float dpp(float x) {
  return __builtin_bit_cast(float, __builtin_amdgcn_mov_dpp(__builtin_bit_cast(int, x), CTRL, 0xf, 0xf, true));
}
constexpr int ROR1 = 0x121, ROR2 = 0x122, ROR4 = 0x124, ROR8 = 0x128;
__device__ __forceinline__ float row16_sum(float x) { x += dpp<ROR8>(x); x += dpp<ROR4>(x); x += dpp<ROR2>(x); x += dpp<ROR1>(x); return x; }
__device__ __forceinline__ float swadd32(float a, float b) {
  auto t = __builtin_amdgcn_permlane32_swap(__float_as_uint(a), __float_as_uint(b), false, false);
  return __uint_as_float(t[0]) + __uint_as_float(t[1]);
}
__device__ __forceinline__ float swadd16(float a, float b) {
  auto t = __builtin_amdgcn_permlane16_swap(__float_as_uint(a), __float_as_uint(b), false, false);
  return __uint_as_float(t[0]) + __uint_as_float(t[1]);
}
__device__ __forceinline__ float xrow_sum(float x) { x = swadd16(x, x); return swadd32(x, x); }
__device__ __forceinline__ float xrow_max(float x) {
  auto s = __builtin_amdgcn_permlane16_swap(__float_as_uint(x), __float_as_uint(x), false, false);
  x = fmaxf(__uint_as_float(s[0]), __uint_as_float(s[1]));
  auto t = __builtin_amdgcn_permlane32_swap(__float_as_uint(x), __float_as_uint(x), false, false);
  return fmaxf(__uint_as_float(t[0]), __uint_as_float(t[1]));
}
__device__ __forceinline__ __amdgpu_buffer_rsrc_t mkrsrc(const void* p, int bytes) { return __builtin_amdgcn_make_buffer_rsrc((void*)p, 0, bytes, 0x00020000); }
__device__ __forceinline__ u32x4 ldw(__amdgpu_buffer_rsrc_t r, int voff, int soff) { return __builtin_amdgcn_raw_buffer_load_b128(r, voff, soff, NT); }
#define LDS_WAIT() asm volatile("s_waitcnt lgkmcnt(0)" ::: "memory")
#define CFENCE() asm volatile("" ::: "memory")
#define BARRIER() asm volatile("s_waitcnt lgkmcnt(0)\n\ts_barrier" ::: "memory")
#define PAUSE(n) do { __builtin_amdgcn_sched_barrier(0); asm volatile("s_sleep %0" :: "i"(n) : "memory"); __builtin_amdgcn_sched_barrier(0); } while (0)

__device__ __forceinline__ void lds_arrive(LAS unsigned char* L, int idx, int lane) {
  LDS_WAIT();
  if (lane == 0) __hip_atomic_fetch_add((LAS int*)(L + L_CTRL) + idx, 1, __ATOMIC_RELAXED, __HIP_MEMORY_SCOPE_WORKGROUP);
}
__device__ __forceinline__ void lds_await(volatile LAS int* ctrl, int idx, int n) {
#pragma unroll 1
  for (int spin = 0; spin < SPIN_CAP; ++spin) { if (__builtin_amdgcn_readfirstlane(ctrl[idx]) >= n) break; __builtin_amdgcn_s_sleep(1); }
  CFENCE();
}

__device__ __forceinline__ void wait_until(unsigned t) {
  __builtin_amdgcn_sched_barrier(0);
#pragma unroll 1
  for (int spin = 0; spin < 4096; ++spin) {
    const int d = (int)(t - (unsigned)__builtin_amdgcn_s_memrealtime());
    if (d <= 0) break;
    if (d > 24) __builtin_amdgcn_s_sleep(6);
  }
  CFENCE();
  __builtin_amdgcn_sched_barrier(0);
}
constexpr int TAU2 = 2 * (TSTREAM * 256 / NUNIT);
__device__ __forceinline__ f32x4 mfma(const u32x4& a, const u32x4& b, const f32x4& c) {
  return __builtin_amdgcn_mfma_f32_16x16x32_bf16(__builtin_bit_cast(bf16x8, a), __builtin_bit_cast(bf16x8, b), c, 0, 0, 0);
}
__device__ __forceinline__ u32x2 ldw2(__amdgpu_buffer_rsrc_t r, int voff, int soff) { return __builtin_amdgcn_raw_buffer_load_b64(r, voff, soff, NT); }

__global__ void __launch_bounds__(NTHR, 4) gated_mix__mha_block_cached__glu_gated_swiglu__decode_kernel(const u16* x, const u16* kc, const u16* vc, const u16* wq, const u16* wo, const u16* w1,
                                                         const u16* w3, const u16* w2, const u16* gw, u16* out, unsigned char* ws) {
  __shared__ __attribute__((aligned(16))) unsigned char lds_raw[LDS_BYTES];
  LAS unsigned char* L = (LAS unsigned char*)lds_raw;
  volatile LAS int* ctrl = (volatile LAS int*)(L + L_CTRL);
  volatile LAS float* ctrlf = (volatile LAS float*)(L + L_CTRL);
  {
    unsigned va, vb, vo, pa, nxa, t0, t1, t2, t3;
#define WAITW "s_waitcnt (wcnt & 15) | ((wcnt >> 4) << 14) | 0x0F70\n\t"
#define SP(v, sb, wait) wait "global_load_lds_dword " v ", " sb " nt\n\tglobal_load_lds_dword " v ", " sb " sc1\n\t"
#define SW "s_waitcnt vmcnt(4)\n\t"
#define UNIT4(v) \
        WAITW "global_load_lds_dwordx4 " v ", s[40:41] nt\n\t" \
        WAITW "global_load_lds_dwordx4 " v ", s[40:41] offset:1024 nt\n\t" \
        WAITW "global_load_lds_dwordx4 " v ", s[40:41] offset:2048 nt\n\t" \
        WAITW "global_load_lds_dwordx4 " v ", s[40:41] offset:3072 nt\n\t"
    asm volatile(
        "s_getpc_b64 s[56:57]\n\t"
        "s_load_dwordx8 s[44:51], %[ka], 0x18\n\t"
        "s_load_dwordx2 s[52:53], %[ka], 0x38\n\t"
        "s_load_dwordx4 s[40:43], %[ka], 0x8\n\t"
        "v_readfirstlane_b32 s54, %[tid]\n\t"
        "s_mov_b32 m0, %c[jk]\n\t"
        "s_lshr_b32 s59, %[bid], 3\n\t"
        "s_cmp_ge_u32 s54, 64\n\t"
        "s_cbranch_scc1 90f\n\t"
        "s_min_u32 s54, s59, %c[nch]\n\t"
        "s_lshl_b32 s54, s54, 10\n\t"
        "v_lshlrev_b32 %[vo], 4, %[tid]\n\t"
        "s_and_b32 s56, s56, -128\n\t"
        "v_add_u32 %[t0], s54, %[vo]\n\t"
        "s_and_b32 s58, %[bid], 7\n\t"
        "global_load_lds_dwordx4 %[t0], s[56:57]\n\t"
        "s_setprio 3\n\t"
        "v_lshlrev_b32 %[t1], 7, %[tid]\n\t"
        "s_lshl_b32 s54, s59, 7\n\t"
        "s_add_u32 s54, s54, s58\n\t"
        "s_lshl_b32 s54, s54, 10\n\t"
        "v_lshrrev_b32 %[va], 3, %[tid]\n\t"
        "v_mul_u32_u24 %[va], 8192 - 1024, %[va]\n\t"
        "v_add3_u32 %[va], %[va], %[t1], s54\n\t"
        "v_add_u32 %[t2], 0x10000, %[va]\n\t"
        "s_lshl_b32 s55, s58, 5\n\t"
        "s_add_u32 s55, s55, s59\n\t"
        "s_mul_i32 s54, s55, 7 * 1024\n\t"
        "v_min_u32 %[t3], 55 * 128, %[t1]\n\t"
        "v_add_u32 %[t3], s54, %[t3]\n\t"
        "s_mul_i32 s54, s58, 448\n\t"
        "s_and_b32 s54, s54, -128\n\t"
        "s_mul_i32 s57, s59, 16 * 3584\n\t"
        "s_add_u32 s54, s54, s57\n\t"
        "v_lshrrev_b32 %[t0], 2, %[tid]\n\t"
        "v_mul_u32_u24 %[t0], 3584 - 512, %[t0]\n\t"
        "v_add3_u32 %[t0], %[t0], %[t1], s54\n\t"
        "v_lshl_add_u32 %[vb], s55, 14, %[t1]\n\t"
        "v_add_u32 %[t1], 0x2000, %[vb]\n\t"
        "s_and_b32 s54, %[bid], 0xc0\n\t"
        "s_lshl3_add_u32 s54, s58, s54\n\t"
        "s_and_b32 s56, s59, 7\n\t"
        "s_add_u32 s54, s54, s56\n\t"
        "s_lshr_b32 s56, s54, 1\n\t"
        "s_lshl_b32 s56, s56, 21\n\t"
        "s_and_b32 s64, s54, 1\n\t"
        "s_bfe_u32 s57, s54, 0x10007\n\t"
        "s_lshl2_add_u32 s64, s57, s64\n\t"
        "s_bfe_u32 s57, s54, 0x10002\n\t"
        "s_lshl4_add_u32 s64, s57, s64\n\t"
        "s_lshl_b32 s64, s64, 16\n\t"
        "s_waitcnt lgkmcnt(0)\n\t"
        SP("%[va]", "s[44:45]", "") SP("%[t2]", "s[44:45]", "") SP("%[t3]", "s[52:53]", "")
        "s_barrier\n\t"
        SP("%[t0]", "s[48:49]", SW) SP("%[t0]", "s[50:51]", SW) SP("%[vb]", "s[46:47]", SW) SP("%[t1]", "s[46:47]", SW)
        "s_add_u32 s40, s40, s56\n\t"
        "s_addc_u32 s41, s41, 0\n\t"
        "s_add_u32 s42, s42, s56\n\t"
        "s_addc_u32 s43, s43, 0\n\t"
        "v_mov_b32 %[va], %[vo]\n\t"
        "v_or_b32 %[vo], s64, %[vo]\n\t"
        ".set so, 0\n\t"
        ".set wcnt, 4\n\t"
        ".rept 4\n\t"
        "s_movk_i32 m0, so\n\t"
        ".if (so & 4096) == 0\n\t"
        WAITW
        ".set wcnt, wcnt + 6\n\t"
        ".else\n\t"
        "s_nop 0\n\t"
        ".endif\n\t"
        "global_load_lds_dwordx4 %[vo], s[40:41] nt\n\t"
        "global_load_lds_dwordx4 %[vo], s[40:41] offset:1024 nt\n\t"
        "global_load_lds_dwordx4 %[vo], s[40:41] offset:2048 nt\n\t"
        "global_load_lds_dwordx4 %[vo], s[40:41] offset:3072 nt\n\t"
        "v_add_u32 %[vo], 0x1000, %[vo]\n\t"
        ".set so, so + 4096\n\t"
        ".endr\n\t"
        "s_waitcnt vmcnt(16)\n\t"
        "s_getpc_b64 s[50:51]\n"
        "20:\n\t"
        "s_add_u32 s50, s50, 23f-20b-%c[wmin]*128\n\t"
        "s_addc_u32 s51, s51, -1\n\t"
        "v_mov_b32 %[pa], %c[ctl]\n\t"
        "v_and_b32 %[nxa], 0x3c0 >> 2, %[vo]\n\t"
        "v_lshrrev_b32 %[nxa], 2, %[nxa]\n\t"
        "v_add_u32 %[nxa], %c[ctl] + 64, %[nxa]\n\t"
        "s_memrealtime s[58:59]\n\t"
        "s_mov_b32 s62, 0\n\t"
        "s_mov_b32 s63, %c[gain]\n\t"
        "s_mov_b32 s44, 4\n\t"
        "s_mov_b32 s45, %c[wmid] >> 8\n\t"
        "s_mov_b32 s46, 0x4000\n\t"
        "s_mov_b32 s47, 2 * %c[tau2]\n\t"
        "s_mov_b32 s48, %c[nslot]\n\t"
        "s_mov_b32 s49, %c[nku]\n\t"
        "s_waitcnt lgkmcnt(0)\n\t"
        "s_mov_b32 s59, 10000\n"
        "22:\n\t"
        "s_memrealtime s[56:57]\n\t"
        "ds_read_b32 %[t3], %[pa] offset:8\n\t"
        "s_lshl_b32 s52, s45, 7\n\t"
        "s_add_u32 s52, s50, s52\n\t"
        "s_addc_u32 s53, s51, 0\n\t"
        "s_add_i32 s54, s45, -1\n\t"
        "s_lshr_b32 s54, s54, 2\n\t"
        "s_sub_i32 s54, s44, s54\n\t"
        "v_mov_b32 %[t1], s54\n\t"
        "v_add_u32 %[t0], 0x1000, %[vo]\n\t"
        "s_add_u32 s55, s46, 0x1000\n\t"
        "s_mov_b32 m0, s46\n\t"
        "s_setpc_b64 s[52:53]\n"
        "24:\n\t"
        "s_waitcnt lgkmcnt(0)\n\t"
        "v_readfirstlane_b32 s57, %[t3]\n\t"
        "v_add_u32 %[t1], 1, %[t1]\n\t"
        "s_lshr_b32 s54, s47, 8\n\t"
        "s_sub_u32 s56, s56, s54\n\t"
        "v_mov_b32 %[t2], s56\n\t"
        "s_sub_i32 s54, s56, s58\n\t"
        "s_mov_b32 s58, s56\n\t"
        "s_sub_i32 s56, s56, s57\n\t"
        "ds_write2_b32 %[pa], %[t1], %[t2] offset1:1\n\t"
        "s_add_i32 s59, s59, s54\n\t"
        "s_add_i32 s59, s59, %c[decay]\n\t"
        "s_cmp_gt_i32 s54, %c[jump]\n\t"
        "s_cselect_b32 s59, s62, s59\n\t"
        "s_min_i32 s62, s56, s59\n\t"
        "s_mul_i32 s56, s62, s63\n\t"
        "s_add_i32 s56, s56, %c[wmid]\n\t"
        "s_ashr_i32 s56, s56, 8\n\t"
        "s_max_i32 s56, s56, %c[wmin]\n\t"
        "s_min_i32 s45, s56, %c[wmax]\n\t"
        "s_add_u32 s47, s47, %c[tau2]\n\t"
        "s_add_u32 s46, s46, 0x2000\n\t"
        "s_and_b32 s46, s46, %c[nslot] * 4096 - 1\n\t"
        "s_add_i32 s44, s44, 2\n\t"
        "s_and_b32 s54, s44, 0xff\n\t"
        "s_and_b32 s55, s44, 0xf0\n\t"
        "s_add_u32 s54, s54, s55\n\t"
        "s_lshl_b32 s54, s54, 12\n\t"
        "s_xor_b32 s54, s54, s64\n\t"
        "v_or_b32 %[vo], s54, %[va]\n\t"
        "s_cmp_lt_i32 s44, s48\n\t"
        "s_cbranch_scc1 22b\n\t"
        "s_cmp_lt_i32 s44, s49\n\t"
        "s_cbranch_scc1 25f\n\t"
        "s_cmp_ge_i32 s44, 2 * %c[nku]\n\t"
        "s_cbranch_scc1 30f\n\t"
        "s_mov_b32 s49, 2 * %c[nku]\n\t"
        "s_mov_b64 s[40:41], s[42:43]\n"
        "25:\n\t"
        "s_cmp_ge_i32 s44, %c[uend]\n\t"
        "s_cselect_b32 s63, %c[gain2], s63\n\t"
        "s_mov_b32 s55, %c[cap]\n"
        "26:\n\t"
        "ds_read_b32 %[t0], %[nxa]\n\t"
        "s_waitcnt lgkmcnt(0)\n\t"
        "s_nop 1\n\t"
        "v_min_i32_dpp %[t0], %[t0], %[t0] row_ror:8 row_mask:0xf bank_mask:0xf\n\t"
        "s_nop 1\n\t"
        "v_min_i32_dpp %[t0], %[t0], %[t0] row_ror:4 row_mask:0xf bank_mask:0xf\n\t"
        "s_nop 1\n\t"
        "v_min_i32_dpp %[t0], %[t0], %[t0] row_ror:2 row_mask:0xf bank_mask:0xf\n\t"
        "s_nop 1\n\t"
        "v_min_i32_dpp %[t0], %[t0], %[t0] row_ror:1 row_mask:0xf bank_mask:0xf\n\t"
        "s_nop 1\n\t"
        "v_readfirstlane_b32 s48, %[t0]\n\t"
        "s_nop 1\n\t"
        "s_add_i32 s48, s48, %c[nslot]\n\t"
        "s_and_b32 s48, s48, -2\n\t"
        "s_min_i32 s48, s48, s49\n\t"
        "s_cmp_lt_i32 s44, s48\n\t"
        "s_cbranch_scc1 22b\n\t"
        "v_mov_b32 %[t1], s44\n\t"
        "s_waitcnt vmcnt(0)\n\t"
        "ds_write_b32 %[pa], %[t1]\n\t"
        "s_sleep 1\n\t"
        "s_sub_u32 s55, s55, 1\n\t"
        "s_cmp_lg_u32 s55, 0\n\t"
        "s_cbranch_scc1 26b\n\t"
        "s_branch 22b\n"
        "30:\n\t"
        ".set k, 6\n\t"
        ".rept 7\n\t"
        ".set wcnt, 4 * k\n\t"
        "v_mov_b32 %[t1], 2 * %c[nku] - k\n\t"
        WAITW
        "ds_write_b32 %[pa], %[t1]\n\t"
        ".set k, k - 1\n\t"
        ".endr\n\t"
        "s_waitcnt lgkmcnt(0)\n\t"
        "s_endpgm\n\t"
        ".p2align 7\n"
        "23:\n\t"
        ".set wcnt, %c[wmin]-1\n\t"
        ".rept %c[wmax] - %c[wmin] + 1\n\t"
        UNIT4("%[vo]")
        "ds_write_b32 %[pa], %[t1]\n\t"
        "s_mov_b32 m0, s55\n\t"
        UNIT4("%[t0]")
        "s_branch 24b\n\t"
        ".p2align 7\n\t"
        ".set wcnt, wcnt+1\n\t"
        ".endr\n"
        "90:\n\t"
        "s_waitcnt lgkmcnt(0)"
        : [va] "=&v"(va), [vb] "=&v"(vb), [vo] "=&v"(vo), [pa] "=&v"(pa), [nxa] "=&v"(nxa), [t0] "=&v"(t0), [t1] "=&v"(t1), [t2] "=&v"(t2), [t3] "=&v"(t3)
        : [tid] "v"(threadIdx.x), [bid] "s"(blockIdx.x), [ka] "s"(__builtin_amdgcn_kernarg_segment_ptr()), [nch] "i"(CODE_KIB - 1), [jk] "i"(RING - UNIT),
          [wmin] "i"(WMIN), [wmax] "i"(WMAX), [gain] "i"(WGAIN), [wmid] "i"(WMID), [tau2] "i"(TAU2),
          [ctl] "i"(L_CTRL), [nslot] "i"(NSLOT), [nku] "i"(NKU), [cap] "i"(SPIN_CAP), [jump] "i"(JUMP), [decay] "i"(DECAY), [uend] "i"(UEND), [gain2] "i"(WGAIN2)
        : "memory", "scc", "s40", "s41", "s42", "s43", "s44", "s45", "s46", "s47", "s48", "s49", "s50", "s51", "s52", "s53", "s54", "s55", "s56", "s57", "s58", "s59", "s62", "s63", "s64");
  }
  __builtin_amdgcn_sched_barrier(0);
  const int tid = threadIdx.x, lane = tid & 63, wave = __builtin_amdgcn_readfirstlane(tid >> 6);
  const int xg = blockIdx.x & 7, kk = blockIdx.x >> 3;
  const int b = kk >> 3, pj = 8 * xg + (kk & 7), h = pj >> 1;
  const int r4 = lane >> 4, c16 = lane & 15;
  asm volatile("" : "+s"(x), "+s"(wq), "+s"(wo), "+s"(w1), "+s"(w3), "+s"(w2), "+s"(gw), "+s"(out), "+s"(ws));
  if (wave == 1) {
    const int wi = lane - 16;
    const int now = (int)(unsigned)__builtin_amdgcn_s_memrealtime() + REF0;
    ctrl[lane] = (lane == 1 || lane == 2) ? now : lane == 8 ? now - REF0 : (lane < 16 || lane >= 32) ? 0 : wi < NCW ? wi : wi < NCW + NHW ? NHC * NCW + NHH * (wi - NCW) : NEVER;
  }
  BARRIER();

  const __amdgpu_buffer_rsrc_t wsr = mkrsrc(ws, WS_BYTES);
  auto wg_max = [&]() __attribute__((always_inline)) {
    float mg = ctrlf[32 + (c16 < NCW + NHW ? c16 : 0)];
    mg = fmaxf(mg, dpp<ROR8>(mg)); mg = fmaxf(mg, dpp<ROR4>(mg)); mg = fmaxf(mg, dpp<ROR2>(mg)); mg = fmaxf(mg, dpp<ROR1>(mg));
    return mg;
  };

  if (wave == XWAVE + 1) {
    unsigned k = (((unsigned)__builtin_amdgcn_s_memrealtime() + 250u) >> 10) + 1;
#pragma unroll 1
    for (int it = 0; it < 32; ++it, ++k) {
      if (__builtin_amdgcn_readfirstlane(ctrl[0]) + (int)((k << 10) - (unsigned)__builtin_amdgcn_s_memrealtime()) / 13 >= NUNIT - 32) break;
      wait_until(k << 10);
      const int own = __builtin_amdgcn_readfirstlane(ctrl[1]);
      if (blockIdx.x < 16 && lane < 8) __builtin_amdgcn_raw_buffer_store_b128(u32x4{(unsigned)own, (unsigned)own, (unsigned)own, (unsigned)own}, wsr, WS_PACE + (k & 31u) * 2048 + blockIdx.x * 128 + lane * 16, 0, SC1);
      wait_until((k << 10) + 200 + 2 * kk);
      const int d = (int)__builtin_amdgcn_raw_buffer_load_b32(wsr, WS_PACE + (k & 31u) * 2048 + (lane & 15) * 128, 0, SC1) - own;
      const bool ok = (unsigned)(d + 600) < 1200u;
      const float sum = row16_sum(ok ? (float)d : 0.f), cnt = row16_sum(ok ? 1.f : 0.f);
      if (cnt >= 8.f) ctrl[2] = own + (int)(sum / cnt);
    }
    return;
  }

  if (wave == XWAVE) {
#pragma unroll 1
    for (int i = 0; i < 20; ++i) PAUSE(4);
    const u32x4 xv = *(const GAS u32x4*)(x + b * D + 8 * lane), gv = *(const GAS u32x4*)(gw + 8 * lane);
    *(LAS u32x4*)(L + L_X + lane * 16) = xv;
    float gd = 0.f;
#pragma unroll
    for (int k = 0; k < 4; ++k) gd = dot2(xv[k], gv[k], gd);
    gd = xrow_sum(row16_sum(gd));
    const float g = 1.f / (1.f + __expf(-gd));
    ctrlf[48] = g;
    lds_arrive(L, C_X, lane);
#pragma unroll 1
    for (int i = 0; i < 16 + kk; ++i) PAUSE(20);
    const u32x2 ep = __builtin_amdgcn_raw_buffer_load_b64(wsr, WS_EPOCH + b * 256, 0, SC1);
    const u64 tag = (((u64)(unsigned)__builtin_amdgcn_readfirstlane(ep[1]) << 32) | (unsigned)__builtin_amdgcn_readfirstlane(ep[0])) + TAG_STEP;
#pragma unroll 1
    for (int spin = 0; spin < SPIN_CAP; ++spin) { if (__builtin_amdgcn_readfirstlane(ctrl[C_MAX]) >= NCW + NHW) break; __builtin_amdgcn_s_sleep(16); }
    CFENCE();
    const float mg = wg_max();
    float l = 0.f;
#pragma unroll 4
    for (int i = 0; i < NKU * 64 / 1024; ++i) {
      const f32x4 sc = *(const LAS f32x4*)(L + L_SC + i * 1024 + lane * 16);
      l += (expd(sc[0] - mg) + expd(sc[1] - mg)) + (expd(sc[2] - mg) + expd(sc[3] - mg));
    }
    l = xrow_sum(row16_sum(l));
    const unsigned tlo = (unsigned)tag, thi = (unsigned)(tag >> 32);
    if (lane < 8) __builtin_amdgcn_raw_buffer_store_b128(u32x4{__float_as_uint(mg), tlo, __float_as_uint(l), thi}, wsr, WS_REC + (b * 64 + pj) * 128 + lane * 16, 0, SC1);
#pragma unroll 1
    for (int i = 0; i < 12 + (kk & 31); ++i) PAUSE(12);
    u32x4 rec;
#pragma unroll 1
    for (int spin = 0; spin < SPIN_CAP; ++spin) {
      CFENCE();
      rec = __builtin_amdgcn_raw_buffer_load_b128(wsr, WS_REC + (b * 64 + (pj ^ 1)) * 128, 0, SC1);
      if (__builtin_amdgcn_ballot_w64(rec[1] != tlo || rec[3] != thi) == 0ull) break;
      __builtin_amdgcn_s_sleep(16);
    }
    const float mo = __uint_as_float(rec[0]), lo = __uint_as_float(rec[2]);
    const float mh = fmaxf(mg, mo), wt = expd(mg - mh);
    ctrlf[49] = g * wt / (wt * l + expd(mo - mh) * lo);
    const u32x4 yt = {tlo & YMASK, (tlo >> 12) & YMASK, (unsigned)(tag >> 24) & YMASK, (thi >> 4) & YMASK};
    *(LAS u32x4*)(L + L_CTRL + 52 * 4) = yt;
    lds_arrive(L, C_CF, lane);
    GAS unsigned* op = (GAS unsigned*)(out + b * D + 8 * pj + 4 * (lane & 1) + 2 * (lane >> 5));
    int off = WS_Y + (b * 64 + (lane >> 1)) * 2048 + pj * 32 + (lane & 1) * 16;
    asm volatile("" : "+v"(op), "+v"(off));
    BARRIER();
    {
      const unsigned now = (unsigned)__builtin_amdgcn_s_memrealtime();
      const int d = __builtin_amdgcn_readfirstlane(ctrl[2]) + TSTREAM + FOFF - (int)now;
      const unsigned t = now + (unsigned)min(max(d, FMIN), FMAX);
#pragma unroll 1
      for (int spin = 0; spin < 4096 && (int)(t - (unsigned)__builtin_amdgcn_s_memrealtime()) > 0; ++spin) {}
    }
    u32x4 ya, yb;
#pragma unroll 1
    for (int spin = 0; spin < SPIN_CAP; ++spin) {
      CFENCE();
      ya = __builtin_amdgcn_raw_buffer_load_b128(wsr, off, 0, SC1);
      yb = __builtin_amdgcn_raw_buffer_load_b128(wsr, off, 32 * 2048, SC1);
      const unsigned bad = (((ya[0] ^ yt[0]) | (ya[1] ^ yt[1])) | ((ya[2] ^ yt[2]) | (ya[3] ^ yt[3])) | ((yb[0] ^ yt[0]) | (yb[1] ^ yt[1])) | ((yb[2] ^ yt[2]) | (yb[3] ^ yt[3]))) & YMASK;
      if (__builtin_amdgcn_ballot_w64(bad != 0u) == 0ull) break;
    }
    float o[4];
#pragma unroll
    for (int k = 0; k < 4; ++k) o[k] = __uint_as_float(ya[k] & ~YMASK) + __uint_as_float(yb[k] & ~YMASK);
    float e0 = swadd32(o[0], o[2]), e1 = swadd32(o[1], o[3]);
    e0 = swadd16(e0, e0); e1 = swadd16(e1, e1);
    e0 += dpp<ROR8>(e0); e1 += dpp<ROR8>(e1); e0 += dpp<ROR4>(e0); e1 += dpp<ROR4>(e1); e0 += dpp<ROR2>(e0); e1 += dpp<ROR2>(e1);
    if ((lane & 30) == 0) *op = pack_bf16(e0, e1);
    if (pj == 0 && lane < 8) __builtin_amdgcn_raw_buffer_store_b128(u32x4{tlo, thi, tlo, thi}, wsr, WS_EPOCH + b * 256 + lane * 16, 0, SC1);
    return;
  }

  const int cw = wave - 1, hw = wave - 1 - NCW;
  int landed = 0;
  auto wait_unit = [&](int u) __attribute__((always_inline)) {
#pragma unroll 1
    for (int spin = 0; landed <= u && spin < SPIN_CAP; ++spin) {
      landed = __builtin_amdgcn_readfirstlane(ctrl[0]);
    }
    CFENCE();
  };
  auto take = [&](int u, int next, u32x4& a0, u32x4& a1, u32x4& a2, u32x4& a3) __attribute__((always_inline)) {
    wait_unit(u);
    const LAS u32x4* sp = (const LAS u32x4*)(L + (u & (NSLOT - 1)) * UNIT + lane * 16);
    a0 = sp[0]; a1 = sp[64]; a2 = sp[128]; a3 = sp[192];
    LDS_WAIT();
    ctrl[16 + cw] = next;
    asm volatile("" : "+v"(a0), "+v"(a1), "+v"(a2), "+v"(a3) :: "memory");
  };
  u32x4 q;
  auto build_q = [&]() __attribute__((always_inline)) {
    lds_await(ctrl, C_Q, NHW);
    const LAS f32x4* qp = (const LAS f32x4*)(L + L_QP + (2 * r4 * DH + 8 * c16) * 4);
    const f32x4 lo = qp[0] + qp[DH / 4], hi = qp[1] + qp[DH / 4 + 1];
    q[0] = pack_bf16(xrow_sum(lo[0]), xrow_sum(lo[1])); q[1] = pack_bf16(xrow_sum(lo[2]), xrow_sum(lo[3]));
    q[2] = pack_bf16(xrow_sum(hi[0]), xrow_sum(hi[1])); q[3] = pack_bf16(xrow_sum(hi[2]), xrow_sum(hi[3]));
  };
  constexpr float SCALE = 0.088388347648318440f;
  float mx = -3.4028234e38f;
  auto score = [&](int u, const u32x4& k0, const u32x4& k1, const u32x4& k2, const u32x4& k3) __attribute__((always_inline)) {
    float d0 = 0.f, d1 = 0.f, d2 = 0.f, d3 = 0.f;
#pragma unroll
    for (int k = 0; k < 4; ++k) { d0 = dot2(k0[k], q[k], d0); d1 = dot2(k1[k], q[k], d1); d2 = dot2(k2[k], q[k], d2); d3 = dot2(k3[k], q[k], d3); }
    const unsigned s01 = pack_bf16(row16_sum(d0), row16_sum(d1)), s23 = pack_bf16(row16_sum(d2), row16_sum(d3));
    const unsigned t01 = pack_bf16(bflo(s01) * SCALE, bfhi(s01) * SCALE), t23 = pack_bf16(bflo(s23) * SCALE, bfhi(s23) * SCALE);
    const f32x4 sc = {bflo(t01), bfhi(t01), bflo(t23), bfhi(t23)};
    mx = fmaxf(fmaxf(mx, fmaxf(sc[0], sc[1])), fmaxf(sc[2], sc[3]));
    if (c16 == 0) *(LAS f32x4*)(L + L_SC + u * 64 + r4 * 16) = sc;
  };

  if (hw < 0) {
    {
      u32x4 hk[4 * NHC];
#pragma unroll
      for (int i = 0; i < NHC; ++i) take(cw + NCW * i, i + 1 < NHC ? cw + NCW * (i + 1) : NHELD + cw, hk[4 * i], hk[4 * i + 1], hk[4 * i + 2], hk[4 * i + 3]);
      build_q();
#pragma unroll
      for (int i = 0; i < NHC; ++i) { score(cw + NCW * i, hk[4 * i], hk[4 * i + 1], hk[4 * i + 2], hk[4 * i + 3]); __builtin_amdgcn_sched_barrier(0); }
    }
#pragma unroll 1
    for (int u = NHELD + cw; u < NKU; u += NCW) {
      u32x4 k0, k1, k2, k3;
      take(u, u + NCW, k0, k1, k2, k3);
      score(u, k0, k1, k2, k3);
    }
    ctrlf[32 + cw] = xrow_max(mx);
    lds_arrive(L, C_MAX, lane);
    lds_await(ctrl, C_MAX, NCW + NHW);
    const float mg = wg_max();
    float acc[8] = {0.f, 0.f, 0.f, 0.f, 0.f, 0.f, 0.f, 0.f};
    LAS unsigned* rp = (LAS unsigned*)(L + L_REC + cw * 272 + (8 * c16 + 2 * r4) * 2);
    asm volatile("" : "+v"(rp));
    float cf = 0.f;
#pragma unroll 1
    for (int u = NKU + cw; u < NUNIT; u += NCW) {
      const f32x4 sc = *(const LAS f32x4*)(L + L_SC + (u - NKU) * 64 + r4 * 16);
      float p[4];
#pragma unroll
      for (int t = 0; t < 4; ++t) { p[t] = expd(sc[t] - mg); asm volatile("" : "+v"(p[t])); }
      if ((u & ~(NCW - 1)) == NUNIT - 32) { lds_await(ctrl, C_CF, 1); cf = ctrlf[49]; }
      u32x4 v[4];
      take(u, u + NCW, v[0], v[1], v[2], v[3]);
#pragma unroll
      for (int t = 0; t < 4; ++t)
#pragma unroll
        for (int k = 0; k < 4; ++k) { acc[2 * k] = fmaf(p[t], bflo(v[t][k]), acc[2 * k]); acc[2 * k + 1] = fmaf(p[t], bfhi(v[t][k]), acc[2 * k + 1]); }
    }
    const float e0 = swadd16(swadd32(acc[0], acc[4]), swadd32(acc[2], acc[6])), e1 = swadd16(swadd32(acc[1], acc[5]), swadd32(acc[3], acc[7]));
    *rp = pack_bf16(e0 * cf, e1 * cf);
    BARRIER();
    return;
  }

  u32x4 wb[16];
  const unsigned tstart = (unsigned)__builtin_amdgcn_readfirstlane(ctrl[8]);
  {
    const __amdgpu_buffer_rsrc_t rq = mkrsrc(wq, D * H * DH * 2);
    const int vo = ((64 * hw + 2 * r4) * (H * DH) + h * DH + 8 * c16) * 2;
#pragma unroll
    for (int i = 0; i < 16; ++i) { if (i == 0) wait_until(tstart + TQ + hw * QA); else PAUSE(QS); wb[i] = ldw(rq, vo, (8 * (i >> 1) + (i & 1)) * (H * DH * 2)); }
    lds_await(ctrl, C_X, 1);
    const LAS unsigned* xl = (const LAS unsigned*)(L + L_X + (64 * hw + 2 * r4) * 2);
    float a[8] = {0.f, 0.f, 0.f, 0.f, 0.f, 0.f, 0.f, 0.f};
#pragma unroll
    for (int i = 0; i < 8; ++i) {
      const unsigned xp = xl[4 * i];
#pragma unroll
      for (int k = 0; k < 4; ++k) {
        a[2 * k] = dot2(perm_lo(wb[2 * i][k], wb[2 * i + 1][k]), xp, a[2 * k]);
        a[2 * k + 1] = dot2(perm_hi(wb[2 * i][k], wb[2 * i + 1][k]), xp, a[2 * k + 1]);
      }
    }
    const float e0 = swadd16(swadd32(a[0], a[4]), swadd32(a[2], a[6])), e1 = swadd16(swadd32(a[1], a[5]), swadd32(a[3], a[7]));
    *(LAS f32x2*)(L + L_QP + (hw * DH + 8 * c16 + 2 * r4) * 4) = f32x2{e0, e1};
    lds_arrive(L, C_Q, lane);
    const int hb = NHC * NCW + NHH * hw;
#pragma unroll
    for (int i = 0; i < NHH; ++i) take(hb + i, i + 1 < NHH ? hb + i + 1 : NEVER, wb[4 * i], wb[4 * i + 1], wb[4 * i + 2], wb[4 * i + 3]);
    build_q();
#pragma unroll
    for (int i = 0; i < NHH; ++i) { score(hb + i, wb[4 * i], wb[4 * i + 1], wb[4 * i + 2], wb[4 * i + 3]); __builtin_amdgcn_sched_barrier(0); }
    ctrlf[32 + cw] = xrow_max(mx);
    lds_arrive(L, C_MAX, lane);
  }
  f32x4 y[4] = {{0.f, 0.f, 0.f, 0.f}, {0.f, 0.f, 0.f, 0.f}, {0.f, 0.f, 0.f, 0.f}, {0.f, 0.f, 0.f, 0.f}};
  if ((pj & 7) != 7) {
    const int sl = 7 * (pj >> 3) + (pj & 7);
    const __amdgpu_buffer_rsrc_t r1 = mkrsrc(w1, D * F * 2), r3 = mkrsrc(w3, D * F * 2), r2 = mkrsrc(w2, F * D * 2);
    const int vo = ((64 * hw + (lane & 60)) * F + FS * sl + 8 * (lane & 3)) * 2;
#pragma unroll
    for (int i = 0; i < 8; ++i) { if (i == 0) wait_until(tstart + T1 + hw * A1); else PAUSE(S1); wb[i] = ldw(i < 4 ? r1 : r3, vo, (i & 3) * (F * 2)); }
    const u32x2 xm = *(const LAS u32x2*)(L + L_X + (64 * hw + (lane & 60)) * 2);
    float a[16];
#pragma unroll
    for (int m = 0; m < 2; ++m)
#pragma unroll
      for (int k = 0; k < 4; ++k) {
        a[8 * m + 2 * k] = dot2(perm_lo(wb[4 * m + 2][k], wb[4 * m + 3][k]), xm[1], dot2(perm_lo(wb[4 * m][k], wb[4 * m + 1][k]), xm[0], 0.f));
        a[8 * m + 2 * k + 1] = dot2(perm_hi(wb[4 * m + 2][k], wb[4 * m + 3][k]), xm[1], dot2(perm_hi(wb[4 * m][k], wb[4 * m + 1][k]), xm[0], 0.f));
      }
    f32x4 e;
#pragma unroll
    for (int k = 0; k < 4; ++k) {
      float t = swadd16(swadd32(a[k], a[8 + k]), swadd32(a[4 + k], a[12 + k]));
      t += dpp<ROR8>(t); e[k] = t + dpp<ROR4>(t);
    }
    if ((lane & 12) == 0) *(LAS f32x4*)(L + L_MP + (hw * 64 + (lane & 32) + 8 * (lane & 3) + 4 * (r4 & 1)) * 4) = e;
    lds_arrive(L, C_MLP, lane);
    u32x2 wr[8];
#pragma unroll
    for (int i = 0; i < 8; ++i) { if (i == 0) wait_until(tstart + T2 + hw * A2); else PAUSE(S2); wr[i] = ldw2(r2, ((FS * sl + 8 * r4) * D + 64 * hw + 4 * c16) * 2, i * (D * 2)); }
    lds_await(ctrl, C_MLP, NHW);
    const LAS float* mp = (const LAS float*)(L + L_MP) + (lane & 31);
    float ga = 0.f, up = 0.f;
#pragma unroll
    for (int w = 0; w < NHW; ++w) { ga += mp[w * 64]; up += mp[w * 64 + 32]; }
    const float hv = ga / (1.f + __expf(-ga)) * up * (1.f - ctrlf[48]);
    const unsigned hh = pack_bf16(hv, 0.f), hl = pack_bf16(hv - bflo(hh), 0.f);
    *(LAS u16*)(L + L_H + hw * 128 + lane * 2) = (u16)(lane < 32 ? hh : hl);
    LDS_WAIT();
    u32x4 ha = *(const LAS u32x4*)(L + L_H + hw * 128 + (c16 & 1) * 64 + r4 * 16);
    if (c16 >= 2) ha = u32x4{0u, 0u, 0u, 0u};
#pragma unroll
    for (int e = 0; e < 4; ++e) {
      u32x4 f;
#pragma unroll
      for (int k = 0; k < 4; ++k) f[k] = (e & 1) ? perm_hi(wr[2 * k][e >> 1], wr[2 * k + 1][e >> 1]) : perm_lo(wr[2 * k][e >> 1], wr[2 * k + 1][e >> 1]);
      y[e] = mfma(ha, f, y[e]);
    }
  }
#pragma unroll
  for (int e = 0; e < 4; ++e) asm volatile("" : "+v"(y[e]));
  {
    const __amdgpu_buffer_rsrc_t ro = mkrsrc(wo, H * DH * D * 2);
#pragma unroll
    for (int t = 0; t < 4; ++t) {
      u32x2 wr[8];
#pragma unroll
      for (int i = 0; i < 8; ++i) { if (t + i == 0) wait_until(tstart + TO + hw * AO); else PAUSE(SO); wr[i] = ldw2(ro, ((h * DH + 32 * r4) * D + 64 * hw + 4 * c16) * 2, (8 * t + i) * (D * 2)); }
#pragma unroll
      for (int e = 0; e < 4; ++e) {
#pragma unroll
        for (int k = 0; k < 4; ++k) wb[4 * t + e][k] = (e & 1) ? perm_hi(wr[2 * k][e >> 1], wr[2 * k + 1][e >> 1]) : perm_lo(wr[2 * k][e >> 1], wr[2 * k + 1][e >> 1]);
        asm volatile("" : "+v"(wb[4 * t + e]));
      }
    }
  }
  lds_await(ctrl, C_CF, 1);
  u32x4 yt = *(const LAS u32x4*)(L + L_CTRL + 52 * 4);
  int yo = WS_Y + (b * 64 + pj) * 2048 + hw * 256 + c16 * 16;
  const LAS u32x4* rec = (const LAS u32x4*)(L + L_REC + (lane & 3) * 272 + r4 * 64);
  unsigned ym = ~YMASK;
  asm volatile("" : "+v"(yt), "+v"(yo), "+v"(rec), "+s"(ym));
  BARRIER();
  {
    const u32x4 a[4] = {rec[0], rec[1], rec[2], rec[3]};
#pragma unroll
    for (int t = 0; t < 4; ++t)
#pragma unroll
      for (int e = 0; e < 4; ++e) y[e] = mfma(a[t], wb[4 * t + e], y[e]);
    u32x4 v;
#pragma unroll
    for (int e = 0; e < 4; ++e) {
      const f32x2 t = y[e].xy + y[e].zw;
      unsigned r;
      asm("v_add_f32 %0, %1, %2\n\tv_add_u32 %0, %5, %0\n\tv_and_or_b32 %0, %0, %3, %4" : "=&v"(r) : "v"(t[0]), "v"(t[1]), "s"(ym), "v"(yt[e]), "i"((YMASK + 1u) / 2u));
      v[e] = r;
    }
    if (lane < 16) __builtin_amdgcn_raw_buffer_store_b128(v, wsr, yo, 0, SC1);
  }
}

extern "C" void kernel_launch(void* const* d_in, const int* in_sizes, int n_in, void* d_out, int out_size, void* d_ws, size_t ws_size,
                              hipStream_t stream) {
  gated_mix__mha_block_cached__glu_gated_swiglu__decode_kernel<<<256, NTHR, 0, stream>>>((const u16*)d_in[0], (const u16*)d_in[1], (const u16*)d_in[2], (const u16*)d_in[3], (const u16*)d_in[4],
                                             (const u16*)d_in[6], (const u16*)d_in[7], (const u16*)d_in[8], (const u16*)d_in[9], (u16*)d_out,
                                             (unsigned char*)d_ws);
}
```

```cpp
#include <hip/hip_runtime.h>
#include <hip/hip_bf16.h>

#define GAS __attribute__((address_space(1)))
#define LAS __attribute__((address_space(3)))
typedef unsigned short u16;
typedef unsigned long long u64;
using u32x4 = __attribute__((ext_vector_type(4))) unsigned;
using u32x2 = __attribute__((ext_vector_type(2))) unsigned;
using f32x4 = __attribute__((ext_vector_type(4))) float;
using f32x2 = __attribute__((ext_vector_type(2))) float;
using bf16x2 = __attribute__((ext_vector_type(2))) __bf16;
using bf16x8 = __attribute__((ext_vector_type(8))) __bf16;

constexpr int B = 4, D = 512, H = 32, SK = 8192, DH = 128, F = 1792;
constexpr int NCW = 4, NHW = 8, XWAVE = 1 + NCW + NHW, NWAVE = XWAVE + 2, NTHR = NWAVE * 64;
constexpr int TQ = 100, QA = 112, QS = 2;
constexpr int T1 = 1400, A1 = 150, S1 = 6;
constexpr int T2 = 2500, A2 = 40, S2 = 1;
constexpr int TO = 2900, AO = 350, SO = 2;
constexpr int TSTREAM = 6860;
constexpr int WMID = 5472, WMIN = 16, WMAX = 28;
constexpr int WGAIN = 10, WGAIN2 = 18, UEND = 420;
constexpr int JUMP = 100, DECAY = 6;
constexpr int REF0 = 180;
constexpr int FOFF = 156, FMIN = 45, FMAX = 150;
constexpr int CODE_KIB = 21;

constexpr int UNIT = 4096;
constexpr int NSLOT = 32, RING = NSLOT * UNIT;
constexpr int NKU = (SK / 2) / 16, NUNIT = 2 * NKU;
constexpr int NHC = 6, NHH = 4, NHELD = NHC * NCW + NHH * NHW;
constexpr int FS = 32;
constexpr int L_CTRL = RING;
constexpr int L_X = L_CTRL + 256;
constexpr int L_QP = L_X + D * 2;
constexpr int L_MP = L_QP + NHW * DH * 4;
constexpr int L_REC = L_MP + NHW * 64 * 4;
constexpr int L_H = L_REC + NCW * 272;
constexpr int L_SC = L_H + NHW * 128;
constexpr int LDS_BYTES = L_SC + NKU * 64;
static_assert(LDS_BYTES <= 159 * 1024, "lds");
static_assert(WMIN <= WMID / 256 && WMID / 256 <= WMAX && WMAX <= 28, "window");
constexpr int WS_EPOCH = 0;
constexpr int WS_REC = 4096;
constexpr int WS_PACE = WS_REC + 256 * 128;
constexpr int WS_Y = WS_PACE + 32 * 2048;
constexpr int WS_BYTES = WS_Y + B * 64 * 2048;
constexpr u64 TAG_STEP = 0x9E3779B97F4A7C15ull;
constexpr unsigned YMASK = 0xFFFu;
constexpr int NT = 2, SC1 = 16;
constexpr int SPIN_CAP = 1 << 22;
constexpr int NEVER = 1 << 20;
constexpr int C_Q = 4, C_MAX = 5, C_MLP = 6, C_X = 7, C_CF = 9;

__device__ __forceinline__ float expd(float d) { return __builtin_amdgcn_exp2f(d * 1.4426950408889634f); }
__device__ __forceinline__ float bflo(unsigned u) { return __builtin_bit_cast(float, u << 16); }
__device__ __forceinline__ float bfhi(unsigned u) { return __builtin_bit_cast(float, u & 0xffff0000u); }
__device__ __forceinline__ float dot2(unsigned a, unsigned b, float acc) {
  return __builtin_amdgcn_fdot2_f32_bf16(__builtin_bit_cast(bf16x2, a), __builtin_bit_cast(bf16x2, b), acc, false);
}
__device__ __forceinline__ unsigned pack_bf16(float lo, float hi) {
  const bf16x2 v = {(__bf16)lo, (__bf16)hi};
  return __builtin_bit_cast(unsigned, v);
}
__device__ __forceinline__ unsigned perm_lo(unsigned a, unsigned b) { return __builtin_amdgcn_perm(b, a, 0x05040100u); }
__device__ __forceinline__ unsigned perm_hi(unsigned a, unsigned b) { return __builtin_amdgcn_perm(b, a, 0x07060302u); }
template <int CTRL> __device__ __forceinline__ float dpp(float x) {
  return __builtin_bit_cast(float, __builtin_amdgcn_mov_dpp(__builtin_bit_cast(int, x), CTRL, 0xf, 0xf, true));
}
constexpr int ROR1 = 0x121, ROR2 = 0x122, ROR4 = 0x124, ROR8 = 0x128;
__device__ __forceinline__ float row16_sum(float x) { x += dpp<ROR8>(x); x += dpp<ROR4>(x); x += dpp<ROR2>(x); x += dpp<ROR1>(x); return x; }
__device__ __forceinline__ float swadd32(float a, float b) {
  auto t = __builtin_amdgcn_permlane32_swap(__float_as_uint(a), __float_as_uint(b), false, false);
  return __uint_as_float(t[0]) + __uint_as_float(t[1]);
}
__device__ __forceinline__ float swadd16(float a, float b) {
  auto t = __builtin_amdgcn_permlane16_swap(__float_as_uint(a), __float_as_uint(b), false, false);
  return __uint_as_float(t[0]) + __uint_as_float(t[1]);
}
__device__ __forceinline__ float xrow_sum(float x) { x = swadd16(x, x); return swadd32(x, x); }
__device__ __forceinline__ float xrow_max(float x) {
  auto s = __builtin_amdgcn_permlane16_swap(__float_as_uint(x), __float_as_uint(x), false, false);
  x = fmaxf(__uint_as_float(s[0]), __uint_as_float(s[1]));
  auto t = __builtin_amdgcn_permlane32_swap(__float_as_uint(x), __float_as_uint(x), false, false);
  return fmaxf(__uint_as_float(t[0]), __uint_as_float(t[1]));
}
__device__ __forceinline__ __amdgpu_buffer_rsrc_t mkrsrc(const void* p, int bytes) { return __builtin_amdgcn_make_buffer_rsrc((void*)p, 0, bytes, 0x00020000); }
__device__ __forceinline__ u32x4 ldw(__amdgpu_buffer_rsrc_t r, int voff, int soff) { return __builtin_amdgcn_raw_buffer_load_b128(r, voff, soff, NT); }
#define LDS_WAIT() asm volatile("s_waitcnt lgkmcnt(0)" ::: "memory")
#define CFENCE() asm volatile("" ::: "memory")
#define BARRIER() asm volatile("s_waitcnt lgkmcnt(0)\n\ts_barrier" ::: "memory")
#define PAUSE(n) do { __builtin_amdgcn_sched_barrier(0); asm volatile("s_sleep %0" :: "i"(n) : "memory"); __builtin_amdgcn_sched_barrier(0); } while (0)

__device__ __forceinline__ void lds_arrive(LAS unsigned char* L, int idx, int lane) {
  LDS_WAIT();
  if (lane == 0) __hip_atomic_fetch_add((LAS int*)(L + L_CTRL) + idx, 1, __ATOMIC_RELAXED, __HIP_MEMORY_SCOPE_WORKGROUP);
}
__device__ __forceinline__ void lds_await(volatile LAS int* ctrl, int idx, int n) {
#pragma unroll 1
  for (int spin = 0; spin < SPIN_CAP; ++spin) { if (__builtin_amdgcn_readfirstlane(ctrl[idx]) >= n) break; __builtin_amdgcn_s_sleep(1); }
  CFENCE();
}

__device__ __forceinline__ void wait_until(unsigned t) {
  __builtin_amdgcn_sched_barrier(0);
#pragma unroll 1
  for (int spin = 0; spin < 4096; ++spin) {
    const int d = (int)(t - (unsigned)__builtin_amdgcn_s_memrealtime());
    if (d <= 0) break;
    if (d > 24) __builtin_amdgcn_s_sleep(6);
  }
  CFENCE();
  __builtin_amdgcn_sched_barrier(0);
}
constexpr int TAU2 = 2 * (TSTREAM * 256 / NUNIT);
__device__ __forceinline__ f32x4 mfma(const u32x4& a, const u32x4& b, const f32x4& c) {
  return __builtin_amdgcn_mfma_f32_16x16x32_bf16(__builtin_bit_cast(bf16x8, a), __builtin_bit_cast(bf16x8, b), c, 0, 0, 0);
}
__device__ __forceinline__ u32x2 ldw2(__amdgpu_buffer_rsrc_t r, int voff, int soff) { return __builtin_amdgcn_raw_buffer_load_b64(r, voff, soff, NT); }

__global__ void __launch_bounds__(NTHR, 4) gated_mix__mha_block_cached__glu_gated_swiglu__decode_kernel(const u16* x, const u16* kc, const u16* vc, const u16* wq, const u16* wo, const u16* w1,
                                                         const u16* w3, const u16* w2, const u16* gw, u16* out, unsigned char* ws) {
  __shared__ __attribute__((aligned(16))) unsigned char lds_raw[LDS_BYTES];
  LAS unsigned char* L = (LAS unsigned char*)lds_raw;
  volatile LAS int* ctrl = (volatile LAS int*)(L + L_CTRL);
  volatile LAS float* ctrlf = (volatile LAS float*)(L + L_CTRL);
  {
    unsigned va, vb, vo, pa, nxa, t0, t1, t2, t3;
#define WAITW "s_waitcnt (wcnt & 15) | ((wcnt >> 4) << 14) | 0x0F70\n\t"
#define SP(v, sb, wait) wait "global_load_lds_dword " v ", " sb " nt\n\tglobal_load_lds_dword " v ", " sb " sc1\n\t"
#define SW "s_waitcnt vmcnt(4)\n\t"
#define UNIT4(v) \
        WAITW "global_load_lds_dwordx4 " v ", s[40:41] nt\n\t" \
        WAITW "global_load_lds_dwordx4 " v ", s[40:41] offset:1024 nt\n\t" \
        WAITW "global_load_lds_dwordx4 " v ", s[40:41] offset:2048 nt\n\t" \
        WAITW "global_load_lds_dwordx4 " v ", s[40:41] offset:3072 nt\n\t"
    asm volatile(
        "s_getpc_b64 s[56:57]\n\t"
        "s_load_dwordx8 s[44:51], %[ka], 0x18\n\t"
        "s_load_dwordx2 s[52:53], %[ka], 0x38\n\t"
        "s_load_dwordx4 s[40:43], %[ka], 0x8\n\t"
        "v_readfirstlane_b32 s54, %[tid]\n\t"
        "s_mov_b32 m0, %c[jk]\n\t"
        "s_lshr_b32 s59, %[bid], 3\n\t"
        "s_cmp_ge_u32 s54, 64\n\t"
        "s_cbranch_scc1 90f\n\t"
        "s_min_u32 s54, s59, %c[nch]\n\t"
        "s_lshl_b32 s54, s54, 10\n\t"
        "v_lshlrev_b32 %[vo], 4, %[tid]\n\t"
        "s_and_b32 s56, s56, -128\n\t"
        "v_add_u32 %[t0], s54, %[vo]\n\t"
        "s_and_b32 s58, %[bid], 7\n\t"
        "global_load_lds_dwordx4 %[t0], s[56:57]\n\t"
        "s_setprio 3\n\t"
        "v_lshlrev_b32 %[t1], 7, %[tid]\n\t"
        "s_lshl_b32 s54, s59, 7\n\t"
        "s_add_u32 s54, s54, s58\n\t"
        "s_lshl_b32 s54, s54, 10\n\t"
        "v_lshrrev_b32 %[va], 3, %[tid]\n\t"
        "v_mul_u32_u24 %[va], 8192 - 1024, %[va]\n\t"
        "v_add3_u32 %[va], %[va], %[t1], s54\n\t"
        "v_add_u32 %[t2], 0x10000, %[va]\n\t"
        "s_lshl_b32 s55, s58, 5\n\t"
        "s_add_u32 s55, s55, s59\n\t"
        "s_mul_i32 s54, s55, 7 * 1024\n\t"
        "v_min_u32 %[t3], 55 * 128, %[t1]\n\t"
        "v_add_u32 %[t3], s54, %[t3]\n\t"
        "s_mul_i32 s54, s58, 448\n\t"
        "s_and_b32 s54, s54, -128\n\t"
        "s_mul_i32 s57, s59, 16 * 3584\n\t"
        "s_add_u32 s54, s54, s57\n\t"
        "v_lshrrev_b32 %[t0], 2, %[tid]\n\t"
        "v_mul_u32_u24 %[t0], 3584 - 512, %[t0]\n\t"
        "v_add3_u32 %[t0], %[t0], %[t1], s54\n\t"
        "v_lshl_add_u32 %[vb], s55, 14, %[t1]\n\t"
        "v_add_u32 %[t1], 0x2000, %[vb]\n\t"
        "s_and_b32 s54, %[bid], 0xc0\n\t"
        "s_lshl3_add_u32 s54, s58, s54\n\t"
        "s_and_b32 s56, s59, 7\n\t"
        "s_add_u32 s54, s54, s56\n\t"
        "s_lshr_b32 s56, s54, 1\n\t"
        "s_lshl_b32 s56, s56, 21\n\t"
        "s_and_b32 s64, s54, 1\n\t"
        "s_bfe_u32 s57, s54, 0x10007\n\t"
        "s_lshl2_add_u32 s64, s57, s64\n\t"
        "s_bfe_u32 s57, s54, 0x10002\n\t"
        "s_lshl4_add_u32 s64, s57, s64\n\t"
        "s_lshl_b32 s64, s64, 16\n\t"
        "s_waitcnt lgkmcnt(0)\n\t"
        SP("%[va]", "s[44:45]", "") SP("%[t2]", "s[44:45]", "") SP("%[t3]", "s[52:53]", "")

        "s_barrier\n\t"
        SP("%[t0]", "s[48:49]", SW) SP("%[t0]", "s[50:51]", SW) SP("%[vb]", "s[46:47]", SW) SP("%[t1]", "s[46:47]", SW)
        "s_add_u32 s40, s40, s56\n\t"
        "s_addc_u32 s41, s41, 0\n\t"
        "s_add_u32 s42, s42, s56\n\t"
        "s_addc_u32 s43, s43, 0\n\t"
        "v_mov_b32 %[va], %[vo]\n\t"
        "v_or_b32 %[vo], s64, %[vo]\n\t"
        ".set so, 0\n\t"
        ".set wcnt, 4\n\t"
        ".rept 4\n\t"
        "s_movk_i32 m0, so\n\t"
        ".if (so & 4096) == 0\n\t"
        WAITW
        ".set wcnt, wcnt + 6\n\t"
        ".else\n\t"
        "s_nop 0\n\t"
        ".endif\n\t"
        "global_load_lds_dwordx4 %[vo], s[40:41] nt\n\t"
        "global_load_lds_dwordx4 %[vo], s[40:41] offset:1024 nt\n\t"
        "global_load_lds_dwordx4 %[vo], s[40:41] offset:2048 nt\n\t"
        "global_load_lds_dwordx4 %[vo], s[40:41] offset:3072 nt\n\t"
        "v_add_u32 %[vo], 0x1000, %[vo]\n\t"
        ".set so, so + 4096\n\t"
        ".endr\n\t"
        "s_waitcnt vmcnt(16)\n\t"
        "s_getpc_b64 s[50:51]\n"
        "20:\n\t"
        "s_add_u32 s50, s50, 23f-20b-%c[wmin]*128\n\t"
        "s_addc_u32 s51, s51, -1\n\t"
        "v_mov_b32 %[pa], %c[ctl]\n\t"
        "v_and_b32 %[nxa], 0x3c0 >> 2, %[vo]\n\t"
        "v_lshrrev_b32 %[nxa], 2, %[nxa]\n\t"
        "v_add_u32 %[nxa], %c[ctl] + 64, %[nxa]\n\t"
        "s_memrealtime s[58:59]\n\t"
        "s_mov_b32 s44, 4\n\t"
        "s_mov_b32 s45, %c[wmid] >> 8\n\t"
        "s_mov_b32 s46, 0x4000\n\t"
        "s_mov_b32 s47, 2 * %c[tau2]\n\t"
        "s_mov_b32 s48, %c[nslot]\n\t"
        "s_mov_b32 s49, %c[nku]\n\t"
        "s_waitcnt lgkmcnt(0)\n\t"
        "s_mov_b32 s62, 0\n\t"
        "s_mov_b32 s63, %c[gain]\n\t"
        "s_mov_b32 s59, 10000\n"
        "22:\n\t"
        "s_memrealtime s[56:57]\n\t"
        "ds_read_b32 %[t3], %[pa] offset:8\n\t"
        "s_lshl_b32 s52, s45, 7\n\t"
        "s_add_u32 s52, s50, s52\n\t"
        "s_addc_u32 s53, s51, 0\n\t"
        "s_add_i32 s54, s45, -1\n\t"
        "s_lshr_b32 s54, s54, 2\n\t"
        "s_sub_i32 s54, s44, s54\n\t"
        "v_mov_b32 %[t1], s54\n\t"
        "v_add_u32 %[t0], 0x1000, %[vo]\n\t"
        "s_add_u32 s55, s46, 0x1000\n\t"
        "s_mov_b32 m0, s46\n\t"
        "s_setpc_b64 s[52:53]\n"
        "24:\n\t"
        "s_waitcnt lgkmcnt(0)\n\t"
        "v_readfirstlane_b32 s57, %[t3]\n\t"
        "v_add_u32 %[t1], 1, %[t1]\n\t"
        "s_lshr_b32 s54, s47, 8\n\t"
        "s_sub_u32 s56, s56, s54\n\t"
        "v_mov_b32 %[t2], s56\n\t"
        "s_sub_i32 s54, s56, s58\n\t"
        "s_mov_b32 s58, s56\n\t"
        "s_sub_i32 s56, s56, s57\n\t"
        "ds_write2_b32 %[pa], %[t1], %[t2] offset1:1\n\t"
        "s_add_i32 s59, s59, s54\n\t"
        "s_add_i32 s59, s59, %c[decay]\n\t"
        "s_cmp_gt_i32 s54, %c[jump]\n\t"
        "s_cselect_b32 s59, s62, s59\n\t"
        "s_min_i32 s62, s56, s59\n\t"
        "s_mul_i32 s56, s62, s63\n\t"
        "s_add_i32 s56, s56, %c[wmid]\n\t"
        "s_ashr_i32 s56, s56, 8\n\t"
        "s_max_i32 s56, s56, %c[wmin]\n\t"
        "s_min_i32 s45, s56, %c[wmax]\n\t"
        "s_add_u32 s47, s47, %c[tau2]\n\t"
        "s_add_u32 s46, s46, 0x2000\n\t"
        "s_and_b32 s46, s46, %c[nslot] * 4096 - 1\n\t"
        "s_add_i32 s44, s44, 2\n\t"
        "s_and_b32 s54, s44, 0xff\n\t"
        "s_and_b32 s55, s44, 0xf0\n\t"
        "s_add_u32 s54, s54, s55\n\t"
        "s_lshl_b32 s54, s54, 12\n\t"
        "s_xor_b32 s54, s54, s64\n\t"
        "v_or_b32 %[vo], s54, %[va]\n\t"
        "s_cmp_lt_i32 s44, s48\n\t"
        "s_cbranch_scc1 22b\n\t"
        "s_cmp_lt_i32 s44, s49\n\t"
        "s_cbranch_scc1 25f\n\t"
        "s_cmp_ge_i32 s44, 2 * %c[nku]\n\t"
        "s_cbranch_scc1 30f\n\t"
        "s_mov_b32 s49, 2 * %c[nku]\n\t"
        "s_mov_b64 s[40:41], s[42:43]\n"
        "25:\n\t"
        "s_cmp_ge_i32 s44, %c[uend]\n\t"
        "s_cselect_b32 s63, %c[gain2], s63\n\t"
        "s_mov_b32 s55, %c[cap]\n"
        "26:\n\t"
        "ds_read_b32 %[t0], %[nxa]\n\t"
        "s_waitcnt lgkmcnt(0)\n\t"
        "s_nop 1\n\t"
        "v_min_i32_dpp %[t0], %[t0], %[t0] row_ror:8 row_mask:0xf bank_mask:0xf\n\t"
        "s_nop 1\n\t"
        "v_min_i32_dpp %[t0], %[t0], %[t0] row_ror:4 row_mask:0xf bank_mask:0xf\n\t"
        "s_nop 1\n\t"
        "v_min_i32_dpp %[t0], %[t0], %[t0] row_ror:2 row_mask:0xf bank_mask:0xf\n\t"
        "s_nop 1\n\t"
        "v_min_i32_dpp %[t0], %[t0], %[t0] row_ror:1 row_mask:0xf bank_mask:0xf\n\t"
        "s_nop 1\n\t"
        "v_readfirstlane_b32 s48, %[t0]\n\t"
        "s_nop 1\n\t"
        "s_add_i32 s48, s48, %c[nslot]\n\t"
        "s_and_b32 s48, s48, -2\n\t"
        "s_min_i32 s48, s48, s49\n\t"
        "s_cmp_lt_i32 s44, s48\n\t"
        "s_cbranch_scc1 22b\n\t"
        "v_mov_b32 %[t1], s44\n\t"
        "s_waitcnt vmcnt(0)\n\t"
        "ds_write_b32 %[pa], %[t1]\n\t"
        "s_sleep 1\n\t"
        "s_sub_u32 s55, s55, 1\n\t"
        "s_cmp_lg_u32 s55, 0\n\t"
        "s_cbranch_scc1 26b\n\t"
        "s_branch 22b\n"
        "30:\n\t"
        ".set k, 6\n\t"
        ".rept 7\n\t"
        ".set wcnt, 4 * k\n\t"
        "v_mov_b32 %[t1], 2 * %c[nku] - k\n\t"
        WAITW
        "ds_write_b32 %[pa], %[t1]\n\t"
        ".set k, k - 1\n\t"
        ".endr\n\t"
        "s_waitcnt lgkmcnt(0)\n\t"
        "s_endpgm\n\t"
        ".p2align 7\n"
        "23:\n\t"
        ".set wcnt, %c[wmin]-1\n\t"
        ".rept %c[wmax] - %c[wmin] + 1\n\t"
        UNIT4("%[vo]")
        "ds_write_b32 %[pa], %[t1]\n\t"
        "s_mov_b32 m0, s55\n\t"
        UNIT4("%[t0]")
        "s_branch 24b\n\t"
        ".p2align 7\n\t"
        ".set wcnt, wcnt+1\n\t"
        ".endr\n"
        "90:\n\t"
        "s_waitcnt lgkmcnt(0)"
        : [va] "=&v"(va), [vb] "=&v"(vb), [vo] "=&v"(vo), [pa] "=&v"(pa), [nxa] "=&v"(nxa), [t0] "=&v"(t0), [t1] "=&v"(t1), [t2] "=&v"(t2), [t3] "=&v"(t3)
        : [tid] "v"(threadIdx.x), [bid] "s"(blockIdx.x), [ka] "s"(__builtin_amdgcn_kernarg_segment_ptr()), [nch] "i"(CODE_KIB - 1), [jk] "i"(RING - UNIT),
          [wmin] "i"(WMIN), [wmax] "i"(WMAX), [gain] "i"(WGAIN), [wmid] "i"(WMID), [tau2] "i"(TAU2),
          [ctl] "i"(L_CTRL), [nslot] "i"(NSLOT), [nku] "i"(NKU), [cap] "i"(SPIN_CAP), [jump] "i"(JUMP), [decay] "i"(DECAY), [uend] "i"(UEND), [gain2] "i"(WGAIN2)
        : "memory", "scc", "s40", "s41", "s42", "s43", "s44", "s45", "s46", "s47", "s48", "s49", "s50", "s51", "s52", "s53", "s54", "s55", "s56", "s57", "s58", "s59", "s62", "s63", "s64");
  }
  __builtin_amdgcn_sched_barrier(0);
  const int tid = threadIdx.x, lane = tid & 63, wave = __builtin_amdgcn_readfirstlane(tid >> 6);
  const int xg = blockIdx.x & 7, kk = blockIdx.x >> 3;
  const int b = kk >> 3, pj = 8 * xg + (kk & 7), h = pj >> 1;
  const int r4 = lane >> 4, c16 = lane & 15;
  asm volatile("" : "+s"(x), "+s"(wq), "+s"(wo), "+s"(w1), "+s"(w3), "+s"(w2), "+s"(gw), "+s"(out), "+s"(ws));
  if (wave == 1) {
    const int wi = lane - 16;
    const int now = (int)(unsigned)__builtin_amdgcn_s_memrealtime() + REF0;
    ctrl[lane] = (lane == 1 || lane == 2) ? now : lane == 8 ? now - REF0 : (lane < 16 || lane >= 32) ? 0 : wi < NCW ? wi : wi < NCW + NHW ? NHC * NCW + NHH * (wi - NCW) : NEVER;
  }
  BARRIER();

  const __amdgpu_buffer_rsrc_t wsr = mkrsrc(ws, WS_BYTES);
  auto wg_max = [&]() __attribute__((always_inline)) {
    float mg = ctrlf[32 + (c16 < NCW + NHW ? c16 : 0)];
    mg = fmaxf(mg, dpp<ROR8>(mg)); mg = fmaxf(mg, dpp<ROR4>(mg)); mg = fmaxf(mg, dpp<ROR2>(mg)); mg = fmaxf(mg, dpp<ROR1>(mg));
    return mg;
  };

  if (wave == XWAVE + 1) {
    unsigned k = (((unsigned)__builtin_amdgcn_s_memrealtime() + 250u) >> 9) + 1;
#pragma unroll 1
    for (int it = 0; it < 32; ++it, ++k) {
      if (__builtin_amdgcn_readfirstlane(ctrl[0]) + (int)((k << 9) - (unsigned)__builtin_amdgcn_s_memrealtime()) / 13 >= NUNIT - 32) break;
      wait_until(k << 9);
      const int own = __builtin_amdgcn_readfirstlane(ctrl[1]);
      if (blockIdx.x < 8 && lane < 8) __builtin_amdgcn_raw_buffer_store_b128(u32x4{(unsigned)own, (unsigned)own, (unsigned)own, (unsigned)own}, wsr, WS_PACE + (k & 31u) * 2048 + blockIdx.x * 128 + lane * 16, 0, SC1);
      wait_until((k << 9) + 200 + 2 * kk);
      const int d = (int)__builtin_amdgcn_raw_buffer_load_b32(wsr, WS_PACE + (k & 31u) * 2048 + (lane & 7) * 128, 0, SC1) - own;
      const bool ok = (unsigned)(d + 600) < 1200u;
      const float sum = row16_sum(ok ? (float)d : 0.f), cnt = row16_sum(ok ? 1.f : 0.f);
      if (cnt >= 8.f) ctrl[2] = own + (int)(sum / cnt);
    }
    return;
  }

  if (wave == XWAVE) {
#pragma unroll 1
    for (int i = 0; i < 20; ++i) PAUSE(4);
    const u32x4 xv = *(const GAS u32x4*)(x + b * D + 8 * lane), gv = *(const GAS u32x4*)(gw + 8 * lane);
    *(LAS u32x4*)(L + L_X + lane * 16) = xv;
    float gd = 0.f;
#pragma unroll
    for (int k = 0; k < 4; ++k) gd = dot2(xv[k], gv[k], gd);
    gd = xrow_sum(row16_sum(gd));
    const float g = 1.f / (1.f + __expf(-gd));
    ctrlf[48] = g;
    lds_arrive(L, C_X, lane);
#pragma unroll 1
    for (int i = 0; i < 16 + kk; ++i) PAUSE(20);
    const u32x2 ep = __builtin_amdgcn_raw_buffer_load_b64(wsr, WS_EPOCH + b * 256, 0, SC1);
    const u64 tag = (((u64)(unsigned)__builtin_amdgcn_readfirstlane(ep[1]) << 32) | (unsigned)__builtin_amdgcn_readfirstlane(ep[0])) + TAG_STEP;
#pragma unroll 1
    for (int spin = 0; spin < SPIN_CAP; ++spin) { if (__builtin_amdgcn_readfirstlane(ctrl[C_MAX]) >= NCW + NHW) break; __builtin_amdgcn_s_sleep(16); }
    CFENCE();
    const float mg = wg_max();
    float l = 0.f;
#pragma unroll 4
    for (int i = 0; i < NKU * 64 / 1024; ++i) {
      const f32x4 sc = *(const LAS f32x4*)(L + L_SC + i * 1024 + lane * 16);
      l += (expd(sc[0] - mg) + expd(sc[1] - mg)) + (expd(sc[2] - mg) + expd(sc[3] - mg));
    }
    l = xrow_sum(row16_sum(l));
    const unsigned tlo = (unsigned)tag, thi = (unsigned)(tag >> 32);
    if (lane < 8) __builtin_amdgcn_raw_buffer_store_b128(u32x4{__float_as_uint(mg), tlo, __float_as_uint(l), thi}, wsr, WS_REC + (b * 64 + pj) * 128 + lane * 16, 0, SC1);
#pragma unroll 1
    for (int i = 0; i < 12 + (kk & 31); ++i) PAUSE(12);
    u32x4 rec;
#pragma unroll 1
    for (int spin = 0; spin < SPIN_CAP; ++spin) {
      CFENCE();
      rec = __builtin_amdgcn_raw_buffer_load_b128(wsr, WS_REC + (b * 64 + (pj ^ 1)) * 128, 0, SC1);
      if (__builtin_amdgcn_ballot_w64(rec[1] != tlo || rec[3] != thi) == 0ull) break;
      __builtin_amdgcn_s_sleep(16);
    }
    const float mo = __uint_as_float(rec[0]), lo = __uint_as_float(rec[2]);
    const float mh = fmaxf(mg, mo), wt = expd(mg - mh);
    ctrlf[49] = g * wt / (wt * l + expd(mo - mh) * lo);
    const u32x4 yt = {tlo & YMASK, (tlo >> 12) & YMASK, (unsigned)(tag >> 24) & YMASK, (thi >> 4) & YMASK};
    *(LAS u32x4*)(L + L_CTRL + 52 * 4) = yt;
    lds_arrive(L, C_CF, lane);
    GAS unsigned* op = (GAS unsigned*)(out + b * D + 8 * pj + 4 * (lane & 1) + 2 * (lane >> 5));
    int off = WS_Y + (b * 64 + (lane >> 1)) * 2048 + pj * 32 + (lane & 1) * 16;
    asm volatile("" : "+v"(op), "+v"(off));
    { const unsigned t = *(volatile GAS unsigned*)op; asm volatile("" :: "v"(t)); }
    BARRIER();
    {
      const unsigned now = (unsigned)__builtin_amdgcn_s_memrealtime();
      const int d = __builtin_amdgcn_readfirstlane(ctrl[2]) + TSTREAM + FOFF - (int)now;
      const unsigned t = now + (unsigned)min(max(d, FMIN), FMAX);
#pragma unroll 1
      for (int spin = 0; spin < 4096 && (int)(t - (unsigned)__builtin_amdgcn_s_memrealtime()) > 0; ++spin) {}
    }
    u32x4 ya, yb;
#pragma unroll 1
    for (int spin = 0; spin < SPIN_CAP; ++spin) {
      CFENCE();
      ya = __builtin_amdgcn_raw_buffer_load_b128(wsr, off, 0, SC1);
      yb = __builtin_amdgcn_raw_buffer_load_b128(wsr, off, 32 * 2048, SC1);
      const unsigned bad = (((ya[0] ^ yt[0]) | (ya[1] ^ yt[1])) | ((ya[2] ^ yt[2]) | (ya[3] ^ yt[3])) | ((yb[0] ^ yt[0]) | (yb[1] ^ yt[1])) | ((yb[2] ^ yt[2]) | (yb[3] ^ yt[3]))) & YMASK;
      if (__builtin_amdgcn_ballot_w64(bad != 0u) == 0ull) break;
    }
    float o[4];
#pragma unroll
    for (int k = 0; k < 4; ++k) o[k] = __uint_as_float(ya[k] & ~YMASK) + __uint_as_float(yb[k] & ~YMASK);
    float e0 = swadd32(o[0], o[2]), e1 = swadd32(o[1], o[3]);
    e0 = swadd16(e0, e0); e1 = swadd16(e1, e1);
    e0 += dpp<ROR8>(e0); e1 += dpp<ROR8>(e1); e0 += dpp<ROR4>(e0); e1 += dpp<ROR4>(e1); e0 += dpp<ROR2>(e0); e1 += dpp<ROR2>(e1);
    if ((lane & 30) == 0) *op = pack_bf16(e0, e1);
    if (pj == 0 && lane < 8) __builtin_amdgcn_raw_buffer_store_b128(u32x4{tlo, thi, tlo, thi}, wsr, WS_EPOCH + b * 256 + lane * 16, 0, SC1);
    return;
  }

  const int cw = wave - 1, hw = wave - 1 - NCW;
  int landed = 0;
  auto wait_unit = [&](int u) __attribute__((always_inline)) {
#pragma unroll 1
    for (int spin = 0; landed <= u && spin < SPIN_CAP; ++spin) {
      landed = __builtin_amdgcn_readfirstlane(ctrl[0]);
    }
    CFENCE();
  };
  auto take = [&](int u, int next, u32x4& a0, u32x4& a1, u32x4& a2, u32x4& a3) __attribute__((always_inline)) {
    wait_unit(u);
    const LAS u32x4* sp = (const LAS u32x4*)(L + (u & (NSLOT - 1)) * UNIT + lane * 16);
    a0 = sp[0]; a1 = sp[64]; a2 = sp[128]; a3 = sp[192];
    LDS_WAIT();
    ctrl[16 + cw] = next;
    asm volatile("" : "+v"(a0), "+v"(a1), "+v"(a2), "+v"(a3) :: "memory");
  };
  u32x4 q;
  auto build_q = [&]() __attribute__((always_inline)) {
    lds_await(ctrl, C_Q, NHW);
    const LAS f32x4* qp = (const LAS f32x4*)(L + L_QP + (2 * r4 * DH + 8 * c16) * 4);
    const f32x4 lo = qp[0] + qp[DH / 4], hi = qp[1] + qp[DH / 4 + 1];
    q[0] = pack_bf16(xrow_sum(lo[0]), xrow_sum(lo[1])); q[1] = pack_bf16(xrow_sum(lo[2]), xrow_sum(lo[3]));
    q[2] = pack_bf16(xrow_sum(hi[0]), xrow_sum(hi[1])); q[3] = pack_bf16(xrow_sum(hi[2]), xrow_sum(hi[3]));
  };
  constexpr float SCALE = 0.088388347648318440f;
  float mx = -3.4028234e38f;
  auto score = [&](int u, const u32x4& k0, const u32x4& k1, const u32x4& k2, const u32x4& k3) __attribute__((always_inline)) {
    float d0 = 0.f, d1 = 0.f, d2 = 0.f, d3 = 0.f;
#pragma unroll
    for (int k = 0; k < 4; ++k) { d0 = dot2(k0[k], q[k], d0); d1 = dot2(k1[k], q[k], d1); d2 = dot2(k2[k], q[k], d2); d3 = dot2(k3[k], q[k], d3); }
    const unsigned s01 = pack_bf16(row16_sum(d0), row16_sum(d1)), s23 = pack_bf16(row16_sum(d2), row16_sum(d3));
    const unsigned t01 = pack_bf16(bflo(s01) * SCALE, bfhi(s01) * SCALE), t23 = pack_bf16(bflo(s23) * SCALE, bfhi(s23) * SCALE);
    const f32x4 sc = {bflo(t01), bfhi(t01), bflo(t23), bfhi(t23)};
    mx = fmaxf(fmaxf(mx, fmaxf(sc[0], sc[1])), fmaxf(sc[2], sc[3]));
    if (c16 == 0) *(LAS f32x4*)(L + L_SC + u * 64 + r4 * 16) = sc;
  };

  if (hw < 0) {
    {
      u32x4 hk[4 * NHC];
#pragma unroll
      for (int i = 0; i < NHC; ++i) take(cw + NCW * i, i + 1 < NHC ? cw + NCW * (i + 1) : NHELD + cw, hk[4 * i], hk[4 * i + 1], hk[4 * i + 2], hk[4 * i + 3]);
      build_q();
#pragma unroll
      for (int i = 0; i < NHC; ++i) { score(cw + NCW * i, hk[4 * i], hk[4 * i + 1], hk[4 * i + 2], hk[4 * i + 3]); __builtin_amdgcn_sched_barrier(0); }
    }
#pragma unroll 1
    for (int u = NHELD + cw; u < NKU; u += NCW) {
      u32x4 k0, k1, k2, k3;
      take(u, u + NCW, k0, k1, k2, k3);
      score(u, k0, k1, k2, k3);
    }
    ctrlf[32 + cw] = xrow_max(mx);
    lds_arrive(L, C_MAX, lane);
    lds_await(ctrl, C_MAX, NCW + NHW);
    const float mg = wg_max();
    float acc[8] = {0.f, 0.f, 0.f, 0.f, 0.f, 0.f, 0.f, 0.f};
    LAS unsigned* rp = (LAS unsigned*)(L + L_REC + cw * 272 + (8 * c16 + 2 * r4) * 2);
    asm volatile("" : "+v"(rp));
    float cf = 0.f;
#pragma unroll 1
    for (int u = NKU + cw; u < NUNIT; u += NCW) {
      const f32x4 sc = *(const LAS f32x4*)(L + L_SC + (u - NKU) * 64 + r4 * 16);
      float p[4];
#pragma unroll
      for (int t = 0; t < 4; ++t) { p[t] = expd(sc[t] - mg); asm volatile("" : "+v"(p[t])); }
      if ((u & ~(NCW - 1)) == NUNIT - 32) { lds_await(ctrl, C_CF, 1); cf = ctrlf[49]; }
      u32x4 v[4];
      take(u, u + NCW, v[0], v[1], v[2], v[3]);
#pragma unroll
      for (int t = 0; t < 4; ++t)
#pragma unroll
        for (int k = 0; k < 4; ++k) { acc[2 * k] = fmaf(p[t], bflo(v[t][k]), acc[2 * k]); acc[2 * k + 1] = fmaf(p[t], bfhi(v[t][k]), acc[2 * k + 1]); }
    }
    const float e0 = swadd16(swadd32(acc[0], acc[4]), swadd32(acc[2], acc[6])), e1 = swadd16(swadd32(acc[1], acc[5]), swadd32(acc[3], acc[7]));
    *rp = pack_bf16(e0 * cf, e1 * cf);
    BARRIER();
    return;
  }

  u32x4 wb[16];
  const unsigned tstart = (unsigned)__builtin_amdgcn_readfirstlane(ctrl[8]);
  {
    const __amdgpu_buffer_rsrc_t rq = mkrsrc(wq, D * H * DH * 2);
    const int vo = ((64 * hw + 2 * r4) * (H * DH) + h * DH + 8 * c16) * 2;
#pragma unroll
    for (int i = 0; i < 16; ++i) { if (i == 0) wait_until(tstart + TQ + hw * QA); else PAUSE(QS); wb[i] = ldw(rq, vo, (8 * (i >> 1) + (i & 1)) * (H * DH * 2)); }
    lds_await(ctrl, C_X, 1);
    const LAS unsigned* xl = (const LAS unsigned*)(L + L_X + (64 * hw + 2 * r4) * 2);
    float a[8] = {0.f, 0.f, 0.f, 0.f, 0.f, 0.f, 0.f, 0.f};
#pragma unroll
    for (int i = 0; i < 8; ++i) {
      const unsigned xp = xl[4 * i];
#pragma unroll
      for (int k = 0; k < 4; ++k) {
        a[2 * k] = dot2(perm_lo(wb[2 * i][k], wb[2 * i + 1][k]), xp, a[2 * k]);
        a[2 * k + 1] = dot2(perm_hi(wb[2 * i][k], wb[2 * i + 1][k]), xp, a[2 * k + 1]);
      }
    }
    const float e0 = swadd16(swadd32(a[0], a[4]), swadd32(a[2], a[6])), e1 = swadd16(swadd32(a[1], a[5]), swadd32(a[3], a[7]));
    *(LAS f32x2*)(L + L_QP + (hw * DH + 8 * c16 + 2 * r4) * 4) = f32x2{e0, e1};
    lds_arrive(L, C_Q, lane);
    const int hb = NHC * NCW + NHH * hw;
#pragma unroll
    for (int i = 0; i < NHH; ++i) take(hb + i, i + 1 < NHH ? hb + i + 1 : NEVER, wb[4 * i], wb[4 * i + 1], wb[4 * i + 2], wb[4 * i + 3]);
    build_q();
#pragma unroll
    for (int i = 0; i < NHH; ++i) { score(hb + i, wb[4 * i], wb[4 * i + 1], wb[4 * i + 2], wb[4 * i + 3]); __builtin_amdgcn_sched_barrier(0); }
    ctrlf[32 + cw] = xrow_max(mx);
    lds_arrive(L, C_MAX, lane);
  }
  f32x4 y[4] = {{0.f, 0.f, 0.f, 0.f}, {0.f, 0.f, 0.f, 0.f}, {0.f, 0.f, 0.f, 0.f}, {0.f, 0.f, 0.f, 0.f}};
  if ((pj & 7) != 7) {
    const int sl = 7 * (pj >> 3) + (pj & 7);
    const __amdgpu_buffer_rsrc_t r1 = mkrsrc(w1, D * F * 2), r3 = mkrsrc(w3, D * F * 2), r2 = mkrsrc(w2, F * D * 2);
    const int vo = ((64 * hw + (lane & 60)) * F + FS * sl + 8 * (lane & 3)) * 2;
#pragma unroll
    for (int i = 0; i < 8; ++i) { if (i == 0) wait_until(tstart + T1 + hw * A1); else PAUSE(S1); wb[i] = ldw(i < 4 ? r1 : r3, vo, (i & 3) * (F * 2)); }
    const u32x2 xm = *(const LAS u32x2*)(L + L_X + (64 * hw + (lane & 60)) * 2);
    float a[16];
#pragma unroll
    for (int m = 0; m < 2; ++m)
#pragma unroll
      for (int k = 0; k < 4; ++k) {
        a[8 * m + 2 * k] = dot2(perm_lo(wb[4 * m + 2][k], wb[4 * m + 3][k]), xm[1], dot2(perm_lo(wb[4 * m][k], wb[4 * m + 1][k]), xm[0], 0.f));
        a[8 * m + 2 * k + 1] = dot2(perm_hi(wb[4 * m + 2][k], wb[4 * m + 3][k]), xm[1], dot2(perm_hi(wb[4 * m][k], wb[4 * m + 1][k]), xm[0], 0.f));
      }
    f32x4 e;
#pragma unroll
    for (int k = 0; k < 4; ++k) {
      float t = swadd16(swadd32(a[k], a[8 + k]), swadd32(a[4 + k], a[12 + k]));
      t += dpp<ROR8>(t); e[k] = t + dpp<ROR4>(t);
    }
    if ((lane & 12) == 0) *(LAS f32x4*)(L + L_MP + (hw * 64 + (lane & 32) + 8 * (lane & 3) + 4 * (r4 & 1)) * 4) = e;
    lds_arrive(L, C_MLP, lane);
    u32x2 wr[8];
#pragma unroll
    for (int i = 0; i < 8; ++i) { if (i == 0) wait_until(tstart + T2 + hw * A2); else PAUSE(S2); wr[i] = ldw2(r2, ((FS * sl + 8 * r4) * D + 64 * hw + 4 * c16) * 2, i * (D * 2)); }
    lds_await(ctrl, C_MLP, NHW);
    const LAS float* mp = (const LAS float*)(L + L_MP) + (lane & 31);
    float ga = 0.f, up = 0.f;
#pragma unroll
    for (int w = 0; w < NHW; ++w) { ga += mp[w * 64]; up += mp[w * 64 + 32]; }
    const float hv = ga / (1.f + __expf(-ga)) * up * (1.f - ctrlf[48]);
    const unsigned hh = pack_bf16(hv, 0.f), hl = pack_bf16(hv - bflo(hh), 0.f);
    *(LAS u16*)(L + L_H + hw * 128 + lane * 2) = (u16)(lane < 32 ? hh : hl);
    LDS_WAIT();
    u32x4 ha = *(const LAS u32x4*)(L + L_H + hw * 128 + (c16 & 1) * 64 + r4 * 16);
    if (c16 >= 2) ha = u32x4{0u, 0u, 0u, 0u};
#pragma unroll
    for (int e = 0; e < 4; ++e) {
      u32x4 f;
#pragma unroll
      for (int k = 0; k < 4; ++k) f[k] = (e & 1) ? perm_hi(wr[2 * k][e >> 1], wr[2 * k + 1][e >> 1]) : perm_lo(wr[2 * k][e >> 1], wr[2 * k + 1][e >> 1]);
      y[e] = mfma(ha, f, y[e]);
    }
  }
#pragma unroll
  for (int e = 0; e < 4; ++e) asm volatile("" : "+v"(y[e]));
  {
    const __amdgpu_buffer_rsrc_t ro = mkrsrc(wo, H * DH * D * 2);
#pragma unroll
    for (int t = 0; t < 4; ++t) {
      u32x2 wr[8];
#pragma unroll
      for (int i = 0; i < 8; ++i) { if (t + i == 0) wait_until(tstart + TO + hw * AO); else PAUSE(SO); wr[i] = ldw2(ro, ((h * DH + 32 * r4) * D + 64 * hw + 4 * c16) * 2, (8 * t + i) * (D * 2)); }
#pragma unroll
      for (int e = 0; e < 4; ++e) {
#pragma unroll
        for (int k = 0; k < 4; ++k) wb[4 * t + e][k] = (e & 1) ? perm_hi(wr[2 * k][e >> 1], wr[2 * k + 1][e >> 1]) : perm_lo(wr[2 * k][e >> 1], wr[2 * k + 1][e >> 1]);
        asm volatile("" : "+v"(wb[4 * t + e]));
      }
    }
  }
  lds_await(ctrl, C_CF, 1);
  u32x4 yt = *(const LAS u32x4*)(L + L_CTRL + 52 * 4);
  int yo = WS_Y + (b * 64 + pj) * 2048 + hw * 256 + c16 * 16;
  const LAS u32x4* rec = (const LAS u32x4*)(L + L_REC + (lane & 3) * 272 + r4 * 64);
  unsigned ym = ~YMASK;
  asm volatile("" : "+v"(yt), "+v"(yo), "+v"(rec), "+s"(ym));
  BARRIER();
  {
    const u32x4 a[4] = {rec[0], rec[1], rec[2], rec[3]};
#pragma unroll
    for (int t = 0; t < 4; ++t)
#pragma unroll
      for (int e = 0; e < 4; ++e) y[e] = mfma(a[t], wb[4 * t + e], y[e]);
    u32x4 v;
#pragma unroll
    for (int e = 0; e < 4; ++e) {
      const f32x2 t = y[e].xy + y[e].zw;
      unsigned r;
      asm("v_add_f32 %0, %1, %2\n\tv_add_u32 %0, %5, %0\n\tv_and_or_b32 %0, %0, %3, %4" : "=&v"(r) : "v"(t[0]), "v"(t[1]), "s"(ym), "v"(yt[e]), "i"((YMASK + 1u) / 2u));
      v[e] = r;
    }
    if (lane < 16) __builtin_amdgcn_raw_buffer_store_b128(v, wsr, yo, 0, SC1);
  }
}

extern "C" void kernel_launch(void* const* d_in, const int* in_sizes, int n_in, void* d_out, int out_size, void* d_ws, size_t ws_size,
                              hipStream_t stream) {
  gated_mix__mha_block_cached__glu_gated_swiglu__decode_kernel<<<256, NTHR, 0, stream>>>((const u16*)d_in[0], (const u16*)d_in[1], (const u16*)d_in[2], (const u16*)d_in[3], (const u16*)d_in[4],
                                             (const u16*)d_in[6], (const u16*)d_in[7], (const u16*)d_in[8], (const u16*)d_in[9], (u16*)d_out,
                                             (unsigned char*)d_ws);
}
```

```cpp
#include <hip/hip_runtime.h>
#include <hip/hip_bf16.h>
#define STAMPS 1
#define MWAVE 13
#define MIDX 7
#define MTYPE 0
#define RWAVE 0
#define RIDX 1
#define MSCALE 10.f

#define GAS __attribute__((address_space(1)))
#define LAS __attribute__((address_space(3)))
typedef unsigned short u16;
typedef unsigned long long u64;
using u32x4 = __attribute__((ext_vector_type(4))) unsigned;
using u32x2 = __attribute__((ext_vector_type(2))) unsigned;
using f32x4 = __attribute__((ext_vector_type(4))) float;
using f32x2 = __attribute__((ext_vector_type(2))) float;
using bf16x2 = __attribute__((ext_vector_type(2))) __bf16;
using bf16x8 = __attribute__((ext_vector_type(8))) __bf16;

constexpr int B = 4, D = 512, H = 32, SK = 8192, DH = 128, F = 1792;
constexpr int NCW = 4, NHW = 8, XWAVE = 1 + NCW + NHW, NWAVE = XWAVE + 2, NTHR = NWAVE * 64;
#define QA 112
#define QB 5
#define T1 1400
#define A1 150
#define B1 18
#define T2 2500
#define A2 40
#define B2 4
#define TO 2900
#define AO 350
#define BO 8
#define TS 68.6
#define REF0 180
#define PLAG 1
#define NOPST 0
#define NOPLD 0
#define PAUX 16
#define RAUX 16
#define PFW 0
#define TQ 100
#define NINIT 6
#define CODE_KIB 21
#define FDEL 0
#define CODEPF 1
#define YST 16
#define YLD 16
#define RED16 0
#define MASKP 0
#define PSLP 0
#define OUTSC 0
#define OUT_STORE(p, v) *(GAS unsigned*)(p) = v
#define CLR0 1
#define MLPFULL 0
#define MLPPF 0
#define TP 1200
#define AP 12
#define GRID 0
#define B2BD 0
#define B2BAUX 16
#define WOB2B 0
#define TOUCHW 1
#define W0 30
#define BARPOS 12
#define NO_WO 0
#define TLBPF 0
#define TMODE 0
#define SPW 4
#define QS 2
#define S1 6
#define S2 1
#define SO 2
#define USL 0
#define JUMP 100
#define DECAY 6
#define HOLD0 10000
#define UEND 420
#define WGAIN2 18
#define WMID2 WMID
#define FOFF 160
#define FMIN 45
#define FMAX 150
#define NRED 64
#define KEARLY 0
#define KLAST 16
#define NOTHR 0
#define WMIN0 16
#define QROW 1
#define QAUX 2
#define QXOR 0
#define HX 0
#define QLO 7
#define HAND 31
#define HOR 0
#define HQ (QLO == 7 ? ((h & HAND) | HOR) : QLO == 1 ? xg + 8 * (h & 3) : 2 * xg + (h & 1) + 16 * ((h >> 1) & 1))
#define QPFM 0
#define HBLK 1
#define HM (HBLK ? NHH : 1)
#define HS (HBLK ? 1 : NHW)
#define RMASK 0
#define SBIT 16
#define XB0 6
#define XM0 0x40000
#define XB1 1
#define XM1 0x100000
#define UMID 128
#define PAT 0
#define SLOTSH 8
#define WMID 21.25
#define WGAIN 10
#define WMIN 16
#define WMAX 28
#define CLRAUX 16
#define PCONT 1
#define XDEL 20
#define XSPR 0
#define XNT 0
#define MIMIC 1
#define NCLR 2
#define NOFLAG 0
#define CSPR 0
constexpr int UNIT = 4096;
constexpr int NSLOT = 32, RING = NSLOT * UNIT;
constexpr int NKU = (SK / 2) / 16, NUNIT = 2 * NKU;
constexpr int NHC = 6, NHH = 4, NHELD = NHC * NCW + NHH * NHW;
constexpr int FS = 32;
constexpr int L_CTRL = RING;
constexpr int L_X = L_CTRL + 256;
constexpr int L_QP = L_X + D * 2;
constexpr int L_MP = L_QP + NHW * DH * 4;
constexpr int L_REC = L_MP + NHW * 64 * 4;
constexpr int L_H = L_REC + NCW * 272;
constexpr int L_SC = L_H + NHW * 128;
constexpr int LDS_BYTES = L_SC + NKU * 64;
static_assert(LDS_BYTES <= 159 * 1024, "lds");
static_assert(WMIN <= (int)WMID && (int)WMID <= WMAX && WMAX <= 28, "window");
constexpr int WS_EPOCH = 0;
constexpr int WS_REC = 4096;
constexpr int WS_PACE = WS_REC + 256 * 128;
constexpr int WS_Y = WS_PACE + 64 * 2048;
constexpr int WS_BYTES = WS_Y + B * 64 * 2048;
constexpr u64 TAG_STEP = 0x9E3779B97F4A7C15ull;
constexpr unsigned YMASK = 0xFFFu;
constexpr int SPIN_CAP = 1 << 22;
constexpr int NEVER = 1 << 20;
constexpr int C_Q = 4, C_MAX = 5, C_MLP = 6, C_X = 7, C_CF = 9;

__device__ __forceinline__ float expd(float d) { return __builtin_amdgcn_exp2f(d * 1.4426950408889634f); }
__device__ __forceinline__ float bflo(unsigned u) { return __builtin_bit_cast(float, u << 16); }
__device__ __forceinline__ float bfhi(unsigned u) { return __builtin_bit_cast(float, u & 0xffff0000u); }
__device__ __forceinline__ float dot2(unsigned a, unsigned b, float acc) {
  return __builtin_amdgcn_fdot2_f32_bf16(__builtin_bit_cast(bf16x2, a), __builtin_bit_cast(bf16x2, b), acc, false);
}
__device__ __forceinline__ unsigned pack_bf16(float lo, float hi) {
  const bf16x2 v = {(__bf16)lo, (__bf16)hi};
  return __builtin_bit_cast(unsigned, v);
}
__device__ __forceinline__ unsigned perm_lo(unsigned a, unsigned b) { return __builtin_amdgcn_perm(b, a, 0x05040100u); }
__device__ __forceinline__ unsigned perm_hi(unsigned a, unsigned b) { return __builtin_amdgcn_perm(b, a, 0x07060302u); }
template <int CTRL> __device__ __forceinline__ float dpp(float x) {
  return __builtin_bit_cast(float, __builtin_amdgcn_mov_dpp(__builtin_bit_cast(int, x), CTRL, 0xf, 0xf, true));
}
constexpr int XOR1 = 0xB1, ROR1 = 0x121, ROR2 = 0x122, ROR4 = 0x124, ROR8 = 0x128;
__device__ __forceinline__ float row16_sum(float x) { x += dpp<ROR8>(x); x += dpp<ROR4>(x); x += dpp<ROR2>(x); x += dpp<ROR1>(x); return x; }
__device__ __forceinline__ float swadd32(float a, float b) {
  auto t = __builtin_amdgcn_permlane32_swap(__float_as_uint(a), __float_as_uint(b), false, false);
  return __uint_as_float(t[0]) + __uint_as_float(t[1]);
}
__device__ __forceinline__ float swadd16(float a, float b) {
  auto t = __builtin_amdgcn_permlane16_swap(__float_as_uint(a), __float_as_uint(b), false, false);
  return __uint_as_float(t[0]) + __uint_as_float(t[1]);
}
__device__ __forceinline__ float xrow_sum(float x) { x = swadd16(x, x); return swadd32(x, x); }
__device__ __forceinline__ float xrow_max(float x) {
  auto s = __builtin_amdgcn_permlane16_swap(__float_as_uint(x), __float_as_uint(x), false, false);
  x = fmaxf(__uint_as_float(s[0]), __uint_as_float(s[1]));
  auto t = __builtin_amdgcn_permlane32_swap(__float_as_uint(x), __float_as_uint(x), false, false);
  return fmaxf(__uint_as_float(t[0]), __uint_as_float(t[1]));
}
__device__ __forceinline__ __amdgpu_buffer_rsrc_t mkrsrc(const void* p, int bytes) { return __builtin_amdgcn_make_buffer_rsrc((void*)p, 0, bytes, 0x00020000); }
__device__ __forceinline__ u32x4 ldw(__amdgpu_buffer_rsrc_t r, int voff, int soff) { return __builtin_amdgcn_raw_buffer_load_b128(r, voff, soff, 2); }
#define LDS_WAIT() asm volatile("s_waitcnt lgkmcnt(0)" ::: "memory")
#define VM_DRAIN() asm volatile("s_waitcnt vmcnt(0)" ::: "memory")
#define CFENCE() asm volatile("" ::: "memory")
#define BARRIER() asm volatile("s_waitcnt lgkmcnt(0)\n\ts_barrier" ::: "memory")
#define PAUSE(n) do { __builtin_amdgcn_sched_barrier(0); asm volatile("s_sleep %0" :: "i"(n) : "memory"); __builtin_amdgcn_sched_barrier(0); } while (0)

__device__ __forceinline__ void lds_arrive(LAS unsigned char* L, int idx, int lane) {
  LDS_WAIT();
  if (lane == 0) __hip_atomic_fetch_add((LAS int*)(L + L_CTRL) + idx, 1, __ATOMIC_RELAXED, __HIP_MEMORY_SCOPE_WORKGROUP);
}
__device__ __forceinline__ void lds_await(volatile LAS int* ctrl, int idx, int n) {
#pragma unroll 1
  for (int spin = 0; spin < SPIN_CAP; ++spin) { if (__builtin_amdgcn_readfirstlane(ctrl[idx]) >= n) break; __builtin_amdgcn_s_sleep(1); }
  CFENCE();
}

__device__ __forceinline__ void wait_until(unsigned t) {
  __builtin_amdgcn_sched_barrier(0);
#pragma unroll 1
  for (int spin = 0; spin < 4096; ++spin) {
    const int d = (int)(t - (unsigned)__builtin_amdgcn_s_memrealtime());
    if (d <= 0) break;
    if (d > 24) __builtin_amdgcn_s_sleep(6);
  }
  CFENCE();
  __builtin_amdgcn_sched_barrier(0);
}
#define STR2(x) #x
#define STR(x) STR2(x)
constexpr int TAU2 = 2 * (int)(TS * 100.0 * 256.0 / NUNIT);
__device__ __forceinline__ f32x4 mfma(const u32x4& a, const u32x4& b, const f32x4& c) {
  return __builtin_amdgcn_mfma_f32_16x16x32_bf16(__builtin_bit_cast(bf16x8, a), __builtin_bit_cast(bf16x8, b), c, 0, 0, 0);
}
__device__ __forceinline__ u32x2 ldw2(__amdgpu_buffer_rsrc_t r, int voff, int soff) { return __builtin_amdgcn_raw_buffer_load_b64(r, voff, soff, 2); }
template <int CTRL> __device__ __forceinline__ int dppi(int x) { return __builtin_amdgcn_mov_dpp(x, CTRL, 0xf, 0xf, true); }

#define STAMP(i) do { __builtin_amdgcn_sched_barrier(0); ((volatile LAS unsigned*)(L + LDS_BYTES))[wave * 16 + (i)] = (unsigned)__builtin_amdgcn_s_memrealtime(); __builtin_amdgcn_sched_barrier(0); } while (0)
#define STAMP_FLUSH() do { LDS_WAIT(); const __amdgpu_buffer_rsrc_t dr = mkrsrc(ws + (4 << 20), 256 * 1024); \
    __builtin_amdgcn_raw_buffer_store_b128(*(volatile LAS u32x4*)(L + LDS_BYTES + lane * 16), dr, blockIdx.x * 1024 + lane * 16, 0, 16); \
    if (blockIdx.x == 0) { const unsigned t0 = __builtin_amdgcn_readfirstlane(((volatile LAS unsigned*)(L + LDS_BYTES))[0]); \
      while ((unsigned)__builtin_amdgcn_s_memrealtime() - t0 < 13000u) __builtin_amdgcn_s_sleep(8); \
      CFENCE(); float tot = 0.f, sum = 0.f, mxv = -1e9f, mnv = -1e9f; \
      for (int i = 0; i < 4; ++i) { const int wg = lane * 4 + i; \
        const unsigned ref = (RWAVE < 0) ? t0 : __builtin_amdgcn_raw_buffer_load_b32(dr, wg * 1024 + ((RWAVE < 0 ? 0 : RWAVE) * 16 + RIDX) * 4, 0, 16); \
        const float v = (float)(int)(__builtin_amdgcn_raw_buffer_load_b32(dr, wg * 1024 + (MWAVE * 16 + MIDX) * 4, 0, 16) - ref); \
        sum += v; mxv = fmaxf(mxv, v); mnv = fmaxf(mnv, -v); tot = fmaxf(tot, (float)(int)(__builtin_amdgcn_raw_buffer_load_b32(dr, wg * 1024 + (13 * 16 + 7) * 4, 0, 16) - t0)); } \
      sum = xrow_sum(row16_sum(sum)) * (1.f / 256.f); \
      float gm = mxv; gm = fmaxf(gm, dpp<ROR8>(gm)); gm = fmaxf(gm, dpp<ROR4>(gm)); gm = fmaxf(gm, dpp<ROR2>(gm)); gm = fmaxf(gm, dpp<ROR1>(gm)); gm = xrow_max(gm); \
      float gn = mnv; gn = fmaxf(gn, dpp<ROR8>(gn)); gn = fmaxf(gn, dpp<ROR4>(gn)); gn = fmaxf(gn, dpp<ROR2>(gn)); gn = fmaxf(gn, dpp<ROR1>(gn)); gn = xrow_max(gn); \
      tot = fmaxf(tot, dpp<ROR8>(tot)); tot = fmaxf(tot, dpp<ROR4>(tot)); tot = fmaxf(tot, dpp<ROR2>(tot)); tot = fmaxf(tot, dpp<ROR1>(tot)); tot = xrow_max(tot); \
      const float met = tot +  (MTYPE == 0 ? sum : MTYPE == 1 ? gm : MTYPE == 2 ? gm - sum : MTYPE == 3 ? sum + gn : gm + gn) * MSCALE; \
      const unsigned tgt = __builtin_amdgcn_readfirstlane((unsigned)(15000.f + met)); \
      while ((unsigned)__builtin_amdgcn_s_memrealtime() - t0 < tgt) __builtin_amdgcn_s_sleep(1); } } while (0)
#define STAMP_LDS 1024

__global__ void __launch_bounds__(NTHR, 4) gated_mix_kernel(const u16* x, const u16* kc, const u16* vc, const u16* wq, const u16* wo, const u16* w1,
                                                         const u16* w3, const u16* w2, const u16* gw, u16* out, unsigned char* ws) {
  __shared__ __attribute__((aligned(16))) unsigned char lds_raw[LDS_BYTES + STAMP_LDS];
  LAS unsigned char* L = (LAS unsigned char*)lds_raw;
  volatile LAS int* ctrl = (volatile LAS int*)(L + L_CTRL);
  volatile LAS float* ctrlf = (volatile LAS float*)(L + L_CTRL);
  u64 t_first; asm volatile("s_memrealtime %0\n\ts_waitcnt lgkmcnt(0)" : "=s"(t_first));
  __builtin_amdgcn_sched_barrier(0);
  if (threadIdx.x < 14 * 64) ((volatile LAS unsigned*)(L + LDS_BYTES))[(threadIdx.x >> 6) * 16] = (unsigned)t_first;
#define DBG(x) x
  {
    unsigned va, vb, vc2, vo, pa, nxa, t0, t1, t2, t3;
#define CODELD "global_load_lds_dwordx4 %[t0], s[56:57]\n\t"
#define ASTAMP(i) DBG("s_memrealtime s[60:61]\n\ts_waitcnt lgkmcnt(0)\n\tv_mov_b32 %[t1], %c[dbg] + 4 * " #i "\n\tv_mov_b32 %[t2], s60\n\tds_write_b32 %[t1], %[t2]\n\t")
#define WAITW "s_waitcnt (wcnt & 15) | ((wcnt >> 4) << 14) | 0x0F70\n\t"
#define SP(v, sb, wait) wait "global_load_lds_dword " v ", " sb " nt\n\tglobal_load_lds_dword " v ", " sb " sc1\n\t"
#define QPF SP("%[va]", "s[44:45]", "") SP("%[t2]", "s[44:45]", "")
#define U0 0x1000
#define U1 0x2000
#define U2 0x4000
#define UH 0
#define SB 16
#define WALK "s_and_b32 s54, s44, 255\n\ts_and_b32 s55, s54, -16\n\ts_add_u32 s54, s54, s55\n\ts_lshl_b32 s54, s54, 12\n\t"
#define SW "s_waitcnt vmcnt(%c[spw])\n\t"
#define UNIT4(v, v2) \
        WAITW "global_load_lds_dwordx4 " v ", s[40:41] nt\n\t" \
        WAITW "global_load_lds_dwordx4 " v ", s[40:41] offset:1024 nt\n\t" \
        WAITW "global_load_lds_dwordx4 " v2 ", s[40:41] offset:2048 nt\n\t" \
        WAITW "global_load_lds_dwordx4 " v2 ", s[40:41] offset:3072 nt\n\t"
    asm volatile(
        "s_getpc_b64 s[56:57]\n\t"
        "s_load_dwordx8 s[44:51], %[ka], 0x18\n\t"
        "s_load_dwordx2 s[52:53], %[ka], 0x38\n\t"
        "s_load_dwordx4 s[40:43], %[ka], 0x8\n\t"
        "v_readfirstlane_b32 s54, %[tid]\n\t"
        "s_mov_b32 m0, %c[jk]\n\t"
        "s_lshr_b32 s59, %[bid], 3\n\t"
        "s_cmp_ge_u32 s54, 64\n\t"
        "s_cbranch_scc1 90f\n\t"
        "s_min_u32 s54, s59, %c[nch]\n\t"
        "s_lshl_b32 s54, s54, 10\n\t"
        "v_lshlrev_b32 %[vo], 4, %[tid]\n\t"
        "s_and_b32 s56, s56, -128\n\t"
        "v_add_u32 %[t0], s54, %[vo]\n\t"
        "s_and_b32 s58, %[bid], 7\n\t"
        CODELD
        "s_setprio 3\n\t"
        "v_lshlrev_b32 %[t1], 7, %[tid]\n\t"
        "v_lshrrev_b32 %[va], 3, %[tid]\n\t"
        "v_lshlrev_b32 %[va], 13, %[va]\n\t"
        "v_and_b32 %[t2], " STR(QLO) ", %[tid]\n\t"
        "v_lshl_add_u32 %[va], %[t2], 7, %[va]\n\t"
        "v_and_b32 %[t2], 7 - " STR(QLO) ", %[tid]\n\t"
        "v_lshl_add_u32 %[va], %[t2], 10, %[va]\n\t"
        "s_lshl_b32 s54, s59, 17\n\t"
        "s_mul_i32 s55, s58, 128 * (" STR(QLO) " + 1)\n\t"
        "s_add_u32 s54, s54, s55\n\t"
        "v_add_u32 %[va], s54, %[va]\n\t"
        "v_add_u32 %[t2], 0x10000, %[va]\n\t"
        "s_lshl_b32 s55, s58, 5\n\t"
        "s_add_u32 s55, s55, s59\n\t"
        "s_mul_i32 s54, s55, 7 * 1024\n\t"
        "v_min_u32 %[t3], 55 * 128, %[t1]\n\t"
        "v_add_u32 %[t3], s54, %[t3]\n\t"
        "s_mul_i32 s54, s58, 448\n\t"
        "s_and_b32 s54, s54, -128\n\t"
        "s_mul_i32 s57, s59, 16 * 3584\n\t"
        "s_add_u32 s54, s54, s57\n\t"
        "v_lshrrev_b32 %[t0], 2, %[tid]\n\t"
        "v_mul_u32_u24 %[t0], 3584 - 512, %[t0]\n\t"
        "v_add3_u32 %[t0], %[t0], %[t1], s54\n\t"
        "v_lshl_add_u32 %[vb], s55, 14, %[t1]\n\t"
        "v_add_u32 %[t1], 0x2000, %[vb]\n\t"
        "s_and_b32 s54, %[bid], 0xc0\n\t"
        "s_lshl3_add_u32 s54, s58, s54\n\t"
        "s_and_b32 s56, s59, 7\n\t"
        "s_add_u32 s54, s54, s56\n\t"
        "s_lshr_b32 s56, s54, 1\n\t"
        "s_lshl_b32 s56, s56, 21\n\t"
        "s_and_b32 s66, s54, 1\n\t"
        "s_lshl_b32 s66, s66, " STR(SB) "\n\t"
        "s_bfe_u32 s57, s54, 0x10000 + 1 + " STR(XB0) "\n\t"
        "s_mul_i32 s57, s57, " STR(XM0) "\n\t"
        "s_xor_b32 s66, s66, s57\n\t"
        "s_bfe_u32 s57, s54, 0x10000 + 1 + " STR(XB1) "\n\t"
        "s_mul_i32 s57, s57, " STR(XM1) "\n\t"
        "s_xor_b32 s66, s66, s57\n\t"
        "s_waitcnt lgkmcnt(0)\n\t"
        QPF SP("%[t3]", "s[52:53]", "")
        ".if %c[spw] <= 4\n\ts_barrier\n\t.endif\n\t"
        SP("%[t0]", "s[48:49]", SW)
        ".if %c[spw] == 6\n\ts_barrier\n\t.endif\n\t"
        SP("%[t0]", "s[50:51]", SW)
        ".if %c[spw] == 8\n\ts_barrier\n\t.endif\n\t"
        SP("%[vb]", "s[46:47]", SW) SP("%[t1]", "s[46:47]", SW)
        ".if %c[spw] > 8\n\ts_barrier\n\t.endif\n\t"
        ASTAMP(6)
        "s_add_u32 s40, s40, s56\n\t"
        "s_addc_u32 s41, s41, 0\n\t"
        "s_add_u32 s42, s42, s56\n\t"
        "s_addc_u32 s43, s43, 0\n\t"
        "v_mov_b32 %[va], %[vo]\n\t"
        "v_or_b32 %[vo], s66, %[vo]\n\t"
        ".set so, 0\n\t"
        ".set wcnt, %c[spw] + " STR(KEARLY) "\n\t"
        ".rept 4\n\t"
        "s_movk_i32 m0, so\n\t"
        ".if (so & 4096) == 0\n\t"
        WAITW
        ".set wcnt, wcnt + 6\n\t"
        ".else\n\t"
        "s_nop 0\n\t"
        ".endif\n\t"
        "v_add_u32 %[vb], " STR(UH) ", %[vo]\n\t"
        "global_load_lds_dwordx4 %[vo], s[40:41] nt\n\t"
        "global_load_lds_dwordx4 %[vo], s[40:41] offset:1024 nt\n\t"
        "global_load_lds_dwordx4 %[vb], s[40:41] offset:2048 nt\n\t"
        "global_load_lds_dwordx4 %[vb], s[40:41] offset:3072 nt\n\t"
        ".if (so & 4096) == 0\n\t"
        "v_add_u32 %[vo], " STR(U0) ", %[vo]\n\t"
        ".elseif so == 4096\n\t"
        "v_add_u32 %[vo], " STR(U1) " - " STR(U0) ", %[vo]\n\t"
        ".else\n\t"
        "v_add_u32 %[vo], " STR(U2) " - " STR(U1) " - " STR(U0) ", %[vo]\n\t"
        ".endif\n\t"
        ".set so, so + 4096\n\t"
        ".endr\n\t"
        "s_waitcnt vmcnt(" STR(KLAST) ")\n\t"
        ASTAMP(7)
        "s_getpc_b64 s[50:51]\n"
        "20:\n\t"
        "s_add_u32 s50, s50, 23f-20b-%c[wmin]*128\n\t"
        "s_addc_u32 s51, s51, -1\n\t"
        "v_mov_b32 %[pa], %c[ctl]\n\t"
        "v_and_b32 %[nxa], 0x3c0 >> 2, %[vo]\n\t"
        "v_lshrrev_b32 %[nxa], 2, %[nxa]\n\t"
        "v_add_u32 %[nxa], %c[ctl] + 64, %[nxa]\n\t"
        "s_memrealtime s[58:59]\n\t"
        "s_mov_b32 s62, 0\n\t"
        "s_mov_b32 s65, " STR(WMIN0) "\n\t"
        "s_mov_b32 s63, %c[gain]\n\t"
        "s_mov_b32 s64, %c[wmid]\n\t"
        "s_mov_b32 s44, 4\n\t"
        "s_mov_b32 s45, %c[wmid] >> 8\n\t"
        "s_mov_b32 s46, 0x4000\n\t"
        "s_mov_b32 s47, 2 * %c[tau2]\n\t"
        "s_mov_b32 s48, %c[nslot]\n\t"
        "s_mov_b32 s49, %c[nku]\n\t"
        "s_waitcnt lgkmcnt(0)\n\t"
        "s_mov_b32 s59, 10000\n"
        "22:\n\t"
        "s_memrealtime s[56:57]\n\t"
        "ds_read_b32 %[t3], %[pa] offset:8\n\t"
        "s_lshl_b32 s52, s45, 7\n\t"
        "s_add_u32 s52, s50, s52\n\t"
        "s_addc_u32 s53, s51, 0\n\t"
        "s_add_i32 s54, s45, -1\n\t"
        "s_lshr_b32 s54, s54, 2\n\t"
        "s_sub_i32 s54, s44, s54\n\t"
        "v_mov_b32 %[t1], s54\n\t"
        "v_add_u32 %[t0], " STR(U0) ", %[vo]\n\t"
        "v_add_u32 %[vb], " STR(UH) ", %[vo]\n\t"
        "v_add_u32 %[vc], " STR(U0) " + " STR(UH) ", %[vo]\n\t"
        "s_add_u32 s55, s46, 0x1000\n\t"
        "s_mov_b32 m0, s46\n\t"
        "s_setpc_b64 s[52:53]\n"
        "24:\n\t"
        "s_waitcnt lgkmcnt(0)\n\t"
        "v_readfirstlane_b32 s57, %[t3]\n\t"
        "v_add_u32 %[t1], 1, %[t1]\n\t"
        "s_lshr_b32 s54, s47, 8\n\t"
        "s_sub_u32 s56, s56, s54\n\t"
        "v_mov_b32 %[t2], s56\n\t"
        "s_sub_i32 s54, s56, s58\n\t"
        "s_mov_b32 s58, s56\n\t"
        "s_sub_i32 s56, s56, s57\n\t"
        "ds_write2_b32 %[pa], %[t1], %[t2] offset1:1\n\t"
        "s_add_i32 s59, s59, s54\n\t"
        "s_add_i32 s59, s59, " STR(DECAY) "\n\t"
        "s_cmp_gt_i32 s54, %c[jump]\n\t"
        "s_cselect_b32 s59, s62, s59\n\t"
        "s_min_i32 s62, s56, s59\n\t"
        "s_mul_i32 s56, s62, s63\n\t"
        "s_add_i32 s56, s56, s64\n\t"
        "s_ashr_i32 s56, s56, 8\n\t"
        "s_max_i32 s56, s56, s65\n\t"
        "s_min_i32 s45, s56, %c[wmax]\n\t"
        "s_add_u32 s47, s47, %c[tau2]\n\t"
        "s_add_u32 s46, s46, 0x2000\n\t"
        "s_and_b32 s46, s46, %c[nslot] * 4096 - 1\n\t"
        "s_add_i32 s44, s44, 2\n\t"
        WALK
        "s_xor_b32 s54, s54, s66\n\t"
        "v_or_b32 %[vo], s54, %[va]\n\t"
        "s_cmp_lt_i32 s44, s48\n\t"
        "s_cbranch_scc1 22b\n\t"
        DBG("s_add_i32 s54, s44, -1\n\ts_lshr_b32 s54, s54, 5\n\ts_lshl_b32 s54, s54, 2\n\ts_add_u32 s54, s54, %c[dbg] + 14 * 64\n\tv_mov_b32 %[t0], s54\n\tv_mov_b32 %[t1], s45\n\t"
            "ds_write2_b32 %[t0], %[t2], %[t1] offset1:16\n\t")
        "s_cmp_lt_i32 s44, s49\n\t"
        "s_cbranch_scc1 25f\n\t"
        "s_cmp_ge_i32 s44, 2 * %c[nku]\n\t"
        "s_cbranch_scc1 30f\n\t"
        "s_mov_b32 s49, 2 * %c[nku]\n\t"
        "s_mov_b64 s[40:41], s[42:43]\n"
        "25:\n\t"
        "s_mov_b32 s65, %c[wmin]\n\t"
        "s_cmp_ge_i32 s44, %c[uend]\n\t"
        "s_cselect_b32 s63, %c[gain2], s63\n\t"
        "s_cmp_ge_i32 s44, " STR(UMID) "\n\t"
        "s_cselect_b32 s64, %c[wmid2], s64\n\t"
        "s_mov_b32 s55, %c[cap]\n"
        "26:\n\t"
        "ds_read_b32 %[t0], %[nxa]\n\t"
        "s_waitcnt lgkmcnt(0)\n\t"
        "s_nop 1\n\t"
        "v_min_i32_dpp %[t0], %[t0], %[t0] row_ror:8 row_mask:0xf bank_mask:0xf\n\t"
        "s_nop 1\n\t"
        "v_min_i32_dpp %[t0], %[t0], %[t0] row_ror:4 row_mask:0xf bank_mask:0xf\n\t"
        "s_nop 1\n\t"
        "v_min_i32_dpp %[t0], %[t0], %[t0] row_ror:2 row_mask:0xf bank_mask:0xf\n\t"
        "s_nop 1\n\t"
        "v_min_i32_dpp %[t0], %[t0], %[t0] row_ror:1 row_mask:0xf bank_mask:0xf\n\t"
        "s_nop 1\n\t"
        "v_readfirstlane_b32 s48, %[t0]\n\t"
        "s_nop 1\n\t"
        "s_add_i32 s48, s48, %c[nslot]\n\t"
        "s_and_b32 s48, s48, -2\n\t"
        "s_min_i32 s48, s48, s49\n\t"
        "s_cmp_lt_i32 s44, s48\n\t"
        "s_cbranch_scc1 22b\n\t"
        "v_mov_b32 %[t1], s44\n\t"
        "s_waitcnt vmcnt(0)\n\t"
        "ds_write_b32 %[pa], %[t1]\n\t"
        "s_sleep 1\n\t"
        "s_sub_u32 s55, s55, 1\n\t"
        "s_cmp_lg_u32 s55, 0\n\t"
        "s_cbranch_scc1 26b\n\t"
        "s_branch 22b\n"
        "30:\n\t"
        ".set k, 6\n\t"
        ".rept 7\n\t"
        ".set wcnt, 4 * k\n\t"
        "v_mov_b32 %[t1], 2 * %c[nku] - k\n\t"
        WAITW
        "ds_write_b32 %[pa], %[t1]\n\t"
        ".set k, k - 1\n\t"
        ".endr\n\t"
        DBG("s_memrealtime s[56:57]\n\ts_waitcnt lgkmcnt(0)\n\tv_mov_b32 %[t0], %c[dbg] + 4\n\tv_mov_b32 %[t1], s56\n\tds_write_b32 %[t0], %[t1]\n\t")
        "s_waitcnt lgkmcnt(0)\n\t"
        "s_endpgm\n\t"
        ".p2align 7\n"
        "23:\n\t"
        ".set wcnt, %c[wmin]-1\n\t"
        ".rept %c[wmax] - %c[wmin] + 1\n\t"
        UNIT4("%[vo]", "%[vb]")
        "ds_write_b32 %[pa], %[t1]\n\t"
        "s_mov_b32 m0, s55\n\t"
        UNIT4("%[t0]", "%[vc]")
        "s_branch 24b\n\t"
        ".p2align 7\n\t"
        ".set wcnt, wcnt+1\n\t"
        ".endr\n"
        "90:\n\t"
        "s_waitcnt lgkmcnt(0)"
        : [va] "=&v"(va), [vb] "=&v"(vb), [vc] "=&v"(vc2), [vo] "=&v"(vo), [pa] "=&v"(pa), [nxa] "=&v"(nxa), [t0] "=&v"(t0), [t1] "=&v"(t1), [t2] "=&v"(t2), [t3] "=&v"(t3)
        : [tid] "v"(threadIdx.x), [bid] "s"(blockIdx.x), [ka] "s"(__builtin_amdgcn_kernarg_segment_ptr()), [nch] "i"(CODE_KIB - 1), [jk] "i"(RING - UNIT),
          [spw] "i"(SPW), [wmin] "i"(WMIN), [wmax] "i"(WMAX), [gain] "i"(WGAIN), [wmid] "i"((int)(WMID * 256)), [tau2] "i"(TAU2),
          [ctl] "i"(L_CTRL), [nslot] "i"(NSLOT), [nku] "i"(NKU), [cap] "i"(SPIN_CAP), [dbg] "i"(LDS_BYTES), [jump] "i"(JUMP), [uend] "i"(UEND), [gain2] "i"(WGAIN2), [wmid2] "i"((int)(WMID2 * 256))
        : "memory", "scc", "s40", "s41", "s42", "s43", "s44", "s45", "s46", "s47", "s48", "s49", "s50", "s51", "s52", "s53", "s54", "s55", "s56", "s57", "s58", "s59", "s60", "s61", "s62", "s63", "s64", "s65", "s66");
  }
  __builtin_amdgcn_sched_barrier(0);
  const int tid = threadIdx.x, lane = tid & 63, wave = __builtin_amdgcn_readfirstlane(tid >> 6);
  const int xg = blockIdx.x & 7, kk = blockIdx.x >> 3;
  const int b = kk >> 3, pj = 8 * xg + (kk & 7), h = pj >> 1, s = pj & 1;
  const int r4 = lane >> 4, c16 = lane & 15;
  asm volatile("" : "+s"(x), "+s"(wq), "+s"(wo), "+s"(w1), "+s"(w3), "+s"(w2), "+s"(gw), "+s"(out), "+s"(ws));
  if (wave == 1) {
    const int wi = lane - 16;
    const int now = (int)(unsigned)__builtin_amdgcn_s_memrealtime() + REF0;
    ctrl[lane] = (lane == 1 || lane == 2) ? now : lane == 8 ? now - REF0 : (lane < 16 || lane >= 32) ? 0 : wi < NCW ? wi : wi < NCW + NHW ? NHC * NCW + HM * (wi - NCW) : NEVER;
  }
  BARRIER();

  const __amdgpu_buffer_rsrc_t wsr = mkrsrc(ws, WS_BYTES);
  auto wg_max = [&]() __attribute__((always_inline)) {
    float mg = ctrlf[32 + (c16 < NCW + NHW ? c16 : 0)];
    mg = fmaxf(mg, dpp<ROR8>(mg)); mg = fmaxf(mg, dpp<ROR4>(mg)); mg = fmaxf(mg, dpp<ROR2>(mg)); mg = fmaxf(mg, dpp<ROR1>(mg));
    return mg;
  };

  if (wave == XWAVE + 1) {
    unsigned k = ((unsigned)__builtin_amdgcn_s_memrealtime() >> SLOTSH) + 1;
#pragma unroll 1
    for (int it = 0; it < 4096 >> (SLOTSH - 2); ++it, ++k) {
      wait_until(k << SLOTSH);
      if (__builtin_amdgcn_readfirstlane(ctrl[0]) >= NUNIT - 36) break;
      const int own = __builtin_amdgcn_readfirstlane(ctrl[1]);
      constexpr unsigned SM = (8192u >> SLOTSH) - 1u;
      if (!NOPST && blockIdx.x < 16 && lane < 8) __builtin_amdgcn_raw_buffer_store_b128(u32x4{(unsigned)own, (unsigned)own, (unsigned)own, (unsigned)own}, wsr, WS_PACE + (k & SM) * 2048 + blockIdx.x * 128 + lane * 16, 0, PAUX);
      if (it < PLAG || NOPLD) continue;
      int e[16];
      asm volatile("s_load_dword %0, %16, 0x0\n\ts_load_dword %1, %16, 0x80\n\ts_load_dword %2, %16, 0x100\n\ts_load_dword %3, %16, 0x180\n\t"
                   "s_load_dword %4, %16, 0x200\n\ts_load_dword %5, %16, 0x280\n\ts_load_dword %6, %16, 0x300\n\ts_load_dword %7, %16, 0x380\n\t"
                   "s_load_dword %8, %16, 0x400\n\ts_load_dword %9, %16, 0x480\n\ts_load_dword %10, %16, 0x500\n\ts_load_dword %11, %16, 0x580\n\t"
                   "s_load_dword %12, %16, 0x600\n\ts_load_dword %13, %16, 0x680\n\ts_load_dword %14, %16, 0x700\n\ts_load_dword %15, %16, 0x780\n\t"
                   "s_waitcnt lgkmcnt(0)"
                   : "=&s"(e[0]), "=&s"(e[1]), "=&s"(e[2]), "=&s"(e[3]), "=&s"(e[4]), "=&s"(e[5]), "=&s"(e[6]), "=&s"(e[7]),
                     "=&s"(e[8]), "=&s"(e[9]), "=&s"(e[10]), "=&s"(e[11]), "=&s"(e[12]), "=&s"(e[13]), "=&s"(e[14]), "=&s"(e[15])
                   : "s"(ws + WS_PACE + ((k - PLAG) & SM) * 2048) : "memory");
      int sum = 0, cnt = 0;
#pragma unroll
      for (int i = 0; i < 16; ++i) { const int d = e[i] - own; const bool ok = (unsigned)(d + 600) < 1200u; sum += ok ? d : 0; cnt += ok ? 1 : 0; }
      if (cnt >= 8) ctrl[2] = own + (int)((float)sum / (float)cnt);
    }
    return;
  }

  if (wave == XWAVE) {
#pragma unroll 1
    for (int i = 0; i < XDEL + XSPR * (kk & 31); ++i) PAUSE(4);
    const u32x4 xv = *(const GAS u32x4*)(x + b * D + 8 * lane), gv = *(const GAS u32x4*)(gw + 8 * lane);
    *(LAS u32x4*)(L + L_X + lane * 16) = xv;
    float gd = 0.f;
#pragma unroll
    for (int k = 0; k < 4; ++k) gd = dot2(xv[k], gv[k], gd);
    gd = xrow_sum(row16_sum(gd));
    const float g = 1.f / (1.f + __expf(-gd));
    ctrlf[48] = g;
    lds_arrive(L, C_X, lane);
    u64 tag;
    asm volatile("s_load_dwordx2 %0, %1, 0x0\n\ts_waitcnt lgkmcnt(0)" : "=s"(tag) : "s"(ws + WS_EPOCH + b * 256) : "memory");
    tag += TAG_STEP;
#pragma unroll 1
    for (int spin = 0; spin < SPIN_CAP; ++spin) { if (__builtin_amdgcn_readfirstlane(ctrl[C_MAX]) >= NCW + NHW) break; __builtin_amdgcn_s_sleep(16); }
    CFENCE();
    const float mg = wg_max();
    float l = 0.f;
#pragma unroll 4
    for (int i = 0; i < NKU * 64 / 1024; ++i) {
      const f32x4 sc = *(const LAS f32x4*)(L + L_SC + i * 1024 + lane * 16);
      l += (expd(sc[0] - mg) + expd(sc[1] - mg)) + (expd(sc[2] - mg) + expd(sc[3] - mg));
    }
    l = xrow_sum(row16_sum(l));
    const unsigned tlo = (unsigned)tag, thi = (unsigned)(tag >> 32);
    if (lane < 8) __builtin_amdgcn_raw_buffer_store_b128(u32x4{__float_as_uint(mg), tlo, __float_as_uint(l), thi}, wsr, WS_REC + (b * 64 + pj) * 128 + lane * 16, 0, 16);
#pragma unroll 1
    for (int i = 0; i < 12 + (kk & 31); ++i) PAUSE(12);
    u32x4 rec;
#pragma unroll 1
    for (int spin = 0; spin < SPIN_CAP; ++spin) {
      CFENCE();
      rec = __builtin_amdgcn_raw_buffer_load_b128(wsr, WS_REC + (b * 64 + (pj ^ 1)) * 128, 0, 16);
      if (__builtin_amdgcn_ballot_w64(rec[1] != tlo || rec[3] != thi) == 0ull) break;
      __builtin_amdgcn_s_sleep(16);
    }
    const float mo = __uint_as_float(rec[0]), lo = __uint_as_float(rec[2]);
    const float mh = fmaxf(mg, mo), wt = expd(mg - mh);
    ctrlf[49] = g * wt / (wt * l + expd(mo - mh) * lo);
    const u32x4 yt = {tlo & YMASK, (tlo >> 12) & YMASK, (unsigned)(tag >> 24) & YMASK, (thi >> 4) & YMASK};
    *(LAS u32x4*)(L + L_CTRL + 52 * 4) = yt;
    lds_arrive(L, C_CF, lane);
    GAS unsigned* op = (GAS unsigned*)(out + b * D + 8 * pj + 4 * (lane & 1) + 2 * (lane >> 5));
    int off = WS_Y + (b * 64 + (lane >> 1)) * 2048 + pj * 32 + (lane & 1) * 16;
    asm volatile("" : "+v"(op), "+v"(off));
    if ((pj & (64 / NRED - 1)) != 0) return;
    STAMP(4);
    BARRIER();
    {
      const unsigned now = (unsigned)__builtin_amdgcn_s_memrealtime();
      const int d = __builtin_amdgcn_readfirstlane(ctrl[2]) + (int)(TS * 100) + FOFF - (int)now;
      const unsigned t = now + (unsigned)min(max(d, FMIN), FMAX);
#pragma unroll 1
      for (int spin = 0; spin < 4096 && (int)(t - (unsigned)__builtin_amdgcn_s_memrealtime()) > 0; ++spin) {}
    }
    STAMP(9);
    constexpr int NL = 128 / NRED;
    u32x4 yv[NL];
    int offs[NL];
#pragma unroll
    for (int i = 0; i < NL; ++i) { offs[i] = off; yv[i] = u32x4{0u, 0u, 0u, 0u}; }
#pragma unroll 1
    for (int spin = 0; spin < SPIN_CAP; ++spin) {
      CFENCE();
      unsigned bad = 0;
#pragma unroll
      for (int i = 0; i < NL; ++i) yv[i] = __builtin_amdgcn_raw_buffer_load_b128(wsr, off, i * (64 / NL) * 2048, 16);
#pragma unroll
      for (int i = 0; i < NL; ++i) bad |= ((yv[i][0] ^ yt[0]) | (yv[i][1] ^ yt[1])) | ((yv[i][2] ^ yt[2]) | (yv[i][3] ^ yt[3]));
      bad &= YMASK;
      { const u64 bal = __builtin_amdgcn_ballot_w64(bad != 0u); if (spin < 5) STAMP(10 + spin); ((volatile LAS unsigned*)(L + LDS_BYTES))[wave * 16 + 15] = spin + 1 + (bal == 1234567 ? 1 : 0); }
      if (__builtin_amdgcn_ballot_w64(bad != 0u) == 0ull) break;
      if (PSLP) __builtin_amdgcn_s_sleep(PSLP);
    }
    float o[4];
#pragma unroll
    for (int k = 0; k < 4; ++k) {
      float t[NL];
#pragma unroll
      for (int i = 0; i < NL; ++i) t[i] = __uint_as_float(yv[i][k] & ~YMASK);
#pragma unroll
      for (int st = NL / 2; st >= 1; st /= 2)
#pragma unroll
        for (int i = 0; i < st; ++i) t[i] += t[i + st];
      o[k] = t[0];
    }
    float e0 = swadd32(o[0], o[2]), e1 = swadd32(o[1], o[3]);
    e0 = swadd16(e0, e0); e1 = swadd16(e1, e1);
    e0 += dpp<ROR8>(e0); e1 += dpp<ROR8>(e1); e0 += dpp<ROR4>(e0); e1 += dpp<ROR4>(e1); e0 += dpp<ROR2>(e0); e1 += dpp<ROR2>(e1);
    if ((lane & 30) == 0) OUT_STORE(op, pack_bf16(e0, e1));
    if (pj == 0 && lane < 8) __builtin_amdgcn_raw_buffer_store_b128(u32x4{tlo, thi, tlo, thi}, wsr, WS_EPOCH + b * 256 + lane * 16, 0, 16);
    STAMP(7);
    VM_DRAIN();
    STAMP(8);
    STAMP_FLUSH();
    return;
  }

  const int cw = wave - 1, hw = wave - 1 - NCW;
  int landed = 0;
  auto wait_unit = [&](int u) __attribute__((always_inline)) {
#pragma unroll 1
    for (int spin = 0; landed <= u && spin < SPIN_CAP; ++spin) {
      landed = __builtin_amdgcn_readfirstlane(ctrl[0]);
      if (landed > u) break;
      if (USL) __builtin_amdgcn_s_sleep(USL);
    }
    CFENCE();
  };
  auto take = [&](int u, int next, u32x4& a0, u32x4& a1, u32x4& a2, u32x4& a3) __attribute__((always_inline)) {
    wait_unit(u);
    const LAS u32x4* sp = (const LAS u32x4*)(L + (u & (NSLOT - 1)) * UNIT + lane * 16);
    a0 = sp[0]; a1 = sp[64]; a2 = sp[128]; a3 = sp[192];
    LDS_WAIT();
    ctrl[16 + cw] = next;
    asm volatile("" : "+v"(a0), "+v"(a1), "+v"(a2), "+v"(a3) :: "memory");
  };
  u32x4 q;
  auto build_q = [&]() __attribute__((always_inline)) {
    lds_await(ctrl, C_Q, NHW);
    const LAS f32x4* qp = (const LAS f32x4*)(L + L_QP + (2 * r4 * DH + 8 * c16) * 4);
    const f32x4 lo = qp[0] + qp[DH / 4], hi = qp[1] + qp[DH / 4 + 1];
    q[0] = pack_bf16(xrow_sum(lo[0]), xrow_sum(lo[1])); q[1] = pack_bf16(xrow_sum(lo[2]), xrow_sum(lo[3]));
    q[2] = pack_bf16(xrow_sum(hi[0]), xrow_sum(hi[1])); q[3] = pack_bf16(xrow_sum(hi[2]), xrow_sum(hi[3]));
  };
  constexpr float SCALE = 0.088388347648318440f;
  float mx = -3.4028234e38f;
  auto score = [&](int u, const u32x4& k0, const u32x4& k1, const u32x4& k2, const u32x4& k3) __attribute__((always_inline)) {
    float d0 = 0.f, d1 = 0.f, d2 = 0.f, d3 = 0.f;
#pragma unroll
    for (int k = 0; k < 4; ++k) { d0 = dot2(k0[k], q[k], d0); d1 = dot2(k1[k], q[k], d1); d2 = dot2(k2[k], q[k], d2); d3 = dot2(k3[k], q[k], d3); }
    const unsigned s01 = pack_bf16(row16_sum(d0), row16_sum(d1)), s23 = pack_bf16(row16_sum(d2), row16_sum(d3));
    const unsigned t01 = pack_bf16(bflo(s01) * SCALE, bfhi(s01) * SCALE), t23 = pack_bf16(bflo(s23) * SCALE, bfhi(s23) * SCALE);
    const f32x4 sc = {bflo(t01), bfhi(t01), bflo(t23), bfhi(t23)};
    mx = fmaxf(fmaxf(mx, fmaxf(sc[0], sc[1])), fmaxf(sc[2], sc[3]));
    if (c16 == 0) *(LAS f32x4*)(L + L_SC + u * 64 + r4 * 16) = sc;
  };

  if (hw < 0) {
    {
      u32x4 hk[4 * NHC];
#pragma unroll
      for (int i = 0; i < NHC; ++i) take(cw + NCW * i, i + 1 < NHC ? cw + NCW * (i + 1) : NHELD + cw, hk[4 * i], hk[4 * i + 1], hk[4 * i + 2], hk[4 * i + 3]);
      STAMP(10);
      build_q();
      STAMP(3);
#pragma unroll
      for (int i = 0; i < NHC; ++i) { score(cw + NCW * i, hk[4 * i], hk[4 * i + 1], hk[4 * i + 2], hk[4 * i + 3]); __builtin_amdgcn_sched_barrier(0); }
    }
#pragma unroll 1
    for (int u = NHELD + cw; u < NKU; u += NCW) {
      u32x4 k0, k1, k2, k3;
      take(u, u + NCW, k0, k1, k2, k3);
      score(u, k0, k1, k2, k3);
    }
    ctrlf[32 + cw] = xrow_max(mx);
    lds_arrive(L, C_MAX, lane);
    STAMP(11);
    lds_await(ctrl, C_MAX, NCW + NHW);
    const float mg = wg_max();
    STAMP(12);
    float acc[8] = {0.f, 0.f, 0.f, 0.f, 0.f, 0.f, 0.f, 0.f};
    LAS unsigned* rp = (LAS unsigned*)(L + L_REC + cw * 272 + (8 * c16 + 2 * r4) * 2);
    asm volatile("" : "+v"(rp));
    float cf = 0.f;
#pragma unroll 1
    for (int u = NKU + cw; u < NUNIT; u += NCW) {
      const f32x4 sc = *(const LAS f32x4*)(L + L_SC + (u - NKU) * 64 + r4 * 16);
      float p[4];
#pragma unroll
      for (int t = 0; t < 4; ++t) { p[t] = expd(sc[t] - mg); asm volatile("" : "+v"(p[t])); }
      if ((u & ~(NCW - 1)) == NUNIT - 32) { lds_await(ctrl, C_CF, 1); cf = ctrlf[49]; }
      u32x4 v[4];
      take(u, u + NCW, v[0], v[1], v[2], v[3]);
#pragma unroll
      for (int t = 0; t < 4; ++t)
#pragma unroll
        for (int k = 0; k < 4; ++k) { acc[2 * k] = fmaf(p[t], bflo(v[t][k]), acc[2 * k]); acc[2 * k + 1] = fmaf(p[t], bfhi(v[t][k]), acc[2 * k + 1]); }
    }
    STAMP(5);
    const float e0 = swadd16(swadd32(acc[0], acc[4]), swadd32(acc[2], acc[6])), e1 = swadd16(swadd32(acc[1], acc[5]), swadd32(acc[3], acc[7]));
    *rp = pack_bf16(e0 * cf, e1 * cf);
    BARRIER();
    return;
  }

  u32x4 wb[16];
  const unsigned tstart = (unsigned)__builtin_amdgcn_readfirstlane(ctrl[8]);
  {
    const __amdgpu_buffer_rsrc_t rq = mkrsrc(wq, D * H * DH * 2);
    const int vo = ((64 * hw + 2 * r4) * (H * DH) + h * DH + 8 * c16) * 2;
#define QR(i) (8 * ((i) >> 1) + ((i) & 1))
#pragma unroll
    for (int i = 0; i < 16; ++i) { if (i == 0) wait_until(tstart + TQ + hw * QA); else PAUSE(QS); wb[i] = __builtin_amdgcn_raw_buffer_load_b128(rq, vo, QR(i) * (H * DH * 2), QAUX); }
    lds_await(ctrl, C_X, 1);
    const LAS unsigned* xl = (const LAS unsigned*)(L + L_X + (QROW == 1 ? 64 * hw + 2 * r4 : QROW == 2 ? 64 * hw + 4 * r4 : QROW == 3 ? 16 * hw + 128 * r4 : 64 * hw + 16 * r4) * 2);
    float a[8] = {0.f, 0.f, 0.f, 0.f, 0.f, 0.f, 0.f, 0.f};
#pragma unroll
    for (int i = 0; i < 8; ++i) {
      const unsigned xp = xl[QR(2 * i) / 2];
#pragma unroll
      for (int k = 0; k < 4; ++k) {
        a[2 * k] = dot2(perm_lo(wb[2 * i][k], wb[2 * i + 1][k]), xp, a[2 * k]);
        a[2 * k + 1] = dot2(perm_hi(wb[2 * i][k], wb[2 * i + 1][k]), xp, a[2 * k + 1]);
      }
    }
    const float e0 = swadd16(swadd32(a[0], a[4]), swadd32(a[2], a[6])), e1 = swadd16(swadd32(a[1], a[5]), swadd32(a[3], a[7]));
    *(LAS f32x2*)(L + L_QP + (hw * DH + 8 * c16 + 2 * r4) * 4) = f32x2{e0, e1};
    lds_arrive(L, C_Q, lane);
    STAMP(2);
    const int hb = NHC * NCW + HM * hw;
#pragma unroll
    for (int i = 0; i < NHH; ++i) take(hb + HS * i, i + 1 < NHH ? hb + HS * (i + 1) : NEVER, wb[4 * i], wb[4 * i + 1], wb[4 * i + 2], wb[4 * i + 3]);
    STAMP(10);
    build_q();
#pragma unroll
    for (int i = 0; i < NHH; ++i) { score(hb + HS * i, wb[4 * i], wb[4 * i + 1], wb[4 * i + 2], wb[4 * i + 3]); __builtin_amdgcn_sched_barrier(0); }
    ctrlf[32 + cw] = xrow_max(mx);
    lds_arrive(L, C_MAX, lane);
  }
  f32x4 y[4] = {{0.f, 0.f, 0.f, 0.f}, {0.f, 0.f, 0.f, 0.f}, {0.f, 0.f, 0.f, 0.f}, {0.f, 0.f, 0.f, 0.f}};
  if ((pj & 7) != 7) {
    const int sl = 7 * (pj >> 3) + (pj & 7);
    const __amdgpu_buffer_rsrc_t r1 = mkrsrc(w1, D * F * 2), r3 = mkrsrc(w3, D * F * 2), r2 = mkrsrc(w2, F * D * 2);
    const int vo = ((64 * hw + (lane & 60)) * F + FS * sl + 8 * (lane & 3)) * 2;
#pragma unroll
    for (int i = 0; i < 8; ++i) { if (i == 0) wait_until(tstart + T1 + hw * A1); else PAUSE(S1); wb[i] = ldw(i < 4 ? r1 : r3, vo, (i & 3) * (F * 2)); }
    const u32x2 xm = *(const LAS u32x2*)(L + L_X + (64 * hw + (lane & 60)) * 2);
    float a[16];
#pragma unroll
    for (int m = 0; m < 2; ++m)
#pragma unroll
      for (int k = 0; k < 4; ++k) {
        a[8 * m + 2 * k] = dot2(perm_lo(wb[4 * m + 2][k], wb[4 * m + 3][k]), xm[1], dot2(perm_lo(wb[4 * m][k], wb[4 * m + 1][k]), xm[0], 0.f));
        a[8 * m + 2 * k + 1] = dot2(perm_hi(wb[4 * m + 2][k], wb[4 * m + 3][k]), xm[1], dot2(perm_hi(wb[4 * m][k], wb[4 * m + 1][k]), xm[0], 0.f));
      }
    f32x4 e;
#pragma unroll
    for (int k = 0; k < 4; ++k) {
      float t = swadd16(swadd32(a[k], a[8 + k]), swadd32(a[4 + k], a[12 + k]));
      t += dpp<ROR8>(t); e[k] = t + dpp<ROR4>(t);
    }
    if ((lane & 12) == 0) *(LAS f32x4*)(L + L_MP + (hw * 64 + (lane & 32) + 8 * (lane & 3) + 4 * (r4 & 1)) * 4) = e;
    lds_arrive(L, C_MLP, lane);
    u32x2 wr[8];
#pragma unroll
    for (int i = 0; i < 8; ++i) { if (i == 0) wait_until(tstart + T2 + hw * A2); else PAUSE(S2); wr[i] = ldw2(r2, ((FS * sl + 8 * r4) * D + 64 * hw + 4 * c16) * 2, i * (D * 2)); }
    lds_await(ctrl, C_MLP, NHW);
    const LAS float* mp = (const LAS float*)(L + L_MP) + (lane & 31);
    float ga = 0.f, up = 0.f;
#pragma unroll
    for (int w = 0; w < NHW; ++w) { ga += mp[w * 64]; up += mp[w * 64 + 32]; }
    const float hv = ga / (1.f + __expf(-ga)) * up * (1.f - ctrlf[48]);
    const unsigned hh = pack_bf16(hv, 0.f), hl = pack_bf16(hv - bflo(hh), 0.f);
    *(LAS u16*)(L + L_H + hw * 128 + lane * 2) = (u16)(lane < 32 ? hh : hl);
    LDS_WAIT();
    u32x4 ha = *(const LAS u32x4*)(L + L_H + hw * 128 + (c16 & 1) * 64 + r4 * 16);
    if (c16 >= 2) ha = u32x4{0u, 0u, 0u, 0u};
#pragma unroll
    for (int e = 0; e < 4; ++e) {
      u32x4 f;
#pragma unroll
      for (int k = 0; k < 4; ++k) f[k] = (e & 1) ? perm_hi(wr[2 * k][e >> 1], wr[2 * k + 1][e >> 1]) : perm_lo(wr[2 * k][e >> 1], wr[2 * k + 1][e >> 1]);
      y[e] = mfma(ha, f, y[e]);
    }
  }
#pragma unroll
  for (int e = 0; e < 4; ++e) asm volatile("" : "+v"(y[e]));
  {
    const __amdgpu_buffer_rsrc_t ro = mkrsrc(wo, H * DH * D * 2);
#pragma unroll
    for (int t = 0; t < 4; ++t) {
      u32x2 wr[8];
#pragma unroll
      for (int i = 0; i < 8; ++i) { if (t + i == 0) wait_until(tstart + TO + hw * AO); else PAUSE(SO); wr[i] = ldw2(ro, ((h * DH + 32 * r4) * D + 64 * hw + 4 * c16) * 2, (8 * t + i) * (D * 2)); }
#pragma unroll
      for (int e = 0; e < 4; ++e) {
#pragma unroll
        for (int k = 0; k < 4; ++k) wb[4 * t + e][k] = (e & 1) ? perm_hi(wr[2 * k][e >> 1], wr[2 * k + 1][e >> 1]) : perm_lo(wr[2 * k][e >> 1], wr[2 * k + 1][e >> 1]);
        asm volatile("" : "+v"(wb[4 * t + e]));
      }
    }
  }
  lds_await(ctrl, C_CF, 1);
  u32x4 yt = *(const LAS u32x4*)(L + L_CTRL + 52 * 4);
  int yo = WS_Y + (b * 64 + pj) * 2048 + hw * 256 + c16 * 16;
  const LAS u32x4* rec = (const LAS u32x4*)(L + L_REC + (lane & 3) * 272 + r4 * 64);
  unsigned ym = ~YMASK;
  asm volatile("" : "+v"(yt), "+v"(yo), "+v"(rec), "+s"(ym));
  STAMP(4);
  BARRIER();
  STAMP(9);
  {
    const u32x4 a[4] = {rec[0], rec[1], rec[2], rec[3]};
#pragma unroll
    for (int t = 0; t < 4; ++t)
#pragma unroll
      for (int e = 0; e < 4; ++e) y[e] = mfma(a[t], wb[4 * t + e], y[e]);
    u32x4 v;
#pragma unroll
    for (int e = 0; e < 4; ++e) {
      const f32x2 t = y[e].xy + y[e].zw;
      unsigned r;
      asm("v_add_f32 %0, %1, %2\n\tv_add_u32 %0, %5, %0\n\tv_and_or_b32 %0, %0, %3, %4" : "=&v"(r) : "v"(t[0]), "v"(t[1]), "s"(ym), "v"(yt[e]), "i"((YMASK + 1u) / 2u));
      v[e] = r;
    }
    if (lane < 16) __builtin_amdgcn_raw_buffer_store_b128(v, wsr, yo, 0, 16);
  }
  STAMP(7);
}

extern "C" void kernel_launch(void* const* d_in, const int* in_sizes, int n_in, void* d_out, int out_size, void* d_ws, size_t ws_size,
                              hipStream_t stream) {
  gated_mix_kernel<<<256, NTHR, 0, stream>>>((const u16*)d_in[0], (const u16*)d_in[1], (const u16*)d_in[2], (const u16*)d_in[3], (const u16*)d_in[4],
                                             (const u16*)d_in[6], (const u16*)d_in[7], (const u16*)d_in[8], (const u16*)d_in[9], (u16*)d_out,
                                             (unsigned char*)d_ws);
}
```

```cpp
#include <hip/hip_runtime.h>
#include <hip/hip_bf16.h>

#define GAS __attribute__((address_space(1)))
#define LAS __attribute__((address_space(3)))
typedef unsigned short u16;
typedef unsigned long long u64;
using u32x4 = __attribute__((ext_vector_type(4))) unsigned;
using u32x2 = __attribute__((ext_vector_type(2))) unsigned;
using f32x4 = __attribute__((ext_vector_type(4))) float;
using f32x2 = __attribute__((ext_vector_type(2))) float;
using bf16x2 = __attribute__((ext_vector_type(2))) __bf16;
using bf16x8 = __attribute__((ext_vector_type(8))) __bf16;

constexpr int B = 4, D = 512, H = 32, SK = 8192, DH = 128, F = 1792;
constexpr int NCW = 4, NHW = 8, XWAVE = 1 + NCW + NHW, NWAVE = XWAVE + 2, NTHR = NWAVE * 64;
constexpr int TQ = 100, QA = 112, QS = 2;
constexpr int T1 = 1400, A1 = 150, S1 = 6;
constexpr int T2 = 2500, A2 = 40, S2 = 1;
constexpr int TO = 2900, AO = 350, SO = 2;
constexpr int TSTREAM = 6860;
constexpr int WMID = 5472, WMIN = 16, WMAX = 28;
constexpr int WGAIN = 10, WGAIN2 = 18, UEND = 420;
constexpr int JUMP = 100, DECAY = 6;
constexpr int REF0 = 180;
constexpr int FOFF = 156, FMIN = 45, FMAX = 150;
constexpr int CODE_KIB = 21;

constexpr int UNIT = 4096;
constexpr int NSLOT = 32, RING = NSLOT * UNIT;
constexpr int NKU = (SK / 2) / 16, NUNIT = 2 * NKU;
constexpr int NHC = 6, NHH = 4, NHELD = NHC * NCW + NHH * NHW;
constexpr int FS = 32;
constexpr int L_CTRL = RING;
constexpr int L_X = L_CTRL + 256;
constexpr int L_QP = L_X + D * 2;
constexpr int L_MP = L_QP + NHW * DH * 4;
constexpr int L_REC = L_MP + NHW * 64 * 4;
constexpr int L_H = L_REC + NCW * 272;
constexpr int L_SC = L_H + NHW * 128;
constexpr int LDS_BYTES = L_SC + NKU * 64;
static_assert(LDS_BYTES <= 159 * 1024, "lds");
static_assert(WMIN <= WMID / 256 && WMID / 256 <= WMAX && WMAX <= 28, "window");
constexpr int WS_EPOCH = 0;
constexpr int WS_REC = 4096;
constexpr int WS_PACE = WS_REC + 256 * 128;
constexpr int WS_Y = WS_PACE + 32 * 2048;
constexpr int WS_BYTES = WS_Y + B * 64 * 2048;
constexpr u64 TAG_STEP = 0x9E3779B97F4A7C15ull;
constexpr unsigned YMASK = 0xFFFu;
constexpr int NT = 2, SC1 = 16;
constexpr int SPIN_CAP = 1 << 22;
constexpr int NEVER = 1 << 20;
constexpr int C_Q = 4, C_MAX = 5, C_MLP = 6, C_X = 7, C_CF = 9;

__device__ __forceinline__ float expd(float d) { return __builtin_amdgcn_exp2f(d * 1.4426950408889634f); }
__device__ __forceinline__ float bflo(unsigned u) { return __builtin_bit_cast(float, u << 16); }
__device__ __forceinline__ float bfhi(unsigned u) { return __builtin_bit_cast(float, u & 0xffff0000u); }
__device__ __forceinline__ float dot2(unsigned a, unsigned b, float acc) {
  return __builtin_amdgcn_fdot2_f32_bf16(__builtin_bit_cast(bf16x2, a), __builtin_bit_cast(bf16x2, b), acc, false);
}
__device__ __forceinline__ unsigned pack_bf16(float lo, float hi) {
  const bf16x2 v = {(__bf16)lo, (__bf16)hi};
  return __builtin_bit_cast(unsigned, v);
}
__device__ __forceinline__ unsigned perm_lo(unsigned a, unsigned b) { return __builtin_amdgcn_perm(b, a, 0x05040100u); }
__device__ __forceinline__ unsigned perm_hi(unsigned a, unsigned b) { return __builtin_amdgcn_perm(b, a, 0x07060302u); }
template <int CTRL> __device__ __forceinline__ float dpp(float x) {
  return __builtin_bit_cast(float, __builtin_amdgcn_mov_dpp(__builtin_bit_cast(int, x), CTRL, 0xf, 0xf, true));
}
constexpr int ROR1 = 0x121, ROR2 = 0x122, ROR4 = 0x124, ROR8 = 0x128;
__device__ __forceinline__ float row16_sum(float x) { x += dpp<ROR8>(x); x += dpp<ROR4>(x); x += dpp<ROR2>(x); x += dpp<ROR1>(x); return x; }
__device__ __forceinline__ float swadd32(float a, float b) {
  auto t = __builtin_amdgcn_permlane32_swap(__float_as_uint(a), __float_as_uint(b), false, false);
  return __uint_as_float(t[0]) + __uint_as_float(t[1]);
}
__device__ __forceinline__ float swadd16(float a, float b) {
  auto t = __builtin_amdgcn_permlane16_swap(__float_as_uint(a), __float_as_uint(b), false, false);
  return __uint_as_float(t[0]) + __uint_as_float(t[1]);
}
__device__ __forceinline__ float xrow_sum(float x) { x = swadd16(x, x); return swadd32(x, x); }
__device__ __forceinline__ float xrow_max(float x) {
  auto s = __builtin_amdgcn_permlane16_swap(__float_as_uint(x), __float_as_uint(x), false, false);
  x = fmaxf(__uint_as_float(s[0]), __uint_as_float(s[1]));
  auto t = __builtin_amdgcn_permlane32_swap(__float_as_uint(x), __float_as_uint(x), false, false);
  return fmaxf(__uint_as_float(t[0]), __uint_as_float(t[1]));
}
__device__ __forceinline__ __amdgpu_buffer_rsrc_t mkrsrc(const void* p, int bytes) { return __builtin_amdgcn_make_buffer_rsrc((void*)p, 0, bytes, 0x00020000); }
__device__ __forceinline__ u32x4 ldw(__amdgpu_buffer_rsrc_t r, int voff, int soff) { return __builtin_amdgcn_raw_buffer_load_b128(r, voff, soff, NT); }
#define LDS_WAIT() asm volatile("s_waitcnt lgkmcnt(0)" ::: "memory")
#define CFENCE() asm volatile("" ::: "memory")
#define BARRIER() asm volatile("s_waitcnt lgkmcnt(0)\n\ts_barrier" ::: "memory")
#define PAUSE(n) do { __builtin_amdgcn_sched_barrier(0); asm volatile("s_sleep %0" :: "i"(n) : "memory"); __builtin_amdgcn_sched_barrier(0); } while (0)

__device__ __forceinline__ void lds_arrive(LAS unsigned char* L, int idx, int lane) {
  LDS_WAIT();
  if (lane == 0) __hip_atomic_fetch_add((LAS int*)(L + L_CTRL) + idx, 1, __ATOMIC_RELAXED, __HIP_MEMORY_SCOPE_WORKGROUP);
}
__device__ __forceinline__ void lds_await(volatile LAS int* ctrl, int idx, int n) {
#pragma unroll 1
  for (int spin = 0; spin < SPIN_CAP; ++spin) { if (__builtin_amdgcn_readfirstlane(ctrl[idx]) >= n) break; __builtin_amdgcn_s_sleep(1); }
  CFENCE();
}

__device__ __forceinline__ void wait_until(unsigned t) {
  __builtin_amdgcn_sched_barrier(0);
#pragma unroll 1
  for (int spin = 0; spin < 4096; ++spin) {
    const int d = (int)(t - (unsigned)__builtin_amdgcn_s_memrealtime());
    if (d <= 0) break;
    if (d > 24) __builtin_amdgcn_s_sleep(6);
  }
  CFENCE();
  __builtin_amdgcn_sched_barrier(0);
}
constexpr int TAU2 = 2 * (TSTREAM * 256 / NUNIT);
__device__ __forceinline__ f32x4 mfma(const u32x4& a, const u32x4& b, const f32x4& c) {
  return __builtin_amdgcn_mfma_f32_16x16x32_bf16(__builtin_bit_cast(bf16x8, a), __builtin_bit_cast(bf16x8, b), c, 0, 0, 0);
}
__device__ __forceinline__ u32x2 ldw2(__amdgpu_buffer_rsrc_t r, int voff, int soff) { return __builtin_amdgcn_raw_buffer_load_b64(r, voff, soff, NT); }

__global__ void __launch_bounds__(NTHR, 4) gated_mix__mha_block_cached__glu_gated_swiglu__decode_kernel(const u16* x, const u16* kc, const u16* vc, const u16* wq, const u16* wo, const u16* w1,
                                                         const u16* w3, const u16* w2, const u16* gw, u16* out, unsigned char* ws) {
  __shared__ __attribute__((aligned(16))) unsigned char lds_raw[LDS_BYTES];
  LAS unsigned char* L = (LAS unsigned char*)lds_raw;
  volatile LAS int* ctrl = (volatile LAS int*)(L + L_CTRL);
  volatile LAS float* ctrlf = (volatile LAS float*)(L + L_CTRL);
  {
    unsigned va, vb, vo, pa, nxa, t0, t1, t2, t3;
#define WAITW "s_waitcnt (wcnt & 15) | ((wcnt >> 4) << 14) | 0x0F70\n\t"
#define SP(v, sb, wait) wait "global_load_lds_dword " v ", " sb " nt\n\tglobal_load_lds_dword " v ", " sb " sc1\n\t"
#define SW "s_waitcnt vmcnt(4)\n\t"
#define UNIT4(v) \
        WAITW "global_load_lds_dwordx4 " v ", s[40:41] nt\n\t" \
        WAITW "global_load_lds_dwordx4 " v ", s[40:41] offset:1024 nt\n\t" \
        WAITW "global_load_lds_dwordx4 " v ", s[40:41] offset:2048 nt\n\t" \
        WAITW "global_load_lds_dwordx4 " v ", s[40:41] offset:3072 nt\n\t"
    asm volatile(
        "s_load_dwordx8 s[44:51], %[ka], 0x18\n\t"
        "s_load_dwordx2 s[52:53], %[ka], 0x38\n\t"
        "s_load_dwordx4 s[40:43], %[ka], 0x8\n\t"
        "v_readfirstlane_b32 s54, %[tid]\n\t"
        "s_getpc_b64 s[56:57]\n\t"
        "s_lshr_b32 s59, %[bid], 3\n\t"
        "s_cmp_ge_u32 s54, 64\n\t"
        "s_cbranch_scc1 90f\n\t"
        "s_min_u32 s54, s59, %c[nch]\n\t"
        "v_lshlrev_b32 %[vo], 4, %[tid]\n\t"
        "s_mov_b32 m0, %c[jk]\n\t"
        "v_lshl_add_u32 %[t0], s54, 10, %[vo]\n\t"
        "s_andn2_b32 s56, s56, 63\n\t"
        "s_and_b32 s58, %[bid], 7\n\t"
        "global_load_lds_dwordx4 %[t0], s[56:57]\n\t"
        "v_lshrrev_b32 %[va], 3, %[tid]\n\t"
        "v_mul_u32_u24 %[va], 56, %[va]\n\t"
        "s_lshl_b32 s54, s59, 7\n\t"
        "s_add_u32 s54, s54, s58\n\t"
        "s_lshl_b32 s54, s54, 3\n\t"
        "v_add3_u32 %[va], %[va], %[tid], s54\n\t"
        "v_lshlrev_b32 %[va], 7, %[va]\n\t"
        "s_waitcnt lgkmcnt(0)\n\t"
        SP("%[va]", "s[44:45]", "")
        "v_add_u32 %[t2], 0x10000, %[va]\n\t"
        "s_setprio 3\n\t"
        SP("%[t2]", "s[44:45]", "")
        "v_lshlrev_b32 %[t1], 7, %[tid]\n\t"
        "s_lshl_b32 s55, s58, 5\n\t"
        "s_add_u32 s55, s55, s59\n\t"
        "s_mul_i32 s54, s55, 7 * 1024\n\t"
        "v_min_u32 %[t3], 55 * 128, %[t1]\n\t"
        "v_add_u32 %[t3], s54, %[t3]\n\t"
        SP("%[t3]", "s[52:53]", "")
        "s_mul_i32 s54, s58, 448\n\t"
        "s_and_b32 s54, s54, -128\n\t"
        "s_mul_i32 s57, s59, 16 * 3584\n\t"
        "s_add_u32 s54, s54, s57\n\t"
        "v_lshrrev_b32 %[t0], 2, %[tid]\n\t"
        "v_mul_u32_u24 %[t0], 3584 - 512, %[t0]\n\t"
        "v_add3_u32 %[t0], %[t0], %[t1], s54\n\t"
        "v_lshl_add_u32 %[vb], s55, 14, %[t1]\n\t"
        "v_add_u32 %[t1], 0x2000, %[vb]\n\t"
        "s_and_b32 s54, %[bid], 0xc0\n\t"
        "s_lshl3_add_u32 s54, s58, s54\n\t"
        "s_and_b32 s56, s59, 7\n\t"
        "s_add_u32 s54, s54, s56\n\t"
        "s_lshr_b32 s56, s54, 1\n\t"
        "s_lshl_b32 s56, s56, 21\n\t"
        "s_and_b32 s64, s54, 1\n\t"
        "s_bfe_u32 s57, s54, 0x10007\n\t"
        "s_lshl2_add_u32 s64, s57, s64\n\t"
        "s_bfe_u32 s57, s54, 0x10002\n\t"
        "s_lshl4_add_u32 s64, s57, s64\n\t"
        "s_lshl_b32 s64, s64, 16\n\t"
        SP("%[t0]", "s[48:49]", SW) SP("%[t0]", "s[50:51]", SW)
        "s_barrier\n\t"
        SP("%[vb]", "s[46:47]", SW) SP("%[t1]", "s[46:47]", SW)
        "s_add_u32 s40, s40, s56\n\t"
        "s_addc_u32 s41, s41, 0\n\t"
        "s_add_u32 s42, s42, s56\n\t"
        "s_addc_u32 s43, s43, 0\n\t"
        "v_mov_b32 %[va], %[vo]\n\t"
        "v_or_b32 %[vo], s64, %[vo]\n\t"
        ".set so, 0\n\t"
        ".set wcnt, 4\n\t"
        ".rept 4\n\t"
        "s_movk_i32 m0, so\n\t"
        ".if (so & 4096) == 0\n\t"
        WAITW
        ".set wcnt, wcnt + 6\n\t"
        ".else\n\t"
        "s_nop 0\n\t"
        ".endif\n\t"
        "global_load_lds_dwordx4 %[vo], s[40:41] nt\n\t"
        "global_load_lds_dwordx4 %[vo], s[40:41] offset:1024 nt\n\t"
        "global_load_lds_dwordx4 %[vo], s[40:41] offset:2048 nt\n\t"
        "global_load_lds_dwordx4 %[vo], s[40:41] offset:3072 nt\n\t"
        "v_add_u32 %[vo], 0x1000, %[vo]\n\t"
        ".set so, so + 4096\n\t"
        ".endr\n\t"
        "s_waitcnt vmcnt(16)\n\t"
        "s_getpc_b64 s[50:51]\n"
        "20:\n\t"
        "s_add_u32 s50, s50, 23f-20b-%c[wmin]*128\n\t"
        "s_addc_u32 s51, s51, -1\n\t"
        "v_mov_b32 %[pa], %c[ctl]\n\t"
        "v_and_b32 %[nxa], 0x3c0 >> 2, %[vo]\n\t"
        "v_lshrrev_b32 %[nxa], 2, %[nxa]\n\t"
        "v_add_u32 %[nxa], %c[ctl] + 64, %[nxa]\n\t"
        "s_memrealtime s[58:59]\n\t"
        "s_mov_b32 s62, 0\n\t"
        "s_mov_b32 s63, %c[gain]\n\t"
        "s_mov_b32 s44, 4\n\t"
        "s_mov_b32 s45, %c[wmid] >> 8\n\t"
        "s_mov_b32 s46, 0x4000\n\t"
        "s_mov_b32 s47, 2 * %c[tau2]\n\t"
        "s_mov_b32 s48, %c[nslot]\n\t"
        "s_mov_b32 s49, %c[nku]\n\t"
        "s_waitcnt lgkmcnt(0)\n\t"
        "s_mov_b32 s59, 10000\n"
        "22:\n\t"
        "s_memrealtime s[56:57]\n\t"
        "ds_read_b32 %[t3], %[pa] offset:8\n\t"
        "s_lshl_b32 s52, s45, 7\n\t"
        "s_add_u32 s52, s50, s52\n\t"
        "s_addc_u32 s53, s51, 0\n\t"
        "s_add_i32 s54, s45, -1\n\t"
        "s_lshr_b32 s54, s54, 2\n\t"
        "s_sub_i32 s54, s44, s54\n\t"
        "v_mov_b32 %[t1], s54\n\t"
        "v_add_u32 %[t0], 0x1000, %[vo]\n\t"
        "s_add_u32 s55, s46, 0x1000\n\t"
        "s_mov_b32 m0, s46\n\t"
        "s_setpc_b64 s[52:53]\n"
        "24:\n\t"
        "s_waitcnt lgkmcnt(0)\n\t"
        "v_readfirstlane_b32 s57, %[t3]\n\t"
        "v_add_u32 %[t1], 1, %[t1]\n\t"
        "s_lshr_b32 s54, s47, 8\n\t"
        "s_sub_u32 s56, s56, s54\n\t"
        "v_mov_b32 %[t2], s56\n\t"
        "s_sub_i32 s54, s56, s58\n\t"
        "s_mov_b32 s58, s56\n\t"
        "s_sub_i32 s56, s56, s57\n\t"
        "ds_write2_b32 %[pa], %[t1], %[t2] offset1:1\n\t"
        "s_add_i32 s59, s59, s54\n\t"
        "s_add_i32 s59, s59, %c[decay]\n\t"
        "s_cmp_gt_i32 s54, %c[jump]\n\t"
        "s_cselect_b32 s59, s62, s59\n\t"
        "s_min_i32 s62, s56, s59\n\t"
        "s_mul_i32 s56, s62, s63\n\t"
        "s_add_i32 s56, s56, %c[wmid]\n\t"
        "s_ashr_i32 s56, s56, 8\n\t"
        "s_max_i32 s56, s56, %c[wmin]\n\t"
        "s_min_i32 s45, s56, %c[wmax]\n\t"
        "s_add_u32 s47, s47, %c[tau2]\n\t"
        "s_add_u32 s46, s46, 0x2000\n\t"
        "s_and_b32 s46, s46, %c[nslot] * 4096 - 1\n\t"
        "s_add_i32 s44, s44, 2\n\t"
        "s_and_b32 s54, s44, 0xff\n\t"
        "s_and_b32 s55, s44, 0xf0\n\t"
        "s_add_u32 s54, s54, s55\n\t"
        "s_lshl_b32 s54, s54, 12\n\t"
        "s_xor_b32 s54, s54, s64\n\t"
        "v_or_b32 %[vo], s54, %[va]\n\t"
        "s_cmp_lt_i32 s44, s48\n\t"
        "s_cbranch_scc1 22b\n\t"
        "s_cmp_lt_i32 s44, s49\n\t"
        "s_cbranch_scc1 25f\n\t"
        "s_cmp_ge_i32 s44, 2 * %c[nku]\n\t"
        "s_cbranch_scc1 30f\n\t"
        "s_mov_b32 s49, 2 * %c[nku]\n\t"
        "s_mov_b64 s[40:41], s[42:43]\n"
        "25:\n\t"
        "s_cmp_ge_i32 s44, %c[uend]\n\t"
        "s_cselect_b32 s63, %c[gain2], s63\n\t"
        "s_mov_b32 s55, %c[cap]\n"
        "26:\n\t"
        "ds_read_b32 %[t0], %[nxa]\n\t"
        "s_waitcnt lgkmcnt(0)\n\t"
        "s_nop 1\n\t"
        "v_min_i32_dpp %[t0], %[t0], %[t0] row_ror:8 row_mask:0xf bank_mask:0xf\n\t"
        "s_nop 1\n\t"
        "v_min_i32_dpp %[t0], %[t0], %[t0] row_ror:4 row_mask:0xf bank_mask:0xf\n\t"
        "s_nop 1\n\t"
        "v_min_i32_dpp %[t0], %[t0], %[t0] row_ror:2 row_mask:0xf bank_mask:0xf\n\t"
        "s_nop 1\n\t"
        "v_min_i32_dpp %[t0], %[t0], %[t0] row_ror:1 row_mask:0xf bank_mask:0xf\n\t"
        "s_nop 1\n\t"
        "v_readfirstlane_b32 s48, %[t0]\n\t"
        "s_nop 1\n\t"
        "s_add_i32 s48, s48, %c[nslot]\n\t"
        "s_and_b32 s48, s48, -2\n\t"
        "s_min_i32 s48, s48, s49\n\t"
        "s_cmp_lt_i32 s44, s48\n\t"
        "s_cbranch_scc1 22b\n\t"
        "v_mov_b32 %[t1], s44\n\t"
        "s_waitcnt vmcnt(0)\n\t"
        "ds_write_b32 %[pa], %[t1]\n\t"
        "s_sleep 1\n\t"
        "s_sub_u32 s55, s55, 1\n\t"
        "s_cmp_lg_u32 s55, 0\n\t"
        "s_cbranch_scc1 26b\n\t"
        "s_branch 22b\n"
        "30:\n\t"
        ".set k, 6\n\t"
        ".rept 7\n\t"
        ".set wcnt, 4 * k\n\t"
        "v_mov_b32 %[t1], 2 * %c[nku] - k\n\t"
        WAITW
        "ds_write_b32 %[pa], %[t1]\n\t"
        ".set k, k - 1\n\t"
        ".endr\n\t"
        "s_waitcnt lgkmcnt(0)\n\t"
        "s_endpgm\n\t"
        ".p2align 7\n"
        "23:\n\t"
        ".set wcnt, %c[wmin]-1\n\t"
        ".rept %c[wmax] - %c[wmin] + 1\n\t"
        UNIT4("%[vo]")
        "ds_write_b32 %[pa], %[t1]\n\t"
        "s_mov_b32 m0, s55\n\t"
        UNIT4("%[t0]")
        "s_branch 24b\n\t"
        ".p2align 7\n\t"
        ".set wcnt, wcnt+1\n\t"
        ".endr\n"
        "90:\n\t"
        "s_waitcnt lgkmcnt(0)"
        : [va] "=&v"(va), [vb] "=&v"(vb), [vo] "=&v"(vo), [pa] "=&v"(pa), [nxa] "=&v"(nxa), [t0] "=&v"(t0), [t1] "=&v"(t1), [t2] "=&v"(t2), [t3] "=&v"(t3)
        : [tid] "v"(threadIdx.x), [bid] "s"(blockIdx.x), [ka] "s"(__builtin_amdgcn_kernarg_segment_ptr()), [nch] "i"(CODE_KIB - 1), [jk] "i"(RING - UNIT),
          [wmin] "i"(WMIN), [wmax] "i"(WMAX), [gain] "i"(WGAIN), [wmid] "i"(WMID), [tau2] "i"(TAU2),
          [ctl] "i"(L_CTRL), [nslot] "i"(NSLOT), [nku] "i"(NKU), [cap] "i"(SPIN_CAP), [jump] "i"(JUMP), [decay] "i"(DECAY), [uend] "i"(UEND), [gain2] "i"(WGAIN2)
        : "memory", "scc", "s40", "s41", "s42", "s43", "s44", "s45", "s46", "s47", "s48", "s49", "s50", "s51", "s52", "s53", "s54", "s55", "s56", "s57", "s58", "s59", "s62", "s63", "s64");
  }
  __builtin_amdgcn_sched_barrier(0);
  const int tid = threadIdx.x, lane = tid & 63, wave = __builtin_amdgcn_readfirstlane(tid >> 6);
  const int xg = blockIdx.x & 7, kk = blockIdx.x >> 3;
  const int b = kk >> 3, pj = 8 * xg + (kk & 7), h = pj >> 1;
  const int r4 = lane >> 4, c16 = lane & 15;
  asm volatile("" : "+s"(x), "+s"(wq), "+s"(wo), "+s"(w1), "+s"(w3), "+s"(w2), "+s"(gw), "+s"(out), "+s"(ws));
  if (wave == 1) {
    const int wi = lane - 16;
    const int now = (int)(unsigned)__builtin_amdgcn_s_memrealtime() + REF0;
    ctrl[lane] = (lane == 1 || lane == 2) ? now : lane == 8 ? now - REF0 : (lane < 16 || lane >= 32) ? 0 : wi < NCW ? wi : wi < NCW + NHW ? NHC * NCW + NHH * (wi - NCW) : NEVER;
  }
  BARRIER();

  const __amdgpu_buffer_rsrc_t wsr = mkrsrc(ws, WS_BYTES);
  auto wg_max = [&]() __attribute__((always_inline)) {
    float mg = ctrlf[32 + (c16 < NCW + NHW ? c16 : 0)];
    mg = fmaxf(mg, dpp<ROR8>(mg)); mg = fmaxf(mg, dpp<ROR4>(mg)); mg = fmaxf(mg, dpp<ROR2>(mg)); mg = fmaxf(mg, dpp<ROR1>(mg));
    return mg;
  };

  if (wave == XWAVE + 1) {
    unsigned k = (((unsigned)__builtin_amdgcn_s_memrealtime() + 250u) >> 9) + 1;
#pragma unroll 1
    for (int it = 0; it < 32; ++it, ++k) {
      if (__builtin_amdgcn_readfirstlane(ctrl[0]) + (int)((k << 9) - (unsigned)__builtin_amdgcn_s_memrealtime()) / 13 >= NUNIT - 32) break;
      wait_until(k << 9);
      const int own = __builtin_amdgcn_readfirstlane(ctrl[1]);
      if (blockIdx.x < 8 && lane < 8) __builtin_amdgcn_raw_buffer_store_b128(u32x4{(unsigned)own, (unsigned)own, (unsigned)own, (unsigned)own}, wsr, WS_PACE + (k & 31u) * 2048 + blockIdx.x * 128 + lane * 16, 0, SC1);
      wait_until((k << 9) + 200 + 2 * kk);
      const int d = (int)__builtin_amdgcn_raw_buffer_load_b32(wsr, WS_PACE + (k & 31u) * 2048 + (lane & 7) * 128, 0, SC1) - own;
      const bool ok = (unsigned)(d + 600) < 1200u;
      const float sum = row16_sum(ok ? (float)d : 0.f), cnt = row16_sum(ok ? 1.f : 0.f);
      if (cnt >= 8.f) ctrl[2] = own + (int)(sum / cnt);
    }
    return;
  }

  if (wave == XWAVE) {
#pragma unroll 1
    for (int i = 0; i < 20; ++i) PAUSE(4);
    const u32x4 xv = *(const GAS u32x4*)(x + b * D + 8 * lane), gv = *(const GAS u32x4*)(gw + 8 * lane);
    *(LAS u32x4*)(L + L_X + lane * 16) = xv;
    float gd = 0.f;
#pragma unroll
    for (int k = 0; k < 4; ++k) gd = dot2(xv[k], gv[k], gd);
    gd = xrow_sum(row16_sum(gd));
    const float g = 1.f / (1.f + __expf(-gd));
    ctrlf[48] = g;
    lds_arrive(L, C_X, lane);
#pragma unroll 1
    for (int i = 0; i < 16 + kk; ++i) PAUSE(20);
    const u32x2 ep = __builtin_amdgcn_raw_buffer_load_b64(wsr, WS_EPOCH + b * 256, 0, SC1);
    const u64 tag = (((u64)(unsigned)__builtin_amdgcn_readfirstlane(ep[1]) << 32) | (unsigned)__builtin_amdgcn_readfirstlane(ep[0])) + TAG_STEP;
#pragma unroll 1
    for (int spin = 0; spin < SPIN_CAP; ++spin) { if (__builtin_amdgcn_readfirstlane(ctrl[C_MAX]) >= NCW + NHW) break; __builtin_amdgcn_s_sleep(16); }
    CFENCE();
    const float mg = wg_max();
    float l = 0.f;
#pragma unroll 4
    for (int i = 0; i < NKU * 64 / 1024; ++i) {
      const f32x4 sc = *(const LAS f32x4*)(L + L_SC + i * 1024 + lane * 16);
      l += (expd(sc[0] - mg) + expd(sc[1] - mg)) + (expd(sc[2] - mg) + expd(sc[3] - mg));
    }
    l = xrow_sum(row16_sum(l));
    const unsigned tlo = (unsigned)tag, thi = (unsigned)(tag >> 32);
    if (lane < 8) __builtin_amdgcn_raw_buffer_store_b128(u32x4{__float_as_uint(mg), tlo, __float_as_uint(l), thi}, wsr, WS_REC + (b * 64 + pj) * 128 + lane * 16, 0, SC1);
#pragma unroll 1
    for (int i = 0; i < 12 + (kk & 31); ++i) PAUSE(12);
    u32x4 rec;
#pragma unroll 1
    for (int spin = 0; spin < SPIN_CAP; ++spin) {
      CFENCE();
      rec = __builtin_amdgcn_raw_buffer_load_b128(wsr, WS_REC + (b * 64 + (pj ^ 1)) * 128, 0, SC1);
      if (__builtin_amdgcn_ballot_w64(rec[1] != tlo || rec[3] != thi) == 0ull) break;
      __builtin_amdgcn_s_sleep(16);
    }
    const float mo = __uint_as_float(rec[0]), lo = __uint_as_float(rec[2]);
    const float mh = fmaxf(mg, mo), wt = expd(mg - mh);
    ctrlf[49] = g * wt / (wt * l + expd(mo - mh) * lo);
    const u32x4 yt = {tlo & YMASK, (tlo >> 12) & YMASK, (unsigned)(tag >> 24) & YMASK, (thi >> 4) & YMASK};
    *(LAS u32x4*)(L + L_CTRL + 52 * 4) = yt;
    lds_arrive(L, C_CF, lane);
    GAS unsigned* op = (GAS unsigned*)(out + b * D + 8 * pj + 4 * (lane & 1) + 2 * (lane >> 5));
    int off = WS_Y + (b * 64 + (lane >> 1)) * 2048 + pj * 32 + (lane & 1) * 16;
    asm volatile("" : "+v"(op), "+v"(off));
    BARRIER();
    {
      const unsigned now = (unsigned)__builtin_amdgcn_s_memrealtime();
      const int d = __builtin_amdgcn_readfirstlane(ctrl[2]) + TSTREAM + FOFF - (int)now;
      const unsigned t = now + (unsigned)min(max(d, FMIN), FMAX);
#pragma unroll 1
      for (int spin = 0; spin < 4096 && (int)(t - (unsigned)__builtin_amdgcn_s_memrealtime()) > 0; ++spin) {}
    }
    u32x4 ya, yb;
#pragma unroll 1
    for (int spin = 0; spin < SPIN_CAP; ++spin) {
      CFENCE();
      ya = __builtin_amdgcn_raw_buffer_load_b128(wsr, off, 0, SC1);
      yb = __builtin_amdgcn_raw_buffer_load_b128(wsr, off, 32 * 2048, SC1);
      const unsigned bad = (((ya[0] ^ yt[0]) | (ya[1] ^ yt[1])) | ((ya[2] ^ yt[2]) | (ya[3] ^ yt[3])) | ((yb[0] ^ yt[0]) | (yb[1] ^ yt[1])) | ((yb[2] ^ yt[2]) | (yb[3] ^ yt[3]))) & YMASK;
      if (__builtin_amdgcn_ballot_w64(bad != 0u) == 0ull) break;
    }
    float o[4];
#pragma unroll
    for (int k = 0; k < 4; ++k) o[k] = __uint_as_float(ya[k] & ~YMASK) + __uint_as_float(yb[k] & ~YMASK);
    float e0 = swadd32(o[0], o[2]), e1 = swadd32(o[1], o[3]);
    e0 = swadd16(e0, e0); e1 = swadd16(e1, e1);
    e0 += dpp<ROR8>(e0); e1 += dpp<ROR8>(e1); e0 += dpp<ROR4>(e0); e1 += dpp<ROR4>(e1); e0 += dpp<ROR2>(e0); e1 += dpp<ROR2>(e1);
    if ((lane & 30) == 0) *op = pack_bf16(e0, e1);
    if (pj == 0 && lane < 8) __builtin_amdgcn_raw_buffer_store_b128(u32x4{tlo, thi, tlo, thi}, wsr, WS_EPOCH + b * 256 + lane * 16, 0, SC1);
    return;
  }

  const int cw = wave - 1, hw = wave - 1 - NCW;
  int landed = 0;
  auto wait_unit = [&](int u) __attribute__((always_inline)) {
#pragma unroll 1
    for (int spin = 0; landed <= u && spin < SPIN_CAP; ++spin) {
      landed = __builtin_amdgcn_readfirstlane(ctrl[0]);
    }
    CFENCE();
  };
  auto take = [&](int u, int next, u32x4& a0, u32x4& a1, u32x4& a2, u32x4& a3) __attribute__((always_inline)) {
    wait_unit(u);
    const LAS u32x4* sp = (const LAS u32x4*)(L + (u & (NSLOT - 1)) * UNIT + lane * 16);
    a0 = sp[0]; a1 = sp[64]; a2 = sp[128]; a3 = sp[192];
    LDS_WAIT();
    ctrl[16 + cw] = next;
    asm volatile("" : "+v"(a0), "+v"(a1), "+v"(a2), "+v"(a3) :: "memory");
  };
  u32x4 q;
  auto build_q = [&]() __attribute__((always_inline)) {
    lds_await(ctrl, C_Q, NHW);
    const LAS f32x4* qp = (const LAS f32x4*)(L + L_QP + (2 * r4 * DH + 8 * c16) * 4);
    const f32x4 lo = qp[0] + qp[DH / 4], hi = qp[1] + qp[DH / 4 + 1];
    q[0] = pack_bf16(xrow_sum(lo[0]), xrow_sum(lo[1])); q[1] = pack_bf16(xrow_sum(lo[2]), xrow_sum(lo[3]));
    q[2] = pack_bf16(xrow_sum(hi[0]), xrow_sum(hi[1])); q[3] = pack_bf16(xrow_sum(hi[2]), xrow_sum(hi[3]));
  };
  constexpr float SCALE = 0.088388347648318440f;
  float mx = -3.4028234e38f;
  auto score = [&](int u, const u32x4& k0, const u32x4& k1, const u32x4& k2, const u32x4& k3) __attribute__((always_inline)) {
    float d0 = 0.f, d1 = 0.f, d2 = 0.f, d3 = 0.f;
#pragma unroll
    for (int k = 0; k < 4; ++k) { d0 = dot2(k0[k], q[k], d0); d1 = dot2(k1[k], q[k], d1); d2 = dot2(k2[k], q[k], d2); d3 = dot2(k3[k], q[k], d3); }
    const unsigned s01 = pack_bf16(row16_sum(d0), row16_sum(d1)), s23 = pack_bf16(row16_sum(d2), row16_sum(d3));
    const unsigned t01 = pack_bf16(bflo(s01) * SCALE, bfhi(s01) * SCALE), t23 = pack_bf16(bflo(s23) * SCALE, bfhi(s23) * SCALE);
    const f32x4 sc = {bflo(t01), bfhi(t01), bflo(t23), bfhi(t23)};
    mx = fmaxf(fmaxf(mx, fmaxf(sc[0], sc[1])), fmaxf(sc[2], sc[3]));
    if (c16 == 0) *(LAS f32x4*)(L + L_SC + u * 64 + r4 * 16) = sc;
  };

  if (hw < 0) {
    {
      u32x4 hk[4 * NHC];
#pragma unroll
      for (int i = 0; i < NHC; ++i) take(cw + NCW * i, i + 1 < NHC ? cw + NCW * (i + 1) : NHELD + cw, hk[4 * i], hk[4 * i + 1], hk[4 * i + 2], hk[4 * i + 3]);
      build_q();
#pragma unroll
      for (int i = 0; i < NHC; ++i) { score(cw + NCW * i, hk[4 * i], hk[4 * i + 1], hk[4 * i + 2], hk[4 * i + 3]); __builtin_amdgcn_sched_barrier(0); }
    }
#pragma unroll 1
    for (int u = NHELD + cw; u < NKU; u += NCW) {
      u32x4 k0, k1, k2, k3;
      take(u, u + NCW, k0, k1, k2, k3);
      score(u, k0, k1, k2, k3);
    }
    ctrlf[32 + cw] = xrow_max(mx);
    lds_arrive(L, C_MAX, lane);
    lds_await(ctrl, C_MAX, NCW + NHW);
    const float mg = wg_max();
    float acc[8] = {0.f, 0.f, 0.f, 0.f, 0.f, 0.f, 0.f, 0.f};
    LAS unsigned* rp = (LAS unsigned*)(L + L_REC + cw * 272 + (8 * c16 + 2 * r4) * 2);
    asm volatile("" : "+v"(rp));
    float cf = 0.f;
#pragma unroll 1
    for (int u = NKU + cw; u < NUNIT; u += NCW) {
      const f32x4 sc = *(const LAS f32x4*)(L + L_SC + (u - NKU) * 64 + r4 * 16);
      float p[4];
#pragma unroll
      for (int t = 0; t < 4; ++t) { p[t] = expd(sc[t] - mg); asm volatile("" : "+v"(p[t])); }
      if ((u & ~(NCW - 1)) == NUNIT - 32) { lds_await(ctrl, C_CF, 1); cf = ctrlf[49]; }
      u32x4 v[4];
      take(u, u + NCW, v[0], v[1], v[2], v[3]);
#pragma unroll
      for (int t = 0; t < 4; ++t)
#pragma unroll
        for (int k = 0; k < 4; ++k) { acc[2 * k] = fmaf(p[t], bflo(v[t][k]), acc[2 * k]); acc[2 * k + 1] = fmaf(p[t], bfhi(v[t][k]), acc[2 * k + 1]); }
    }
    const float e0 = swadd16(swadd32(acc[0], acc[4]), swadd32(acc[2], acc[6])), e1 = swadd16(swadd32(acc[1], acc[5]), swadd32(acc[3], acc[7]));
    *rp = pack_bf16(e0 * cf, e1 * cf);
    BARRIER();
    return;
  }

  u32x4 wb[16];
  const unsigned tstart = (unsigned)__builtin_amdgcn_readfirstlane(ctrl[8]);
  {
    const __amdgpu_buffer_rsrc_t rq = mkrsrc(wq, D * H * DH * 2);
    const int vo = ((64 * hw + 2 * r4) * (H * DH) + h * DH + 8 * c16) * 2;
#pragma unroll
    for (int i = 0; i < 16; ++i) { if (i == 0) wait_until(tstart + TQ + hw * QA); else PAUSE(QS); wb[i] = ldw(rq, vo, (8 * (i >> 1) + (i & 1)) * (H * DH * 2)); }
    lds_await(ctrl, C_X, 1);
    const LAS unsigned* xl = (const LAS unsigned*)(L + L_X + (64 * hw + 2 * r4) * 2);
    float a[8] = {0.f, 0.f, 0.f, 0.f, 0.f, 0.f, 0.f, 0.f};
#pragma unroll
    for (int i = 0; i < 8; ++i) {
      const unsigned xp = xl[4 * i];
#pragma unroll
      for (int k = 0; k < 4; ++k) {
        a[2 * k] = dot2(perm_lo(wb[2 * i][k], wb[2 * i + 1][k]), xp, a[2 * k]);
        a[2 * k + 1] = dot2(perm_hi(wb[2 * i][k], wb[2 * i + 1][k]), xp, a[2 * k + 1]);
      }
    }
    const float e0 = swadd16(swadd32(a[0], a[4]), swadd32(a[2], a[6])), e1 = swadd16(swadd32(a[1], a[5]), swadd32(a[3], a[7]));
    *(LAS f32x2*)(L + L_QP + (hw * DH + 8 * c16 + 2 * r4) * 4) = f32x2{e0, e1};
    lds_arrive(L, C_Q, lane);
    const int hb = NHC * NCW + NHH * hw;
#pragma unroll
    for (int i = 0; i < NHH; ++i) take(hb + i, i + 1 < NHH ? hb + i + 1 : NEVER, wb[4 * i], wb[4 * i + 1], wb[4 * i + 2], wb[4 * i + 3]);
    build_q();
#pragma unroll
    for (int i = 0; i < NHH; ++i) { score(hb + i, wb[4 * i], wb[4 * i + 1], wb[4 * i + 2], wb[4 * i + 3]); __builtin_amdgcn_sched_barrier(0); }
    ctrlf[32 + cw] = xrow_max(mx);
    lds_arrive(L, C_MAX, lane);
  }
  f32x4 y[4] = {{0.f, 0.f, 0.f, 0.f}, {0.f, 0.f, 0.f, 0.f}, {0.f, 0.f, 0.f, 0.f}, {0.f, 0.f, 0.f, 0.f}};
  if ((pj & 7) != 7) {
    const int sl = 7 * (pj >> 3) + (pj & 7);
    const __amdgpu_buffer_rsrc_t r1 = mkrsrc(w1, D * F * 2), r3 = mkrsrc(w3, D * F * 2), r2 = mkrsrc(w2, F * D * 2);
    const int vo = ((64 * hw + (lane & 60)) * F + FS * sl + 8 * (lane & 3)) * 2;
#pragma unroll
    for (int i = 0; i < 8; ++i) { if (i == 0) wait_until(tstart + T1 + hw * A1); else PAUSE(S1); wb[i] = ldw(i < 4 ? r1 : r3, vo, (i & 3) * (F * 2)); }
    const u32x2 xm = *(const LAS u32x2*)(L + L_X + (64 * hw + (lane & 60)) * 2);
    float a[16];
#pragma unroll
    for (int m = 0; m < 2; ++m)
#pragma unroll
      for (int k = 0; k < 4; ++k) {
        a[8 * m + 2 * k] = dot2(perm_lo(wb[4 * m + 2][k], wb[4 * m + 3][k]), xm[1], dot2(perm_lo(wb[4 * m][k], wb[4 * m + 1][k]), xm[0], 0.f));
        a[8 * m + 2 * k + 1] = dot2(perm_hi(wb[4 * m + 2][k], wb[4 * m + 3][k]), xm[1], dot2(perm_hi(wb[4 * m][k], wb[4 * m + 1][k]), xm[0], 0.f));
      }
    f32x4 e;
#pragma unroll
    for (int k = 0; k < 4; ++k) {
      float t = swadd16(swadd32(a[k], a[8 + k]), swadd32(a[4 + k], a[12 + k]));
      t += dpp<ROR8>(t); e[k] = t + dpp<ROR4>(t);
    }
    if ((lane & 12) == 0) *(LAS f32x4*)(L + L_MP + (hw * 64 + (lane & 32) + 8 * (lane & 3) + 4 * (r4 & 1)) * 4) = e;
    lds_arrive(L, C_MLP, lane);
    u32x2 wr[8];
#pragma unroll
    for (int i = 0; i < 8; ++i) { if (i == 0) wait_until(tstart + T2 + hw * A2); else PAUSE(S2); wr[i] = ldw2(r2, ((FS * sl + 8 * r4) * D + 64 * hw + 4 * c16) * 2, i * (D * 2)); }
    lds_await(ctrl, C_MLP, NHW);
    const LAS float* mp = (const LAS float*)(L + L_MP) + (lane & 31);
    float ga = 0.f, up = 0.f;
#pragma unroll
    for (int w = 0; w < NHW; ++w) { ga += mp[w * 64]; up += mp[w * 64 + 32]; }
    const float hv = ga / (1.f + __expf(-ga)) * up * (1.f - ctrlf[48]);
    const unsigned hh = pack_bf16(hv, 0.f), hl = pack_bf16(hv - bflo(hh), 0.f);
    *(LAS u16*)(L + L_H + hw * 128 + lane * 2) = (u16)(lane < 32 ? hh : hl);
    LDS_WAIT();
    u32x4 ha = *(const LAS u32x4*)(L + L_H + hw * 128 + (c16 & 1) * 64 + r4 * 16);
    if (c16 >= 2) ha = u32x4{0u, 0u, 0u, 0u};
#pragma unroll
    for (int e = 0; e < 4; ++e) {
      u32x4 f;
#pragma unroll
      for (int k = 0; k < 4; ++k) f[k] = (e & 1) ? perm_hi(wr[2 * k][e >> 1], wr[2 * k + 1][e >> 1]) : perm_lo(wr[2 * k][e >> 1], wr[2 * k + 1][e >> 1]);
      y[e] = mfma(ha, f, y[e]);
    }
  }
#pragma unroll
  for (int e = 0; e < 4; ++e) asm volatile("" : "+v"(y[e]));
  {
    const __amdgpu_buffer_rsrc_t ro = mkrsrc(wo, H * DH * D * 2);
#pragma unroll
    for (int t = 0; t < 4; ++t) {
      u32x2 wr[8];
#pragma unroll
      for (int i = 0; i < 8; ++i) { if (t + i == 0) wait_until(tstart + TO + hw * AO); else PAUSE(SO); wr[i] = ldw2(ro, ((h * DH + 32 * r4) * D + 64 * hw + 4 * c16) * 2, (8 * t + i) * (D * 2)); }
#pragma unroll
      for (int e = 0; e < 4; ++e) {
#pragma unroll
        for (int k = 0; k < 4; ++k) wb[4 * t + e][k] = (e & 1) ? perm_hi(wr[2 * k][e >> 1], wr[2 * k + 1][e >> 1]) : perm_lo(wr[2 * k][e >> 1], wr[2 * k + 1][e >> 1]);
        asm volatile("" : "+v"(wb[4 * t + e]));
      }
    }
  }
  lds_await(ctrl, C_CF, 1);
  u32x4 yt = *(const LAS u32x4*)(L + L_CTRL + 52 * 4);
  int yo = WS_Y + (b * 64 + pj) * 2048 + hw * 256 + c16 * 16;
  const LAS u32x4* rec = (const LAS u32x4*)(L + L_REC + (lane & 3) * 272 + r4 * 64);
  unsigned ym = ~YMASK;
  asm volatile("" : "+v"(yt), "+v"(yo), "+v"(rec), "+s"(ym));
  BARRIER();
  {
    const u32x4 a[4] = {rec[0], rec[1], rec[2], rec[3]};
#pragma unroll
    for (int t = 0; t < 4; ++t)
#pragma unroll
      for (int e = 0; e < 4; ++e) y[e] = mfma(a[t], wb[4 * t + e], y[e]);
    u32x4 v;
#pragma unroll
    for (int e = 0; e < 4; ++e) {
      const f32x2 t = y[e].xy + y[e].zw;
      unsigned r;
      asm("v_add_f32 %0, %1, %2\n\tv_add_u32 %0, %5, %0\n\tv_and_or_b32 %0, %0, %3, %4" : "=&v"(r) : "v"(t[0]), "v"(t[1]), "s"(ym), "v"(yt[e]), "i"((YMASK + 1u) / 2u));
      v[e] = r;
    }
    if (lane < 16) __builtin_amdgcn_raw_buffer_store_b128(v, wsr, yo, 0, SC1);
  }
}

extern "C" void kernel_launch(void* const* d_in, const int* in_sizes, int n_in, void* d_out, int out_size, void* d_ws, size_t ws_size,
                              hipStream_t stream) {
  gated_mix__mha_block_cached__glu_gated_swiglu__decode_kernel<<<256, NTHR, 0, stream>>>((const u16*)d_in[0], (const u16*)d_in[1], (const u16*)d_in[2], (const u16*)d_in[3], (const u16*)d_in[4],
                                             (const u16*)d_in[6], (const u16*)d_in[7], (const u16*)d_in[8], (const u16*)d_in[9], (u16*)d_out,
                                             (unsigned char*)d_ws);
}
```

```cpp
#include <hip/hip_runtime.h>
#include <hip/hip_bf16.h>
#define STAMPS 1
#define MWAVE 0
#define MIDX 0
#define MTYPE 0
#define RWAVE 0
#define RIDX 0
#define MSCALE 0.f

#define GAS __attribute__((address_space(1)))
#define LAS __attribute__((address_space(3)))
typedef unsigned short u16;
typedef unsigned long long u64;
using u32x4 = __attribute__((ext_vector_type(4))) unsigned;
using u32x2 = __attribute__((ext_vector_type(2))) unsigned;
using f32x4 = __attribute__((ext_vector_type(4))) float;
using f32x2 = __attribute__((ext_vector_type(2))) float;
using bf16x2 = __attribute__((ext_vector_type(2))) __bf16;
using bf16x8 = __attribute__((ext_vector_type(8))) __bf16;

constexpr int B = 4, D = 512, H = 32, SK = 8192, DH = 128, F = 1792;
constexpr int NCW = 4, NHW = 8, XWAVE = 1 + NCW + NHW, NWAVE = XWAVE + 2, NTHR = NWAVE * 64;
#define QA 112
#define QB 5
#define T1 1400
#define A1 150
#define B1 18
#define T2 2500
#define A2 40
#define B2 4
#define TO 2900
#define AO 350
#define BO 8
#define TS 68.6
#define REF0 180
#define PLAG 1
#define NOPST 0
#define NOPLD 0
#define PAUX 16
#define RAUX 16
#define PFW 0
#define TQ 100
#define NINIT 6
#define CODE_KIB 21
#define FDEL 0
#define CODEPF 1
#define YST 16
#define YLD 16
#define RED16 0
#define MASKP 0
#define PSLP 0
#define OUTSC 0
#define OUT_STORE(p, v) *(GAS unsigned*)(p) = v
#define CLR0 1
#define MLPFULL 0
#define MLPPF 0
#define TP 1200
#define AP 12
#define GRID 0
#define B2BD 0
#define B2BAUX 16
#define WOB2B 0
#define TOUCHW 1
#define W0 30
#define BARPOS 12
#define NO_WO 0
#define TLBPF 0
#define TMODE 0
#define SPW 4
#define QS 2
#define S1 6
#define S2 1
#define SO 2
#define USL 0
#define JUMP 100
#define DECAY 6
#define HOLD0 10000
#define UEND 420
#define WGAIN2 18
#define WMID2 WMID
#define FOFF 162
#define FMIN 45
#define FMAX 150
#define NRED 64
#define KEARLY 0
#define KLAST 16
#define NOTHR 0
#define WMIN0 16
#define QROW 1
#define QAUX 2
#define QXOR 0
#define HX 0
#define QLO 7
#define HAND 31
#define HOR 0
#define HQ (QLO == 7 ? ((h & HAND) | HOR) : QLO == 1 ? xg + 8 * (h & 3) : 2 * xg + (h & 1) + 16 * ((h >> 1) & 1))
#define QPFM 0
#define HBLK 1
#define HM (HBLK ? NHH : 1)
#define HS (HBLK ? 1 : NHW)
#define RMASK 0
#define SBIT 16
#define XB0 6
#define XM0 0x40000
#define XB1 1
#define XM1 0x100000
#define UMID 128
#define PAT 0
#define SLOTSH 8
#define WMID 21.5
#define WGAIN 10
#define WMIN 16
#define WMAX 28
#define CLRAUX 16
#define PCONT 1
#define XDEL 20
#define XSPR 0
#define XNT 0
#define MIMIC 1
#define NCLR 2
#define NOFLAG 0
#define CSPR 0
constexpr int UNIT = 4096;
constexpr int NSLOT = 32, RING = NSLOT * UNIT;
constexpr int NKU = (SK / 2) / 16, NUNIT = 2 * NKU;
constexpr int NHC = 6, NHH = 4, NHELD = NHC * NCW + NHH * NHW;
constexpr int FS = 32;
constexpr int L_CTRL = RING;
constexpr int L_X = L_CTRL + 256;
constexpr int L_QP = L_X + D * 2;
constexpr int L_MP = L_QP + NHW * DH * 4;
constexpr int L_REC = L_MP + NHW * 64 * 4;
constexpr int L_H = L_REC + NCW * 272;
constexpr int L_SC = L_H + NHW * 128;
constexpr int LDS_BYTES = L_SC + NKU * 64;
static_assert(LDS_BYTES <= 159 * 1024, "lds");
static_assert(WMIN <= (int)WMID && (int)WMID <= WMAX && WMAX <= 28, "window");
constexpr int WS_EPOCH = 0;
constexpr int WS_REC = 4096;
constexpr int WS_PACE = WS_REC + 256 * 128;
constexpr int WS_Y = WS_PACE + 64 * 2048;
constexpr int WS_BYTES = WS_Y + B * 64 * 2048;
constexpr u64 TAG_STEP = 0x9E3779B97F4A7C15ull;
constexpr unsigned YMASK = 0xFFFu;
constexpr int SPIN_CAP = 1 << 22;
constexpr int NEVER = 1 << 20;
constexpr int C_Q = 4, C_MAX = 5, C_MLP = 6, C_X = 7, C_CF = 9;

__device__ __forceinline__ float expd(float d) { return __builtin_amdgcn_exp2f(d * 1.4426950408889634f); }
__device__ __forceinline__ float bflo(unsigned u) { return __builtin_bit_cast(float, u << 16); }
__device__ __forceinline__ float bfhi(unsigned u) { return __builtin_bit_cast(float, u & 0xffff0000u); }
__device__ __forceinline__ float dot2(unsigned a, unsigned b, float acc) {
  return __builtin_amdgcn_fdot2_f32_bf16(__builtin_bit_cast(bf16x2, a), __builtin_bit_cast(bf16x2, b), acc, false);
}
__device__ __forceinline__ unsigned pack_bf16(float lo, float hi) {
  const bf16x2 v = {(__bf16)lo, (__bf16)hi};
  return __builtin_bit_cast(unsigned, v);
}
__device__ __forceinline__ unsigned perm_lo(unsigned a, unsigned b) { return __builtin_amdgcn_perm(b, a, 0x05040100u); }
__device__ __forceinline__ unsigned perm_hi(unsigned a, unsigned b) { return __builtin_amdgcn_perm(b, a, 0x07060302u); }
template <int CTRL> __device__ __forceinline__ float dpp(float x) {
  return __builtin_bit_cast(float, __builtin_amdgcn_mov_dpp(__builtin_bit_cast(int, x), CTRL, 0xf, 0xf, true));
}
constexpr int XOR1 = 0xB1, ROR1 = 0x121, ROR2 = 0x122, ROR4 = 0x124, ROR8 = 0x128;
__device__ __forceinline__ float row16_sum(float x) { x += dpp<ROR8>(x); x += dpp<ROR4>(x); x += dpp<ROR2>(x); x += dpp<ROR1>(x); return x; }
__device__ __forceinline__ float swadd32(float a, float b) {
  auto t = __builtin_amdgcn_permlane32_swap(__float_as_uint(a), __float_as_uint(b), false, false);
  return __uint_as_float(t[0]) + __uint_as_float(t[1]);
}
__device__ __forceinline__ float swadd16(float a, float b) {
  auto t = __builtin_amdgcn_permlane16_swap(__float_as_uint(a), __float_as_uint(b), false, false);
  return __uint_as_float(t[0]) + __uint_as_float(t[1]);
}
__device__ __forceinline__ float xrow_sum(float x) { x = swadd16(x, x); return swadd32(x, x); }
__device__ __forceinline__ float xrow_max(float x) {
  auto s = __builtin_amdgcn_permlane16_swap(__float_as_uint(x), __float_as_uint(x), false, false);
  x = fmaxf(__uint_as_float(s[0]), __uint_as_float(s[1]));
  auto t = __builtin_amdgcn_permlane32_swap(__float_as_uint(x), __float_as_uint(x), false, false);
  return fmaxf(__uint_as_float(t[0]), __uint_as_float(t[1]));
}
__device__ __forceinline__ __amdgpu_buffer_rsrc_t mkrsrc(const void* p, int bytes) { return __builtin_amdgcn_make_buffer_rsrc((void*)p, 0, bytes, 0x00020000); }
__device__ __forceinline__ u32x4 ldw(__amdgpu_buffer_rsrc_t r, int voff, int soff) { return __builtin_amdgcn_raw_buffer_load_b128(r, voff, soff, 2); }
#define LDS_WAIT() asm volatile("s_waitcnt lgkmcnt(0)" ::: "memory")
#define VM_DRAIN() asm volatile("s_waitcnt vmcnt(0)" ::: "memory")
#define CFENCE() asm volatile("" ::: "memory")
#define BARRIER() asm volatile("s_waitcnt lgkmcnt(0)\n\ts_barrier" ::: "memory")
#define PAUSE(n) do { __builtin_amdgcn_sched_barrier(0); asm volatile("s_sleep %0" :: "i"(n) : "memory"); __builtin_amdgcn_sched_barrier(0); } while (0)

__device__ __forceinline__ void lds_arrive(LAS unsigned char* L, int idx, int lane) {
  LDS_WAIT();
  if (lane == 0) __hip_atomic_fetch_add((LAS int*)(L + L_CTRL) + idx, 1, __ATOMIC_RELAXED, __HIP_MEMORY_SCOPE_WORKGROUP);
}
__device__ __forceinline__ void lds_await(volatile LAS int* ctrl, int idx, int n) {
#pragma unroll 1
  for (int spin = 0; spin < SPIN_CAP; ++spin) { if (__builtin_amdgcn_readfirstlane(ctrl[idx]) >= n) break; __builtin_amdgcn_s_sleep(1); }
  CFENCE();
}

__device__ __forceinline__ void wait_until(unsigned t) {
  __builtin_amdgcn_sched_barrier(0);
#pragma unroll 1
  for (int spin = 0; spin < 4096; ++spin) {
    const int d = (int)(t - (unsigned)__builtin_amdgcn_s_memrealtime());
    if (d <= 0) break;
    if (d > 24) __builtin_amdgcn_s_sleep(6);
  }
  CFENCE();
  __builtin_amdgcn_sched_barrier(0);
}
#define STR2(x) #x
#define STR(x) STR2(x)
constexpr int TAU2 = 2 * (int)(TS * 100.0 * 256.0 / NUNIT);
__device__ __forceinline__ f32x4 mfma(const u32x4& a, const u32x4& b, const f32x4& c) {
  return __builtin_amdgcn_mfma_f32_16x16x32_bf16(__builtin_bit_cast(bf16x8, a), __builtin_bit_cast(bf16x8, b), c, 0, 0, 0);
}
__device__ __forceinline__ u32x2 ldw2(__amdgpu_buffer_rsrc_t r, int voff, int soff) { return __builtin_amdgcn_raw_buffer_load_b64(r, voff, soff, 2); }
template <int CTRL> __device__ __forceinline__ int dppi(int x) { return __builtin_amdgcn_mov_dpp(x, CTRL, 0xf, 0xf, true); }

#define STAMP(i) do { __builtin_amdgcn_sched_barrier(0); ((volatile LAS unsigned*)(L + LDS_BYTES))[wave * 16 + (i)] = (unsigned)__builtin_amdgcn_s_memrealtime(); __builtin_amdgcn_sched_barrier(0); } while (0)
#define STAMP_FLUSH() do { LDS_WAIT(); const __amdgpu_buffer_rsrc_t dr = mkrsrc(ws + (4 << 20), 256 * 1024); \
    __builtin_amdgcn_raw_buffer_store_b128(*(volatile LAS u32x4*)(L + LDS_BYTES + lane * 16), dr, blockIdx.x * 1024 + lane * 16, 0, 16); \
    if (blockIdx.x == 0) { const unsigned t0 = __builtin_amdgcn_readfirstlane(((volatile LAS unsigned*)(L + LDS_BYTES))[0]); \
      while ((unsigned)__builtin_amdgcn_s_memrealtime() - t0 < 13000u) __builtin_amdgcn_s_sleep(8); \
      CFENCE(); float sum = 0.f, mxv = -1e9f, mnv = -1e9f; \
      for (int i = 0; i < 4; ++i) { const int wg = lane * 4 + i; \
        const unsigned ref = (RWAVE < 0) ? t0 : __builtin_amdgcn_raw_buffer_load_b32(dr, wg * 1024 + ((RWAVE < 0 ? 0 : RWAVE) * 16 + RIDX) * 4, 0, 16); \
        const float v = (float)(int)(__builtin_amdgcn_raw_buffer_load_b32(dr, wg * 1024 + (MWAVE * 16 + MIDX) * 4, 0, 16) - ref); \
        sum += v; mxv = fmaxf(mxv, v); mnv = fmaxf(mnv, -v); } \
      sum = xrow_sum(row16_sum(sum)) * (1.f / 256.f); \
      float gm = mxv; gm = fmaxf(gm, dpp<ROR8>(gm)); gm = fmaxf(gm, dpp<ROR4>(gm)); gm = fmaxf(gm, dpp<ROR2>(gm)); gm = fmaxf(gm, dpp<ROR1>(gm)); gm = xrow_max(gm); \
      float gn = mnv; gn = fmaxf(gn, dpp<ROR8>(gn)); gn = fmaxf(gn, dpp<ROR4>(gn)); gn = fmaxf(gn, dpp<ROR2>(gn)); gn = fmaxf(gn, dpp<ROR1>(gn)); gn = xrow_max(gn); \
      const float met = (MTYPE == 0 ? sum : MTYPE == 1 ? gm : MTYPE == 2 ? gm - sum : MTYPE == 3 ? sum + gn : gm + gn) * MSCALE; \
      const unsigned tgt = __builtin_amdgcn_readfirstlane((unsigned)(15000.f + met + 100.f * (float)(((u64)vc >> 12) & 511))); \
      while ((unsigned)__builtin_amdgcn_s_memrealtime() - t0 < tgt) __builtin_amdgcn_s_sleep(1); } } while (0)
#define STAMP_LDS 1024

__global__ void __launch_bounds__(NTHR, 4) gated_mix_kernel(const u16* x, const u16* kc, const u16* vc, const u16* wq, const u16* wo, const u16* w1,
                                                         const u16* w3, const u16* w2, const u16* gw, u16* out, unsigned char* ws) {
  __shared__ __attribute__((aligned(16))) unsigned char lds_raw[LDS_BYTES + STAMP_LDS];
  LAS unsigned char* L = (LAS unsigned char*)lds_raw;
  volatile LAS int* ctrl = (volatile LAS int*)(L + L_CTRL);
  volatile LAS float* ctrlf = (volatile LAS float*)(L + L_CTRL);
  u64 t_first; asm volatile("s_memrealtime %0\n\ts_waitcnt lgkmcnt(0)" : "=s"(t_first));
  __builtin_amdgcn_sched_barrier(0);
  if (threadIdx.x < 14 * 64) ((volatile LAS unsigned*)(L + LDS_BYTES))[(threadIdx.x >> 6) * 16] = (unsigned)t_first;
#define DBG(x) x
  {
    unsigned va, vb, vc2, vo, pa, nxa, t0, t1, t2, t3;
#define CODELD "global_load_lds_dwordx4 %[t0], s[56:57]\n\t"
#define ASTAMP(i) DBG("s_memrealtime s[60:61]\n\ts_waitcnt lgkmcnt(0)\n\tv_mov_b32 %[t1], %c[dbg] + 4 * " #i "\n\tv_mov_b32 %[t2], s60\n\tds_write_b32 %[t1], %[t2]\n\t")
#define WAITW "s_waitcnt (wcnt & 15) | ((wcnt >> 4) << 14) | 0x0F70\n\t"
#define SP(v, sb, wait) wait "global_load_lds_dword " v ", " sb " nt\n\tglobal_load_lds_dword " v ", " sb " sc1\n\t"
#define QPF SP("%[va]", "s[44:45]", "") SP("%[t2]", "s[44:45]", "")
#define U0 0x1000
#define U1 0x2000
#define U2 0x4000
#define UH 0
#define SB 16
#define WALK "s_and_b32 s54, s44, 255\n\ts_and_b32 s55, s54, -16\n\ts_add_u32 s54, s54, s55\n\ts_lshl_b32 s54, s54, 12\n\t"
#define SW "s_waitcnt vmcnt(%c[spw])\n\t"
#define UNIT4(v, v2) \
        WAITW "global_load_lds_dwordx4 " v ", s[40:41] nt\n\t" \
        WAITW "global_load_lds_dwordx4 " v ", s[40:41] offset:1024 nt\n\t" \
        WAITW "global_load_lds_dwordx4 " v2 ", s[40:41] offset:2048 nt\n\t" \
        WAITW "global_load_lds_dwordx4 " v2 ", s[40:41] offset:3072 nt\n\t"
    asm volatile(
        "s_getpc_b64 s[56:57]\n\t"
        "s_load_dwordx8 s[44:51], %[ka], 0x18\n\t"
        "s_load_dwordx2 s[52:53], %[ka], 0x38\n\t"
        "s_load_dwordx4 s[40:43], %[ka], 0x8\n\t"
        "v_readfirstlane_b32 s54, %[tid]\n\t"
        "s_mov_b32 m0, %c[jk]\n\t"
        "s_lshr_b32 s59, %[bid], 3\n\t"
        "s_cmp_ge_u32 s54, 64\n\t"
        "s_cbranch_scc1 90f\n\t"
        "s_min_u32 s54, s59, %c[nch]\n\t"
        "s_lshl_b32 s54, s54, 10\n\t"
        "v_lshlrev_b32 %[vo], 4, %[tid]\n\t"
        "s_and_b32 s56, s56, -128\n\t"
        "v_add_u32 %[t0], s54, %[vo]\n\t"
        "s_and_b32 s58, %[bid], 7\n\t"
        CODELD
        "s_setprio 3\n\t"
        "v_lshlrev_b32 %[t1], 7, %[tid]\n\t"
        "v_lshrrev_b32 %[va], 3, %[tid]\n\t"
        "v_lshlrev_b32 %[va], 13, %[va]\n\t"
        "v_and_b32 %[t2], " STR(QLO) ", %[tid]\n\t"
        "v_lshl_add_u32 %[va], %[t2], 7, %[va]\n\t"
        "v_and_b32 %[t2], 7 - " STR(QLO) ", %[tid]\n\t"
        "v_lshl_add_u32 %[va], %[t2], 10, %[va]\n\t"
        "s_lshl_b32 s54, s59, 17\n\t"
        "s_mul_i32 s55, s58, 128 * (" STR(QLO) " + 1)\n\t"
        "s_add_u32 s54, s54, s55\n\t"
        "v_add_u32 %[va], s54, %[va]\n\t"
        "v_add_u32 %[t2], 0x10000, %[va]\n\t"
        "s_lshl_b32 s55, s58, 5\n\t"
        "s_add_u32 s55, s55, s59\n\t"
        "s_mul_i32 s54, s55, 7 * 1024\n\t"
        "v_min_u32 %[t3], 55 * 128, %[t1]\n\t"
        "v_add_u32 %[t3], s54, %[t3]\n\t"
        "s_mul_i32 s54, s58, 448\n\t"
        "s_and_b32 s54, s54, -128\n\t"
        "s_mul_i32 s57, s59, 16 * 3584\n\t"
        "s_add_u32 s54, s54, s57\n\t"
        "v_lshrrev_b32 %[t0], 2, %[tid]\n\t"
        "v_mul_u32_u24 %[t0], 3584 - 512, %[t0]\n\t"
        "v_add3_u32 %[t0], %[t0], %[t1], s54\n\t"
        "v_lshl_add_u32 %[vb], s55, 14, %[t1]\n\t"
        "v_add_u32 %[t1], 0x2000, %[vb]\n\t"
        "s_and_b32 s54, %[bid], 0xc0\n\t"
        "s_lshl3_add_u32 s54, s58, s54\n\t"
        "s_and_b32 s56, s59, 7\n\t"
        "s_add_u32 s54, s54, s56\n\t"
        "s_lshr_b32 s56, s54, 1\n\t"
        "s_lshl_b32 s56, s56, 21\n\t"
        "s_and_b32 s66, s54, 1\n\t"
        "s_lshl_b32 s66, s66, " STR(SB) "\n\t"
        "s_bfe_u32 s57, s54, 0x10000 + 1 + " STR(XB0) "\n\t"
        "s_mul_i32 s57, s57, " STR(XM0) "\n\t"
        "s_xor_b32 s66, s66, s57\n\t"
        "s_bfe_u32 s57, s54, 0x10000 + 1 + " STR(XB1) "\n\t"
        "s_mul_i32 s57, s57, " STR(XM1) "\n\t"
        "s_xor_b32 s66, s66, s57\n\t"
        "s_waitcnt lgkmcnt(0)\n\t"
        QPF SP("%[t3]", "s[52:53]", "")
        ".if %c[spw] <= 4\n\ts_barrier\n\t.endif\n\t"
        SP("%[t0]", "s[48:49]", SW)
        ".if %c[spw] == 6\n\ts_barrier\n\t.endif\n\t"
        SP("%[t0]", "s[50:51]", SW)
        ".if %c[spw] == 8\n\ts_barrier\n\t.endif\n\t"
        SP("%[vb]", "s[46:47]", SW) SP("%[t1]", "s[46:47]", SW)
        ".if %c[spw] > 8\n\ts_barrier\n\t.endif\n\t"
        ASTAMP(6)
        "s_add_u32 s40, s40, s56\n\t"
        "s_addc_u32 s41, s41, 0\n\t"
        "s_add_u32 s42, s42, s56\n\t"
        "s_addc_u32 s43, s43, 0\n\t"
        "v_mov_b32 %[va], %[vo]\n\t"
        "v_or_b32 %[vo], s66, %[vo]\n\t"
        ".set so, 0\n\t"
        ".set wcnt, %c[spw] + " STR(KEARLY) "\n\t"
        ".rept 4\n\t"
        "s_movk_i32 m0, so\n\t"
        ".if (so & 4096) == 0\n\t"
        WAITW
        ".set wcnt, wcnt + 6\n\t"
        ".else\n\t"
        "s_nop 0\n\t"
        ".endif\n\t"
        "v_add_u32 %[vb], " STR(UH) ", %[vo]\n\t"
        "global_load_lds_dwordx4 %[vo], s[40:41] nt\n\t"
        "global_load_lds_dwordx4 %[vo], s[40:41] offset:1024 nt\n\t"
        "global_load_lds_dwordx4 %[vb], s[40:41] offset:2048 nt\n\t"
        "global_load_lds_dwordx4 %[vb], s[40:41] offset:3072 nt\n\t"
        ".if (so & 4096) == 0\n\t"
        "v_add_u32 %[vo], " STR(U0) ", %[vo]\n\t"
        ".elseif so == 4096\n\t"
        "v_add_u32 %[vo], " STR(U1) " - " STR(U0) ", %[vo]\n\t"
        ".else\n\t"
        "v_add_u32 %[vo], " STR(U2) " - " STR(U1) " - " STR(U0) ", %[vo]\n\t"
        ".endif\n\t"
        ".set so, so + 4096\n\t"
        ".endr\n\t"
        "s_waitcnt vmcnt(" STR(KLAST) ")\n\t"
        ASTAMP(7)
        "s_getpc_b64 s[50:51]\n"
        "20:\n\t"
        "s_add_u32 s50, s50, 23f-20b-%c[wmin]*128\n\t"
        "s_addc_u32 s51, s51, -1\n\t"
        "v_mov_b32 %[pa], %c[ctl]\n\t"
        "v_and_b32 %[nxa], 0x3c0 >> 2, %[vo]\n\t"
        "v_lshrrev_b32 %[nxa], 2, %[nxa]\n\t"
        "v_add_u32 %[nxa], %c[ctl] + 64, %[nxa]\n\t"
        "s_memrealtime s[58:59]\n\t"
        "s_mov_b32 s62, 0\n\t"
        "s_mov_b32 s65, " STR(WMIN0) "\n\t"
        "s_mov_b32 s63, %c[gain]\n\t"
        "s_mov_b32 s64, %c[wmid]\n\t"
        "s_mov_b32 s44, 4\n\t"
        "s_mov_b32 s45, %c[wmid] >> 8\n\t"
        "s_mov_b32 s46, 0x4000\n\t"
        "s_mov_b32 s47, 2 * %c[tau2]\n\t"
        "s_mov_b32 s48, %c[nslot]\n\t"
        "s_mov_b32 s49, %c[nku]\n\t"
        "s_waitcnt lgkmcnt(0)\n\t"
        "s_mov_b32 s59, 10000\n"
        "22:\n\t"
        "s_memrealtime s[56:57]\n\t"
        "ds_read_b32 %[t3], %[pa] offset:8\n\t"
        "s_lshl_b32 s52, s45, 7\n\t"
        "s_add_u32 s52, s50, s52\n\t"
        "s_addc_u32 s53, s51, 0\n\t"
        "s_add_i32 s54, s45, -1\n\t"
        "s_lshr_b32 s54, s54, 2\n\t"
        "s_sub_i32 s54, s44, s54\n\t"
        "v_mov_b32 %[t1], s54\n\t"
        "v_add_u32 %[t0], " STR(U0) ", %[vo]\n\t"
        "v_add_u32 %[vb], " STR(UH) ", %[vo]\n\t"
        "v_add_u32 %[vc], " STR(U0) " + " STR(UH) ", %[vo]\n\t"
        "s_add_u32 s55, s46, 0x1000\n\t"
        "s_mov_b32 m0, s46\n\t"
        "s_setpc_b64 s[52:53]\n"
        "24:\n\t"
        "s_waitcnt lgkmcnt(0)\n\t"
        "v_readfirstlane_b32 s57, %[t3]\n\t"
        "v_add_u32 %[t1], 1, %[t1]\n\t"
        "s_lshr_b32 s54, s47, 8\n\t"
        "s_sub_u32 s56, s56, s54\n\t"
        "v_mov_b32 %[t2], s56\n\t"
        "s_sub_i32 s54, s56, s58\n\t"
        "s_mov_b32 s58, s56\n\t"
        "s_sub_i32 s56, s56, s57\n\t"
        "ds_write2_b32 %[pa], %[t1], %[t2] offset1:1\n\t"
        "s_add_i32 s59, s59, s54\n\t"
        "s_add_i32 s59, s59, " STR(DECAY) "\n\t"
        "s_cmp_gt_i32 s54, %c[jump]\n\t"
        "s_cselect_b32 s59, s62, s59\n\t"
        "s_min_i32 s62, s56, s59\n\t"
        "s_mul_i32 s56, s62, s63\n\t"
        "s_add_i32 s56, s56, s64\n\t"
        "s_ashr_i32 s56, s56, 8\n\t"
        "s_max_i32 s56, s56, s65\n\t"
        "s_min_i32 s45, s56, %c[wmax]\n\t"
        "s_add_u32 s47, s47, %c[tau2]\n\t"
        "s_add_u32 s46, s46, 0x2000\n\t"
        "s_and_b32 s46, s46, %c[nslot] * 4096 - 1\n\t"
        "s_add_i32 s44, s44, 2\n\t"
        WALK
        "s_xor_b32 s54, s54, s66\n\t"
        "v_or_b32 %[vo], s54, %[va]\n\t"
        "s_cmp_lt_i32 s44, s48\n\t"
        "s_cbranch_scc1 22b\n\t"
        DBG("s_add_i32 s54, s44, -1\n\ts_lshr_b32 s54, s54, 5\n\ts_lshl_b32 s54, s54, 2\n\ts_add_u32 s54, s54, %c[dbg] + 14 * 64\n\tv_mov_b32 %[t0], s54\n\tv_mov_b32 %[t1], s45\n\t"
            "ds_write2_b32 %[t0], %[t2], %[t1] offset1:16\n\t")
        "s_cmp_lt_i32 s44, s49\n\t"
        "s_cbranch_scc1 25f\n\t"
        "s_cmp_ge_i32 s44, 2 * %c[nku]\n\t"
        "s_cbranch_scc1 30f\n\t"
        "s_mov_b32 s49, 2 * %c[nku]\n\t"
        "s_mov_b64 s[40:41], s[42:43]\n"
        "25:\n\t"
        "s_mov_b32 s65, %c[wmin]\n\t"
        "s_cmp_ge_i32 s44, %c[uend]\n\t"
        "s_cselect_b32 s63, %c[gain2], s63\n\t"
        "s_cmp_ge_i32 s44, " STR(UMID) "\n\t"
        "s_cselect_b32 s64, %c[wmid2], s64\n\t"
        "s_mov_b32 s55, %c[cap]\n"
        "26:\n\t"
        "ds_read_b32 %[t0], %[nxa]\n\t"
        "s_waitcnt lgkmcnt(0)\n\t"
        "s_nop 1\n\t"
        "v_min_i32_dpp %[t0], %[t0], %[t0] row_ror:8 row_mask:0xf bank_mask:0xf\n\t"
        "s_nop 1\n\t"
        "v_min_i32_dpp %[t0], %[t0], %[t0] row_ror:4 row_mask:0xf bank_mask:0xf\n\t"
        "s_nop 1\n\t"
        "v_min_i32_dpp %[t0], %[t0], %[t0] row_ror:2 row_mask:0xf bank_mask:0xf\n\t"
        "s_nop 1\n\t"
        "v_min_i32_dpp %[t0], %[t0], %[t0] row_ror:1 row_mask:0xf bank_mask:0xf\n\t"
        "s_nop 1\n\t"
        "v_readfirstlane_b32 s48, %[t0]\n\t"
        "s_nop 1\n\t"
        "s_add_i32 s48, s48, %c[nslot]\n\t"
        "s_and_b32 s48, s48, -2\n\t"
        "s_min_i32 s48, s48, s49\n\t"
        "s_cmp_lt_i32 s44, s48\n\t"
        "s_cbranch_scc1 22b\n\t"
        "v_mov_b32 %[t1], s44\n\t"
        "s_waitcnt vmcnt(0)\n\t"
        "ds_write_b32 %[pa], %[t1]\n\t"
        "s_sleep 1\n\t"
        "s_sub_u32 s55, s55, 1\n\t"
        "s_cmp_lg_u32 s55, 0\n\t"
        "s_cbranch_scc1 26b\n\t"
        "s_branch 22b\n"
        "30:\n\t"
        ".set k, 6\n\t"
        ".rept 7\n\t"
        ".set wcnt, 4 * k\n\t"
        "v_mov_b32 %[t1], 2 * %c[nku] - k\n\t"
        WAITW
        "ds_write_b32 %[pa], %[t1]\n\t"
        ".set k, k - 1\n\t"
        ".endr\n\t"
        DBG("s_memrealtime s[56:57]\n\ts_waitcnt lgkmcnt(0)\n\tv_mov_b32 %[t0], %c[dbg] + 4\n\tv_mov_b32 %[t1], s56\n\tds_write_b32 %[t0], %[t1]\n\t")
        "s_waitcnt lgkmcnt(0)\n\t"
        "s_endpgm\n\t"
        ".p2align 7\n"
        "23:\n\t"
        ".set wcnt, %c[wmin]-1\n\t"
        ".rept %c[wmax] - %c[wmin] + 1\n\t"
        UNIT4("%[vo]", "%[vb]")
        "ds_write_b32 %[pa], %[t1]\n\t"
        "s_mov_b32 m0, s55\n\t"
        UNIT4("%[t0]", "%[vc]")
        "s_branch 24b\n\t"
        ".p2align 7\n\t"
        ".set wcnt, wcnt+1\n\t"
        ".endr\n"
        "90:\n\t"
        "s_waitcnt lgkmcnt(0)"
        : [va] "=&v"(va), [vb] "=&v"(vb), [vc] "=&v"(vc2), [vo] "=&v"(vo), [pa] "=&v"(pa), [nxa] "=&v"(nxa), [t0] "=&v"(t0), [t1] "=&v"(t1), [t2] "=&v"(t2), [t3] "=&v"(t3)
        : [tid] "v"(threadIdx.x), [bid] "s"(blockIdx.x), [ka] "s"(__builtin_amdgcn_kernarg_segment_ptr()), [nch] "i"(CODE_KIB - 1), [jk] "i"(RING - UNIT),
          [spw] "i"(SPW), [wmin] "i"(WMIN), [wmax] "i"(WMAX), [gain] "i"(WGAIN), [wmid] "i"((int)(WMID * 256)), [tau2] "i"(TAU2),
          [ctl] "i"(L_CTRL), [nslot] "i"(NSLOT), [nku] "i"(NKU), [cap] "i"(SPIN_CAP), [dbg] "i"(LDS_BYTES), [jump] "i"(JUMP), [uend] "i"(UEND), [gain2] "i"(WGAIN2), [wmid2] "i"((int)(WMID2 * 256))
        : "memory", "scc", "s40", "s41", "s42", "s43", "s44", "s45", "s46", "s47", "s48", "s49", "s50", "s51", "s52", "s53", "s54", "s55", "s56", "s57", "s58", "s59", "s60", "s61", "s62", "s63", "s64", "s65", "s66");
  }
  __builtin_amdgcn_sched_barrier(0);
  const int tid = threadIdx.x, lane = tid & 63, wave = __builtin_amdgcn_readfirstlane(tid >> 6);
  const int xg = blockIdx.x & 7, kk = blockIdx.x >> 3;
  const int b = kk >> 3, pj = 8 * xg + (kk & 7), h = pj >> 1, s = pj & 1;
  const int r4 = lane >> 4, c16 = lane & 15;
  asm volatile("" : "+s"(x), "+s"(wq), "+s"(wo), "+s"(w1), "+s"(w3), "+s"(w2), "+s"(gw), "+s"(out), "+s"(ws));
  if (wave == 1) {
    const int wi = lane - 16;
    const int now = (int)(unsigned)__builtin_amdgcn_s_memrealtime() + REF0;
    ctrl[lane] = (lane == 1 || lane == 2) ? now : lane == 8 ? now - REF0 : (lane < 16 || lane >= 32) ? 0 : wi < NCW ? wi : wi < NCW + NHW ? NHC * NCW + HM * (wi - NCW) : NEVER;
  }
  BARRIER();

  const __amdgpu_buffer_rsrc_t wsr = mkrsrc(ws, WS_BYTES);
  auto wg_max = [&]() __attribute__((always_inline)) {
    float mg = ctrlf[32 + (c16 < NCW + NHW ? c16 : 0)];
    mg = fmaxf(mg, dpp<ROR8>(mg)); mg = fmaxf(mg, dpp<ROR4>(mg)); mg = fmaxf(mg, dpp<ROR2>(mg)); mg = fmaxf(mg, dpp<ROR1>(mg));
    return mg;
  };

  if (wave == XWAVE + 1) {
    unsigned k = ((unsigned)__builtin_amdgcn_s_memrealtime() >> SLOTSH) + 1;
#pragma unroll 1
    for (int it = 0; it < 4096 >> (SLOTSH - 2); ++it, ++k) {
      wait_until(k << SLOTSH);
      if (__builtin_amdgcn_readfirstlane(ctrl[0]) >= NUNIT - 36) break;
      const int own = __builtin_amdgcn_readfirstlane(ctrl[1]);
      constexpr unsigned SM = (8192u >> SLOTSH) - 1u;
      if (!NOPST && blockIdx.x < 16 && lane < 8) __builtin_amdgcn_raw_buffer_store_b128(u32x4{(unsigned)own, (unsigned)own, (unsigned)own, (unsigned)own}, wsr, WS_PACE + (k & SM) * 2048 + blockIdx.x * 128 + lane * 16, 0, PAUX);
      if (it < PLAG || NOPLD) continue;
      int e[16];
      asm volatile("s_load_dword %0, %16, 0x0\n\ts_load_dword %1, %16, 0x80\n\ts_load_dword %2, %16, 0x100\n\ts_load_dword %3, %16, 0x180\n\t"
                   "s_load_dword %4, %16, 0x200\n\ts_load_dword %5, %16, 0x280\n\ts_load_dword %6, %16, 0x300\n\ts_load_dword %7, %16, 0x380\n\t"
                   "s_load_dword %8, %16, 0x400\n\ts_load_dword %9, %16, 0x480\n\ts_load_dword %10, %16, 0x500\n\ts_load_dword %11, %16, 0x580\n\t"
                   "s_load_dword %12, %16, 0x600\n\ts_load_dword %13, %16, 0x680\n\ts_load_dword %14, %16, 0x700\n\ts_load_dword %15, %16, 0x780\n\t"
                   "s_waitcnt lgkmcnt(0)"
                   : "=&s"(e[0]), "=&s"(e[1]), "=&s"(e[2]), "=&s"(e[3]), "=&s"(e[4]), "=&s"(e[5]), "=&s"(e[6]), "=&s"(e[7]),
                     "=&s"(e[8]), "=&s"(e[9]), "=&s"(e[10]), "=&s"(e[11]), "=&s"(e[12]), "=&s"(e[13]), "=&s"(e[14]), "=&s"(e[15])
                   : "s"(ws + WS_PACE + ((k - PLAG) & SM) * 2048) : "memory");
      int sum = 0, cnt = 0;
#pragma unroll
      for (int i = 0; i < 16; ++i) { const int d = e[i] - own; const bool ok = (unsigned)(d + 600) < 1200u; sum += ok ? d : 0; cnt += ok ? 1 : 0; }
      if (cnt >= 8) ctrl[2] = own + (int)((float)sum / (float)cnt);
    }
    return;
  }

  if (wave == XWAVE) {
#pragma unroll 1
    for (int i = 0; i < XDEL + XSPR * (kk & 31); ++i) PAUSE(4);
    const u32x4 xv = *(const GAS u32x4*)(x + b * D + 8 * lane), gv = *(const GAS u32x4*)(gw + 8 * lane);
    *(LAS u32x4*)(L + L_X + lane * 16) = xv;
    float gd = 0.f;
#pragma unroll
    for (int k = 0; k < 4; ++k) gd = dot2(xv[k], gv[k], gd);
    gd = xrow_sum(row16_sum(gd));
    const float g = 1.f / (1.f + __expf(-gd));
    ctrlf[48] = g;
    lds_arrive(L, C_X, lane);
    u64 tag;
    asm volatile("s_load_dwordx2 %0, %1, 0x0\n\ts_waitcnt lgkmcnt(0)" : "=s"(tag) : "s"(ws + WS_EPOCH + b * 256) : "memory");
    tag += TAG_STEP;
#pragma unroll 1
    for (int spin = 0; spin < SPIN_CAP; ++spin) { if (__builtin_amdgcn_readfirstlane(ctrl[C_MAX]) >= NCW + NHW) break; __builtin_amdgcn_s_sleep(16); }
    CFENCE();
    const float mg = wg_max();
    float l = 0.f;
#pragma unroll 4
    for (int i = 0; i < NKU * 64 / 1024; ++i) {
      const f32x4 sc = *(const LAS f32x4*)(L + L_SC + i * 1024 + lane * 16);
      l += (expd(sc[0] - mg) + expd(sc[1] - mg)) + (expd(sc[2] - mg) + expd(sc[3] - mg));
    }
    l = xrow_sum(row16_sum(l));
    const unsigned tlo = (unsigned)tag, thi = (unsigned)(tag >> 32);
    if (lane < 8) __builtin_amdgcn_raw_buffer_store_b128(u32x4{__float_as_uint(mg), tlo, __float_as_uint(l), thi}, wsr, WS_REC + (b * 64 + pj) * 128 + lane * 16, 0, 16);
#pragma unroll 1
    for (int i = 0; i < 12 + (kk & 31); ++i) PAUSE(12);
    u32x4 rec;
#pragma unroll 1
    for (int spin = 0; spin < SPIN_CAP; ++spin) {
      CFENCE();
      rec = __builtin_amdgcn_raw_buffer_load_b128(wsr, WS_REC + (b * 64 + (pj ^ 1)) * 128, 0, 16);
      if (__builtin_amdgcn_ballot_w64(rec[1] != tlo || rec[3] != thi) == 0ull) break;
      __builtin_amdgcn_s_sleep(16);
    }
    const float mo = __uint_as_float(rec[0]), lo = __uint_as_float(rec[2]);
    const float mh = fmaxf(mg, mo), wt = expd(mg - mh);
    ctrlf[49] = g * wt / (wt * l + expd(mo - mh) * lo);
    const u32x4 yt = {tlo & YMASK, (tlo >> 12) & YMASK, (unsigned)(tag >> 24) & YMASK, (thi >> 4) & YMASK};
    *(LAS u32x4*)(L + L_CTRL + 52 * 4) = yt;
    lds_arrive(L, C_CF, lane);
    GAS unsigned* op = (GAS unsigned*)(out + b * D + 8 * pj + 4 * (lane & 1) + 2 * (lane >> 5));
    int off = WS_Y + (b * 64 + (lane >> 1)) * 2048 + pj * 32 + (lane & 1) * 16;
    asm volatile("" : "+v"(op), "+v"(off));
    if ((pj & (64 / NRED - 1)) != 0) return;
    STAMP(4);
    BARRIER();
    {
      const unsigned now = (unsigned)__builtin_amdgcn_s_memrealtime();
      const int d = __builtin_amdgcn_readfirstlane(ctrl[2]) + (int)(TS * 100) + FOFF - (int)now;
      const unsigned t = now + (unsigned)min(max(d, FMIN), FMAX);
#pragma unroll 1
      for (int spin = 0; spin < 4096 && (int)(t - (unsigned)__builtin_amdgcn_s_memrealtime()) > 0; ++spin) {}
    }
    STAMP(9);
    constexpr int NL = 128 / NRED;
    u32x4 yv[NL];
    int offs[NL];
#pragma unroll
    for (int i = 0; i < NL; ++i) { offs[i] = off; yv[i] = u32x4{0u, 0u, 0u, 0u}; }
#pragma unroll 1
    for (int spin = 0; spin < SPIN_CAP; ++spin) {
      CFENCE();
      unsigned bad = 0;
#pragma unroll
      for (int i = 0; i < NL; ++i) yv[i] = __builtin_amdgcn_raw_buffer_load_b128(wsr, off, i * (64 / NL) * 2048, 16);
#pragma unroll
      for (int i = 0; i < NL; ++i) bad |= ((yv[i][0] ^ yt[0]) | (yv[i][1] ^ yt[1])) | ((yv[i][2] ^ yt[2]) | (yv[i][3] ^ yt[3]));
      bad &= YMASK;
      { const u64 bal = __builtin_amdgcn_ballot_w64(bad != 0u); if (spin < 5) STAMP(10 + spin); ((volatile LAS unsigned*)(L + LDS_BYTES))[wave * 16 + 15] = spin + 1 + (bal == 1234567 ? 1 : 0); }
      if (__builtin_amdgcn_ballot_w64(bad != 0u) == 0ull) break;
      if (PSLP) __builtin_amdgcn_s_sleep(PSLP);
    }
    float o[4];
#pragma unroll
    for (int k = 0; k < 4; ++k) {
      float t[NL];
#pragma unroll
      for (int i = 0; i < NL; ++i) t[i] = __uint_as_float(yv[i][k] & ~YMASK);
#pragma unroll
      for (int st = NL / 2; st >= 1; st /= 2)
#pragma unroll
        for (int i = 0; i < st; ++i) t[i] += t[i + st];
      o[k] = t[0];
    }
    float e0 = swadd32(o[0], o[2]), e1 = swadd32(o[1], o[3]);
    e0 = swadd16(e0, e0); e1 = swadd16(e1, e1);
    e0 += dpp<ROR8>(e0); e1 += dpp<ROR8>(e1); e0 += dpp<ROR4>(e0); e1 += dpp<ROR4>(e1); e0 += dpp<ROR2>(e0); e1 += dpp<ROR2>(e1);
    if ((lane & 30) == 0) OUT_STORE(op, pack_bf16(e0, e1));
    if (pj == 0 && lane < 8) __builtin_amdgcn_raw_buffer_store_b128(u32x4{tlo, thi, tlo, thi}, wsr, WS_EPOCH + b * 256 + lane * 16, 0, 16);
    STAMP(7);
    VM_DRAIN();
    STAMP(8);
    STAMP_FLUSH();
    return;
  }

  const int cw = wave - 1, hw = wave - 1 - NCW;
  int landed = 0;
  auto wait_unit = [&](int u) __attribute__((always_inline)) {
#pragma unroll 1
    for (int spin = 0; landed <= u && spin < SPIN_CAP; ++spin) {
      landed = __builtin_amdgcn_readfirstlane(ctrl[0]);
      if (landed > u) break;
      if (USL) __builtin_amdgcn_s_sleep(USL);
    }
    CFENCE();
  };
  auto take = [&](int u, int next, u32x4& a0, u32x4& a1, u32x4& a2, u32x4& a3) __attribute__((always_inline)) {
    wait_unit(u);
    const LAS u32x4* sp = (const LAS u32x4*)(L + (u & (NSLOT - 1)) * UNIT + lane * 16);
    a0 = sp[0]; a1 = sp[64]; a2 = sp[128]; a3 = sp[192];
    LDS_WAIT();
    ctrl[16 + cw] = next;
    asm volatile("" : "+v"(a0), "+v"(a1), "+v"(a2), "+v"(a3) :: "memory");
  };
  u32x4 q;
  auto build_q = [&]() __attribute__((always_inline)) {
    lds_await(ctrl, C_Q, NHW);
    const LAS f32x4* qp = (const LAS f32x4*)(L + L_QP + (2 * r4 * DH + 8 * c16) * 4);
    const f32x4 lo = qp[0] + qp[DH / 4], hi = qp[1] + qp[DH / 4 + 1];
    q[0] = pack_bf16(xrow_sum(lo[0]), xrow_sum(lo[1])); q[1] = pack_bf16(xrow_sum(lo[2]), xrow_sum(lo[3]));
    q[2] = pack_bf16(xrow_sum(hi[0]), xrow_sum(hi[1])); q[3] = pack_bf16(xrow_sum(hi[2]), xrow_sum(hi[3]));
  };
  constexpr float SCALE = 0.088388347648318440f;
  float mx = -3.4028234e38f;
  auto score = [&](int u, const u32x4& k0, const u32x4& k1, const u32x4& k2, const u32x4& k3) __attribute__((always_inline)) {
    float d0 = 0.f, d1 = 0.f, d2 = 0.f, d3 = 0.f;
#pragma unroll
    for (int k = 0; k < 4; ++k) { d0 = dot2(k0[k], q[k], d0); d1 = dot2(k1[k], q[k], d1); d2 = dot2(k2[k], q[k], d2); d3 = dot2(k3[k], q[k], d3); }
    const unsigned s01 = pack_bf16(row16_sum(d0), row16_sum(d1)), s23 = pack_bf16(row16_sum(d2), row16_sum(d3));
    const unsigned t01 = pack_bf16(bflo(s01) * SCALE, bfhi(s01) * SCALE), t23 = pack_bf16(bflo(s23) * SCALE, bfhi(s23) * SCALE);
    const f32x4 sc = {bflo(t01), bfhi(t01), bflo(t23), bfhi(t23)};
    mx = fmaxf(fmaxf(mx, fmaxf(sc[0], sc[1])), fmaxf(sc[2], sc[3]));
    if (c16 == 0) *(LAS f32x4*)(L + L_SC + u * 64 + r4 * 16) = sc;
  };

  if (hw < 0) {
    {
      u32x4 hk[4 * NHC];
#pragma unroll
      for (int i = 0; i < NHC; ++i) take(cw + NCW * i, i + 1 < NHC ? cw + NCW * (i + 1) : NHELD + cw, hk[4 * i], hk[4 * i + 1], hk[4 * i + 2], hk[4 * i + 3]);
      STAMP(10);
      build_q();
      STAMP(3);
#pragma unroll
      for (int i = 0; i < NHC; ++i) { score(cw + NCW * i, hk[4 * i], hk[4 * i + 1], hk[4 * i + 2], hk[4 * i + 3]); __builtin_amdgcn_sched_barrier(0); }
    }
#pragma unroll 1
    for (int u = NHELD + cw; u < NKU; u += NCW) {
      u32x4 k0, k1, k2, k3;
      take(u, u + NCW, k0, k1, k2, k3);
      score(u, k0, k1, k2, k3);
    }
    ctrlf[32 + cw] = xrow_max(mx);
    lds_arrive(L, C_MAX, lane);
    STAMP(11);
    lds_await(ctrl, C_MAX, NCW + NHW);
    const float mg = wg_max();
    STAMP(12);
    float acc[8] = {0.f, 0.f, 0.f, 0.f, 0.f, 0.f, 0.f, 0.f};
    LAS unsigned* rp = (LAS unsigned*)(L + L_REC + cw * 272 + (8 * c16 + 2 * r4) * 2);
    asm volatile("" : "+v"(rp));
    float cf = 0.f;
#pragma unroll 1
    for (int u = NKU + cw; u < NUNIT; u += NCW) {
      const f32x4 sc = *(const LAS f32x4*)(L + L_SC + (u - NKU) * 64 + r4 * 16);
      float p[4];
#pragma unroll
      for (int t = 0; t < 4; ++t) { p[t] = expd(sc[t] - mg); asm volatile("" : "+v"(p[t])); }
      if ((u & ~(NCW - 1)) == NUNIT - 32) { lds_await(ctrl, C_CF, 1); cf = ctrlf[49]; }
      u32x4 v[4];
      take(u, u + NCW, v[0], v[1], v[2], v[3]);
#pragma unroll
      for (int t = 0; t < 4; ++t)
#pragma unroll
        for (int k = 0; k < 4; ++k) { acc[2 * k] = fmaf(p[t], bflo(v[t][k]), acc[2 * k]); acc[2 * k + 1] = fmaf(p[t], bfhi(v[t][k]), acc[2 * k + 1]); }
    }
    STAMP(5);
    const float e0 = swadd16(swadd32(acc[0], acc[4]), swadd32(acc[2], acc[6])), e1 = swadd16(swadd32(acc[1], acc[5]), swadd32(acc[3], acc[7]));
    *rp = pack_bf16(e0 * cf, e1 * cf);
    BARRIER();
    return;
  }

  u32x4 wb[16];
  const unsigned tstart = (unsigned)__builtin_amdgcn_readfirstlane(ctrl[8]);
  {
    const __amdgpu_buffer_rsrc_t rq = mkrsrc(wq, D * H * DH * 2);
    const int vo = ((64 * hw + 2 * r4) * (H * DH) + h * DH + 8 * c16) * 2;
#define QR(i) (8 * ((i) >> 1) + ((i) & 1))
#pragma unroll
    for (int i = 0; i < 16; ++i) { if (i == 0) wait_until(tstart + TQ + hw * QA); else PAUSE(QS); wb[i] = __builtin_amdgcn_raw_buffer_load_b128(rq, vo, QR(i) * (H * DH * 2), QAUX); }
    lds_await(ctrl, C_X, 1);
    const LAS unsigned* xl = (const LAS unsigned*)(L + L_X + (QROW == 1 ? 64 * hw + 2 * r4 : QROW == 2 ? 64 * hw + 4 * r4 : QROW == 3 ? 16 * hw + 128 * r4 : 64 * hw + 16 * r4) * 2);
    float a[8] = {0.f, 0.f, 0.f, 0.f, 0.f, 0.f, 0.f, 0.f};
#pragma unroll
    for (int i = 0; i < 8; ++i) {
      const unsigned xp = xl[QR(2 * i) / 2];
#pragma unroll
      for (int k = 0; k < 4; ++k) {
        a[2 * k] = dot2(perm_lo(wb[2 * i][k], wb[2 * i + 1][k]), xp, a[2 * k]);
        a[2 * k + 1] = dot2(perm_hi(wb[2 * i][k], wb[2 * i + 1][k]), xp, a[2 * k + 1]);
      }
    }
    const float e0 = swadd16(swadd32(a[0], a[4]), swadd32(a[2], a[6])), e1 = swadd16(swadd32(a[1], a[5]), swadd32(a[3], a[7]));
    *(LAS f32x2*)(L + L_QP + (hw * DH + 8 * c16 + 2 * r4) * 4) = f32x2{e0, e1};
    lds_arrive(L, C_Q, lane);
    STAMP(2);
    const int hb = NHC * NCW + HM * hw;
#pragma unroll
    for (int i = 0; i < NHH; ++i) take(hb + HS * i, i + 1 < NHH ? hb + HS * (i + 1) : NEVER, wb[4 * i], wb[4 * i + 1], wb[4 * i + 2], wb[4 * i + 3]);
    STAMP(10);
    build_q();
#pragma unroll
    for (int i = 0; i < NHH; ++i) { score(hb + HS * i, wb[4 * i], wb[4 * i + 1], wb[4 * i + 2], wb[4 * i + 3]); __builtin_amdgcn_sched_barrier(0); }
    ctrlf[32 + cw] = xrow_max(mx);
    lds_arrive(L, C_MAX, lane);
  }
  f32x4 y[4] = {{0.f, 0.f, 0.f, 0.f}, {0.f, 0.f, 0.f, 0.f}, {0.f, 0.f, 0.f, 0.f}, {0.f, 0.f, 0.f, 0.f}};
  if ((pj & 7) != 7) {
    const int sl = 7 * (pj >> 3) + (pj & 7);
    const __amdgpu_buffer_rsrc_t r1 = mkrsrc(w1, D * F * 2), r3 = mkrsrc(w3, D * F * 2), r2 = mkrsrc(w2, F * D * 2);
    const int vo = ((64 * hw + (lane & 60)) * F + FS * sl + 8 * (lane & 3)) * 2;
#pragma unroll
    for (int i = 0; i < 8; ++i) { if (i == 0) wait_until(tstart + T1 + hw * A1); else PAUSE(S1); wb[i] = ldw(i < 4 ? r1 : r3, vo, (i & 3) * (F * 2)); }
    const u32x2 xm = *(const LAS u32x2*)(L + L_X + (64 * hw + (lane & 60)) * 2);
    float a[16];
#pragma unroll
    for (int m = 0; m < 2; ++m)
#pragma unroll
      for (int k = 0; k < 4; ++k) {
        a[8 * m + 2 * k] = dot2(perm_lo(wb[4 * m + 2][k], wb[4 * m + 3][k]), xm[1], dot2(perm_lo(wb[4 * m][k], wb[4 * m + 1][k]), xm[0], 0.f));
        a[8 * m + 2 * k + 1] = dot2(perm_hi(wb[4 * m + 2][k], wb[4 * m + 3][k]), xm[1], dot2(perm_hi(wb[4 * m][k], wb[4 * m + 1][k]), xm[0], 0.f));
      }
    f32x4 e;
#pragma unroll
    for (int k = 0; k < 4; ++k) {
      float t = swadd16(swadd32(a[k], a[8 + k]), swadd32(a[4 + k], a[12 + k]));
      t += dpp<ROR8>(t); e[k] = t + dpp<ROR4>(t);
    }
    if ((lane & 12) == 0) *(LAS f32x4*)(L + L_MP + (hw * 64 + (lane & 32) + 8 * (lane & 3) + 4 * (r4 & 1)) * 4) = e;
    lds_arrive(L, C_MLP, lane);
    u32x2 wr[8];
#pragma unroll
    for (int i = 0; i < 8; ++i) { if (i == 0) wait_until(tstart + T2 + hw * A2); else PAUSE(S2); wr[i] = ldw2(r2, ((FS * sl + 8 * r4) * D + 64 * hw + 4 * c16) * 2, i * (D * 2)); }
    lds_await(ctrl, C_MLP, NHW);
    const LAS float* mp = (const LAS float*)(L + L_MP) + (lane & 31);
    float ga = 0.f, up = 0.f;
#pragma unroll
    for (int w = 0; w < NHW; ++w) { ga += mp[w * 64]; up += mp[w * 64 + 32]; }
    const float hv = ga / (1.f + __expf(-ga)) * up * (1.f - ctrlf[48]);
    const unsigned hh = pack_bf16(hv, 0.f), hl = pack_bf16(hv - bflo(hh), 0.f);
    *(LAS u16*)(L + L_H + hw * 128 + lane * 2) = (u16)(lane < 32 ? hh : hl);
    LDS_WAIT();
    u32x4 ha = *(const LAS u32x4*)(L + L_H + hw * 128 + (c16 & 1) * 64 + r4 * 16);
    if (c16 >= 2) ha = u32x4{0u, 0u, 0u, 0u};
#pragma unroll
    for (int e = 0; e < 4; ++e) {
      u32x4 f;
#pragma unroll
      for (int k = 0; k < 4; ++k) f[k] = (e & 1) ? perm_hi(wr[2 * k][e >> 1], wr[2 * k + 1][e >> 1]) : perm_lo(wr[2 * k][e >> 1], wr[2 * k + 1][e >> 1]);
      y[e] = mfma(ha, f, y[e]);
    }
  }
#pragma unroll
  for (int e = 0; e < 4; ++e) asm volatile("" : "+v"(y[e]));
  {
    const __amdgpu_buffer_rsrc_t ro = mkrsrc(wo, H * DH * D * 2);
#pragma unroll
    for (int t = 0; t < 4; ++t) {
      u32x2 wr[8];
#pragma unroll
      for (int i = 0; i < 8; ++i) { if (t + i == 0) wait_until(tstart + TO + hw * AO); else PAUSE(SO); wr[i] = ldw2(ro, ((h * DH + 32 * r4) * D + 64 * hw + 4 * c16) * 2, (8 * t + i) * (D * 2)); }
#pragma unroll
      for (int e = 0; e < 4; ++e) {
#pragma unroll
        for (int k = 0; k < 4; ++k) wb[4 * t + e][k] = (e & 1) ? perm_hi(wr[2 * k][e >> 1], wr[2 * k + 1][e >> 1]) : perm_lo(wr[2 * k][e >> 1], wr[2 * k + 1][e >> 1]);
        asm volatile("" : "+v"(wb[4 * t + e]));
      }
    }
  }
  lds_await(ctrl, C_CF, 1);
  u32x4 yt = *(const LAS u32x4*)(L + L_CTRL + 52 * 4);
  int yo = WS_Y + (b * 64 + pj) * 2048 + hw * 256 + c16 * 16;
  const LAS u32x4* rec = (const LAS u32x4*)(L + L_REC + (lane & 3) * 272 + r4 * 64);
  unsigned ym = ~YMASK;
  asm volatile("" : "+v"(yt), "+v"(yo), "+v"(rec), "+s"(ym));
  STAMP(4);
  BARRIER();
  STAMP(9);
  {
    const u32x4 a[4] = {rec[0], rec[1], rec[2], rec[3]};
#pragma unroll
    for (int t = 0; t < 4; ++t)
#pragma unroll
      for (int e = 0; e < 4; ++e) y[e] = mfma(a[t], wb[4 * t + e], y[e]);
    u32x4 v;
#pragma unroll
    for (int e = 0; e < 4; ++e) {
      const f32x2 t = y[e].xy + y[e].zw;
      unsigned r;
      asm("v_add_f32 %0, %1, %2\n\tv_add_u32 %0, %5, %0\n\tv_and_or_b32 %0, %0, %3, %4" : "=&v"(r) : "v"(t[0]), "v"(t[1]), "s"(ym), "v"(yt[e]), "i"((YMASK + 1u) / 2u));
      v[e] = r;
    }
    if (lane < 16) __builtin_amdgcn_raw_buffer_store_b128(v, wsr, yo, 0, 16);
  }
  STAMP(7);
}

extern "C" void kernel_launch(void* const* d_in, const int* in_sizes, int n_in, void* d_out, int out_size, void* d_ws, size_t ws_size,
                              hipStream_t stream) {
  gated_mix_kernel<<<256, NTHR, 0, stream>>>((const u16*)d_in[0], (const u16*)d_in[1], (const u16*)d_in[2], (const u16*)d_in[3], (const u16*)d_in[4],
                                             (const u16*)d_in[6], (const u16*)d_in[7], (const u16*)d_in[8], (const u16*)d_in[9], (u16*)d_out,
                                             (unsigned char*)d_ws);
}
```

```cpp
#include <hip/hip_runtime.h>
#include <hip/hip_bf16.h>

#define GAS __attribute__((address_space(1)))
#define LAS __attribute__((address_space(3)))
typedef unsigned short u16;
typedef unsigned long long u64;
using u32x4 = __attribute__((ext_vector_type(4))) unsigned;
using u32x2 = __attribute__((ext_vector_type(2))) unsigned;
using f32x4 = __attribute__((ext_vector_type(4))) float;
using f32x2 = __attribute__((ext_vector_type(2))) float;
using bf16x2 = __attribute__((ext_vector_type(2))) __bf16;
using bf16x8 = __attribute__((ext_vector_type(8))) __bf16;

constexpr int B = 4, D = 512, H = 32, SK = 8192, DH = 128, F = 1792;
constexpr int NCW = 4, NHW = 8, XWAVE = 1 + NCW + NHW, NWAVE = XWAVE + 2, NTHR = NWAVE * 64;
constexpr int TQ = 100, QA = 112, QS = 2;
constexpr int T1 = 1400, A1 = 150, S1 = 6;
constexpr int T2 = 2500, A2 = 40, S2 = 1;
constexpr int TO = 2900, AO = 350, SO = 2;
constexpr int TSTREAM = 6860;
constexpr int WMID = 5472, WMIN = 16, WMAX = 28;
constexpr int WGAIN = 10, WGAIN2 = 18, UEND = 420;
constexpr int JUMP = 100, DECAY = 6;
constexpr int REF0 = 180;
constexpr int FOFF = 156, FMIN = 45, FMAX = 150;
constexpr int CODE_BYTES = 21056;

constexpr int UNIT = 4096;
constexpr int NSLOT = 32, RING = NSLOT * UNIT;
constexpr int NKU = (SK / 2) / 16, NUNIT = 2 * NKU;
constexpr int NHC = 6, NHH = 4, NHELD = NHC * NCW + NHH * NHW;
constexpr int FS = 32;
constexpr int L_CTRL = RING;
constexpr int L_X = L_CTRL + 256;
constexpr int L_QP = L_X + D * 2;
constexpr int L_MP = L_QP + NHW * DH * 4;
constexpr int L_REC = L_MP + NHW * 64 * 4;
constexpr int L_H = L_REC + NCW * 272;
constexpr int L_SC = L_H + NHW * 128;
constexpr int LDS_BYTES = L_SC + NKU * 64;
static_assert(LDS_BYTES <= 159 * 1024, "lds");
static_assert(WMIN <= WMID / 256 && WMID / 256 <= WMAX && WMAX <= 28, "window");
constexpr int WS_EPOCH = 0;
constexpr int WS_REC = 4096;
constexpr int WS_PACE = WS_REC + 256 * 128;
constexpr int WS_Y = WS_PACE + 32 * 2048;
constexpr int WS_BYTES = WS_Y + B * 64 * 2048;
constexpr u64 TAG_STEP = 0x9E3779B97F4A7C15ull;
constexpr unsigned YMASK = 0xFFFu;
constexpr int NT = 2, SC1 = 16;
constexpr int SPIN_CAP = 1 << 22;
constexpr int NEVER = 1 << 20;
constexpr int C_Q = 4, C_MAX = 5, C_MLP = 6, C_X = 7, C_CF = 9;

__device__ __forceinline__ float expd(float d) { return __builtin_amdgcn_exp2f(d * 1.4426950408889634f); }
__device__ __forceinline__ float bflo(unsigned u) { return __builtin_bit_cast(float, u << 16); }
__device__ __forceinline__ float bfhi(unsigned u) { return __builtin_bit_cast(float, u & 0xffff0000u); }
__device__ __forceinline__ float dot2(unsigned a, unsigned b, float acc) {
  return __builtin_amdgcn_fdot2_f32_bf16(__builtin_bit_cast(bf16x2, a), __builtin_bit_cast(bf16x2, b), acc, false);
}
__device__ __forceinline__ unsigned pack_bf16(float lo, float hi) {
  const bf16x2 v = {(__bf16)lo, (__bf16)hi};
  return __builtin_bit_cast(unsigned, v);
}
__device__ __forceinline__ unsigned perm_lo(unsigned a, unsigned b) { return __builtin_amdgcn_perm(b, a, 0x05040100u); }
__device__ __forceinline__ unsigned perm_hi(unsigned a, unsigned b) { return __builtin_amdgcn_perm(b, a, 0x07060302u); }
template <int CTRL> __device__ __forceinline__ float dpp(float x) {
  return __builtin_bit_cast(float, __builtin_amdgcn_mov_dpp(__builtin_bit_cast(int, x), CTRL, 0xf, 0xf, true));
}
constexpr int ROR1 = 0x121, ROR2 = 0x122, ROR4 = 0x124, ROR8 = 0x128;
__device__ __forceinline__ float row16_sum(float x) { x += dpp<ROR8>(x); x += dpp<ROR4>(x); x += dpp<ROR2>(x); x += dpp<ROR1>(x); return x; }
__device__ __forceinline__ float swadd32(float a, float b) {
  auto t = __builtin_amdgcn_permlane32_swap(__float_as_uint(a), __float_as_uint(b), false, false);
  return __uint_as_float(t[0]) + __uint_as_float(t[1]);
}
__device__ __forceinline__ float swadd16(float a, float b) {
  auto t = __builtin_amdgcn_permlane16_swap(__float_as_uint(a), __float_as_uint(b), false, false);
  return __uint_as_float(t[0]) + __uint_as_float(t[1]);
}
__device__ __forceinline__ float xrow_sum(float x) { x = swadd16(x, x); return swadd32(x, x); }
__device__ __forceinline__ float xrow_max(float x) {
  auto s = __builtin_amdgcn_permlane16_swap(__float_as_uint(x), __float_as_uint(x), false, false);
  x = fmaxf(__uint_as_float(s[0]), __uint_as_float(s[1]));
  auto t = __builtin_amdgcn_permlane32_swap(__float_as_uint(x), __float_as_uint(x), false, false);
  return fmaxf(__uint_as_float(t[0]), __uint_as_float(t[1]));
}
__device__ __forceinline__ __amdgpu_buffer_rsrc_t mkrsrc(const void* p, int bytes) { return __builtin_amdgcn_make_buffer_rsrc((void*)p, 0, bytes, 0x00020000); }
__device__ __forceinline__ u32x4 ldw(__amdgpu_buffer_rsrc_t r, int voff, int soff) { return __builtin_amdgcn_raw_buffer_load_b128(r, voff, soff, NT); }
#define LDS_WAIT() asm volatile("s_waitcnt lgkmcnt(0)" ::: "memory")
#define CFENCE() asm volatile("" ::: "memory")
#define BARRIER() asm volatile("s_waitcnt lgkmcnt(0)\n\ts_barrier" ::: "memory")
#define PAUSE(n) do { __builtin_amdgcn_sched_barrier(0); asm volatile("s_sleep %0" :: "i"(n) : "memory"); __builtin_amdgcn_sched_barrier(0); } while (0)

__device__ __forceinline__ void lds_arrive(LAS unsigned char* L, int idx, int lane) {
  LDS_WAIT();
  if (lane == 0) __hip_atomic_fetch_add((LAS int*)(L + L_CTRL) + idx, 1, __ATOMIC_RELAXED, __HIP_MEMORY_SCOPE_WORKGROUP);
}
__device__ __forceinline__ void lds_await(volatile LAS int* ctrl, int idx, int n) {
#pragma unroll 1
  for (int spin = 0; spin < SPIN_CAP; ++spin) { if (__builtin_amdgcn_readfirstlane(ctrl[idx]) >= n) break; __builtin_amdgcn_s_sleep(1); }
  CFENCE();
}

__device__ __forceinline__ void wait_until(unsigned t) {
  __builtin_amdgcn_sched_barrier(0);
#pragma unroll 1
  for (int spin = 0; spin < 4096; ++spin) {
    const int d = (int)(t - (unsigned)__builtin_amdgcn_s_memrealtime());
    if (d <= 0) break;
    if (d > 24) __builtin_amdgcn_s_sleep(6);
  }
  CFENCE();
  __builtin_amdgcn_sched_barrier(0);
}
constexpr int TAU2 = 2 * (TSTREAM * 256 / NUNIT);
__device__ __forceinline__ f32x4 mfma(const u32x4& a, const u32x4& b, const f32x4& c) {
  return __builtin_amdgcn_mfma_f32_16x16x32_bf16(__builtin_bit_cast(bf16x8, a), __builtin_bit_cast(bf16x8, b), c, 0, 0, 0);
}
__device__ __forceinline__ u32x2 ldw2(__amdgpu_buffer_rsrc_t r, int voff, int soff) { return __builtin_amdgcn_raw_buffer_load_b64(r, voff, soff, NT); }

__global__ void __launch_bounds__(NTHR, 4) gated_mix__mha_block_cached__glu_gated_swiglu__decode_kernel(const u16* x, const u16* kc, const u16* vc, const u16* wq, const u16* wo, const u16* w1,
                                                         const u16* w3, const u16* w2, const u16* gw, u16* out, unsigned char* ws) {
  __shared__ __attribute__((aligned(16))) unsigned char lds_raw[LDS_BYTES];
  LAS unsigned char* L = (LAS unsigned char*)lds_raw;
  volatile LAS int* ctrl = (volatile LAS int*)(L + L_CTRL);
  volatile LAS float* ctrlf = (volatile LAS float*)(L + L_CTRL);
  {
    unsigned va, vb, vo, pa, nxa, t0, t1, t2, t3;
#define WAITW "s_waitcnt (wcnt & 15) | ((wcnt >> 4) << 14) | 0x0F70\n\t"
#define SP(v, sb, wait) wait "global_load_lds_dword " v ", " sb " nt\n\tglobal_load_lds_dword " v ", " sb " sc1\n\t"
#define SW "s_waitcnt vmcnt(4)\n\t"
#define UNIT4(v) \
        WAITW "global_load_lds_dwordx4 " v ", s[40:41] nt\n\t" \
        WAITW "global_load_lds_dwordx4 " v ", s[40:41] offset:1024 nt\n\t" \
        WAITW "global_load_lds_dwordx4 " v ", s[40:41] offset:2048 nt\n\t" \
        WAITW "global_load_lds_dwordx4 " v ", s[40:41] offset:3072 nt\n\t"
    asm volatile(
        "s_load_dwordx8 s[44:51], %[ka], 0x18\n\t"
        "s_load_dwordx2 s[52:53], %[ka], 0x38\n\t"
        "s_load_dwordx4 s[40:43], %[ka], 0x8\n\t"
        "v_readfirstlane_b32 s54, %[tid]\n\t"
        "s_getpc_b64 s[56:57]\n\t"
        "s_lshr_b32 s59, %[bid], 3\n\t"
        "s_cmp_ge_u32 s54, 64\n\t"
        "s_cbranch_scc1 90f\n\t"
        "s_lshl_b32 s54, s59, 6\n\t"
        "s_min_u32 s54, s54, %c[nch]\n\t"
        "v_lshlrev_b32 %[vo], 4, %[tid]\n\t"
        "s_mov_b32 m0, %c[jk]\n\t"
        "v_lshl_add_u32 %[t0], s54, 4, %[vo]\n\t"
        "s_andn2_b32 s56, s56, 63\n\t"
        "s_and_b32 s58, %[bid], 7\n\t"
        "global_load_lds_dwordx4 %[t0], s[56:57]\n\t"
        "v_lshrrev_b32 %[va], 3, %[tid]\n\t"
        "v_mul_u32_u24 %[va], 56, %[va]\n\t"
        "s_lshl_b32 s54, s59, 7\n\t"
        "s_add_u32 s54, s54, s58\n\t"
        "s_lshl_b32 s54, s54, 3\n\t"
        "v_add3_u32 %[va], %[va], %[tid], s54\n\t"
        "v_lshlrev_b32 %[va], 7, %[va]\n\t"
        "s_waitcnt lgkmcnt(0)\n\t"
        SP("%[va]", "s[44:45]", "")
        "v_add_u32 %[t2], 0x10000, %[va]\n\t"
        "s_setprio 3\n\t"
        SP("%[t2]", "s[44:45]", "")
        "v_lshlrev_b32 %[t1], 7, %[tid]\n\t"
        "s_lshl_b32 s55, s58, 5\n\t"
        "s_add_u32 s55, s55, s59\n\t"
        "s_mul_i32 s54, s55, 7 * 1024\n\t"
        "v_min_u32 %[t3], 55 * 128, %[t1]\n\t"
        "v_add_u32 %[t3], s54, %[t3]\n\t"
        SP("%[t3]", "s[52:53]", "")
        "s_mul_i32 s54, s58, 448\n\t"
        "s_and_b32 s54, s54, -128\n\t"
        "s_mul_i32 s57, s59, 16 * 3584\n\t"
        "s_add_u32 s54, s54, s57\n\t"
        "v_lshrrev_b32 %[t0], 2, %[tid]\n\t"
        "v_mul_u32_u24 %[t0], 3584 - 512, %[t0]\n\t"
        "v_add3_u32 %[t0], %[t0], %[t1], s54\n\t"
        "v_lshl_add_u32 %[vb], s55, 14, %[t1]\n\t"
        "v_add_u32 %[t1], 0x2000, %[vb]\n\t"
        "s_and_b32 s54, %[bid], 0xc0\n\t"
        "s_lshl3_add_u32 s54, s58, s54\n\t"
        "s_and_b32 s56, s59, 7\n\t"
        "s_add_u32 s54, s54, s56\n\t"
        "s_lshr_b32 s56, s54, 1\n\t"
        "s_lshl_b32 s56, s56, 21\n\t"
        "s_and_b32 s64, s54, 1\n\t"
        "s_bfe_u32 s57, s54, 0x10007\n\t"
        "s_lshl2_add_u32 s64, s57, s64\n\t"
        "s_bfe_u32 s57, s54, 0x10002\n\t"
        "s_lshl4_add_u32 s64, s57, s64\n\t"
        "s_lshl_b32 s64, s64, 16\n\t"
        SP("%[t0]", "s[48:49]", SW) SP("%[t0]", "s[50:51]", SW)
        "s_barrier\n\t"
        SP("%[vb]", "s[46:47]", SW) SP("%[t1]", "s[46:47]", SW)
        "s_add_u32 s40, s40, s56\n\t"
        "s_addc_u32 s41, s41, 0\n\t"
        "s_add_u32 s42, s42, s56\n\t"
        "s_addc_u32 s43, s43, 0\n\t"
        "v_mov_b32 %[va], %[vo]\n\t"
        "v_or_b32 %[vo], s64, %[vo]\n\t"
        ".set so, 0\n\t"
        ".set wcnt, 4\n\t"
        ".rept 4\n\t"
        "s_movk_i32 m0, so\n\t"
        ".if (so & 4096) == 0\n\t"
        WAITW
        ".set wcnt, wcnt + 6\n\t"
        ".else\n\t"
        "s_nop 0\n\t"
        ".endif\n\t"
        "global_load_lds_dwordx4 %[vo], s[40:41] nt\n\t"
        "global_load_lds_dwordx4 %[vo], s[40:41] offset:1024 nt\n\t"
        "global_load_lds_dwordx4 %[vo], s[40:41] offset:2048 nt\n\t"
        "global_load_lds_dwordx4 %[vo], s[40:41] offset:3072 nt\n\t"
        "v_add_u32 %[vo], 0x1000, %[vo]\n\t"
        ".set so, so + 4096\n\t"
        ".endr\n\t"
        "s_waitcnt vmcnt(16)\n\t"
        "s_getpc_b64 s[50:51]\n"
        "20:\n\t"
        "s_add_u32 s50, s50, 23f-20b-%c[wmin]*128\n\t"
        "s_addc_u32 s51, s51, -1\n\t"
        "v_mov_b32 %[pa], %c[ctl]\n\t"
        "v_and_b32 %[nxa], 0x3c0 >> 2, %[vo]\n\t"
        "v_lshrrev_b32 %[nxa], 2, %[nxa]\n\t"
        "v_add_u32 %[nxa], %c[ctl] + 64, %[nxa]\n\t"
        "s_memrealtime s[58:59]\n\t"
        "s_mov_b32 s62, 0\n\t"
        "s_mov_b32 s63, %c[gain]\n\t"
        "s_mov_b32 s44, 4\n\t"
        "s_mov_b32 s45, %c[wmid] >> 8\n\t"
        "s_mov_b32 s46, 0x4000\n\t"
        "s_mov_b32 s47, 2 * %c[tau2]\n\t"
        "s_mov_b32 s48, %c[nslot]\n\t"
        "s_mov_b32 s49, %c[nku]\n\t"
        "s_waitcnt lgkmcnt(0)\n\t"
        "s_mov_b32 s59, 10000\n"
        "22:\n\t"
        "s_memrealtime s[56:57]\n\t"
        "ds_read_b32 %[t3], %[pa] offset:8\n\t"
        "s_lshl_b32 s52, s45, 7\n\t"
        "s_add_u32 s52, s50, s52\n\t"
        "s_addc_u32 s53, s51, 0\n\t"
        "s_add_i32 s54, s45, -1\n\t"
        "s_lshr_b32 s54, s54, 2\n\t"
        "s_sub_i32 s54, s44, s54\n\t"
        "v_mov_b32 %[t1], s54\n\t"
        "v_add_u32 %[t0], 0x1000, %[vo]\n\t"
        "s_add_u32 s55, s46, 0x1000\n\t"
        "s_mov_b32 m0, s46\n\t"
        "s_setpc_b64 s[52:53]\n"
        "24:\n\t"
        "s_waitcnt lgkmcnt(0)\n\t"
        "v_readfirstlane_b32 s57, %[t3]\n\t"
        "v_add_u32 %[t1], 1, %[t1]\n\t"
        "s_lshr_b32 s54, s47, 8\n\t"
        "s_sub_u32 s56, s56, s54\n\t"
        "v_mov_b32 %[t2], s56\n\t"
        "s_sub_i32 s54, s56, s58\n\t"
        "s_mov_b32 s58, s56\n\t"
        "s_sub_i32 s56, s56, s57\n\t"
        "ds_write2_b32 %[pa], %[t1], %[t2] offset1:1\n\t"
        "s_add_i32 s59, s59, s54\n\t"
        "s_add_i32 s59, s59, %c[decay]\n\t"
        "s_cmp_gt_i32 s54, %c[jump]\n\t"
        "s_cselect_b32 s59, s62, s59\n\t"
        "s_min_i32 s62, s56, s59\n\t"
        "s_mul_i32 s56, s62, s63\n\t"
        "s_add_i32 s56, s56, %c[wmid]\n\t"
        "s_ashr_i32 s56, s56, 8\n\t"
        "s_max_i32 s56, s56, %c[wmin]\n\t"
        "s_min_i32 s45, s56, %c[wmax]\n\t"
        "s_add_u32 s47, s47, %c[tau2]\n\t"
        "s_add_u32 s46, s46, 0x2000\n\t"
        "s_and_b32 s46, s46, %c[nslot] * 4096 - 1\n\t"
        "s_add_i32 s44, s44, 2\n\t"
        "s_and_b32 s54, s44, 0xff\n\t"
        "s_and_b32 s55, s44, 0xf0\n\t"
        "s_add_u32 s54, s54, s55\n\t"
        "s_lshl_b32 s54, s54, 12\n\t"
        "s_xor_b32 s54, s54, s64\n\t"
        "v_or_b32 %[vo], s54, %[va]\n\t"
        "s_cmp_lt_i32 s44, s48\n\t"
        "s_cbranch_scc1 22b\n\t"
        "s_cmp_lt_i32 s44, s49\n\t"
        "s_cbranch_scc1 25f\n\t"
        "s_cmp_ge_i32 s44, 2 * %c[nku]\n\t"
        "s_cbranch_scc1 30f\n\t"
        "s_mov_b32 s49, 2 * %c[nku]\n\t"
        "s_mov_b64 s[40:41], s[42:43]\n"
        "25:\n\t"
        "s_cmp_ge_i32 s44, %c[uend]\n\t"
        "s_cselect_b32 s63, %c[gain2], s63\n\t"
        "s_mov_b32 s55, %c[cap]\n"
        "26:\n\t"
        "ds_read_b32 %[t0], %[nxa]\n\t"
        "s_waitcnt lgkmcnt(0)\n\t"
        "s_nop 1\n\t"
        "v_min_i32_dpp %[t0], %[t0], %[t0] row_ror:8 row_mask:0xf bank_mask:0xf\n\t"
        "s_nop 1\n\t"
        "v_min_i32_dpp %[t0], %[t0], %[t0] row_ror:4 row_mask:0xf bank_mask:0xf\n\t"
        "s_nop 1\n\t"
        "v_min_i32_dpp %[t0], %[t0], %[t0] row_ror:2 row_mask:0xf bank_mask:0xf\n\t"
        "s_nop 1\n\t"
        "v_min_i32_dpp %[t0], %[t0], %[t0] row_ror:1 row_mask:0xf bank_mask:0xf\n\t"
        "s_nop 1\n\t"
        "v_readfirstlane_b32 s48, %[t0]\n\t"
        "s_nop 1\n\t"
        "s_add_i32 s48, s48, %c[nslot]\n\t"
        "s_and_b32 s48, s48, -2\n\t"
        "s_min_i32 s48, s48, s49\n\t"
        "s_cmp_lt_i32 s44, s48\n\t"
        "s_cbranch_scc1 22b\n\t"
        "v_mov_b32 %[t1], s44\n\t"
        "s_waitcnt vmcnt(0)\n\t"
        "ds_write_b32 %[pa], %[t1]\n\t"
        "s_sleep 1\n\t"
        "s_sub_u32 s55, s55, 1\n\t"
        "s_cmp_lg_u32 s55, 0\n\t"
        "s_cbranch_scc1 26b\n\t"
        "s_branch 22b\n"
        "30:\n\t"
        ".set k, 6\n\t"
        ".rept 7\n\t"
        ".set wcnt, 4 * k\n\t"
        "v_mov_b32 %[t1], 2 * %c[nku] - k\n\t"
        WAITW
        "ds_write_b32 %[pa], %[t1]\n\t"
        ".set k, k - 1\n\t"
        ".endr\n\t"
        "s_waitcnt lgkmcnt(0)\n\t"
        "s_endpgm\n\t"
        ".p2align 7\n"
        "23:\n\t"
        ".set wcnt, %c[wmin]-1\n\t"
        ".rept %c[wmax] - %c[wmin] + 1\n\t"
        UNIT4("%[vo]")
        "ds_write_b32 %[pa], %[t1]\n\t"
        "s_mov_b32 m0, s55\n\t"
        UNIT4("%[t0]")
        "s_branch 24b\n\t"
        ".p2align 7\n\t"
        ".set wcnt, wcnt+1\n\t"
        ".endr\n"
        "90:\n\t"
        "s_waitcnt lgkmcnt(0)"
        : [va] "=&v"(va), [vb] "=&v"(vb), [vo] "=&v"(vo), [pa] "=&v"(pa), [nxa] "=&v"(nxa), [t0] "=&v"(t0), [t1] "=&v"(t1), [t2] "=&v"(t2), [t3] "=&v"(t3)
        : [tid] "v"(threadIdx.x), [bid] "s"(blockIdx.x), [ka] "s"(__builtin_amdgcn_kernarg_segment_ptr()), [nch] "i"((CODE_BYTES - 1024) / 16), [jk] "i"(RING - UNIT),
          [wmin] "i"(WMIN), [wmax] "i"(WMAX), [gain] "i"(WGAIN), [wmid] "i"(WMID), [tau2] "i"(TAU2),
          [ctl] "i"(L_CTRL), [nslot] "i"(NSLOT), [nku] "i"(NKU), [cap] "i"(SPIN_CAP), [jump] "i"(JUMP), [decay] "i"(DECAY), [uend] "i"(UEND), [gain2] "i"(WGAIN2)
        : "memory", "scc", "s40", "s41", "s42", "s43", "s44", "s45", "s46", "s47", "s48", "s49", "s50", "s51", "s52", "s53", "s54", "s55", "s56", "s57", "s58", "s59", "s62", "s63", "s64");
  }
  __builtin_amdgcn_sched_barrier(0);
  const int tid = threadIdx.x, lane = tid & 63, wave = __builtin_amdgcn_readfirstlane(tid >> 6);
  const int xg = blockIdx.x & 7, kk = blockIdx.x >> 3;
  const int b = kk >> 3, pj = 8 * xg + (kk & 7), h = pj >> 1;
  const int r4 = lane >> 4, c16 = lane & 15;
  asm volatile("" : "+s"(x), "+s"(wq), "+s"(wo), "+s"(w1), "+s"(w3), "+s"(w2), "+s"(gw), "+s"(out), "+s"(ws));
  if (wave == 1) {
    const int wi = lane - 16;
    const int now = (int)(unsigned)__builtin_amdgcn_s_memrealtime() + REF0;
    ctrl[lane] = (lane == 1 || lane == 2) ? now : lane == 8 ? now - REF0 : (lane < 16 || lane >= 32) ? 0 : wi < NCW ? wi : wi < NCW + NHW ? NHC * NCW + NHH * (wi - NCW) : NEVER;
  }
  BARRIER();

  const __amdgpu_buffer_rsrc_t wsr = mkrsrc(ws, WS_BYTES);
  auto wg_max = [&]() __attribute__((always_inline)) {
    float mg = ctrlf[32 + (c16 < NCW + NHW ? c16 : 0)];
    mg = fmaxf(mg, dpp<ROR8>(mg)); mg = fmaxf(mg, dpp<ROR4>(mg)); mg = fmaxf(mg, dpp<ROR2>(mg)); mg = fmaxf(mg, dpp<ROR1>(mg));
    return mg;
  };

  if (wave == XWAVE + 1) {
    unsigned k = (((unsigned)__builtin_amdgcn_s_memrealtime() + 250u) >> 9) + 1;
#pragma unroll 1
    for (int it = 0; it < 32; ++it, ++k) {
      if (__builtin_amdgcn_readfirstlane(ctrl[0]) + (int)((k << 9) - (unsigned)__builtin_amdgcn_s_memrealtime()) / 13 >= NUNIT - 32) break;
      wait_until(k << 9);
      const int own = __builtin_amdgcn_readfirstlane(ctrl[1]);
      if (blockIdx.x < 8 && lane < 8) __builtin_amdgcn_raw_buffer_store_b128(u32x4{(unsigned)own, (unsigned)own, (unsigned)own, (unsigned)own}, wsr, WS_PACE + (k & 31u) * 2048 + blockIdx.x * 128 + lane * 16, 0, SC1);
      wait_until((k << 9) + 200 + 2 * kk);
      const int d = (int)__builtin_amdgcn_raw_buffer_load_b32(wsr, WS_PACE + (k & 31u) * 2048 + (lane & 7) * 128, 0, SC1) - own;
      const bool ok = (unsigned)(d + 600) < 1200u;
      const float sum = row16_sum(ok ? (float)d : 0.f), cnt = row16_sum(ok ? 1.f : 0.f);
      if (cnt >= 8.f) ctrl[2] = own + (int)(sum / cnt);
    }
    return;
  }

  if (wave == XWAVE) {
#pragma unroll 1
    for (int i = 0; i < 20; ++i) PAUSE(4);
    const u32x4 xv = *(const GAS u32x4*)(x + b * D + 8 * lane), gv = *(const GAS u32x4*)(gw + 8 * lane);
    *(LAS u32x4*)(L + L_X + lane * 16) = xv;
    float gd = 0.f;
#pragma unroll
    for (int k = 0; k < 4; ++k) gd = dot2(xv[k], gv[k], gd);
    gd = xrow_sum(row16_sum(gd));
    const float g = 1.f / (1.f + __expf(-gd));
    ctrlf[48] = g;
    lds_arrive(L, C_X, lane);
#pragma unroll 1
    for (int i = 0; i < 16 + kk; ++i) PAUSE(20);
    const u32x2 ep = __builtin_amdgcn_raw_buffer_load_b64(wsr, WS_EPOCH + b * 256, 0, SC1);
    const u64 tag = (((u64)(unsigned)__builtin_amdgcn_readfirstlane(ep[1]) << 32) | (unsigned)__builtin_amdgcn_readfirstlane(ep[0])) + TAG_STEP;
#pragma unroll 1
    for (int spin = 0; spin < SPIN_CAP; ++spin) { if (__builtin_amdgcn_readfirstlane(ctrl[C_MAX]) >= NCW + NHW) break; __builtin_amdgcn_s_sleep(16); }
    CFENCE();
    const float mg = wg_max();
    float l = 0.f;
#pragma unroll 4
    for (int i = 0; i < NKU * 64 / 1024; ++i) {
      const f32x4 sc = *(const LAS f32x4*)(L + L_SC + i * 1024 + lane * 16);
      l += (expd(sc[0] - mg) + expd(sc[1] - mg)) + (expd(sc[2] - mg) + expd(sc[3] - mg));
    }
    l = xrow_sum(row16_sum(l));
    const unsigned tlo = (unsigned)tag, thi = (unsigned)(tag >> 32);
    if (lane < 8) __builtin_amdgcn_raw_buffer_store_b128(u32x4{__float_as_uint(mg), tlo, __float_as_uint(l), thi}, wsr, WS_REC + (b * 64 + pj) * 128 + lane * 16, 0, SC1);
#pragma unroll 1
    for (int i = 0; i < 12 + (kk & 31); ++i) PAUSE(12);
    u32x4 rec;
#pragma unroll 1
    for (int spin = 0; spin < SPIN_CAP; ++spin) {
      CFENCE();
      rec = __builtin_amdgcn_raw_buffer_load_b128(wsr, WS_REC + (b * 64 + (pj ^ 1)) * 128, 0, SC1);
      if (__builtin_amdgcn_ballot_w64(rec[1] != tlo || rec[3] != thi) == 0ull) break;
      __builtin_amdgcn_s_sleep(16);
    }
    const float mo = __uint_as_float(rec[0]), lo = __uint_as_float(rec[2]);
    const float mh = fmaxf(mg, mo), wt = expd(mg - mh);
    ctrlf[49] = g * wt / (wt * l + expd(mo - mh) * lo);
    const u32x4 yt = {tlo & YMASK, (tlo >> 12) & YMASK, (unsigned)(tag >> 24) & YMASK, (thi >> 4) & YMASK};
    *(LAS u32x4*)(L + L_CTRL + 52 * 4) = yt;
    lds_arrive(L, C_CF, lane);
    GAS unsigned* op = (GAS unsigned*)(out + b * D + 8 * pj + 4 * (lane & 1) + 2 * (lane >> 5));
    int off = WS_Y + (b * 64 + (lane >> 1)) * 2048 + pj * 32 + (lane & 1) * 16;
    asm volatile("" : "+v"(op), "+v"(off));
    BARRIER();
    {
      const unsigned now = (unsigned)__builtin_amdgcn_s_memrealtime();
      const int d = __builtin_amdgcn_readfirstlane(ctrl[2]) + TSTREAM + FOFF - (int)now;
      const unsigned t = now + (unsigned)min(max(d, FMIN), FMAX);
#pragma unroll 1
      for (int spin = 0; spin < 4096 && (int)(t - (unsigned)__builtin_amdgcn_s_memrealtime()) > 0; ++spin) {}
    }
    u32x4 ya, yb;
#pragma unroll 1
    for (int spin = 0; spin < SPIN_CAP; ++spin) {
      CFENCE();
      ya = __builtin_amdgcn_raw_buffer_load_b128(wsr, off, 0, SC1);
      yb = __builtin_amdgcn_raw_buffer_load_b128(wsr, off, 32 * 2048, SC1);
      const unsigned bad = (((ya[0] ^ yt[0]) | (ya[1] ^ yt[1])) | ((ya[2] ^ yt[2]) | (ya[3] ^ yt[3])) | ((yb[0] ^ yt[0]) | (yb[1] ^ yt[1])) | ((yb[2] ^ yt[2]) | (yb[3] ^ yt[3]))) & YMASK;
      if (__builtin_amdgcn_ballot_w64(bad != 0u) == 0ull) break;
    }
    float o[4];
#pragma unroll
    for (int k = 0; k < 4; ++k) o[k] = __uint_as_float(ya[k] & ~YMASK) + __uint_as_float(yb[k] & ~YMASK);
    float e0 = swadd32(o[0], o[2]), e1 = swadd32(o[1], o[3]);
    e0 = swadd16(e0, e0); e1 = swadd16(e1, e1);
    e0 += dpp<ROR8>(e0); e1 += dpp<ROR8>(e1); e0 += dpp<ROR4>(e0); e1 += dpp<ROR4>(e1); e0 += dpp<ROR2>(e0); e1 += dpp<ROR2>(e1);
    if ((lane & 30) == 0) *op = pack_bf16(e0, e1);
    if (pj == 0 && lane < 8) __builtin_amdgcn_raw_buffer_store_b128(u32x4{tlo, thi, tlo, thi}, wsr, WS_EPOCH + b * 256 + lane * 16, 0, SC1);
    return;
  }

  const int cw = wave - 1, hw = wave - 1 - NCW;
  int landed = 0;
  auto wait_unit = [&](int u) __attribute__((always_inline)) {
#pragma unroll 1
    for (int spin = 0; landed <= u && spin < SPIN_CAP; ++spin) {
      landed = __builtin_amdgcn_readfirstlane(ctrl[0]);
    }
    CFENCE();
  };
  auto take = [&](int u, int next, u32x4& a0, u32x4& a1, u32x4& a2, u32x4& a3) __attribute__((always_inline)) {
    wait_unit(u);
    const LAS u32x4* sp = (const LAS u32x4*)(L + (u & (NSLOT - 1)) * UNIT + lane * 16);
    a0 = sp[0]; a1 = sp[64]; a2 = sp[128]; a3 = sp[192];
    LDS_WAIT();
    ctrl[16 + cw] = next;
    asm volatile("" : "+v"(a0), "+v"(a1), "+v"(a2), "+v"(a3) :: "memory");
  };
  u32x4 q;
  auto build_q = [&]() __attribute__((always_inline)) {
    lds_await(ctrl, C_Q, NHW);
    const LAS f32x4* qp = (const LAS f32x4*)(L + L_QP + (2 * r4 * DH + 8 * c16) * 4);
    const f32x4 lo = qp[0] + qp[DH / 4], hi = qp[1] + qp[DH / 4 + 1];
    q[0] = pack_bf16(xrow_sum(lo[0]), xrow_sum(lo[1])); q[1] = pack_bf16(xrow_sum(lo[2]), xrow_sum(lo[3]));
    q[2] = pack_bf16(xrow_sum(hi[0]), xrow_sum(hi[1])); q[3] = pack_bf16(xrow_sum(hi[2]), xrow_sum(hi[3]));
  };
  constexpr float SCALE = 0.088388347648318440f;
  float mx = -3.4028234e38f;
  auto score = [&](int u, const u32x4& k0, const u32x4& k1, const u32x4& k2, const u32x4& k3) __attribute__((always_inline)) {
    float d0 = 0.f, d1 = 0.f, d2 = 0.f, d3 = 0.f;
#pragma unroll
    for (int k = 0; k < 4; ++k) { d0 = dot2(k0[k], q[k], d0); d1 = dot2(k1[k], q[k], d1); d2 = dot2(k2[k], q[k], d2); d3 = dot2(k3[k], q[k], d3); }
    const unsigned s01 = pack_bf16(row16_sum(d0), row16_sum(d1)), s23 = pack_bf16(row16_sum(d2), row16_sum(d3));
    const unsigned t01 = pack_bf16(bflo(s01) * SCALE, bfhi(s01) * SCALE), t23 = pack_bf16(bflo(s23) * SCALE, bfhi(s23) * SCALE);
    const f32x4 sc = {bflo(t01), bfhi(t01), bflo(t23), bfhi(t23)};
    mx = fmaxf(fmaxf(mx, fmaxf(sc[0], sc[1])), fmaxf(sc[2], sc[3]));
    if (c16 == 0) *(LAS f32x4*)(L + L_SC + u * 64 + r4 * 16) = sc;
  };

  if (hw < 0) {
    {
      u32x4 hk[4 * NHC];
#pragma unroll
      for (int i = 0; i < NHC; ++i) take(cw + NCW * i, i + 1 < NHC ? cw + NCW * (i + 1) : NHELD + cw, hk[4 * i], hk[4 * i + 1], hk[4 * i + 2], hk[4 * i + 3]);
      build_q();
#pragma unroll
      for (int i = 0; i < NHC; ++i) { score(cw + NCW * i, hk[4 * i], hk[4 * i + 1], hk[4 * i + 2], hk[4 * i + 3]); __builtin_amdgcn_sched_barrier(0); }
    }
#pragma unroll 1
    for (int u = NHELD + cw; u < NKU; u += NCW) {
      u32x4 k0, k1, k2, k3;
      take(u, u + NCW, k0, k1, k2, k3);
      score(u, k0, k1, k2, k3);
    }
    ctrlf[32 + cw] = xrow_max(mx);
    lds_arrive(L, C_MAX, lane);
    lds_await(ctrl, C_MAX, NCW + NHW);
    const float mg = wg_max();
    float acc[8] = {0.f, 0.f, 0.f, 0.f, 0.f, 0.f, 0.f, 0.f};
    LAS unsigned* rp = (LAS unsigned*)(L + L_REC + cw * 272 + (8 * c16 + 2 * r4) * 2);
    asm volatile("" : "+v"(rp));
    float cf = 0.f;
#pragma unroll 1
    for (int u = NKU + cw; u < NUNIT; u += NCW) {
      const f32x4 sc = *(const LAS f32x4*)(L + L_SC + (u - NKU) * 64 + r4 * 16);
      float p[4];
#pragma unroll
      for (int t = 0; t < 4; ++t) { p[t] = expd(sc[t] - mg); asm volatile("" : "+v"(p[t])); }
      if ((u & ~(NCW - 1)) == NUNIT - 32) { lds_await(ctrl, C_CF, 1); cf = ctrlf[49]; }
      u32x4 v[4];
      take(u, u + NCW, v[0], v[1], v[2], v[3]);
#pragma unroll
      for (int t = 0; t < 4; ++t)
#pragma unroll
        for (int k = 0; k < 4; ++k) { acc[2 * k] = fmaf(p[t], bflo(v[t][k]), acc[2 * k]); acc[2 * k + 1] = fmaf(p[t], bfhi(v[t][k]), acc[2 * k + 1]); }
    }
    const float e0 = swadd16(swadd32(acc[0], acc[4]), swadd32(acc[2], acc[6])), e1 = swadd16(swadd32(acc[1], acc[5]), swadd32(acc[3], acc[7]));
    *rp = pack_bf16(e0 * cf, e1 * cf);
    BARRIER();
    return;
  }

  u32x4 wb[16];
  const unsigned tstart = (unsigned)__builtin_amdgcn_readfirstlane(ctrl[8]);
  {
    const __amdgpu_buffer_rsrc_t rq = mkrsrc(wq, D * H * DH * 2);
    const int vo = ((64 * hw + 2 * r4) * (H * DH) + h * DH + 8 * c16) * 2;
#pragma unroll
    for (int i = 0; i < 16; ++i) { if (i == 0) wait_until(tstart + TQ + hw * QA); else PAUSE(QS); wb[i] = ldw(rq, vo, (8 * (i >> 1) + (i & 1)) * (H * DH * 2)); }
    lds_await(ctrl, C_X, 1);
    const LAS unsigned* xl = (const LAS unsigned*)(L + L_X + (64 * hw + 2 * r4) * 2);
    float a[8] = {0.f, 0.f, 0.f, 0.f, 0.f, 0.f, 0.f, 0.f};
#pragma unroll
    for (int i = 0; i < 8; ++i) {
      const unsigned xp = xl[4 * i];
#pragma unroll
      for (int k = 0; k < 4; ++k) {
        a[2 * k] = dot2(perm_lo(wb[2 * i][k], wb[2 * i + 1][k]), xp, a[2 * k]);
        a[2 * k + 1] = dot2(perm_hi(wb[2 * i][k], wb[2 * i + 1][k]), xp, a[2 * k + 1]);
      }
    }
    const float e0 = swadd16(swadd32(a[0], a[4]), swadd32(a[2], a[6])), e1 = swadd16(swadd32(a[1], a[5]), swadd32(a[3], a[7]));
    *(LAS f32x2*)(L + L_QP + (hw * DH + 8 * c16 + 2 * r4) * 4) = f32x2{e0, e1};
    lds_arrive(L, C_Q, lane);
    const int hb = NHC * NCW + NHH * hw;
#pragma unroll
    for (int i = 0; i < NHH; ++i) take(hb + i, i + 1 < NHH ? hb + i + 1 : NEVER, wb[4 * i], wb[4 * i + 1], wb[4 * i + 2], wb[4 * i + 3]);
    build_q();
#pragma unroll
    for (int i = 0; i < NHH; ++i) { score(hb + i, wb[4 * i], wb[4 * i + 1], wb[4 * i + 2], wb[4 * i + 3]); __builtin_amdgcn_sched_barrier(0); }
    ctrlf[32 + cw] = xrow_max(mx);
    lds_arrive(L, C_MAX, lane);
  }
  f32x4 y[4] = {{0.f, 0.f, 0.f, 0.f}, {0.f, 0.f, 0.f, 0.f}, {0.f, 0.f, 0.f, 0.f}, {0.f, 0.f, 0.f, 0.f}};
  if ((pj & 7) != 7) {
    const int sl = 7 * (pj >> 3) + (pj & 7);
    const __amdgpu_buffer_rsrc_t r1 = mkrsrc(w1, D * F * 2), r3 = mkrsrc(w3, D * F * 2), r2 = mkrsrc(w2, F * D * 2);
    const int vo = ((64 * hw + (lane & 60)) * F + FS * sl + 8 * (lane & 3)) * 2;
#pragma unroll
    for (int i = 0; i < 8; ++i) { if (i == 0) wait_until(tstart + T1 + hw * A1); else PAUSE(S1); wb[i] = ldw(i < 4 ? r1 : r3, vo, (i & 3) * (F * 2)); }
    const u32x2 xm = *(const LAS u32x2*)(L + L_X + (64 * hw + (lane & 60)) * 2);
    float a[16];
#pragma unroll
    for (int m = 0; m < 2; ++m)
#pragma unroll
      for (int k = 0; k < 4; ++k) {
        a[8 * m + 2 * k] = dot2(perm_lo(wb[4 * m + 2][k], wb[4 * m + 3][k]), xm[1], dot2(perm_lo(wb[4 * m][k], wb[4 * m + 1][k]), xm[0], 0.f));
        a[8 * m + 2 * k + 1] = dot2(perm_hi(wb[4 * m + 2][k], wb[4 * m + 3][k]), xm[1], dot2(perm_hi(wb[4 * m][k], wb[4 * m + 1][k]), xm[0], 0.f));
      }
    f32x4 e;
#pragma unroll
    for (int k = 0; k < 4; ++k) {
      float t = swadd16(swadd32(a[k], a[8 + k]), swadd32(a[4 + k], a[12 + k]));
      t += dpp<ROR8>(t); e[k] = t + dpp<ROR4>(t);
    }
    if ((lane & 12) == 0) *(LAS f32x4*)(L + L_MP + (hw * 64 + (lane & 32) + 8 * (lane & 3) + 4 * (r4 & 1)) * 4) = e;
    lds_arrive(L, C_MLP, lane);
    u32x2 wr[8];
#pragma unroll
    for (int i = 0; i < 8; ++i) { if (i == 0) wait_until(tstart + T2 + hw * A2); else PAUSE(S2); wr[i] = ldw2(r2, ((FS * sl + 8 * r4) * D + 64 * hw + 4 * c16) * 2, i * (D * 2)); }
    lds_await(ctrl, C_MLP, NHW);
    const LAS float* mp = (const LAS float*)(L + L_MP) + (lane & 31);
    float ga = 0.f, up = 0.f;
#pragma unroll
    for (int w = 0; w < NHW; ++w) { ga += mp[w * 64]; up += mp[w * 64 + 32]; }
    const float hv = ga / (1.f + __expf(-ga)) * up * (1.f - ctrlf[48]);
    const unsigned hh = pack_bf16(hv, 0.f), hl = pack_bf16(hv - bflo(hh), 0.f);
    *(LAS u16*)(L + L_H + hw * 128 + lane * 2) = (u16)(lane < 32 ? hh : hl);
    LDS_WAIT();
    u32x4 ha = *(const LAS u32x4*)(L + L_H + hw * 128 + (c16 & 1) * 64 + r4 * 16);
    if (c16 >= 2) ha = u32x4{0u, 0u, 0u, 0u};
#pragma unroll
    for (int e = 0; e < 4; ++e) {
      u32x4 f;
#pragma unroll
      for (int k = 0; k < 4; ++k) f[k] = (e & 1) ? perm_hi(wr[2 * k][e >> 1], wr[2 * k + 1][e >> 1]) : perm_lo(wr[2 * k][e >> 1], wr[2 * k + 1][e >> 1]);
      y[e] = mfma(ha, f, y[e]);
    }
  }
#pragma unroll
  for (int e = 0; e < 4; ++e) asm volatile("" : "+v"(y[e]));
  {
    const __amdgpu_buffer_rsrc_t ro = mkrsrc(wo, H * DH * D * 2);
#pragma unroll
    for (int t = 0; t < 4; ++t) {
      u32x2 wr[8];
#pragma unroll
      for (int i = 0; i < 8; ++i) { if (t + i == 0) wait_until(tstart + TO + hw * AO); else PAUSE(SO); wr[i] = ldw2(ro, ((h * DH + 32 * r4) * D + 64 * hw + 4 * c16) * 2, (8 * t + i) * (D * 2)); }
#pragma unroll
      for (int e = 0; e < 4; ++e) {
#pragma unroll
        for (int k = 0; k < 4; ++k) wb[4 * t + e][k] = (e & 1) ? perm_hi(wr[2 * k][e >> 1], wr[2 * k + 1][e >> 1]) : perm_lo(wr[2 * k][e >> 1], wr[2 * k + 1][e >> 1]);
        asm volatile("" : "+v"(wb[4 * t + e]));
      }
    }
  }
  lds_await(ctrl, C_CF, 1);
  u32x4 yt = *(const LAS u32x4*)(L + L_CTRL + 52 * 4);
  int yo = WS_Y + (b * 64 + pj) * 2048 + hw * 256 + c16 * 16;
  const LAS u32x4* rec = (const LAS u32x4*)(L + L_REC + (lane & 3) * 272 + r4 * 64);
  unsigned ym = ~YMASK;
  asm volatile("" : "+v"(yt), "+v"(yo), "+v"(rec), "+s"(ym));
  BARRIER();
  {
    const u32x4 a[4] = {rec[0], rec[1], rec[2], rec[3]};
#pragma unroll
    for (int t = 0; t < 4; ++t)
#pragma unroll
      for (int e = 0; e < 4; ++e) y[e] = mfma(a[t], wb[4 * t + e], y[e]);
    u32x4 v;
#pragma unroll
    for (int e = 0; e < 4; ++e) {
      const f32x2 t = y[e].xy + y[e].zw;
      unsigned r;
      asm("v_add_f32 %0, %1, %2\n\tv_add_u32 %0, %5, %0\n\tv_and_or_b32 %0, %0, %3, %4" : "=&v"(r) : "v"(t[0]), "v"(t[1]), "s"(ym), "v"(yt[e]), "i"((YMASK + 1u) / 2u));
      v[e] = r;
    }
    if (lane < 16) __builtin_amdgcn_raw_buffer_store_b128(v, wsr, yo, 0, SC1);
  }
}

extern "C" void kernel_launch(void* const* d_in, const int* in_sizes, int n_in, void* d_out, int out_size, void* d_ws, size_t ws_size,
                              hipStream_t stream) {
  gated_mix__mha_block_cached__glu_gated_swiglu__decode_kernel<<<256, NTHR, 0, stream>>>((const u16*)d_in[0], (const u16*)d_in[1], (const u16*)d_in[2], (const u16*)d_in[3], (const u16*)d_in[4],
                                             (const u16*)d_in[6], (const u16*)d_in[7], (const u16*)d_in[8], (const u16*)d_in[9], (u16*)d_out,
                                             (unsigned char*)d_ws);
}
```
